```python
import torch

def _softmax(x, axis=-1):
    return torch.softmax(x, dim=axis)

def _gelu(x):
    return torch.nn.functional.gelu(x, approximate='tanh')

def _silu(x):
    return torch.nn.functional.silu(x)

def _sigmoid(x):
    return torch.sigmoid(x)

def _layernorm(x, axis=-1, eps=1e-05):
    m = x.mean(dim=axis, keepdim=True)
    v = x.var(dim=axis, keepdim=True, unbiased=False)
    return (x - m) / torch.sqrt(v + eps)

def _scatter_add(tgt, idx, src, axis=0):
    return tgt.clone().index_add_(axis, idx, src)

def _segment_sum(src, seg, n):
    out = torch.zeros((n,) + src.shape[1:], dtype=src.dtype)
    return out.index_add_(0, seg, src)

def _pad(x, pw):
    flat = tuple((v for pair in reversed(pw) for v in pair))
    return torch.nn.functional.pad(x, flat)
kernel_name = 'sequential_preLN__spectral_gfnet__mixer_gmlp__prompt'

def setup_inputs():

    _w = lambda a: (lambda t: t.to(torch.bfloat16) if t.is_floating_point() else t)(torch.as_tensor(a))

    def _A_setup():
        B, S, D = (1, 2048, 640)
        Df = D // 2 + 1
        return {'x': _w((lambda *s: torch.randn(*s, dtype=torch.bfloat16) * 0.05)(B, S, D)), 'w_r': _w((lambda *s: torch.randn(*s, dtype=torch.bfloat16) * 0.4)(S, Df) * 0.02), 'w_i': _w((lambda *s: torch.randn(*s, dtype=torch.bfloat16) * 0.4)(S, Df) * 0.02)}
    _A_in = _A_setup()

    def _B_setup():
        B, S, D, F = (1, 2048, 640, 1728)
        Fh = F // 2
        return {'x': _w((lambda *s: torch.randn(*s, dtype=torch.bfloat16) * 0.05)(B, S, D)), 'w_in': _w((lambda *s: torch.randn(*s, dtype=torch.bfloat16) * 0.4)(D, 2 * Fh)), 'w_sp': _w(0.01 * (lambda *s: torch.randn(*s, dtype=torch.bfloat16) * 0.4)(S, S)), 'b_sp': _w(1.0 + 0.01 * (lambda *s: torch.randn(*s, dtype=torch.bfloat16) * 0.05)(S)), 'w_out': _w((lambda *s: torch.randn(*s, dtype=torch.bfloat16) * 0.05)(Fh, D))}
    _B_in = _B_setup()
    return {'A_x': _A_in['x'], 'A_w_r': _A_in['w_r'], 'A_w_i': _A_in['w_i'], 'B_x': _B_in['x'], 'B_w_in': _B_in['w_in'], 'B_w_sp': _B_in['w_sp'], 'B_b_sp': _B_in['b_sp'], 'B_w_out': _B_in['w_out']}

def reference(A_x, A_w_r, A_w_i, B_x, B_w_in, B_w_sp, B_b_sp, B_w_out):

    def _A_ref(x, w_r, w_i):
        B, S, D = x.shape
        X = (lambda x, axes=(-2, -1): torch.fft.rfft2(x.float(), dim=axes))(x, axes=(-2, -1))
        W = w_r + 1j * w_i
        X = X * W
        y = (lambda x, s, axes=(-2, -1): torch.fft.irfft2(x, s=s, dim=axes).to(torch.bfloat16))(X, (S, D), axes=(-2, -1))
        return y

    def _B_ref(x, w_in, w_sp, b_sp, w_out):
        B, S, D = x.shape
        F = w_in.shape[-1]
        Fh = F // 2
        z = _gelu(torch.einsum('bsd,df->bsf', x, w_in))
        z1 = z[..., :Fh].clone()
        z2 = _layernorm(z[..., Fh:], axis=-1)
        g = torch.einsum('ts,bsf->btf', w_sp, z2) + b_sp[None, :, None]
        return torch.einsum('bsf,fd->bsd', z1 * g, w_out)
    x = A_x
    h = x + _A_ref(_layernorm(x, axis=-1), A_w_r, A_w_i)
    h2 = h + _B_ref(_layernorm(h, axis=-1), B_w_in, B_w_sp, B_b_sp, B_w_out)
    return h2
```

```cpp
#include <hip/hip_runtime.h>
#include <hip/hip_bf16.h>
#include <stdint.h>

typedef unsigned short u16;
__device__ __forceinline__ float bf2f(u16 v){ return __uint_as_float(((unsigned)v)<<16); }
__device__ __forceinline__ u16 f2bf(float f){ __hip_bfloat16 b = __float2bfloat16(f); return *reinterpret_cast<u16*>(&b); }
__device__ __forceinline__ float rbf(float f){ return bf2f(f2bf(f)); }

constexpr int S=2048, D=640, DF=321, F=1728, FH=864;

__device__ float block_sum(float v, float* sh){
  for(int o=32;o>0;o>>=1) v += __shfl_xor(v,o);
  int w=threadIdx.x>>6;
  __syncthreads();
  if((threadIdx.x&63)==0) sh[w]=v;
  __syncthreads();
  float r=0; for(int i=0;i<(int)(blockDim.x>>6);++i) r+=sh[i];
  return r;
}
__global__ void k_ln(const u16* in, int ld, int n, u16* out, int ldo){
  __shared__ float sh[8];
  int r=blockIdx.x;
  float s=0; for(int i=threadIdx.x;i<n;i+=blockDim.x) s+=bf2f(in[(size_t)r*ld+i]);
  float mean=block_sum(s,sh)/n;
  float q=0; for(int i=threadIdx.x;i<n;i+=blockDim.x){ float d=bf2f(in[(size_t)r*ld+i])-mean; q+=d*d; }
  float var=block_sum(q,sh)/n;
  float mb=rbf(mean), vb=rbf(var);
  float den=rbf(sqrtf(rbf(vb+1e-5f)));
  for(int i=threadIdx.x;i<n;i+=blockDim.x){ float d=rbf(bf2f(in[(size_t)r*ld+i])-mb); out[(size_t)r*ldo+i]=f2bf(d/den); }
}
__global__ void k_rowdft(const u16* u, float2* X1){
  __shared__ float su[D]; __shared__ float2 tw[D];
  int s=blockIdx.x;
  for(int i=threadIdx.x;i<D;i+=blockDim.x){ su[i]=bf2f(u[s*D+i]); float a=-2.f*(float)i/D; tw[i]=make_float2(cospif(a),sinpif(a)); }
  __syncthreads();
  for(int k=threadIdx.x;k<DF;k+=blockDim.x){
    float re=0,im=0; int idx=0;
    for(int d=0;d<D;++d){ re+=su[d]*tw[idx].x; im+=su[d]*tw[idx].y; idx+=k; if(idx>=D) idx-=D; }
    X1[(size_t)k*S+s]=make_float2(re,im);
  }
}
__global__ void k_coldft(const float2* in, float2* out, float sign, const u16* wr, const u16* wi){
  __shared__ float2 sx[S]; __shared__ float2 tw[S];
  int k=blockIdx.x;
  for(int i=threadIdx.x;i<S;i+=blockDim.x){ sx[i]=in[(size_t)k*S+i]; float a=sign*2.f*(float)i/S; tw[i]=make_float2(cospif(a),sinpif(a)); }
  __syncthreads();
  for(int j=threadIdx.x;j<S;j+=blockDim.x){
    float re=0,im=0; int idx=0;
    for(int s=0;s<S;++s){ float2 a=sx[s], t=tw[idx]; re+=a.x*t.x-a.y*t.y; im+=a.x*t.y+a.y*t.x; idx=(idx+j)&(S-1); }
    if(wr){ float a=bf2f(wr[j*DF+k]), b=bf2f(wi[j*DF+k]); float r2=re*a-im*b, i2=re*b+im*a; re=r2; im=i2; }
    out[(size_t)k*S+j]=make_float2(re,im);
  }
}
__global__ void k_rowidft(const float2* Z, const u16* x, u16* h){
  __shared__ float2 sz[DF]; __shared__ float2 tw[D];
  int s=blockIdx.x;
  for(int i=threadIdx.x;i<D;i+=blockDim.x){ float a=2.f*(float)i/D; tw[i]=make_float2(cospif(a),sinpif(a)); }
  for(int i=threadIdx.x;i<DF;i+=blockDim.x) sz[i]=Z[(size_t)i*S+s];
  __syncthreads();
  for(int d=threadIdx.x;d<D;d+=blockDim.x){
    float acc=0; int idx=d;
    for(int k=1;k<320;++k){ acc+=sz[k].x*tw[idx].x-sz[k].y*tw[idx].y; idx+=d; if(idx>=D) idx-=D; }
    acc=2.f*acc+sz[0].x+((d&1)?-sz[320].x:sz[320].x);
    float y=rbf(acc*(1.f/(S*D)));
    h[s*D+d]=f2bf(bf2f(x[s*D+d])+y);
  }
}
__global__ void k_gemm(const u16* A,int lda,const u16* B,int ldb,u16* C,int ldc,int M,int N,int K,int mode,const u16* aux,int ldaux,const u16* bias){
  __shared__ float sa[16][17], sb[16][17];
  int tx=threadIdx.x&15, ty=threadIdx.x>>4;
  int row=blockIdx.y*16+ty, col=blockIdx.x*16+tx;
  float acc=0;
  for(int k0=0;k0<K;k0+=16){
    sa[ty][tx]=bf2f(A[(size_t)row*lda+k0+tx]);
    sb[ty][tx]=bf2f(B[(size_t)(k0+ty)*ldb+col]);
    __syncthreads();
    for(int k=0;k<16;++k) acc+=sa[ty][k]*sb[k][tx];
    __syncthreads();
  }
  float c=rbf(acc);
  if(mode==0){ float t=tanhf(0.7978845608028654f*(c+0.044715f*c*c*c)); c=0.5f*c*(1.f+t); }
  else if(mode==1){ c=rbf(c+bf2f(bias[row])); c=c*bf2f(aux[(size_t)row*ldaux+col]); }
  else { c=c+bf2f(aux[(size_t)row*ldaux+col]); }
  C[(size_t)row*ldc+col]=f2bf(c);
}

extern "C" void kernel_launch(void* const* d_in, const int* in_sizes, int n_in,
                              void* d_out, int out_size, void* d_ws, size_t ws_size,
                              hipStream_t stream) {
  const u16* x=(const u16*)d_in[0]; const u16* wr=(const u16*)d_in[1]; const u16* wi=(const u16*)d_in[2];
  const u16* w_in=(const u16*)d_in[4]; const u16* w_sp=(const u16*)d_in[5]; const u16* b_sp=(const u16*)d_in[6]; const u16* w_out=(const u16*)d_in[7];
  char* ws=(char*)d_ws;
  u16* u=(u16*)ws;
  float2* X1=(float2*)(ws+(4<<20));
  float2* X2=(float2*)(ws+(12<<20));
  float2* X3=(float2*)(ws+(20<<20));
  u16* h=(u16*)(ws+(28<<20));
  u16* l2=(u16*)(ws+(32<<20));
  u16* z=(u16*)(ws+(36<<20));
  u16* z2=(u16*)(ws+(44<<20));
  u16* p=(u16*)(ws+(48<<20));
  k_ln<<<S,256,0,stream>>>(x,D,D,u,D);
  k_rowdft<<<S,256,0,stream>>>(u,X1);
  k_coldft<<<DF,1024,0,stream>>>(X1,X2,-1.f,wr,wi);
  k_coldft<<<DF,1024,0,stream>>>(X2,X3,1.f,nullptr,nullptr);
  k_rowidft<<<S,256,0,stream>>>(X3,x,h);
  k_ln<<<S,256,0,stream>>>(h,D,D,l2,D);
  k_gemm<<<dim3(F/16,S/16),256,0,stream>>>(l2,D,w_in,F,z,F,S,F,D,0,nullptr,0,nullptr);
  k_ln<<<S,256,0,stream>>>(z+FH,F,FH,z2,FH);
  k_gemm<<<dim3(FH/16,S/16),256,0,stream>>>(w_sp,S,z2,FH,p,FH,S,FH,S,1,z,F,b_sp);
  k_gemm<<<dim3(D/16,S/16),256,0,stream>>>(p,FH,w_out,D,(u16*)d_out,D,S,D,FH,2,h,D,nullptr);
}
```

```cpp
#include <hip/hip_runtime.h>
#include <hip/hip_bf16.h>
#include <stdint.h>
typedef unsigned short u16;
typedef unsigned int u32;
typedef _Float16 h2 __attribute__((ext_vector_type(2)));
typedef _Float16 h8 __attribute__((ext_vector_type(8)));
typedef __bf16 bf2 __attribute__((ext_vector_type(2)));
typedef __bf16 bf8 __attribute__((ext_vector_type(8)));
typedef float f2 __attribute__((ext_vector_type(2)));
typedef float f4 __attribute__((ext_vector_type(4)));
typedef short s4 __attribute__((ext_vector_type(4)));
typedef u32 u2v __attribute__((ext_vector_type(2)));
typedef u32 u4v __attribute__((ext_vector_type(4)));
#define DI __device__ __forceinline__
#define LDS_AS __attribute__((address_space(3)))

constexpr int S = 2048, D = 640, DF = 321, FF = 1728, FH = 864;
constexpr int TROW_UNITS = 648;
constexpr int NUNITS = 641;
constexpr int NPREP = 168;

DI float bflo(u32 v) { return __uint_as_float(v << 16); }
DI float bfhi(u32 v) { return __uint_as_float(v & 0xffff0000u); }
DI u32 pk_bf(float a, float b) { f2 v = {a, b}; return __builtin_bit_cast(u32, __builtin_convertvector(v, bf2)); }
DI u32 pk_h(float a, float b) { f2 v = {a, b}; return __builtin_bit_cast(u32, __builtin_convertvector(v, h2)); }
DI float rbf(float a) { return bflo(pk_bf(a, a)); }
DI float cosr(float rev) { return __builtin_amdgcn_cosf(rev); }
DI float sinr(float rev) { return __builtin_amdgcn_sinf(rev); }
DI f4 mfma_h(u4v a, u4v b, f4 c) {
  return __builtin_amdgcn_mfma_f32_16x16x32_f16(__builtin_bit_cast(h8, a), __builtin_bit_cast(h8, b), c, 0, 0, 0);
}
template <int CTRL> DI float dpp_add(float v) {
  int t = __builtin_amdgcn_update_dpp(0, __float_as_int(v), CTRL, 0xf, 0xf, true);
  return v + __int_as_float(t);
}
DI float sum32(float v) {
  v = dpp_add<0xB1>(v);
  v = dpp_add<0x4E>(v);
  v = dpp_add<0x141>(v);
  v = dpp_add<0x140>(v);
  u32 b = __float_as_uint(v);
  auto r = __builtin_amdgcn_permlane16_swap(b, b, false, false);
  return __uint_as_float(r[0]) + __uint_as_float(r[1]);
}
template <int CTRL> DI u32 dpp_maxu(u32 v) {
  u32 t = (u32)__builtin_amdgcn_update_dpp(0, (int)v, CTRL, 0xf, 0xf, true);
  return v > t ? v : t;
}
DI u32 max64u(u32 v) {
  v = dpp_maxu<0xB1>(v); v = dpp_maxu<0x4E>(v); v = dpp_maxu<0x141>(v); v = dpp_maxu<0x140>(v);
  auto r = __builtin_amdgcn_permlane16_swap(v, v, false, false);
  v = r[0] > r[1] ? r[0] : r[1];
  auto s = __builtin_amdgcn_permlane32_swap(v, v, false, false);
  return s[0] > s[1] ? s[0] : s[1];
}
DI void ln_stats(const float (&v)[20], float inv_n, float& mb, float& rinv) {
  float s = 0.f;
#pragma unroll
  for (int i = 0; i < 20; ++i) s += v[i];
  float mean = sum32(s) * inv_n;
  float q = 0.f;
#pragma unroll
  for (int i = 0; i < 20; ++i) { float d = v[i] - mean; q = fmaf(d, d, q); }
  float var = sum32(q) * inv_n;
  mb = rbf(mean);
  float den = rbf(sqrtf(rbf(rbf(var) + 1e-5f)));
  rinv = 1.0f / den;
}

struct __attribute__((packed, aligned(2))) U4U { u32 a, b, c, d; };

__global__ __launch_bounds__(256) void k1_kernel(const u16* __restrict__ x, const u16* __restrict__ wr,
                                                 const u16* __restrict__ wi, u32* __restrict__ T,
                                                 u32* __restrict__ Wp, u32* __restrict__ W2, u32* __restrict__ wmax) {
  __shared__ __attribute__((aligned(16))) char smem[10272 + 18 * 1024];
  const int tid = threadIdx.x, lane = tid & 63;
  const int wave = __builtin_amdgcn_readfirstlane(tid >> 6);
  const int li = lane & 15, g = lane >> 4;
  if (blockIdx.x >= 256) {
    const int pb = blockIdx.x - 256;
    const int cp = pb & 7, cc = pb >> 3;
    const int c0 = cc * 16 < 305 ? cc * 16 : 305;
    const int e = tid;
    const size_t roff = (size_t)(cp + 8 * e) * DF + c0;
    const U4U* pr = (const U4U*)(wr + roff);
    const U4U* pi = (const U4U*)(wi + roff);
    U4U r0 = pr[0], r1 = pr[1], i0 = pi[0], i1 = pi[1];
    u32 rr[8] = {r0.a, r0.b, r0.c, r0.d, r1.a, r1.b, r1.c, r1.d};
    u32 ii[8] = {i0.a, i0.b, i0.c, i0.d, i1.a, i1.b, i1.c, i1.d};
    u32 mx = 0;
#pragma unroll
    for (int j = 0; j < 8; ++j) {
      u32 a = rr[j] & 0x7fff7fffu, b = ii[j] & 0x7fff7fffu;
      mx = max(mx, max(max(a & 0xffffu, a >> 16), max(b & 0xffffu, b >> 16)));
    }
    const bool mir = cp > 4;
    const int ep = mir ? 255 - e : e;
    const int dpos = ((ep & 15) + 16 * (ep >> 6)) * 4 + ((ep >> 4) & 3);
    const int e2 = (cp == 0) ? ((256 - e) & 255) : 255 - e;
    const int dpos2 = ((e2 & 15) + 16 * (e2 >> 6)) * 4 + ((e2 >> 4) & 3);
#pragma unroll
    for (int j = 0; j < 16; ++j) {
      u32 a = rr[j >> 1], b = ii[j >> 1];
      u32 v = (j & 1) ? ((a >> 16) | (b & 0xffff0000u)) : ((a & 0xffffu) | (b << 16));
      const int k2 = c0 + j;
      const bool edge = (k2 == 0) || (k2 == 320);
      int tr;
      if (cp == 0 || cp == 4) {
        tr = (480 + (k2 >> 1)) * 4 + (k2 & 1) * 2 + (cp == 4);
      } else {
        const int q = mir ? 8 - cp : cp;
        const int col = mir ? 640 - k2 : k2;
        const int ka = col & 15, kb = col >> 4;
        tr = ((q - 1) * 160 + (kb >> 2) * 16 + ka) * 4 + (kb & 3);
      }
      if (!(mir && edge)) Wp[(size_t)tr * 256 + dpos] = mir ? (v ^ 0x80000000u) : v;
      if (edge && (mir || cp == 0 || cp == 4)) {
        const int cls = mir ? 8 - cp : cp;
        W2[(cls * 2 + (k2 == 320)) * 256 + dpos2] = v ^ 0x80000000u;
      }
    }
    mx = max64u(mx);
    u32* sm = (u32*)smem;
    if (lane == 0) sm[wave] = mx;
    __syncthreads();
    if (tid == 0) wmax[pb] = max(max(sm[0], sm[1]), max(sm[2], sm[3]));
    return;
  }
  const int b = (blockIdx.x & 7) * 32 + (blockIdx.x >> 3);
  const int a = 2 * wave + (lane >> 5), j = lane & 31;
  const u16* xr = x + (size_t)(256 * a + b) * D;
  u4v x0 = *(const u4v*)(xr + 8 * j);
  u4v x1 = *(const u4v*)(xr + 256 + 8 * j);
  u2v x2 = *(const u2v*)(xr + 512 + 4 * j);
  char* f40 = smem + 10272;
  for (int pr = wave; pr < 9; pr += 4) {
    const int to = pr / 3, tk = pr - 3 * to;
    const int kb = 16 * to + li;
    u4v are, aim;
#pragma unroll
    for (int jj = 0; jj < 4; ++jj) {
      const int db = 16 * tk + 4 * g + jj;
      const float ang = (float)(kb * db) * (1.0f / 40.0f);
      const float sc = db < 40 ? 0.125f : 0.f;
      const float c = cosr(ang) * sc, s = sinr(ang) * sc;
      are[jj] = pk_h(c, s);
      aim[jj] = pk_h(-s, c);
    }
    *(u4v*)(f40 + (pr * 2 + 0) * 1024 + lane * 16) = are;
    *(u4v*)(f40 + (pr * 2 + 1) * 1024 + lane * 16) = aim;
  }
  if (tid < 4) *(u32*)(smem + 10240 + tid * 4) = 0u;
  const int q = wave;
  u4v mR, mI;
  {
    const int a0 = g >> 1;
    const int phi = (q == 0) ? (a0 ? 2 : -2) : 2 * a0 * q;
    float cs[8], sn[8];
#pragma unroll
    for (int jj = 0; jj < 8; ++jj) {
      const int da = 8 * (g & 1) + jj;
      const float ang = (float)((da * li + phi) & 15) * 0.0625f;
      cs[jj] = cosr(ang); sn[jj] = -sinr(ang);
    }
#pragma unroll
    for (int jj = 0; jj < 4; ++jj) { mR[jj] = pk_h(cs[2 * jj], cs[2 * jj + 1]); mI[jj] = pk_h(sn[2 * jj], sn[2 * jj + 1]); }
  }
  f2 tw[12];
  {
    const float sc = (q == 0) ? 0.125f * 0.70710678118f : 0.125f;
#pragma unroll
    for (int t = 0; t < 3; ++t)
#pragma unroll
      for (int r = 0; r < 4; ++r) {
        const int db = 16 * t + 4 * g + r;
        const float ang = (float)(db * li) * (1.0f / 640.0f);
        tw[t * 4 + r] = f2{cosr(ang) * sc, -sinr(ang) * sc};
      }
  }
  {
    float v[20];
#pragma unroll
    for (int i = 0; i < 4; ++i) { v[2 * i] = bflo(x0[i]); v[2 * i + 1] = bfhi(x0[i]); v[8 + 2 * i] = bflo(x1[i]); v[9 + 2 * i] = bfhi(x1[i]); }
    v[16] = bflo(x2[0]); v[17] = bfhi(x2[0]); v[18] = bflo(x2[1]); v[19] = bfhi(x2[1]);
    float mb, rinv;
    ln_stats(v, 1.0f / 640.0f, mb, rinv);
    u32 o[10];
#pragma unroll
    for (int i = 0; i < 10; ++i) {
      u32 t = pk_bf(v[2 * i] - mb, v[2 * i + 1] - mb);
      u32 n = pk_bf(bflo(t) * rinv, bfhi(t) * rinv);
      o[i] = pk_h(bflo(n), bfhi(n));
    }
    char* ub = smem + a * 1280;
    *(u4v*)(ub + 16 * j) = u4v{o[0], o[1], o[2], o[3]};
    *(u4v*)(ub + 512 + 16 * j) = u4v{o[4], o[5], o[6], o[7]};
    *(u2v*)(ub + 1024 + 8 * j) = u2v{o[8], o[9]};
  }
  __syncthreads();
  u4v b2[3];
  {
    const int qq = li >> 2, p = li & 3;
    const char* abase = smem + (8 * g + qq) * 80 + 8 * p;
    f4 are[3], aim[3];
#pragma unroll
    for (int t = 0; t < 3; ++t) { are[t] = f4{0, 0, 0, 0}; aim[t] = f4{0, 0, 0, 0}; }
#pragma unroll
    for (int ks = 0; ks < 4; ++ks) {
      const int n = (q * ks) & 3;
      const u32 sre = (n >= 2) ? 0x80008000u : 0u;
      const u32 sim = (n == 1 || n == 2) ? 0x80008000u : 0u;
      u4v bre, bim;
#pragma unroll
      for (int jj = 0; jj < 4; ++jj) {
        bre[jj] = ((n & 1) ? mI[jj] : mR[jj]) ^ sre;
        bim[jj] = ((n & 1) ? mR[jj] : mI[jj]) ^ sim;
      }
#pragma unroll
      for (int t = 0; t < 3; ++t) {
        s4 lo = __builtin_amdgcn_ds_read_tr16_b64_v4i16((LDS_AS s4*)(abase + ks * 2560 + t * 32));
        s4 hi = __builtin_amdgcn_ds_read_tr16_b64_v4i16((LDS_AS s4*)(abase + ks * 2560 + t * 32 + 320));
        u2v l2 = __builtin_bit_cast(u2v, lo), h2v = __builtin_bit_cast(u2v, hi);
        u4v af = u4v{l2[0], l2[1], h2v[0], h2v[1]};
        are[t] = mfma_h(af, bre, are[t]);
        aim[t] = mfma_h(af, bim, aim[t]);
      }
    }
#pragma unroll
    for (int t = 0; t < 3; ++t)
#pragma unroll
      for (int r = 0; r < 4; ++r) {
        const f2 w = tw[t * 4 + r];
        const float re = are[t][r] * w[0] - aim[t][r] * w[1];
        const float im = are[t][r] * w[1] + aim[t][r] * w[0];
        b2[t][r] = pk_h(re, im);
      }
  }
  u32* Trow = T + (size_t)b * TROW_UNITS * 4;
#pragma unroll
  for (int to = 0; to < 3; ++to) {
    f4 ore = f4{0, 0, 0, 0}, oim = f4{0, 0, 0, 0};
#pragma unroll
    for (int tk = 0; tk < 3; ++tk) {
      const u4v fre = *(const u4v*)(f40 + ((to * 3 + tk) * 2 + 0) * 1024 + lane * 16);
      const u4v fim = *(const u4v*)(f40 + ((to * 3 + tk) * 2 + 1) * 1024 + lane * 16);
      ore = mfma_h(fre, b2[tk], ore);
      oim = mfma_h(fim, b2[tk], oim);
    }
    u4v o;
#pragma unroll
    for (int r = 0; r < 4; ++r) o[r] = pk_h(ore[r], oim[r]);
    if (to < 2 || lane < 32) {
      if (q > 0) {
        *(u4v*)(Trow + ((q - 1) * 160 + 64 * to + lane) * 4) = o;
      } else {
#pragma unroll
        for (int r = 0; r < 4; ++r) {
          const int k2 = li + 16 * (16 * to + 4 * g + r);
          const int pos = k2 <= 320 ? 2 * k2 : 2 * (640 - k2) + 1;
          Trow[480 * 4 + pos] = o[r];
          if (k2 == 0 || k2 == 320) Trow[480 * 4 + pos + 1] = o[r];
        }
      }
    }
  }
}

__global__ __launch_bounds__(128) void k2_kernel(const u32* __restrict__ T, const u32* __restrict__ Wp,
                                                 const u32* __restrict__ W2, const u32* __restrict__ wmax,
                                                 u32* __restrict__ Tp, float* __restrict__ unscale) {
  const int tid = threadIdx.x, lane = tid & 63;
  const int wave = __builtin_amdgcn_readfirstlane(tid >> 6);
  const int li = lane & 15, g = lane >> 4;
  const int P = (blockIdx.x & 7) * 41 + (blockIdx.x >> 3);
  const int U = 2 * P + wave;
  if (U >= NUNITS) return;
  u4v L[4];
#pragma unroll
  for (int jj = 0; jj < 4; ++jj) {
    const int bb = 16 * (4 * g + jj) + li;
    L[jj] = *(const u4v*)(T + ((size_t)bb * TROW_UNITS + U) * 4);
  }
  u4v Wv[4];
#pragma unroll
  for (int t = 0; t < 4; ++t) Wv[t] = *(const u4v*)(Wp + ((size_t)(U * 4 + t) * 64 + lane) * 4);
  u32 mx = max(max(wmax[lane], wmax[64 + lane]), lane < NPREP - 128 ? wmax[128 + lane] : 0u);
  const bool special = U >= 480;
  const int cB = special ? 4 : (U / 160 + 1);
  u4v Fre, Fim, P1re[2], P1im[2], P4re[2], P4im[2];
  f2 twf[2][4], twi[2][4];
#pragma unroll
  for (int jj = 0; jj < 4; ++jj) {
    const int m = 4 * g + jj;
    {
      const float ang = (float)((li * m) & 15) * 0.0625f;
      const float c = cosr(ang), s = sinr(ang);
      Fre[jj] = pk_h(c, s); Fim[jj] = pk_h(-s, c);
    }
    {
      const float ang = (float)((m * (cB + 8 * li)) & 127) * (1.0f / 128.0f);
      const float c = cosr(ang), s = sinr(ang);
      P1re[1][jj] = pk_h(c, s); P1im[1][jj] = pk_h(-s, c);
    }
    {
      const float ang = (float)((li * (cB + 8 * m)) & 127) * (1.0f / 128.0f);
      const float c = cosr(ang), s = sinr(ang);
      P4re[1][jj] = pk_h(c, s); P4im[1][jj] = pk_h(-s, c);
    }
    {
      const float ang = (float)(m * (cB + 8 * li)) * (1.0f / 2048.0f);
      twf[1][jj] = f2{cosr(ang) * 0.25f, -sinr(ang) * 0.25f};
    }
    {
      const float ang = (float)(li * (cB + 8 * m)) * (1.0f / 2048.0f);
      twi[1][jj] = f2{cosr(ang) * 0.25f, sinr(ang) * 0.25f};
    }
    if (special) {
      const float ang = (float)(li * m) * (1.0f / 256.0f);
      const float c = cosr(ang) * 0.25f, s = sinr(ang) * 0.25f;
      twf[0][jj] = f2{c, -s}; twi[0][jj] = f2{c, s};
      P1re[0][jj] = Fre[jj]; P1im[0][jj] = Fim[jj]; P4re[0][jj] = Fre[jj]; P4im[0][jj] = Fim[jj];
    } else {
      twf[0][jj] = twf[1][jj]; twi[0][jj] = twi[1][jj];
      P1re[0][jj] = P1re[1][jj]; P1im[0][jj] = P1im[1][jj]; P4re[0][jj] = P4re[1][jj]; P4im[0][jj] = P4im[1][jj];
    }
  }
  mx = max64u(mx);
  int E = (int)(mx >> 7);
  E = E < 1 ? 1 : (E > 253 ? 253 : E);
  const float sig = __uint_as_float((u32)(254 - E) << 23) * 0.25f;
  if (U == 0 && lane == 0) unscale[0] = __uint_as_float((u32)E << 23);
  f2 wf[4][4];
#pragma unroll
  for (int t = 0; t < 4; ++t)
#pragma unroll
    for (int r = 0; r < 4; ++r) wf[t][r] = f2{bflo(Wv[t][r]), bfhi(Wv[t][r])};
  {
    const int ur = special ? U - 480 : U % 160;
    const bool e0 = ur == 0, e320 = special ? ur == 160 : ur == 80;
    if (e0 || e320) {
      const int cls = special ? 0 : cB;
      const u4v w2 = *(const u4v*)(W2 + (cls * 2 + (e320 ? 1 : 0)) * 256 + lane * 4);
#pragma unroll
      for (int r = 0; r < 4; ++r) wf[0][r] = (wf[0][r] + f2{bflo(w2[r]), bfhi(w2[r])}) * 0.5f;
      if (special) {
        const u4v w4 = *(const u4v*)(W2 + (4 * 2 + (e320 ? 1 : 0)) * 256 + lane * 4);
#pragma unroll
        for (int r = 0; r < 4; ++r) wf[1][r] = (wf[1][r] + f2{bflo(w4[r]), bfhi(w4[r])}) * 0.5f;
      }
    }
  }
#pragma unroll
  for (int t = 0; t < 4; ++t)
#pragma unroll
    for (int r = 0; r < 4; ++r) wf[t][r] *= sig;
  u4v X[4];
  if (special) {
#pragma unroll
    for (int jj = 0; jj < 4; ++jj)
#pragma unroll
      for (int pp = 0; pp < 2; ++pp) {
        const u32 au = L[jj][2 * pp], bu = L[jj][2 * pp + 1] ^ 0x80000000u;
        const h2 A = __builtin_bit_cast(h2, au);
        const h2 Bc = __builtin_bit_cast(h2, bu);
        X[2 * pp][jj] = __builtin_bit_cast(u32, A + Bc);
        X[2 * pp + 1][jj] = __builtin_bit_cast(u32, A - Bc);
      }
  } else {
#pragma unroll
    for (int jj = 0; jj < 4; ++jj)
#pragma unroll
      for (int t = 0; t < 4; ++t) X[t][jj] = L[jj][t];
  }
  f4 ore[4], oim[4];
  const f4 z4 = f4{0, 0, 0, 0};
#pragma unroll
  for (int t = 0; t < 4; ++t) {
    const int cs = t & 1;
    f4 re = mfma_h(X[t], P1re[cs], z4), im = mfma_h(X[t], P1im[cs], z4);
    u4v v;
#pragma unroll
    for (int r = 0; r < 4; ++r) {
      const f2 w = twf[cs][r];
      v[r] = pk_h(re[r] * w[0] - im[r] * w[1], re[r] * w[1] + im[r] * w[0]);
    }
    re = mfma_h(Fre, v, z4); im = mfma_h(Fim, v, z4);
#pragma unroll
    for (int r = 0; r < 4; ++r) {
      const f2 w = wf[t][r];
      v[r] = pk_h(re[r] * w[1] + im[r] * w[0], re[r] * w[0] - im[r] * w[1]);
    }
    im = mfma_h(v, Fre, z4); re = mfma_h(v, Fim, z4);
#pragma unroll
    for (int r = 0; r < 4; ++r) {
      const f2 w = twi[cs][r];
      v[r] = pk_h(re[r] * w[1] + im[r] * w[0], re[r] * w[0] - im[r] * w[1]);
    }
    oim[t] = mfma_h(P4re[cs], v, z4); ore[t] = mfma_h(P4im[cs], v, z4);
  }
  u32* dst = Tp + ((size_t)U * 256 + lane) * 4;
#pragma unroll
  for (int r = 0; r < 4; ++r) {
    u4v o;
    if (special) {
#pragma unroll
      for (int pp = 0; pp < 2; ++pp) {
        const float yr = ore[2 * pp][r], yi = oim[2 * pp][r], zr = ore[2 * pp + 1][r], zi = oim[2 * pp + 1][r];
        o[2 * pp] = pk_h((yr + zr) * 0.125f, (yi + zi) * 0.125f);
        o[2 * pp + 1] = pk_h((yr - zr) * 0.125f, (zi - yi) * 0.125f);
      }
    } else {
#pragma unroll
      for (int t = 0; t < 4; ++t) o[t] = pk_h(ore[t][r] * 0.25f, oim[t][r] * 0.25f);
    }
    *(u4v*)(dst + r * 256) = o;
  }
}

__global__ __launch_bounds__(256) void k3_kernel(const u16* __restrict__ x, const u32* __restrict__ Tp,
                                                 const float* __restrict__ unscale, u16* __restrict__ h,
                                                 u16* __restrict__ l2, float* __restrict__ ydbg) {
  __shared__ __attribute__((aligned(16))) char smem[10752 + 10240];
  const int tid = threadIdx.x, lane = tid & 63;
  const int wave = __builtin_amdgcn_readfirstlane(tid >> 6);
  const int li = lane & 15, g = lane >> 4;
  const int b = (blockIdx.x & 7) * 32 + (blockIdx.x >> 3);
  const int posb = ((b >> 4) & 3) * 64 + (b >> 6) * 16 + (b & 15);
#pragma unroll
  for (int i = 0; i < 2; ++i) {
    int U = (wave * 2 + i) * 64 + lane;
    U = U < 480 ? U : 479;
    __builtin_amdgcn_global_load_lds((const u32*)(Tp + ((size_t)U * 256 + posb) * 4),
                                     (LDS_AS u32*)(smem + (wave * 2 + i) * 1024), 16, 0, 0);
  }
  for (int i = wave; i < 10; i += 4) {
    const int idx = i * 64 + lane;
    const int uq = idx >> 2, r = idx & 3;
    const int k2 = (uq & 15) + 16 * (4 * (uq >> 4) + r);
    const int pos = k2 <= 320 ? 2 * k2 : 2 * (640 - k2) + 1;
    __builtin_amdgcn_global_load_lds((const u32*)(Tp + ((size_t)(480 + (pos >> 2)) * 256 + posb) * 4 + (pos & 3)),
                                     (LDS_AS u32*)(smem + 8192 + i * 256), 4, 0, 0);
  }
  const int a = 2 * wave + (lane >> 5), j = lane & 31;
  const size_t rowoff = (size_t)(256 * a + b) * D;
  const u16* xr = x + rowoff;
  u4v x0 = *(const u4v*)(xr + 8 * j);
  u4v x1 = *(const u4v*)(xr + 256 + 8 * j);
  u2v x2 = *(const u2v*)(xr + 512 + 4 * j);
  const float usc = unscale[0];
  if (wave < 3) {
    const int t = wave;
    const int db = 16 * t + li;
    u4v Bre[3], Bim[3], Gre, Gim;
    f2 tw[4];
#pragma unroll
    for (int jj = 0; jj < 4; ++jj) {
#pragma unroll
      for (int tk = 0; tk < 3; ++tk) {
        const int kb = 16 * tk + 4 * g + jj;
        const float ang = (float)(kb * db) * (1.0f / 40.0f);
        const float sc = kb < 40 ? 1.f : 0.f;
        const float c = cosr(ang) * sc, s = sinr(ang) * sc;
        Bre[tk][jj] = pk_h(c, -s);
        Bim[tk][jj] = pk_h(s, c);
      }
      const int m = 4 * g + jj;
      {
        const float ang = (float)((li * m) & 15) * 0.0625f;
        const float c = cosr(ang), s = sinr(ang);
        Gre[jj] = pk_h(c, -s); Gim[jj] = pk_h(s, c);
      }
      {
        const float ang = (float)(db * m) * (1.0f / 640.0f);
        tw[jj] = f2{cosr(ang) * 0.125f, sinr(ang) * 0.125f};
      }
    }
    asm volatile("s_waitcnt vmcnt(0)" ::: "memory");
    __syncthreads();
    f4 vre[4], vim[4];
    const f4 z4 = f4{0, 0, 0, 0};
#pragma unroll
    for (int q = 0; q < 4; ++q) {
      const char* base = smem + (q == 0 ? 8192 : (q - 1) * 2560);
      f4 re = z4, im = z4;
#pragma unroll
      for (int tk = 0; tk < 3; ++tk) {
        const u4v af = *(const u4v*)(base + (tk < 2 ? (64 * tk + lane) : (128 + (lane & 31))) * 16);
        re = mfma_h(af, Bre[tk], re);
        im = mfma_h(af, Bim[tk], im);
      }
      u4v v;
#pragma unroll
      for (int r = 0; r < 4; ++r) {
        const f2 w = tw[r];
        v[r] = pk_h(re[r] * w[0] - im[r] * w[1], re[r] * w[1] + im[r] * w[0]);
      }
      vre[q] = mfma_h(v, Gre, z4);
      vim[q] = mfma_h(v, Gim, z4);
    }
    const float fs = usc * (2.0f * 1024.0f * 128.0f / (2048.0f * 640.0f));
    const float hh = 0.70710678118f;
    f4 E = vre[0] + vim[0], O = vre[0] - vim[0];
    f4 s13 = vre[1] + vre[3], dx = vre[1] - vre[3], sy = vim[1] + vim[3], dy = vim[3] - vim[1];
    f4 Ep = E + vre[2], Em = E - vre[2], Om = O - vim[2], Op = O + vim[2];
    f4 A = (dx - sy) * hh, B = (dx + sy) * hh;
    f4 y[8];
    y[0] = Ep + s13; y[4] = Ep - s13; y[2] = Em + dy; y[6] = Em - dy;
    y[1] = Om + A; y[5] = Om - A; y[3] = Op - B; y[7] = Op + B;
    if (t < 2 || lane < 32) {
      char* yb = smem + 10752 + (40 * li + 16 * t + 4 * g) * 2;
#pragma unroll
      for (int aa = 0; aa < 8; ++aa) {
        f4 v = y[aa] * fs;
        *(u2v*)(yb + aa * 1280) = u2v{pk_bf(v[0], v[1]), pk_bf(v[2], v[3])};
        if (ydbg) *(f4*)(ydbg + (size_t)(256 * aa + b) * D + 40 * li + 16 * t + 4 * g) = v;
      }
    }
  } else {
    asm volatile("s_waitcnt vmcnt(0)" ::: "memory");
    __syncthreads();
  }
  __syncthreads();
  {
    const char* yb = smem + 10752 + a * 1280;
    const u4v y0 = *(const u4v*)(yb + 16 * j);
    const u4v y1 = *(const u4v*)(yb + 512 + 16 * j);
    const u2v y2 = *(const u2v*)(yb + 1024 + 8 * j);
    u32 hx[10];
#pragma unroll
    for (int i = 0; i < 4; ++i) {
      hx[i] = pk_bf(bflo(x0[i]) + bflo(y0[i]), bfhi(x0[i]) + bfhi(y0[i]));
      hx[4 + i] = pk_bf(bflo(x1[i]) + bflo(y1[i]), bfhi(x1[i]) + bfhi(y1[i]));
    }
    hx[8] = pk_bf(bflo(x2[0]) + bflo(y2[0]), bfhi(x2[0]) + bfhi(y2[0]));
    hx[9] = pk_bf(bflo(x2[1]) + bflo(y2[1]), bfhi(x2[1]) + bfhi(y2[1]));
    u16* hr = h + rowoff;
    *(u4v*)(hr + 8 * j) = u4v{hx[0], hx[1], hx[2], hx[3]};
    *(u4v*)(hr + 256 + 8 * j) = u4v{hx[4], hx[5], hx[6], hx[7]};
    *(u2v*)(hr + 512 + 4 * j) = u2v{hx[8], hx[9]};
    float v[20];
#pragma unroll
    for (int i = 0; i < 10; ++i) { v[2 * i] = bflo(hx[i]); v[2 * i + 1] = bfhi(hx[i]); }
    float mb, rinv;
    ln_stats(v, 1.0f / 640.0f, mb, rinv);
    u32 o[10];
#pragma unroll
    for (int i = 0; i < 10; ++i) {
      u32 t = pk_bf(v[2 * i] - mb, v[2 * i + 1] - mb);
      o[i] = pk_bf(bflo(t) * rinv, bfhi(t) * rinv);
    }
    u16* lr = l2 + rowoff;
    *(u4v*)(lr + 8 * j) = u4v{o[0], o[1], o[2], o[3]};
    *(u4v*)(lr + 256 + 8 * j) = u4v{o[4], o[5], o[6], o[7]};
    *(u2v*)(lr + 512 + 4 * j) = u2v{o[8], o[9]};
  }
}

typedef unsigned long long u64;
typedef __attribute__((address_space(1))) unsigned long long gu64;
#define GLDS16(src, dst) __builtin_amdgcn_global_load_lds((const u32*)(src), (LDS_AS u32*)(dst), 16, 0, 0)
#define WAITV(n) asm volatile("s_waitcnt vmcnt(%0)" ::"n"(n) : "memory")
#define BAR() __builtin_amdgcn_s_barrier()
DI f4 mfma_b(u4v a, u4v b, f4 c) {
  return __builtin_amdgcn_mfma_f32_16x16x32_bf16(__builtin_bit_cast(bf8, a), __builtin_bit_cast(bf8, b), c, 0, 0, 0);
}
DI float xsum_g(float v) {
  u32 b = __float_as_uint(v);
  auto r = __builtin_amdgcn_permlane16_swap(b, b, false, false);
  v = __uint_as_float(r[0]) + __uint_as_float(r[1]);
  b = __float_as_uint(v);
  auto s = __builtin_amdgcn_permlane32_swap(b, b, false, false);
  return __uint_as_float(s[0]) + __uint_as_float(s[1]);
}
DI f2 gelu2(f2 x) {
  const float k1 = -2.0f * 0.7978845608028654f * 1.4426950408889634f;
  const float k2 = k1 * 0.044715f;
  f2 p = x * x * k2 + k1;
  f2 a = x * p;
  f2 e = f2{__builtin_amdgcn_exp2f(a[0]), __builtin_amdgcn_exp2f(a[1])} + 1.0f;
  f2 r = f2{__builtin_amdgcn_rcpf(e[0]), __builtin_amdgcn_rcpf(e[1])};
  return x * r;
}
DI u2v gelu4(f4 a) {
  u32 t0 = pk_bf(a[0], a[1]), t1 = pk_bf(a[2], a[3]);
  f2 g0 = gelu2(f2{bflo(t0), bfhi(t0)}), g1 = gelu2(f2{bflo(t1), bfhi(t1)});
  return u2v{pk_bf(g0[0], g0[1]), pk_bf(g1[0], g1[1])};
}

template <bool PERM>
DI void retile_task(const u16* __restrict__ W, int ldw, int k0, int c0, u32* __restrict__ dst, char* lds, int lane) {
#pragma unroll
  for (int i = 0; i < 4; ++i)
    GLDS16(W + (size_t)(k0 + 8 * i + (lane >> 3)) * ldw + c0 + (lane & 7) * 8, lds + i * 1024);
  WAITV(0);
  const int li = lane & 15, g = lane >> 4, q = li >> 2, p = li & 3;
  const int r0 = PERM ? 4 * g + q : 8 * g + q;
  const int r1 = PERM ? 16 + 4 * g + q : 8 * g + 4 + q;
#pragma unroll
  for (int f = 0; f < 4; ++f) {
    s4 lo = __builtin_amdgcn_ds_read_tr16_b64_v4i16((LDS_AS s4*)(lds + r0 * 128 + f * 32 + 8 * p));
    s4 hi = __builtin_amdgcn_ds_read_tr16_b64_v4i16((LDS_AS s4*)(lds + r1 * 128 + f * 32 + 8 * p));
    u2v a = __builtin_bit_cast(u2v, lo), b = __builtin_bit_cast(u2v, hi);
    *(u4v*)(dst + f * 256 + lane * 4) = u4v{a[0], a[1], b[0], b[1]};
  }
}
constexpr int NRT1 = 20 * 27, NRT3 = 27 * 10;
__global__ __launch_bounds__(128) void prep_kernel(const u16* __restrict__ w_in, const u16* __restrict__ w_out,
                                                   u32* __restrict__ Wt1, u32* __restrict__ Wt3, u32* __restrict__ stats) {
  __shared__ __attribute__((aligned(16))) char smem[8192];
  const int lane = threadIdx.x & 63, wave = __builtin_amdgcn_readfirstlane(threadIdx.x >> 6);
  const int task = blockIdx.x * 2 + wave;
  if (task < NRT1) {
    const int ks = task / 27, cg = task - ks * 27;
    retile_task<false>(w_in, FF, ks * 32, cg * 64, Wt1 + (size_t)(ks * 108 + cg * 4) * 256, smem + wave * 4096, lane);
  } else if (task < NRT1 + NRT3) {
    const int t = task - NRT1, ks = t / 10, cg = t - ks * 10;
    retile_task<true>(w_out, D, ks * 32, cg * 64, Wt3 + (size_t)(ks * 40 + cg * 4) * 256, smem + wave * 4096, lane);
  }
  if (task < 256) *(u4v*)(stats + task * 256 + lane * 4) = u4v{~0u, ~0u, ~0u, ~0u};
}

constexpr int G1_NST = 5, G1_STB = 30 * 1024;
__global__ __launch_bounds__(256) void g1_kernel(const u16* __restrict__ l2, const u32* __restrict__ Wt1,
                                                 u32* __restrict__ z1f, u16* __restrict__ z2t, u64* stats) {
  __shared__ __attribute__((aligned(16))) char smem[G1_NST * G1_STB];
  const int tid = threadIdx.x, lane = tid & 63;
  const int wave = __builtin_amdgcn_readfirstlane(tid >> 6);
  const int li = lane & 15, g = lane >> 4;
  const int xcd = blockIdx.x & 7, idx = blockIdx.x >> 3;
  const int mt = (xcd >> 1) * 4 + (idx >> 3), nb = (xcd & 1) * 8 + (idx & 7);
  const int zs = nb < 6 ? 4 * nb : 24 + 3 * (nb - 6);
  const int z1s = nb < 6 ? 3 * nb : (nb < 12 ? 18 + 4 * (nb - 6) : 42 + 3 * (nb - 12));
  const int type = nb < 6 ? 0 : (nb < 12 ? 1 : 2);
  auto slot_frag = [&](int s) { return s < 3 ? 54 + zs + s : (s == 3 ? (type == 0 ? 54 + zs + 3 : (type == 1 ? z1s + 3 : z1s)) : z1s + s - 4); };
  const u16* asrc = l2 + (size_t)(mt * 128 + wave * 32 + (lane >> 3)) * D + ((lane & 7) ^ (lane >> 3)) * 8;
  const u32* bsrc[4];
#pragma unroll
  for (int j = 0; j < 4; ++j) {
    int i = wave + 4 * j; i = i < 14 ? i : 0;
    const int kk = i / 7, s = i - 7 * kk;
    bsrc[j] = Wt1 + (size_t)(kk * 108 + slot_frag(s)) * 256 + lane * 4;
  }
  auto stage = [&](int kt, int buf) {
    char* sb = smem + buf * G1_STB;
#pragma unroll
    for (int i = 0; i < 4; ++i) GLDS16(asrc + (size_t)i * 8 * D + kt * 64, sb + (wave * 4 + i) * 1024);
#pragma unroll
    for (int j = 0; j < 3; ++j) GLDS16(bsrc[j] + (size_t)kt * 2 * 108 * 256, sb + 16384 + (wave + 4 * j) * 1024);
    if (wave < 2) GLDS16(bsrc[3] + (size_t)kt * 2 * 108 * 256, sb + 16384 + (wave + 12) * 1024);
  };
  f4 acc[7][2];
#pragma unroll
  for (int s = 0; s < 7; ++s) { acc[s][0] = f4{0, 0, 0, 0}; acc[s][1] = f4{0, 0, 0, 0}; }
  const int aoff = (wave * 32 + li) * 128 + ((g ^ (li & 7)) * 16);
  auto compute = [&](int buf) {
    const char* sb = smem + buf * G1_STB;
#pragma unroll
    for (int kk = 0; kk < 2; ++kk) {
      const u4v a0 = *(const u4v*)(sb + (aoff ^ (kk * 64)));
      const u4v a1 = *(const u4v*)(sb + (aoff ^ (kk * 64)) + 2048);
#pragma unroll
      for (int s = 0; s < 7; ++s) {
        const u4v w = *(const u4v*)(sb + 16384 + (kk * 7 + s) * 1024 + lane * 16);
        acc[s][0] = mfma_b(w, a0, acc[s][0]);
        acc[s][1] = mfma_b(w, a1, acc[s][1]);
      }
    }
  };
#pragma unroll
  for (int s = 0; s < G1_NST - 1; ++s) stage(s, s);
#pragma unroll
  for (int kt = 0; kt < 10; ++kt) {
    const int rem = (10 - 1 - kt) < (G1_NST - 2) ? (10 - 1 - kt) : (G1_NST - 2);
    if (wave < 2) {
      if (rem == 3) WAITV(24); else if (rem == 2) WAITV(16); else if (rem == 1) WAITV(8); else WAITV(0);
    } else {
      if (rem == 3) WAITV(21); else if (rem == 2) WAITV(14); else if (rem == 1) WAITV(7); else WAITV(0);
    }
    asm volatile("s_waitcnt lgkmcnt(0)" ::: "memory");
    BAR();
    if (kt + G1_NST - 1 < 10) stage(kt + G1_NST - 1, (kt + G1_NST - 1) % G1_NST);
    compute(kt % G1_NST);
  }
  const int n2 = type == 0 ? 4 : 3;
  u2v zv[4][2];
  float lsum[2], lm2[2];
#pragma unroll
  for (int sf = 0; sf < 2; ++sf) {
    float s = 0.f;
#pragma unroll
    for (int j = 0; j < 4; ++j) {
      zv[j][sf] = gelu4(acc[j][sf]);
      float t = (bflo(zv[j][sf][0]) + bfhi(zv[j][sf][0])) + (bflo(zv[j][sf][1]) + bfhi(zv[j][sf][1]));
      if (j < 3 || type == 0) s += t;
    }
    s = xsum_g(s);
    const float lmean = s * (type == 0 ? (1.0f / 64.0f) : (1.0f / 48.0f));
    float q = 0.f;
#pragma unroll
    for (int j = 0; j < 4; ++j) {
      float d0 = bflo(zv[j][sf][0]) - lmean, d1 = bfhi(zv[j][sf][0]) - lmean, d2 = bflo(zv[j][sf][1]) - lmean, d3 = bfhi(zv[j][sf][1]) - lmean;
      float t = (d0 * d0 + d1 * d1) + (d2 * d2 + d3 * d3);
      if (j < 3 || type == 0) q += t;
    }
    lsum[sf] = s; lm2[sf] = xsum_g(q);
  }
  gu64* st = (gu64*)stats + (size_t)mt * 16 * 128 + wave * 32;
  if (lane < 32) {
    u32 a = __float_as_uint(g ? lsum[1] : lsum[0]), b = __float_as_uint(g ? lm2[1] : lm2[0]);
    if (a == ~0u) a = 0x7fc00000u;
    __hip_atomic_store(st + nb * 128 + lane, ((u64)b << 32) | a, __ATOMIC_RELAXED, __HIP_MEMORY_SCOPE_AGENT);
  }
  {
    const int sf0 = mt * 8 + wave * 2;
#pragma unroll
    for (int sf = 0; sf < 2; ++sf) {
#pragma unroll
      for (int j = 0; j < 3; ++j)
        *(u2v*)(z1f + ((size_t)(sf0 + sf) * 54 + z1s + j) * 128 + lane * 2) = gelu4(acc[4 + j][sf]);
      if (type == 1) *(u2v*)(z1f + ((size_t)(sf0 + sf) * 54 + z1s + 3) * 128 + lane * 2) = zv[3][sf];
    }
  }
  u64 gv[2][4];
  for (int spin = 0; spin < (1 << 20); ++spin) {
    bool ok = true;
#pragma unroll
    for (int sf = 0; sf < 2; ++sf)
#pragma unroll
      for (int i = 0; i < 4; ++i) {
        gv[sf][i] = __hip_atomic_load(st + (4 * g + i) * 128 + sf * 16 + li, __ATOMIC_RELAXED, __HIP_MEMORY_SCOPE_AGENT);
        ok &= ((u32)gv[sf][i] != ~0u);
      }
    if (__all(ok)) break;
    __builtin_amdgcn_s_sleep(1);
  }
  __syncthreads();
  char* tb = smem + wave * 8192;
#pragma unroll
  for (int sf = 0; sf < 2; ++sf) {
    float s = 0.f;
#pragma unroll
    for (int i = 0; i < 4; ++i) s += __uint_as_float((u32)gv[sf][i]);
    const float mean = xsum_g(s) * (1.0f / 864.0f);
    float q = 0.f;
#pragma unroll
    for (int i = 0; i < 4; ++i) {
      const bool big = (4 * g + i) < 6;
      const float dm = __uint_as_float((u32)gv[sf][i]) * (big ? (1.0f / 64.0f) : (1.0f / 48.0f)) - mean;
      q += __uint_as_float((u32)(gv[sf][i] >> 32)) + (big ? 64.f : 48.f) * dm * dm;
    }
    const float var = xsum_g(q) * (1.0f / 864.0f);
    const float mb = rbf(mean);
    const float rinv = 1.0f / rbf(sqrtf(rbf(rbf(var) + 1e-5f)));
#pragma unroll
    for (int j = 0; j < 4; ++j) {
      u32 t0 = pk_bf(bflo(zv[j][sf][0]) - mb, bfhi(zv[j][sf][0]) - mb);
      u32 t1 = pk_bf(bflo(zv[j][sf][1]) - mb, bfhi(zv[j][sf][1]) - mb);
      u2v o = u2v{pk_bf(bflo(t0) * rinv, bfhi(t0) * rinv), pk_bf(bflo(t1) * rinv, bfhi(t1) * rinv)};
      *(u2v*)(tb + (sf * 16 + li) * 144 + j * 32 + g * 8) = o;
    }
  }
  {
    const int q = li >> 2, p = li & 3;
#pragma unroll
    for (int j = 0; j < 4; ++j) {
      s4 lo = __builtin_amdgcn_ds_read_tr16_b64_v4i16((LDS_AS s4*)(tb + (8 * g + q) * 144 + j * 32 + 8 * p));
      s4 hi = __builtin_amdgcn_ds_read_tr16_b64_v4i16((LDS_AS s4*)(tb + (8 * g + 4 + q) * 144 + j * 32 + 8 * p));
      u2v a = __builtin_bit_cast(u2v, lo), b = __builtin_bit_cast(u2v, hi);
      if (j < n2) *(u4v*)(z2t + (size_t)((zs + j) * 16 + li) * S + mt * 128 + wave * 32 + 8 * g) = u4v{a[0], a[1], b[0], b[1]};
    }
  }
}

constexpr int G2_NST = 6, G2_STB = 22 * 1024;
__global__ __launch_bounds__(256) void g2_kernel(const u16* __restrict__ w_sp, const u16* __restrict__ z2t,
                                                 const u32* __restrict__ z1f, const u16* __restrict__ b_sp,
                                                 u32* __restrict__ pf) {
  __shared__ __attribute__((aligned(16))) char smem[G2_NST * G2_STB];
  const int tid = threadIdx.x, lane = tid & 63;
  const int wave = __builtin_amdgcn_readfirstlane(tid >> 6);
  const int li = lane & 15, g = lane >> 4;
  const int xcd = blockIdx.x & 7, idx = blockIdx.x >> 3;
  const int tt = (xcd >> 1) * 8 + (idx >> 2), ft = (xcd & 1) * 4 + (idx & 3);
  const int wt = wave & 1, wf = wave >> 1;
  u2v z1v[4][2];
  float bias[2];
#pragma unroll
  for (int tf = 0; tf < 2; ++tf) {
    const int tfr = tt * 4 + wt * 2 + tf;
#pragma unroll
    for (int j = 0; j < 4; ++j) {
      int ff = ft * 7 + wf * 3 + j; ff = ff < 54 ? ff : 53;
      z1v[j][tf] = *(const u2v*)(z1f + ((size_t)tfr * 54 + ff) * 128 + lane * 2);
    }
    bias[tf] = bflo(b_sp[tfr * 16 + li]);
  }
  const u16* src[6];
  const int swz = ((lane & 7) ^ (lane >> 3)) * 8;
#pragma unroll
  for (int j = 0; j < 6; ++j) {
    int i = wave + 4 * j; i = i < 22 ? i : 0;
    if (j < 2) src[j] = w_sp + (size_t)(tt * 64 + 8 * i + (lane >> 3)) * S + swz;
    else { int r = ft * 112 + 8 * (i - 8) + (lane >> 3); r = r < FH ? r : FH - 1; r = r < 0 ? 0 : r; src[j] = z2t + (size_t)r * S + swz; }
  }
  auto stage = [&](int kt, int buf) {
    char* sb = smem + buf * G2_STB + wave * 1024;
#pragma unroll
    for (int j = 0; j < 5; ++j) GLDS16(src[j] + kt * 64, sb + j * 4096);
    if (wave < 2) GLDS16(src[5] + kt * 64, sb + 5 * 4096);
  };
  f4 acc[4][2];
#pragma unroll
  for (int j = 0; j < 4; ++j) { acc[j][0] = f4{0, 0, 0, 0}; acc[j][1] = f4{0, 0, 0, 0}; }
  const int sw = (g ^ (li & 7)) * 16;
  const int toff = (wt * 32 + li) * 128 + sw;
  const int foff = 8192 + (wf * 48 + li) * 128 + sw;
  auto compute = [&](int buf) {
    const char* sb = smem + buf * G2_STB;
#pragma unroll
    for (int kk = 0; kk < 2; ++kk) {
      const u4v b0 = *(const u4v*)(sb + (toff ^ (kk * 64)));
      const u4v b1 = *(const u4v*)(sb + (toff ^ (kk * 64)) + 2048);
#pragma unroll
      for (int j = 0; j < 4; ++j) {
        const u4v a = *(const u4v*)(sb + (foff ^ (kk * 64)) + j * 2048);
        acc[j][0] = mfma_b(a, b0, acc[j][0]);
        acc[j][1] = mfma_b(a, b1, acc[j][1]);
      }
    }
  };
#pragma unroll
  for (int s = 0; s < G2_NST - 1; ++s) stage(s, s);
  int buf = 0, nbuf = G2_NST - 1;
  for (int kt = 0; kt < 32 - (G2_NST - 1); ++kt) {
    if (wave < 2) WAITV(6 * (G2_NST - 2)); else WAITV(5 * (G2_NST - 2));
    asm volatile("s_waitcnt lgkmcnt(0)" ::: "memory");
    BAR();
    stage(kt + G2_NST - 1, nbuf);
    compute(buf);
    nbuf = buf;
    buf = buf + 1 == G2_NST ? 0 : buf + 1;
  }
#pragma unroll
  for (int rem = G2_NST - 2; rem >= 0; --rem) {
    if (wave < 2) { if (rem == 4) WAITV(24); else if (rem == 3) WAITV(18); else if (rem == 2) WAITV(12); else if (rem == 1) WAITV(6); else WAITV(0); }
    else { if (rem == 4) WAITV(20); else if (rem == 3) WAITV(15); else if (rem == 2) WAITV(10); else if (rem == 1) WAITV(5); else WAITV(0); }
    BAR();
    compute(buf);
    buf = buf + 1 == G2_NST ? 0 : buf + 1;
  }
#pragma unroll
  for (int tf = 0; tf < 2; ++tf) {
    const int tfr = tt * 4 + wt * 2 + tf;
#pragma unroll
    for (int j = 0; j < 4; ++j) {
      const int ff = ft * 7 + wf * 3 + j;
      if ((wf == 0 || j > 0) && ff < 54) {
        const f4 a = acc[j][tf];
        u32 t0 = pk_bf(a[0], a[1]), t1 = pk_bf(a[2], a[3]);
        t0 = pk_bf(bflo(t0) + bias[tf], bfhi(t0) + bias[tf]);
        t1 = pk_bf(bflo(t1) + bias[tf], bfhi(t1) + bias[tf]);
        const u2v z = z1v[j][tf];
        u2v o = u2v{pk_bf(bflo(t0) * bflo(z[0]), bfhi(t0) * bfhi(z[0])), pk_bf(bflo(t1) * bflo(z[1]), bfhi(t1) * bfhi(z[1]))};
        *(u2v*)(pf + ((size_t)tfr * 27 + (ff >> 1)) * 256 + lane * 4 + (ff & 1) * 2) = o;
      }
    }
  }
}

constexpr int G3_NST = 5, G3_STB = 27 * 1024;
__global__ __launch_bounds__(256) void g3_kernel(const u32* __restrict__ pf, const u32* __restrict__ Wt3,
                                                 const u16* __restrict__ h, u16* __restrict__ out) {
  __shared__ __attribute__((aligned(16))) char smem[G3_NST * G3_STB];
  const int tid = threadIdx.x, lane = tid & 63;
  const int wave = __builtin_amdgcn_readfirstlane(tid >> 6);
  const int li = lane & 15, g = lane >> 4;
  const int xcd = blockIdx.x & 7, idx = blockIdx.x >> 3;
  const int tt = xcd * 4 + (idx >> 3), dt = idx & 7;
  const int trow = tt * 64 + wave * 16 + li;
  u2v hv[5];
#pragma unroll
  for (int j = 0; j < 5; ++j) hv[j] = *(const u2v*)(h + (size_t)trow * D + dt * 80 + j * 16 + g * 4);
  const u32* psrc = pf + (size_t)(tt * 4 + wave) * 27 * 256 + lane * 4;
  const u32* wsrc[4];
#pragma unroll
  for (int j = 0; j < 4; ++j) {
    int i = wave + 4 * j; i = i < 15 ? i : 0;
    const int kk = i / 5, df = i - 5 * kk;
    wsrc[j] = Wt3 + (size_t)(kk * 40 + dt * 5 + df) * 256 + lane * 4;
  }
  auto stage = [&](int kt, int buf) {
    char* sb = smem + buf * G3_STB;
#pragma unroll
    for (int i = 0; i < 3; ++i) GLDS16(psrc + (kt * 3 + i) * 256, sb + (wave * 3 + i) * 1024);
#pragma unroll
    for (int j = 0; j < 3; ++j) GLDS16(wsrc[j] + (size_t)kt * 3 * 40 * 256, sb + 12288 + (wave + 4 * j) * 1024);
    if (wave < 3) GLDS16(wsrc[3] + (size_t)kt * 3 * 40 * 256, sb + 12288 + (wave + 12) * 1024);
  };
  f4 acc[5];
#pragma unroll
  for (int j = 0; j < 5; ++j) acc[j] = f4{0, 0, 0, 0};
  auto compute = [&](int buf) {
    const char* sb = smem + buf * G3_STB + lane * 16;
#pragma unroll
    for (int kk = 0; kk < 3; ++kk) {
      const u4v b = *(const u4v*)(sb + (wave * 3 + kk) * 1024);
#pragma unroll
      for (int j = 0; j < 5; ++j) {
        const u4v a = *(const u4v*)(sb + 12288 + (kk * 5 + j) * 1024);
        acc[j] = mfma_b(a, b, acc[j]);
      }
    }
  };
#pragma unroll
  for (int s = 0; s < G3_NST - 1; ++s) stage(s, s);
#pragma unroll
  for (int kt = 0; kt < 9; ++kt) {
    const int rem = (9 - 1 - kt) < (G3_NST - 2) ? (9 - 1 - kt) : (G3_NST - 2);
    if (wave < 3) {
      if (rem == 3) WAITV(21); else if (rem == 2) WAITV(14); else if (rem == 1) WAITV(7); else WAITV(0);
    } else {
      if (rem == 3) WAITV(18); else if (rem == 2) WAITV(12); else if (rem == 1) WAITV(6); else WAITV(0);
    }
    asm volatile("s_waitcnt lgkmcnt(0)" ::: "memory");
    BAR();
    if (kt + G3_NST - 1 < 9) stage(kt + G3_NST - 1, (kt + G3_NST - 1) % G3_NST);
    compute(kt % G3_NST);
  }
#pragma unroll
  for (int j = 0; j < 5; ++j) {
    u32 t0 = pk_bf(acc[j][0], acc[j][1]), t1 = pk_bf(acc[j][2], acc[j][3]);
    u2v o = u2v{pk_bf(bflo(t0) + bflo(hv[j][0]), bfhi(t0) + bfhi(hv[j][0])), pk_bf(bflo(t1) + bflo(hv[j][1]), bfhi(t1) + bfhi(hv[j][1]))};
    *(u2v*)(out + (size_t)trow * D + dt * 80 + j * 16 + g * 4) = o;
  }
}


extern "C" { long long dbg_offsets[16] = {28<<20, 32<<20, -1, 44<<20, -1, -1, -1}; }

extern "C" void kernel_launch(void* const* d_in, const int* in_sizes, int n_in,
                              void* d_out, int out_size, void* d_ws, size_t ws_size,
                              hipStream_t stream) {
  const u16* x=(const u16*)d_in[0]; const u16* wr=(const u16*)d_in[1]; const u16* wi=(const u16*)d_in[2];
  const u16* w_in=(const u16*)d_in[4]; const u16* w_sp=(const u16*)d_in[5]; const u16* b_sp=(const u16*)d_in[6]; const u16* w_out=(const u16*)d_in[7];
  char* ws=(char*)d_ws;
  u32* T=(u32*)ws;
  u32* Tp=(u32*)(ws+(4<<20));
  u32* Wp=(u32*)(ws+(8<<20));
  u32* W2=(u32*)(ws+(12<<20));
  u32* wmax=(u32*)(ws+(13<<20));
  float* unscale=(float*)(ws+(13<<20)+4096);
  u32* stats=(u32*)(ws+(14<<20));
  u32* Wt1=(u32*)(ws+(16<<20));
  u32* Wt3=(u32*)(ws+(20<<20));
  u16* h=(u16*)(ws+(28<<20));
  u16* l2=(u16*)(ws+(32<<20));
  u32* z1f=(u32*)(ws+(36<<20));
  u16* z2t=(u16*)(ws+(44<<20));
  u32* pf=(u32*)(ws+(48<<20));
  k1_kernel<<<256+NPREP,256,0,stream>>>(x,wr,wi,T,Wp,W2,wmax);
  prep_kernel<<<(NRT1+NRT3)/2,128,0,stream>>>(w_in,w_out,Wt1,Wt3,stats);
  k2_kernel<<<328,128,0,stream>>>(T,Wp,W2,wmax,Tp,unscale);
  k3_kernel<<<256,256,0,stream>>>(x,Tp,unscale,h,l2,nullptr);
  g1_kernel<<<256,256,0,stream>>>(l2,Wt1,z1f,z2t,(u64*)stats);
  g2_kernel<<<256,256,0,stream>>>(w_sp,z2t,z1f,b_sp,pf);
  g3_kernel<<<256,256,0,stream>>>(pf,Wt3,h,(u16*)d_out);
}
```

```cpp
#include <hip/hip_runtime.h>
#include <hip/hip_bf16.h>
#include <stdint.h>
#define STAMP(k, i)
#define STAMPC(k, i)

#define PF_WSP_K2 352
#define PF_WSP_K3 1
#define PF_WT1 0
typedef unsigned short u16;
typedef unsigned int u32;
typedef _Float16 h2 __attribute__((ext_vector_type(2)));
typedef _Float16 h8 __attribute__((ext_vector_type(8)));
typedef __bf16 bf2 __attribute__((ext_vector_type(2)));
typedef __bf16 bf8 __attribute__((ext_vector_type(8)));
typedef float f2 __attribute__((ext_vector_type(2)));
typedef float f4 __attribute__((ext_vector_type(4)));
typedef short s4 __attribute__((ext_vector_type(4)));
typedef u32 u2v __attribute__((ext_vector_type(2)));
typedef u32 u4v __attribute__((ext_vector_type(4)));
#define DI __device__ __forceinline__
#define LDS_AS __attribute__((address_space(3)))
#define PIN(x) asm volatile("" : "+v"(x))
#define GLDS16(src, dst) __builtin_amdgcn_global_load_lds((const u32*)(src), (LDS_AS u32*)(dst), 16, 0, 0)
#define WAITV(n) asm volatile("s_waitcnt vmcnt(%0)" ::"n"(n) : "memory")

constexpr int S = 2048, D = 640, DF = 321, FF = 1728, FH = 864;
constexpr int TROW_UNITS = 648;
constexpr int NUNITS = 641;
constexpr int NPREP = 168;

constexpr size_t WS_T = 0, WS_TP = 4u << 20, WS_WP = 8u << 20, WS_W2 = 12u << 20, WS_WMAX = 13u << 20, WS_UNS = (13u << 20) + 8192,
                 WS_STATS = 14u << 20, WS_CNT = 15u << 20, WS_WT1 = 16u << 20, WS_WT3 = 20u << 20, WS_H = 28u << 20, WS_L2 = 32u << 20,
                 WS_Z1 = 36u << 20, WS_Z2 = 44u << 20, WS_P = 48u << 20;
#define PINS(x) asm volatile("" ::"s"(x))
#define GLDS4(src, dst) __builtin_amdgcn_global_load_lds((const u32*)(src), (LDS_AS u32*)(dst), 4, 0, 0)
#define touch(p) GLDS4(p, scratch)

constexpr int AUX_SC1 = 16;
DI __amdgpu_buffer_rsrc_t ws_rsrc(char* ws) { return __builtin_amdgcn_make_buffer_rsrc((void*)ws, 0, 64 << 20, 0x00020000); }
#define ST_AUX 16
DI void st16(__amdgpu_buffer_rsrc_t rs, u32 off, u4v v) { __builtin_amdgcn_raw_buffer_store_b128(v, rs, off, 0, ST_AUX); }
DI void st8(__amdgpu_buffer_rsrc_t rs, u32 off, u2v v) { __builtin_amdgcn_raw_buffer_store_b64(v, rs, off, 0, ST_AUX); }
DI float bflo(u32 v) { return __uint_as_float(v << 16); }
DI float bfhi(u32 v) { return __uint_as_float(v & 0xffff0000u); }
DI u32 pk_bf(float a, float b) { f2 v = {a, b}; return __builtin_bit_cast(u32, __builtin_convertvector(v, bf2)); }
DI u32 pk_h(float a, float b) { f2 v = {a, b}; return __builtin_bit_cast(u32, __builtin_convertvector(v, h2)); }
DI float rbf(float a) { return bflo(pk_bf(a, a)); }
DI float cosr(float rev) { return __builtin_amdgcn_cosf(rev); }
DI float sinr(float rev) { return __builtin_amdgcn_sinf(rev); }
DI f4 mfma_h(u4v a, u4v b, f4 c) {
  return __builtin_amdgcn_mfma_f32_16x16x32_f16(__builtin_bit_cast(h8, a), __builtin_bit_cast(h8, b), c, 0, 0, 0);
}
template <int CTRL> DI float dpp_add(float v) {
  int t = __builtin_amdgcn_update_dpp(0, __float_as_int(v), CTRL, 0xf, 0xf, true);
  return v + __int_as_float(t);
}
DI float xsum_g(float v) {
  u32 b = __float_as_uint(v);
  auto r = __builtin_amdgcn_permlane16_swap(b, b, false, false);
  v = __uint_as_float(r[0]) + __uint_as_float(r[1]);
  b = __float_as_uint(v);
  auto s = __builtin_amdgcn_permlane32_swap(b, b, false, false);
  return __uint_as_float(s[0]) + __uint_as_float(s[1]);
}
DI float sum64(float v) {
  v = dpp_add<0xB1>(v);
  v = dpp_add<0x4E>(v);
  v = dpp_add<0x141>(v);
  v = dpp_add<0x140>(v);
  return xsum_g(v);
}
template <int CTRL> DI u32 dpp_maxu(u32 v) {
  u32 t = (u32)__builtin_amdgcn_update_dpp(0, (int)v, CTRL, 0xf, 0xf, true);
  return v > t ? v : t;
}
DI u32 max64u(u32 v) {
  v = dpp_maxu<0xB1>(v); v = dpp_maxu<0x4E>(v); v = dpp_maxu<0x141>(v); v = dpp_maxu<0x140>(v);
  auto r = __builtin_amdgcn_permlane16_swap(v, v, false, false);
  v = r[0] > r[1] ? r[0] : r[1];
  auto s = __builtin_amdgcn_permlane32_swap(v, v, false, false);
  return s[0] > s[1] ? s[0] : s[1];
}
DI float sum32(float v) {
  v = dpp_add<0xB1>(v); v = dpp_add<0x4E>(v); v = dpp_add<0x141>(v); v = dpp_add<0x140>(v);
  u32 b = __float_as_uint(v);
  auto r = __builtin_amdgcn_permlane16_swap(b, b, false, false);
  return __uint_as_float(r[0]) + __uint_as_float(r[1]);
}
DI void ln_row(f2 (&v)[10]) {
  f2 s = ((v[0] + v[1]) + (v[2] + v[3])) + ((v[4] + v[5]) + (v[6] + v[7])) + (v[8] + v[9]);
  const float mean = sum32(s[0] + s[1]) * (1.0f / 640.0f);
  f2 q0 = f2{0.f, 0.f}, q1 = f2{0.f, 0.f};
#pragma unroll
  for (int i = 0; i < 10; i += 2) { v[i] -= mean; v[i + 1] -= mean; q0 += v[i] * v[i]; q1 += v[i + 1] * v[i + 1]; }
  q0 += q1;
  const float var = sum32(q0[0] + q0[1]) * (1.0f / 640.0f);
  const float rinv = __builtin_amdgcn_rcpf(rbf(__builtin_amdgcn_sqrtf(rbf(rbf(var) + 1e-5f))));
  const float off = (mean - rbf(mean)) * rinv;
#pragma unroll
  for (int i = 0; i < 10; ++i) v[i] = v[i] * rinv + off;
}
#define SB() __builtin_amdgcn_sched_barrier(0)
template <bool SWAP> DI u4v cmul_pack(f4 re, f4 im, f4 wr, f4 wi) {
  const f4 ore = re * wr - im * wi, oim = re * wi + im * wr;
  u4v v;
#pragma unroll
  for (int r = 0; r < 4; ++r) v[r] = SWAP ? pk_h(oim[r], ore[r]) : pk_h(ore[r], oim[r]);
  return v;
}

struct __attribute__((packed, aligned(2))) U4U { u32 a, b, c, d; };

constexpr int K1_F40 = 10272, K1_TW = K1_F40 + 18432, K1_R16 = K1_TW + 6144, K1_LDS = K1_R16 + 4096;
__global__ __launch_bounds__(256, 2) void k1_kernel(const u16* __restrict__ x, const u16* __restrict__ wr,
                                                    const u16* __restrict__ wi, char* __restrict__ ws) {
  __shared__ __attribute__((aligned(16))) char smem[K1_LDS];
  const int tid = threadIdx.x, lane = tid & 63;
  const int wave = __builtin_amdgcn_readfirstlane(tid >> 6);
  const int li = lane & 15, g = lane >> 4;
  STAMP(0,0);
  PINS(x); PINS(wr); PINS(wi); PINS(ws);
  if (blockIdx.x >= 256) {
    const int pb = blockIdx.x - 256;
    const int cp = pb & 7, cc = pb >> 3;
    const int c0 = cc < 20 ? cc * 16 : 305;
    const int e = tid;
    const size_t roff = (size_t)(cp + 8 * e) * DF + c0;
    const U4U* pr = (const U4U*)(wr + roff);
    const U4U* pi = (const U4U*)(wi + roff);
    U4U r0 = pr[0], r1 = pr[1], i0 = pi[0], i1 = pi[1];
    const bool mir = cp > 4;
    const bool sp = (cp & 3) == 0;
    const int ep = mir ? 255 - e : e;
    const u32 flip = mir ? 0x80000000u : 0u;
    const int q1 = (mir ? 8 - cp : cp) - 1;
    const int kb = mir ? 39 - cc : cc;
    const int t1 = sp ? 1920 + 32 * cc + (cp >> 2) : (q1 * 160 + (kb >> 2) * 16 + (mir ? 16 : 0)) * 4 + (kb & 3);
    const int step = sp ? 2 : (mir ? -4 : 4);
    const int t0 = mir ? (q1 * 160 + ((kb + 1) >> 2) * 16) * 4 + ((kb + 1) & 3) : t1;
    u32* dst = (u32*)(ws + WS_WP) + ((ep & 15) + 16 * (ep >> 6)) * 4 + ((ep >> 4) & 3);
    u32 rr[8] = {r0.a, r0.b, r0.c, r0.d, r1.a, r1.b, r1.c, r1.d};
    u32 ii[8] = {i0.a, i0.b, i0.c, i0.d, i1.a, i1.b, i1.c, i1.d};
    u32 vals[16];
#pragma unroll
    for (int j = 0; j < 16; ++j) {
      u32 a = rr[j >> 1], b = ii[j >> 1];
      vals[j] = ((j & 1) ? __builtin_amdgcn_perm(b, a, 0x07060302u) : __builtin_amdgcn_perm(b, a, 0x05040100u)) ^ flip;
    }
    const bool edge = (cc == 0 || cc == 20);
    const u32 v0 = cc < 20 ? vals[0] : vals[15];
    if (!(mir && edge)) dst[(ptrdiff_t)t0 * 256] = v0;
    if (cc < 20) {
      u32* d = dst + (ptrdiff_t)t1 * 256;
#pragma unroll
      for (int j = 1; j < 16; ++j) d[(ptrdiff_t)step * 256 * j] = vals[j];
    }
    if (edge && (mir || sp)) {
      const int e2 = (cp == 0) ? ((256 - e) & 255) : 255 - e;
      const int dpos2 = ((e2 & 15) + 16 * (e2 >> 6)) * 4 + ((e2 >> 4) & 3);
      const int cls = mir ? 8 - cp : cp;
      ((u32*)(ws + WS_W2))[(cls * 2 + (cc == 20)) * 256 + dpos2] = v0 ^ flip ^ 0x80000000u;
    }
    u32 mx = 0;
#pragma unroll
    for (int j = 0; j < 16; ++j) {
      const u32 a = vals[j] & 0x7fff7fffu;
      mx = max(mx, max(a & 0xffffu, a >> 16));
    }
    mx = max64u(mx);
    if (lane == 0) ((u32*)(ws + WS_WMAX))[pb * 4 + wave] = mx;
    STAMP(0,5);
    return;
  }
  const int b = (blockIdx.x & 7) * 32 + (blockIdx.x >> 3);
  const int a = 2 * wave + (lane >> 5), j = lane & 31;
  const u16* xr = x + (size_t)(256 * a + b) * D;
  const u4v x0 = *(const u4v*)(xr + 8 * j);
  const u4v x1 = *(const u4v*)(xr + 256 + 8 * j);
  const u2v x2 = *(const u2v*)(xr + 512 + 4 * j);
#pragma unroll
  for (int it = 0; it < 3; ++it) {
    const int item = wave + 4 * it;
    if (it < 2 || wave == 0) {
      const int to = item / 3, tk = item - 3 * to;
      const int kb = 16 * to + li;
      const float s8 = (tk < 2 || g < 2) ? 0.125f : 0.f;
      u4v fre, fim;
#pragma unroll
      for (int jj = 0; jj < 4; ++jj) {
        const float ang = (float)(kb * (16 * tk + 4 * g + jj)) * (1.0f / 40.0f);
        const float c = cosr(ang) * s8, s = sinr(ang) * s8;
        fre[jj] = pk_h(c, s);
        fim[jj] = pk_h(-s, c);
      }
      *(u4v*)(smem + K1_F40 + item * 2048 + lane * 16) = fre;
      *(u4v*)(smem + K1_F40 + item * 2048 + 1024 + lane * 16) = fim;
    } else {
      const int t = wave - 1;
      f4 twr, twi;
#pragma unroll
      for (int r = 0; r < 4; ++r) {
        const float ang = (float)((16 * t + 4 * g + r) * li) * (1.0f / 640.0f);
        twr[r] = cosr(ang) * 0.125f; twi[r] = -sinr(ang) * 0.125f;
      }
      *(f4*)(smem + K1_TW + t * 2048 + lane * 16) = twr;
      *(f4*)(smem + K1_TW + t * 2048 + 1024 + lane * 16) = twi;
    }
  }
  {
    const int ph = 2 * wave + (lane >> 5);
    float cs[8];
#pragma unroll
    for (int jj = 0; jj < 8; ++jj) cs[jj] = cosr((float)(((8 * (g & 1) + jj) * li + 2 * ph) & 15) * 0.0625f);
    *(u4v*)(smem + K1_R16 + wave * 1024 + lane * 16) = u4v{pk_h(cs[0], cs[1]), pk_h(cs[2], cs[3]), pk_h(cs[4], cs[5]), pk_h(cs[6], cs[7])};
  }
  if (tid < 4) *(u32*)(smem + 10240 + tid * 4) = 0u;
  asm volatile("s_waitcnt lgkmcnt(0)" ::: "memory");
  __builtin_amdgcn_s_barrier();
  asm volatile("" ::: "memory");
  const int q = wave;
  u4v bre[4], bim[4], fre[9], fim[9];
  f4 twr[3], twi[3];
  u32 toff[3][4];
  {
    const int a0 = g >> 1;
#pragma unroll
    for (int ks = 0; ks < 4; ++ks) {
      const int ph = (q == 0) ? (a0 ? 1 : 7) : (2 * ks + a0) * q;
      bre[ks] = *(const u4v*)(smem + K1_R16 + (ph & 7) * 512 + (lane & 31) * 16);
      bim[ks] = *(const u4v*)(smem + K1_R16 + ((ph + 2) & 7) * 512 + (lane & 31) * 16);
    }
#pragma unroll
    for (int i = 0; i < 9; ++i) {
      fre[i] = *(const u4v*)(smem + K1_F40 + i * 2048 + lane * 16);
      fim[i] = *(const u4v*)(smem + K1_F40 + i * 2048 + 1024 + lane * 16);
    }
    const float qs = q == 0 ? 0.70710678118f : 1.f;
#pragma unroll
    for (int t = 0; t < 3; ++t) {
      twr[t] = *(const f4*)(smem + K1_TW + t * 2048 + lane * 16) * qs;
      twi[t] = *(const f4*)(smem + K1_TW + t * 2048 + 1024 + lane * 16) * qs;
      PIN(twr[t]); PIN(twi[t]);
#pragma unroll
      for (int r = 0; r < 4; ++r) {
        const int k2 = li + 16 * (16 * t + 4 * g + r);
        const int pos = k2 <= 320 ? 2 * k2 : 2 * (640 - k2) + 1;
        toff[t][r] = (u32)WS_T + (u32)b * (TROW_UNITS * 16) + (q > 0 ? ((q - 1) * 160 + 64 * t + lane) * 16 : 480 * 16 + pos * 4);
        if (t == 2 && lane >= 32) toff[t][r] = 0xfffffff0u;
        PIN(toff[t][r]);
      }
    }
#pragma unroll
    for (int ks = 0; ks < 4; ++ks) { PIN(bre[ks]); PIN(bim[ks]); }
#pragma unroll
    for (int i = 0; i < 9; ++i) { PIN(fre[i]); PIN(fim[i]); }
  }
  STAMP(0,1);
  {
    f2 v[10];
#pragma unroll
    for (int i = 0; i < 4; ++i) { v[i] = f2{bflo(x0[i]), bfhi(x0[i])}; v[4 + i] = f2{bflo(x1[i]), bfhi(x1[i])}; }
    v[8] = f2{bflo(x2[0]), bfhi(x2[0])}; v[9] = f2{bflo(x2[1]), bfhi(x2[1])};
    ln_row(v);
    char* ub = smem + a * 1280;
    *(u4v*)(ub + 16 * j) = u4v{pk_h(v[0][0], v[0][1]), pk_h(v[1][0], v[1][1]), pk_h(v[2][0], v[2][1]), pk_h(v[3][0], v[3][1])};
    *(u4v*)(ub + 512 + 16 * j) = u4v{pk_h(v[4][0], v[4][1]), pk_h(v[5][0], v[5][1]), pk_h(v[6][0], v[6][1]), pk_h(v[7][0], v[7][1])};
    *(u2v*)(ub + 1024 + 8 * j) = u2v{pk_h(v[8][0], v[8][1]), pk_h(v[9][0], v[9][1])};
  }
  __syncthreads();
  STAMP(0,2);
  u4v b2[3];
  {
    const int qq = li >> 2, p = li & 3;
    const char* abase = smem + (8 * g + qq) * 80 + 8 * p;
    u4v af[3][4];
#pragma unroll
    for (int t = 0; t < 3; ++t)
#pragma unroll
      for (int ks = 0; ks < 4; ++ks) {
        s4 lo = __builtin_amdgcn_ds_read_tr16_b64_v4i16((LDS_AS s4*)(abase + ks * 2560 + t * 32));
        s4 hi = __builtin_amdgcn_ds_read_tr16_b64_v4i16((LDS_AS s4*)(abase + ks * 2560 + 320 + t * 32));
        u2v l2 = __builtin_bit_cast(u2v, lo), h2v = __builtin_bit_cast(u2v, hi);
        af[t][ks] = u4v{l2[0], l2[1], h2v[0], h2v[1]};
      }
    SB();
    f4 are[3], aim[3];
#pragma unroll
    for (int t = 0; t < 3; ++t) {
      are[t] = f4{0, 0, 0, 0}; aim[t] = f4{0, 0, 0, 0};
#pragma unroll
      for (int ks = 0; ks < 4; ++ks) {
        are[t] = mfma_h(af[t][ks], bre[ks], are[t]);
        aim[t] = mfma_h(af[t][ks], bim[ks], aim[t]);
      }
    }
#pragma unroll
    for (int t = 0; t < 3; ++t) b2[t] = cmul_pack<false>(are[t], aim[t], twr[t], twi[t]);
  }
  const __amdgpu_buffer_rsrc_t rs = ws_rsrc(ws);
  f4 ore[3], oim[3];
#pragma unroll
  for (int tk = 0; tk < 3; ++tk)
#pragma unroll
    for (int to = 0; to < 3; ++to) {
      ore[to] = mfma_h(fre[to * 3 + tk], b2[tk], tk ? ore[to] : f4{0, 0, 0, 0});
      oim[to] = mfma_h(fim[to * 3 + tk], b2[tk], tk ? oim[to] : f4{0, 0, 0, 0});
    }
  u4v o[3];
#pragma unroll
  for (int to = 0; to < 3; ++to)
#pragma unroll
    for (int r = 0; r < 4; ++r) o[to][r] = pk_h(ore[to][r], oim[to][r]);
  if (q > 0) {
#pragma unroll
    for (int to = 0; to < 3; ++to) st16(rs, toff[to][0], o[to]);
  } else {
#pragma unroll
    for (int to = 0; to < 3; ++to)
#pragma unroll
      for (int r = 0; r < 4; ++r) __builtin_amdgcn_raw_buffer_store_b32(o[to][r], rs, toff[to][r], 0, 0);
    __builtin_amdgcn_raw_buffer_store_b32(lane ? o[1][0] : o[0][0], rs, lane == 0 ? toff[0][0] + 4 : (lane == 16 ? toff[1][0] + 4 : 0xfffffff0u), 0, 0);
  }
  STAMP(0,4);
}

constexpr int NRT1 = 20 * 27, NRT3 = 27 * 10;
constexpr int K2_FFT_BLOCKS = 168, K2_PREP_BLOCKS = 88;

__global__ __launch_bounds__(256, 2) void k2_kernel(char* __restrict__ ws, const u16* __restrict__ w_in,
                                                    const u16* __restrict__ w_out, const u16* __restrict__ w_sp) {
  PINS(ws); PINS(w_in); PINS(w_out); PINS(w_sp);
  const u32* T = (const u32*)(ws + WS_T); const u32* Wp = (const u32*)(ws + WS_WP); const u32* W2 = (const u32*)(ws + WS_W2);
  const u32* wmax = (const u32*)(ws + WS_WMAX); u32* Tp = (u32*)(ws + WS_TP); float* unscale = (float*)(ws + WS_UNS);
  u32* Wt1 = (u32*)(ws + WS_WT1); u32* Wt3 = (u32*)(ws + WS_WT3); u32* stats = (u32*)(ws + WS_STATS);
  extern __shared__ __attribute__((aligned(16))) char smem[];
  const int tid = threadIdx.x, lane = tid & 63;
  const int wave = __builtin_amdgcn_readfirstlane(tid >> 6);
  const int li = lane & 15, g = lane >> 4;
  if (blockIdx.x >= K2_FFT_BLOCKS) {
    STAMP(1,0);
    const int wid = (blockIdx.x - K2_FFT_BLOCKS) * 4 + wave;
    const __amdgpu_buffer_rsrc_t rs = ws_rsrc(ws);
    char* lds = smem + wave * 12288;
#pragma unroll
    for (int i = 0; i < 3; ++i) {
      const int task = wid + i * (K2_PREP_BLOCKS * 4);
      const bool one = task < NRT1;
      const int t = one ? task : (task < NRT1 + NRT3 ? task - NRT1 : 0);
      const int ks = one ? t / 27 : t / 10, cg = t - ks * (one ? 27 : 10);
      const u16* src = (one ? w_in : w_out) + (size_t)(ks * 32 + (lane >> 3)) * (one ? FF : D) + cg * 64 + (lane & 7) * 8;
#pragma unroll
      for (int j = 0; j < 4; ++j) GLDS16(src + (size_t)j * 8 * (one ? FF : D), lds + i * 4096 + j * 1024);
    }
    { char* scratch = smem + 49152;
      touch((const char*)w_sp + ((size_t)wid * 64 + lane) * 128); }
    st16(rs, wid < 256 ? (u32)WS_STATS + wid * 1024 + lane * 16 : 0xfffffff0u, u4v{~0u, ~0u, ~0u, ~0u});
    WAITV(2);
    const int q = li >> 2, p = li & 3;
#pragma unroll
    for (int i = 0; i < 3; ++i) {
      const int task = wid + i * (K2_PREP_BLOCKS * 4);
      const bool one = task < NRT1;
      const int t = one ? task : task - NRT1;
      const int ks = one ? t / 27 : t / 10, cg = t - ks * (one ? 27 : 10);
      const u32 dst = (one ? (u32)WS_WT1 + (ks * 108 + cg * 4) * 1024 : (u32)WS_WT3 + (ks * 40 + cg * 4) * 1024) + lane * 16;
      const int r0 = one ? 8 * g + q : 4 * g + q, r1 = one ? r0 + 4 : r0 + 16;
#pragma unroll
      for (int f = 0; f < 4; ++f) {
        s4 lo = __builtin_amdgcn_ds_read_tr16_b64_v4i16((LDS_AS s4*)(lds + i * 4096 + r0 * 128 + f * 32 + 8 * p));
        s4 hi = __builtin_amdgcn_ds_read_tr16_b64_v4i16((LDS_AS s4*)(lds + i * 4096 + r1 * 128 + f * 32 + 8 * p));
        u2v a = __builtin_bit_cast(u2v, lo), b = __builtin_bit_cast(u2v, hi);
        if (task < NRT1 + NRT3) st16(rs, dst + f * 1024, u4v{a[0], a[1], b[0], b[1]});
      }
    }
    STAMP(1,1);
    return;
  }
  const int P = (blockIdx.x & 7) * 21 + (blockIdx.x >> 3);
  if (P > 160) return;
  const int U = 4 * P + wave;
  STAMP(2,0);
#pragma unroll
  for (int i = 0; i < 4; ++i) {
    const int bb = (wave * 4 + i) * 16 + (lane >> 2);
    GLDS16(T + ((size_t)bb * TROW_UNITS + 4 * P + ((lane & 3) ^ ((lane >> 4) & 3))) * 4, smem + (wave * 4 + i) * 1024);
  }
  u4v mxv[3];
#pragma unroll
  for (int i = 0; i < 3; ++i) mxv[i] = *(const u4v*)(wmax + (i < 2 || lane < 40 ? i * 256 + lane * 4 : 0));
  u4v Wv[4];
#pragma unroll
  for (int t = 0; t < 4; ++t) Wv[t] = *(const u4v*)(Wp + ((size_t)((U < NUNITS ? U : 0) * 4 + t) * 64 + lane) * 4);
  STAMP(2,5);
  const bool special = P >= 120;
  const int cB = special ? 4 : (P / 40 + 1);
  const float osc = special ? 0.125f : 0.25f;
  {
    char* cb = smem + 16384;
    const int mA = (wave & 1) ? li : 4 * g, mB = (wave & 1) ? 4 * g : li;
    const float inv = wave < 2 ? (1.0f / 128.0f) : (1.0f / 2048.0f);
    const float amp = wave == 0 ? 1.f : (wave == 1 ? osc : 0.25f);
    f4 c, s;
#pragma unroll
    for (int jj = 0; jj < 4; ++jj) {
      const int a = (wave & 1) ? mA * (cB + 8 * (mB + jj)) : (mA + jj) * (cB + 8 * mB);
      const float ang = (float)a * inv;
      c[jj] = cosr(ang) * amp; s[jj] = sinr(ang) * amp;
    }
    if (wave < 2) {
      *(u4v*)(cb + wave * 2048 + lane * 16) = u4v{pk_h(c[0], s[0]), pk_h(c[1], s[1]), pk_h(c[2], s[2]), pk_h(c[3], s[3])};
      *(u4v*)(cb + wave * 2048 + 1024 + lane * 16) = u4v{pk_h(-s[0], c[0]), pk_h(-s[1], c[1]), pk_h(-s[2], c[2]), pk_h(-s[3], c[3])};
    } else {
      *(f4*)(cb + wave * 2048 + lane * 16) = c;
      *(f4*)(cb + wave * 2048 + 1024 + lane * 16) = s;
    }
    if (wave < 3 && (wave == 0 || special)) {
      const float inv2 = wave < 2 ? 0.0625f : (1.0f / 256.0f);
#pragma unroll
      for (int jj = 0; jj < 4; ++jj) {
        const float ang = (float)(li * (4 * g + jj)) * inv2;
        c[jj] = cosr(ang) * amp; s[jj] = sinr(ang) * amp;
      }
      if (wave < 2) {
        *(u4v*)(cb + (4 + wave) * 2048 + lane * 16) = u4v{pk_h(c[0], s[0]), pk_h(c[1], s[1]), pk_h(c[2], s[2]), pk_h(c[3], s[3])};
        *(u4v*)(cb + (4 + wave) * 2048 + 1024 + lane * 16) = u4v{pk_h(-s[0], c[0]), pk_h(-s[1], c[1]), pk_h(-s[2], c[2]), pk_h(-s[3], c[3])};
      } else {
        *(f4*)(cb + 6 * 2048 + lane * 16) = c;
        *(f4*)(cb + 6 * 2048 + 1024 + lane * 16) = s;
      }
    }
  }
  asm volatile("s_waitcnt lgkmcnt(0)" ::: "memory");
  __builtin_amdgcn_s_barrier();
  asm volatile("" ::: "memory");
  STAMP(2,7);
  u4v Fre, Fim, P1re[2], P1im[2], P4re[2], P4im[2];
  f4 tfr[2], tfs[2], tir[2], tis[2];
  {
    const char* cb = smem + 16384 + lane * 16;
    Fre = *(const u4v*)(cb + 4 * 2048); Fim = *(const u4v*)(cb + 4 * 2048 + 1024);
    P1re[1] = *(const u4v*)(cb); P1im[1] = *(const u4v*)(cb + 1024);
    P4re[1] = *(const u4v*)(cb + 2048); P4im[1] = *(const u4v*)(cb + 2048 + 1024);
    tfr[1] = *(const f4*)(cb + 2 * 2048); tfs[1] = *(const f4*)(cb + 2 * 2048 + 1024);
    tir[1] = *(const f4*)(cb + 3 * 2048); tis[1] = *(const f4*)(cb + 3 * 2048 + 1024);
    const int o1 = special ? 4 * 2048 : 0, o4 = special ? 5 * 2048 : 2048, of = special ? 6 * 2048 : 2 * 2048, oi = special ? 6 * 2048 : 3 * 2048;
    P1re[0] = *(const u4v*)(cb + o1); P1im[0] = *(const u4v*)(cb + o1 + 1024);
    P4re[0] = *(const u4v*)(cb + o4); P4im[0] = *(const u4v*)(cb + o4 + 1024);
    tfr[0] = *(const f4*)(cb + of); tfs[0] = *(const f4*)(cb + of + 1024);
    tir[0] = *(const f4*)(cb + oi); tis[0] = *(const f4*)(cb + oi + 1024);
    PIN(Fre); PIN(Fim);
#pragma unroll
    for (int c = 0; c < 2; ++c) { PIN(P1re[c]); PIN(P1im[c]); PIN(P4re[c]); PIN(P4im[c]); PIN(tfr[c]); PIN(tfs[c]); PIN(tir[c]); PIN(tis[c]); }
  }
  STAMP(2,1);
  u32 mx = 0;
#pragma unroll
  for (int i = 0; i < 3; ++i) mx = max(max(max(mxv[i][0], mxv[i][1]), max(mxv[i][2], mxv[i][3])), mx);
  mx = max64u(mx);
  int E = (int)(mx >> 7);
  E = E < 1 ? 1 : (E > 253 ? 253 : E);
  const float sig = __uint_as_float((u32)(254 - E) << 23) * 0.25f;
  if (U == 0 && lane == 0) unscale[0] = __uint_as_float((u32)E << 23);
  f4 wr4[4], wi4[4];
#pragma unroll
  for (int t = 0; t < 4; ++t)
#pragma unroll
    for (int r = 0; r < 4; ++r) { wr4[t][r] = bflo(Wv[t][r]); wi4[t][r] = bfhi(Wv[t][r]); }
  {
    const int ur = special ? U - 480 : U % 160;
    const bool e0 = ur == 0, e320 = special ? ur == 160 : ur == 80;
    if (e0 || e320) {
      const int cls = special ? 0 : cB;
      const u4v w2 = *(const u4v*)(W2 + (cls * 2 + (e320 ? 1 : 0)) * 256 + lane * 4);
      const u4v w4 = *(const u4v*)(W2 + (4 * 2 + (e320 ? 1 : 0)) * 256 + lane * 4);
#pragma unroll
      for (int r = 0; r < 4; ++r) {
        wr4[0][r] = (wr4[0][r] + bflo(w2[r])) * 0.5f; wi4[0][r] = (wi4[0][r] + bfhi(w2[r])) * 0.5f;
        if (special) { wr4[1][r] = (wr4[1][r] + bflo(w4[r])) * 0.5f; wi4[1][r] = (wi4[1][r] + bfhi(w4[r])) * 0.5f; }
      }
    }
  }
#pragma unroll
  for (int t = 0; t < 4; ++t) { wr4[t] *= sig; wi4[t] *= sig; PIN(wr4[t]); PIN(wi4[t]); }
  asm volatile("s_waitcnt vmcnt(0)" ::: "memory");
  __builtin_amdgcn_s_barrier();
  asm volatile("" ::: "memory");
  if (U >= NUNITS) return;
  STAMP(2,2);
  u4v L[4];
#pragma unroll
  for (int jj = 0; jj < 4; ++jj) L[jj] = *(const u4v*)(smem + (16 * (4 * g + jj) + li) * 64 + ((wave ^ (li >> 2)) & 3) * 16);
  u4v X[4];
  if (special) {
#pragma unroll
    for (int jj = 0; jj < 4; ++jj)
#pragma unroll
      for (int pp = 0; pp < 2; ++pp) {
        const u32 au = L[jj][2 * pp], bu = L[jj][2 * pp + 1] ^ 0x80000000u;
        const h2 A = __builtin_bit_cast(h2, au);
        const h2 Bc = __builtin_bit_cast(h2, bu);
        X[2 * pp][jj] = __builtin_bit_cast(u32, A + Bc);
        X[2 * pp + 1][jj] = __builtin_bit_cast(u32, A - Bc);
      }
  } else {
#pragma unroll
    for (int jj = 0; jj < 4; ++jj)
#pragma unroll
      for (int t = 0; t < 4; ++t) X[t][jj] = L[jj][t];
  }
  const f4 z4 = f4{0, 0, 0, 0};
  f4 re[4], im[4];
#pragma unroll
  for (int t = 0; t < 4; ++t) { re[t] = mfma_h(X[t], P1re[t & 1], z4); im[t] = mfma_h(X[t], P1im[t & 1], z4); }
#pragma unroll
  for (int t = 0; t < 4; ++t) {
    const u4v v = cmul_pack<false>(re[t], im[t], tfr[t & 1], -tfs[t & 1]);
    re[t] = mfma_h(Fre, v, z4); im[t] = mfma_h(Fim, v, z4);
  }
#pragma unroll
  for (int t = 0; t < 4; ++t) {
    const u4v v = cmul_pack<true>(re[t], im[t], wr4[t], wi4[t]);
    im[t] = mfma_h(v, Fre, z4); re[t] = mfma_h(v, Fim, z4);
  }
#pragma unroll
  for (int t = 0; t < 4; ++t) {
    const u4v v = cmul_pack<true>(re[t], im[t], tir[t & 1], tis[t & 1]);
    im[t] = mfma_h(P4re[t & 1], v, z4); re[t] = mfma_h(P4im[t & 1], v, z4);
  }
  STAMP(2,3);
  if (special) {
#pragma unroll
    for (int pp = 0; pp < 2; ++pp) {
      const f4 yr = re[2 * pp], yi = im[2 * pp], zr = re[2 * pp + 1], zi = im[2 * pp + 1];
      re[2 * pp] = yr + zr; im[2 * pp] = yi + zi;
      re[2 * pp + 1] = yr - zr; im[2 * pp + 1] = zi - yi;
    }
  }
  const __amdgpu_buffer_rsrc_t rs = ws_rsrc(ws);
  const u32 dst = (u32)WS_TP + (U * 256 + lane) * 16;
#pragma unroll
  for (int r = 0; r < 4; ++r) {
    u4v o;
#pragma unroll
    for (int t = 0; t < 4; ++t) o[t] = pk_h(re[t][r], im[t][r]);
    st16(rs, dst + r * 1024, o);
  }
  STAMP(2,4);
}

__global__ __launch_bounds__(256, 2) void k3_kernel(const u16* __restrict__ x, char* __restrict__ ws, const u16* __restrict__ w_sp) {
  PINS(x); PINS(ws); PINS(w_sp);
  const u32* Tp = (const u32*)(ws + WS_TP);
  constexpr int K3_Y = 10752, K3_CB = K3_Y + 10240;
  __shared__ __attribute__((aligned(16))) char smem[K3_CB + 8192 + 512];
  const int tid = threadIdx.x, lane = tid & 63;
  const int wave = __builtin_amdgcn_readfirstlane(tid >> 6);
  const int li = lane & 15, g = lane >> 4;
  const int b = (blockIdx.x & 7) * 32 + (blockIdx.x >> 3);
  const int posb = ((b >> 4) & 3) * 64 + (b >> 6) * 16 + (b & 15);
  STAMP(3,0);
#pragma unroll
  for (int i = 0; i < 2; ++i) {
    int U = (wave * 2 + i) * 64 + lane;
    U = U < 480 ? U : 479;
    GLDS16(Tp + ((size_t)U * 256 + posb) * 4, smem + (wave * 2 + i) * 1024);
  }
#pragma unroll
  for (int it = 0; it < 4; ++it) {
    const int i = it < 2 ? wave * 2 + it : 6 + it;
    if (it < 2 || wave == 3) {
      const int k2 = (lane >> 2) + 16 * (4 * i + (lane & 3));
      const int pos = k2 <= 320 ? 2 * k2 : 2 * (640 - k2) + 1;
      GLDS4(Tp + ((size_t)(480 + (pos >> 2)) * 256 + posb) * 4 + (pos & 3), smem + 8192 + i * 256);
    }
  }
  char* scratch = smem + K3_CB + 8192;
  if (wave == 0) GLDS4(ws + WS_UNS, scratch + 256);
  const int a = 2 * wave + (lane >> 5), j = lane & 31;
  const int row = 256 * a + b;
  const u16* xr = x + (size_t)row * D;
  const u4v x0 = *(const u4v*)(xr + 8 * j);
  const u4v x1 = *(const u4v*)(xr + 256 + 8 * j);
  const u2v x2 = *(const u2v*)(xr + 512 + 4 * j);
  STAMP(3,5);
  char* cb = smem + K3_CB;
  const int t = wave;
  u4v Bre[3], Bim[3];
  if (wave < 3) {
#pragma unroll
    for (int tk = 0; tk < 3; ++tk) {
      const float sc = (tk < 2 || g < 2) ? 1.f : 0.f;
#pragma unroll
      for (int jj = 0; jj < 4; ++jj) {
        const float ang = (float)((16 * tk + 4 * g + jj) * (16 * t + li)) * (1.0f / 40.0f);
        const float c = cosr(ang) * sc, s = sinr(ang) * sc;
        Bre[tk][jj] = pk_h(c, -s);
        Bim[tk][jj] = pk_h(s, c);
      }
      PIN(Bre[tk]); PIN(Bim[tk]);
    }
  } else {
#pragma unroll
    for (int tt = 0; tt < 3; ++tt) {
      f4 twr, twi;
#pragma unroll
      for (int jj = 0; jj < 4; ++jj) {
        const float ang = (float)((16 * tt + li) * (4 * g + jj)) * (1.0f / 640.0f);
        twr[jj] = cosr(ang) * 0.125f; twi[jj] = sinr(ang) * 0.125f;
      }
      *(f4*)(cb + tt * 2048 + lane * 16) = twr;
      *(f4*)(cb + tt * 2048 + 1024 + lane * 16) = twi;
    }
    u4v gre, gim;
#pragma unroll
    for (int jj = 0; jj < 4; ++jj) {
      const float ang = (float)(li * (4 * g + jj)) * 0.0625f;
      const float c = cosr(ang), s = sinr(ang);
      gre[jj] = pk_h(c, -s); gim[jj] = pk_h(s, c);
    }
    *(u4v*)(cb + 6144 + lane * 16) = gre;
    *(u4v*)(cb + 7168 + lane * 16) = gim;
  }
  STAMP(3,1);
  asm volatile("s_waitcnt vmcnt(0)" ::: "memory");
  __syncthreads();
  STAMP(3,2);
  char* yb = smem + K3_Y;
  if (wave < 3) {
    const f4 z4 = f4{0, 0, 0, 0};
    u4v af[4][3];
#pragma unroll
    for (int q = 0; q < 4; ++q) {
      const char* base = smem + (q == 0 ? 8192 : (q - 1) * 2560);
#pragma unroll
      for (int tk = 0; tk < 3; ++tk) af[q][tk] = *(const u4v*)(base + (tk < 2 ? (64 * tk + lane) : (128 + (lane & 31))) * 16);
    }
    const f4 twr = *(const f4*)(cb + t * 2048 + lane * 16), twi = *(const f4*)(cb + t * 2048 + 1024 + lane * 16);
    const u4v Gre = *(const u4v*)(cb + 6144 + lane * 16), Gim = *(const u4v*)(cb + 7168 + lane * 16);
    const float usc = *(const float*)(scratch + 256);
    SB();
    f4 vre[4], vim[4];
#pragma unroll
    for (int q = 0; q < 4; ++q) {
      f4 re = z4, im = z4;
#pragma unroll
      for (int tk = 0; tk < 3; ++tk) {
        re = mfma_h(af[q][tk], Bre[tk], re);
        im = mfma_h(af[q][tk], Bim[tk], im);
      }
      vre[q] = re; vim[q] = im;
    }
#pragma unroll
    for (int q = 0; q < 4; ++q) {
      const u4v v = cmul_pack<false>(vre[q], vim[q], twr, twi);
      vre[q] = mfma_h(v, Gre, z4);
      vim[q] = mfma_h(v, Gim, z4);
    }
    const float fs = usc * (2.0f * 1024.0f * 128.0f / (2048.0f * 640.0f));
    const float fh = fs * 0.70710678118f;
    const f4 E = (vre[0] + vim[0]) * fs, O = (vre[0] - vim[0]) * fs;
    const f4 s13 = vre[1] + vre[3], dx = vre[1] - vre[3], sy = vim[1] + vim[3], dy = vim[3] - vim[1];
    const f4 Ep = E + vre[2] * fs, Em = E - vre[2] * fs, Om = O - vim[2] * fs, Op = O + vim[2] * fs;
    const f4 A = dx - sy, B = dx + sy;
    f4 y[8];
    y[0] = Ep + s13 * fs; y[4] = Ep - s13 * fs; y[2] = Em + dy * fs; y[6] = Em - dy * fs;
    y[1] = Om + A * fh; y[5] = Om - A * fh; y[3] = Op - B * fh; y[7] = Op + B * fh;
    if (t < 2 || lane < 32) {
      char* yp = yb + (40 * li + 16 * t + 4 * g) * 2;
#pragma unroll
      for (int aa = 0; aa < 8; ++aa) *(u2v*)(yp + aa * 1280) = u2v{pk_bf(y[aa][0], y[aa][1]), pk_bf(y[aa][2], y[aa][3])};
    }
  }
  else {
#pragma unroll
    for (int i = 0; i < 3; ++i) { const int task = PF_WSP_K2 + blockIdx.x * 3 + i; if (task < 1024) touch((const char*)w_sp + ((size_t)task * 64 + lane) * 128); }
  }
  asm volatile("s_waitcnt lgkmcnt(0)" ::: "memory");
  __builtin_amdgcn_s_barrier();
  asm volatile("" ::: "memory");
  STAMP(3,3);
  {
    const __amdgpu_buffer_rsrc_t rs = ws_rsrc(ws);
    const u4v y0 = *(const u4v*)(yb + a * 1280 + 16 * j);
    const u4v y1 = *(const u4v*)(yb + a * 1280 + 512 + 16 * j);
    const u2v y2 = *(const u2v*)(yb + a * 1280 + 1024 + 8 * j);
    u32 hx[10];
#pragma unroll
    for (int i = 0; i < 4; ++i) {
      hx[i] = pk_bf(bflo(x0[i]) + bflo(y0[i]), bfhi(x0[i]) + bfhi(y0[i]));
      hx[4 + i] = pk_bf(bflo(x1[i]) + bflo(y1[i]), bfhi(x1[i]) + bfhi(y1[i]));
    }
    hx[8] = pk_bf(bflo(x2[0]) + bflo(y2[0]), bfhi(x2[0]) + bfhi(y2[0]));
    hx[9] = pk_bf(bflo(x2[1]) + bflo(y2[1]), bfhi(x2[1]) + bfhi(y2[1]));
    auto pairup = [&](u32 v0, u32 v1) {
      return u4v{v0, v1, (u32)__builtin_amdgcn_update_dpp(0, (int)v0, 0xB1, 0xf, 0xf, true), (u32)__builtin_amdgcn_update_dpp(0, (int)v1, 0xB1, 0xf, 0xf, true)};
    };
    const u32 drop = (lane & 1) ? 0xfffffff0u : 0u;
    const u32 ho = (u32)WS_H + row * (D * 2);
    st16(rs, ho + 16 * j, u4v{hx[0], hx[1], hx[2], hx[3]});
    st16(rs, ho + 512 + 16 * j, u4v{hx[4], hx[5], hx[6], hx[7]});
    st16(rs, (ho + 1024 + 8 * j) | drop, pairup(hx[8], hx[9]));
    f2 v[10];
#pragma unroll
    for (int i = 0; i < 10; ++i) v[i] = f2{bflo(hx[i]), bfhi(hx[i])};
    ln_row(v);
#pragma unroll
    for (int i = 0; i < 10; ++i) hx[i] = pk_bf(v[i][0], v[i][1]);
    const u32 lo = (u32)WS_L2 + (row >> 4) * 20480 + (row & 15) * 64;
    st16(rs, lo + (j >> 2) * 1024 + (j & 3) * 16, u4v{hx[0], hx[1], hx[2], hx[3]});
    st16(rs, lo + (8 + (j >> 2)) * 1024 + (j & 3) * 16, u4v{hx[4], hx[5], hx[6], hx[7]});
    st16(rs, (lo + (16 + (j >> 3)) * 1024 + (j & 7) * 8) | drop, pairup(hx[8], hx[9]));
  }
  STAMP(3,4);
}

#include <type_traits>
typedef unsigned long long u64;
typedef __attribute__((address_space(1))) unsigned long long gu64;
#define WAITL0() do { __builtin_amdgcn_sched_barrier(0); asm volatile("s_waitcnt lgkmcnt(0)" ::: "memory"); __builtin_amdgcn_sched_barrier(0); } while (0)
#define BAR() do { asm volatile("" ::: "memory"); __builtin_amdgcn_sched_barrier(0); __builtin_amdgcn_s_barrier(); __builtin_amdgcn_sched_barrier(0); asm volatile("" ::: "memory"); } while (0)
#define SGB(mask, n) __builtin_amdgcn_sched_group_barrier(mask, n, 0)
DI f4 mfma_b(u4v a, u4v b, f4 c) {
  return __builtin_amdgcn_mfma_f32_16x16x32_bf16(__builtin_bit_cast(bf8, a), __builtin_bit_cast(bf8, b), c, 0, 0, 0);
}
DI f2 gelu2(f2 x) {
  const float k1 = -2.0f * 0.7978845608028654f * 1.4426950408889634f;
  const float k2 = k1 * 0.044715f;
  f2 p = x * x * k2 + k1;
  f2 a = x * p;
  f2 e = f2{__builtin_amdgcn_exp2f(a[0]), __builtin_amdgcn_exp2f(a[1])} + 1.0f;
  f2 r = f2{__builtin_amdgcn_rcpf(e[0]), __builtin_amdgcn_rcpf(e[1])};
  return x * r;
}
DI u2v gelu4(f4 a) {
  f2 g0 = gelu2(f2{a[0], a[1]}), g1 = gelu2(f2{a[2], a[3]});
  return u2v{pk_bf(g0[0], g0[1]), pk_bf(g1[0], g1[1])};
}
template <int N> DI void waitv() { asm volatile("s_waitcnt vmcnt(%0)" ::"n"(N) : "memory"); }

template <int KT, int NST, int NLA, class F>
DI void loader_loop(bool big, F&& stage) {
#pragma unroll
  for (int s = 0; s < NST - 1; ++s) stage(s, s);
  int nbuf = NST - 1;
  for (int kt = 0; kt < KT - (NST - 1); ++kt) {
    if (big) waitv<NLA*(NST - 2)>(); else waitv<(NLA - 1) * (NST - 2)>();
    BAR();
    stage(kt + NST - 1, nbuf);
    nbuf = nbuf + 1 == NST ? 0 : nbuf + 1;
  }
  if constexpr (NST >= 7) { if (big) waitv<NLA * 5>(); else waitv<(NLA - 1) * 5>(); BAR(); }
  if constexpr (NST >= 6) { if (big) waitv<NLA * 4>(); else waitv<(NLA - 1) * 4>(); BAR(); }
  if constexpr (NST >= 5) { if (big) waitv<NLA * 3>(); else waitv<(NLA - 1) * 3>(); BAR(); }
  if (big) waitv<NLA * 2>(); else waitv<(NLA - 1) * 2>(); BAR();
  if (big) waitv<NLA>(); else waitv<NLA - 1>(); BAR();
  waitv<0>(); BAR();
}

#define G1_DEPTH 3
__global__ __launch_bounds__(512) void g1_kernel(char* __restrict__ ws) {
  PINS(ws);
  const u32* l2f = (const u32*)(ws + WS_L2); const u32* Wt1 = (const u32*)(ws + WS_WT1);
  u32* z1f = (u32*)(ws + WS_Z1); u16* z2t = (u16*)(ws + WS_Z2); u64* stats = (u64*)(ws + WS_STATS);
  __shared__ __attribute__((aligned(16))) char smem[140 * 1024 + 4 * 4608];
  const int tid = threadIdx.x, lane = tid & 63;
  const int wave = __builtin_amdgcn_readfirstlane(tid >> 6);
  const int li = lane & 15, g = lane >> 4;
  STAMP(4,0);
  const int xcd = blockIdx.x & 7, idx = blockIdx.x >> 3;
#define G1_MAP 1
  const int mt = G1_MAP == 0 ? xcd * 2 + (idx >> 4) : (G1_MAP == 1 ? (xcd >> 1) * 4 + (idx >> 3) : (xcd >> 2) * 8 + (idx >> 2));
  const int nb = G1_MAP == 0 ? (idx & 15) : (G1_MAP == 1 ? (xcd & 1) * 8 + (idx & 7) : (xcd & 3) * 4 + (idx & 3));
  const int row0 = 1024 * (mt & 1) + 32 * (mt >> 1);
  const int zs = nb < 6 ? 4 * nb : 24 + 3 * (nb - 6);
  const int z1s = nb < 6 ? 3 * nb : (nb < 12 ? 18 + 4 * (nb - 6) : 42 + 3 * (nb - 12));
  const int type = nb < 6 ? 0 : (nb < 12 ? 1 : 2);
  if (wave >= 4) {
    const int lw = wave - 4;
    auto slot_frag = [&](int s) { return s < 3 ? 54 + zs + s : (s == 3 ? (type == 0 ? 54 + zs + 3 : (type == 1 ? z1s + 3 : z1s)) : z1s + s - 4); };
    const u32* bsrc[4];
#pragma unroll
    for (int j = 0; j < 4; ++j) {
      int i = lw + 4 * j; i = i < 14 ? i : 0;
      const int kk = i / 7, s = i - 7 * kk;
      bsrc[j] = Wt1 + (size_t)(kk * 108 + slot_frag(s)) * 256 + lane * 4;
    }
    auto stage = [&](int kt) {
      char* sb = smem + kt * 14336 + lw * 1024;
#pragma unroll
      for (int j = 0; j < 3; ++j) GLDS16(bsrc[j] + (size_t)kt * 2 * 108 * 256, sb + j * 4096);
      if (lw < 2) GLDS16(bsrc[3] + (size_t)kt * 2 * 108 * 256, sb + 3 * 4096);
    };
#pragma unroll
    for (int s = 0; s < G1_DEPTH; ++s) stage(s);
    for (int kt = 0; kt < 10 - G1_DEPTH; ++kt) {
      if (lw < 2) waitv<4 * (G1_DEPTH - 1)>(); else waitv<3 * (G1_DEPTH - 1)>();
      BAR();
      stage(kt + G1_DEPTH);
    }
    if constexpr (G1_DEPTH >= 5) { if (lw < 2) waitv<16>(); else waitv<12>(); BAR(); }
    if constexpr (G1_DEPTH >= 4) { if (lw < 2) waitv<12>(); else waitv<9>(); BAR(); }
    if (lw < 2) waitv<8>(); else waitv<6>(); BAR();
    if (lw < 2) waitv<4>(); else waitv<3>(); BAR();
    waitv<0>(); BAR();
    return;
  }
  const int rg0 = (row0 + wave * 256) >> 4;
  const u32* asrc = l2f + (size_t)rg0 * 20 * 256 + li * 16 + g * 4;
#define G1_AD 5
  u4v fa[G1_AD][2];
#pragma unroll
  for (int k = 0; k < G1_AD; ++k) { fa[k][0] = *(const u4v*)(asrc + k * 256); fa[k][1] = *(const u4v*)(asrc + (20 + k) * 256); __builtin_amdgcn_sched_barrier(0); }
  f4 acc[7][2];
#pragma unroll
  for (int s = 0; s < 7; ++s) { acc[s][0] = f4{0, 0, 0, 0}; acc[s][1] = f4{0, 0, 0, 0}; }
  u4v fw[2][7];
  const char* wb = smem + lane * 16;
  auto rd = [&](int k) {
#pragma unroll
    for (int s = 0; s < 7; ++s) fw[k & 1][s] = *(const u4v*)(wb + (k * 7 + s) * 1024);
  };
  auto mm = [&](int k) {
#pragma unroll
    for (int s = 0; s < 7; ++s) {
      acc[s][0] = mfma_b(fw[k & 1][s], fa[k % G1_AD][0], acc[s][0]);
      acc[s][1] = mfma_b(fw[k & 1][s], fa[k % G1_AD][1], acc[s][1]);
    }
    if (k + G1_AD < 20) { fa[k % G1_AD][0] = *(const u4v*)(asrc + (k + G1_AD) * 256); fa[k % G1_AD][1] = *(const u4v*)(asrc + (20 + G1_AD + k) * 256); }
  };
  BAR(); STAMP(4,3); rd(0);
#pragma unroll
  for (int k = 1; k < 20; ++k) {
    if (!(k & 1)) BAR(); else __builtin_amdgcn_sched_barrier(0);
    if (k == 8) { STAMP(4,6); STAMPC(7,0); }
    if (k == 14) { STAMP(4,7); STAMPC(7,1); }
    rd(k);
    mm(k - 1);
#pragma unroll
    for (int i = 0; i < 7; ++i) { SGB(0x100, 1); SGB(0x8, 2); }
  }
  __builtin_amdgcn_sched_barrier(0);
  mm(19);
  STAMP(4,1);
  const int n2 = type == 0 ? 4 : 3;
  f4 zv[4][2];
  u2v z3[2];
  float lsum[2], lm2[2];
#pragma unroll
  for (int sf = 0; sf < 2; ++sf) {
    f2 s = f2{0.f, 0.f};
#pragma unroll
    for (int j = 0; j < 4; ++j) {
      const u2v t = gelu4(acc[j][sf]);
      if (j == 3) z3[sf] = t;
      zv[j][sf] = f4{bflo(t[0]), bfhi(t[0]), bflo(t[1]), bfhi(t[1])};
      if (j < 3 || type == 0) s += f2{zv[j][sf][0], zv[j][sf][1]} + f2{zv[j][sf][2], zv[j][sf][3]};
    }
    const float st = xsum_g(s[0] + s[1]);
    const float lmean = st * (type == 0 ? (1.0f / 64.0f) : (1.0f / 48.0f));
    f2 q = f2{0.f, 0.f};
#pragma unroll
    for (int j = 0; j < 4; ++j) {
      const f4 d = zv[j][sf] - lmean;
      if (j < 3 || type == 0) { q += f2{d[0], d[1]} * f2{d[0], d[1]}; q += f2{d[2], d[3]} * f2{d[2], d[3]}; }
    }
    lsum[sf] = st; lm2[sf] = xsum_g(q[0] + q[1]);
  }
  gu64* st = (gu64*)stats + (size_t)mt * 16 * 128 + wave * 32;
  if (lane < 32) {
    u32 a = __float_as_uint(g ? lsum[1] : lsum[0]), b = __float_as_uint(g ? lm2[1] : lm2[0]);
    if (a == ~0u) a = 0x7fc00000u;
    __hip_atomic_store(st + nb * 128 + lane, ((u64)b << 32) | a, __ATOMIC_RELAXED, __HIP_MEMORY_SCOPE_AGENT);
  }
  __builtin_amdgcn_sched_barrier(0);
  STAMP(4,2);
  const __amdgpu_buffer_rsrc_t rs = ws_rsrc(ws);
  {
    const u32 zo = (u32)WS_Z1 + ((rg0 >> 1) * 54 + z1s) * 1024 + lane * 16;
#pragma unroll
    for (int j = 0; j < 3; ++j) {
      const u2v a = gelu4(acc[4 + j][0]), b = gelu4(acc[4 + j][1]);
      st16(rs, zo + j * 1024, u4v{a[0], a[1], b[0], b[1]});
    }
    if (type == 1) st16(rs, zo + 3 * 1024, u4v{z3[0][0], z3[0][1], z3[1][0], z3[1][1]});
  }
  __builtin_amdgcn_sched_barrier(0);
  u64 gv[2][4];
  for (int spin = 0; spin < (1 << 20); ++spin) {
    bool ok = true;
#pragma unroll
    for (int sf = 0; sf < 2; ++sf)
#pragma unroll
      for (int i = 0; i < 4; ++i) {
        gv[sf][i] = __hip_atomic_load(st + (4 * g + i) * 128 + sf * 16 + li, __ATOMIC_RELAXED, __HIP_MEMORY_SCOPE_AGENT);
        ok &= ((u32)gv[sf][i] != ~0u);
      }
    if (__all(ok)) break;
    __builtin_amdgcn_s_sleep(1);
  }
  STAMP(4,4);
  char* tb = smem + 140 * 1024 + wave * 4608;
#pragma unroll
  for (int sf = 0; sf < 2; ++sf) {
    float s = 0.f;
#pragma unroll
    for (int i = 0; i < 4; ++i) s += __uint_as_float((u32)gv[sf][i]);
    const float mean = xsum_g(s) * (1.0f / 864.0f);
    float q = 0.f;
#pragma unroll
    for (int i = 0; i < 4; ++i) {
      const bool big = (4 * g + i) < 6;
      const float dm = __uint_as_float((u32)gv[sf][i]) * (big ? (1.0f / 64.0f) : (1.0f / 48.0f)) - mean;
      q += __uint_as_float((u32)(gv[sf][i] >> 32)) + (big ? 64.f : 48.f) * dm * dm;
    }
    const float var = xsum_g(q) * (1.0f / 864.0f);
    const float mb = rbf(mean);
    const float rinv = __builtin_amdgcn_rcpf(rbf(__builtin_amdgcn_sqrtf(rbf(rbf(var) + 1e-5f))));
#pragma unroll
    for (int j = 0; j < 4; ++j) {
      const f4 o = (zv[j][sf] - mb) * rinv;
      *(u2v*)(tb + (sf * 16 + li) * 144 + j * 32 + g * 8) = u2v{pk_bf(o[0], o[1]), pk_bf(o[2], o[3])};
    }
  }
  {
    const int q = li >> 2, p = li & 3;
#pragma unroll
    for (int j = 0; j < 4; ++j) {
      s4 lo = __builtin_amdgcn_ds_read_tr16_b64_v4i16((LDS_AS s4*)(tb + (8 * g + q) * 144 + j * 32 + 8 * p));
      s4 hi = __builtin_amdgcn_ds_read_tr16_b64_v4i16((LDS_AS s4*)(tb + (8 * g + 4 + q) * 144 + j * 32 + 8 * p));
      u2v a = __builtin_bit_cast(u2v, lo), b = __builtin_bit_cast(u2v, hi);
      if (j < n2) st16(rs, (u32)WS_Z2 + (((zs + j) * 16 + li) * S + row0 + wave * 256 + 8 * g) * 2, u4v{a[0], a[1], b[0], b[1]});
    }
  }
  STAMP(4,5);
}

constexpr int G2_NST = 7, G2_STB = 22 * 1024;
__global__ __launch_bounds__(512) void g2_kernel(const u16* __restrict__ w_sp, const u16* __restrict__ b_sp, char* __restrict__ ws) {
  PINS(w_sp); PINS(b_sp); PINS(ws);
  const u16* z2t = (const u16*)(ws + WS_Z2); const u32* z1f = (const u32*)(ws + WS_Z1);
  __shared__ __attribute__((aligned(16))) char smem[G2_NST * G2_STB];
  const int tid = threadIdx.x, lane = tid & 63;
  const int wave = __builtin_amdgcn_readfirstlane(tid >> 6);
  const int li = lane & 15, g = lane >> 4;
  STAMP(5,0);
  const int xcd = blockIdx.x & 7, idx = blockIdx.x >> 3;
  const int tt = (xcd >> 1) * 8 + (idx >> 2), ft = (xcd & 1) * 4 + (idx & 3);
  if (wave >= 4) {
    const int lw = wave - 4;
    const u16* src[6];
    const int swz = ((lane & 7) ^ (lane >> 3)) * 8;
#pragma unroll
    for (int j = 0; j < 6; ++j) {
      int i = lw + 4 * j; i = i < 22 ? i : 8;
      if (j < 2) src[j] = w_sp + (size_t)(tt * 64 + 8 * i + (lane >> 3)) * S + swz;
      else { int r = ft * 112 + 8 * (i - 8) + (lane >> 3); r = r < FH ? r : FH - 1; src[j] = z2t + (size_t)r * S + swz; }
    }
    auto stage = [&](int kt, int buf) {
      char* sb = smem + buf * G2_STB + lw * 1024;
      const int kk = kt * 64;
#pragma unroll
      for (int j = 0; j < 5; ++j) GLDS16(src[j] + kk, sb + j * 4096);
      if (lw < 2) GLDS16(src[5] + kk, sb + 5 * 4096);
    };
    const bool big = lw < 2;
    stage(0, 0); stage(1, 1); stage(2, 2);
    if (big) waitv<12>(); else waitv<10>(); BAR();
    stage(3, 3); stage(4, 4);
    if (big) waitv<18>(); else waitv<15>(); BAR();
    stage(5, 5); stage(6, 6);
    if (big) waitv<24>(); else waitv<20>(); BAR();
    stage(7, 0); stage(8, 1);
    int nbuf = 2;
    for (int kt = 3; kt < 26; ++kt) {
      if (big) waitv<30>(); else waitv<25>();
      BAR();
      stage(kt + 6, nbuf);
      nbuf = nbuf + 1 == G2_NST ? 0 : nbuf + 1;
    }
    if (big) waitv<30>(); else waitv<25>(); BAR();
    if (big) waitv<24>(); else waitv<20>(); BAR();
    if (big) waitv<18>(); else waitv<15>(); BAR();
    if (big) waitv<12>(); else waitv<10>(); BAR();
    if (big) waitv<6>(); else waitv<5>(); BAR();
    waitv<0>(); BAR();
    return;
  }
  const int wt = wave & 1, wf = wave >> 1;
  const int n0 = (ft & 1) ? 3 : 4;
  const int f0 = wf ? n0 : 0, nf = wf ? 7 - n0 : n0;
  u4v z1v[4];
  float bias[2];
#pragma unroll
  for (int j = 0; j < 4; ++j) {
    int ff = ft * 7 + f0 + j; ff = ff < 54 ? ff : 53;
    z1v[j] = *(const u4v*)(z1f + ((size_t)(tt * 2 + wt) * 54 + ff) * 256 + lane * 4);
  }
  bias[0] = bflo(b_sp[tt * 64 + wt * 32 + li]); bias[1] = bflo(b_sp[tt * 64 + wt * 32 + 16 + li]);
  f4 acc[4][2];
#pragma unroll
  for (int j = 0; j < 4; ++j) { acc[j][0] = f4{0, 0, 0, 0}; acc[j][1] = f4{0, 0, 0, 0}; }
  const int sw = (g ^ (li & 7)) * 16;
  const int toff = (wt * 32 + li) * 128 + sw;
  const int foff = 8192 + (f0 * 16 + li) * 128 + sw;
  auto body = [&](auto NFc) {
    constexpr int NF = decltype(NFc)::value;
    u4v fb[2][2][2], fa[2][2][NF];
    auto rd = [&](int set, int buf) {
      const char* sb = smem + buf * G2_STB;
#pragma unroll
      for (int kk = 0; kk < 2; ++kk) {
        fb[set][kk][0] = *(const u4v*)(sb + (toff ^ (kk * 64)));
        fb[set][kk][1] = *(const u4v*)(sb + (toff ^ (kk * 64)) + 2048);
#pragma unroll
        for (int j = 0; j < NF; ++j) fa[set][kk][j] = *(const u4v*)(sb + (foff ^ (kk * 64)) + j * 2048);
      }
    };
    auto mm = [&](int set) {
#pragma unroll
      for (int kk = 0; kk < 2; ++kk)
#pragma unroll
        for (int j = 0; j < NF; ++j) {
          acc[j][0] = mfma_b(fa[set][kk][j], fb[set][kk][0], acc[j][0]);
          acc[j][1] = mfma_b(fa[set][kk][j], fb[set][kk][1], acc[j][1]);
        }
    };
    auto sched = [&]() {
#pragma unroll
      for (int i = 0; i < 2 * NF + 4; ++i) { SGB(0x100, 1); SGB(0x8, 1); }
      SGB(0x8, 2 * NF - 4);
    };
    BAR(); STAMP(5,3); rd(0, 0); WAITL0();
    int buf = 1;
    for (int kt = 1; kt < 31; kt += 2) {
      if (kt == 7) STAMP(5,4);
      if (kt == 23) STAMP(5,6);
      BAR(); rd(1, buf); mm(0); sched(); WAITL0();
      buf = buf + 1 == G2_NST ? 0 : buf + 1;
      BAR(); rd(0, buf); mm(1); sched(); WAITL0();
      buf = buf + 1 == G2_NST ? 0 : buf + 1;
    }
    BAR(); rd(1, buf); mm(0); sched(); WAITL0();
    mm(1);
  };
  if (nf == 4) body(std::integral_constant<int, 4>{}); else body(std::integral_constant<int, 3>{});
  STAMP(5,1);
  const __amdgpu_buffer_rsrc_t rs = ws_rsrc(ws);
  const int ff0 = ft * 7 + f0;
#pragma unroll
  for (int tf = 0; tf < 2; ++tf) {
    const int tfr = tt * 4 + wt * 2 + tf;
    u2v o[4];
#pragma unroll
    for (int j = 0; j < 4; ++j) {
      const u32 za = z1v[j][2 * tf], zb = z1v[j][2 * tf + 1];
      const f4 v = (acc[j][tf] + bias[tf]) * f4{bflo(za), bfhi(za), bflo(zb), bfhi(zb)};
      o[j] = u2v{pk_bf(v[0], v[1]), pk_bf(v[2], v[3])};
    }
    const u32 base = (u32)WS_P + (tfr * 27 + (ff0 >> 1)) * 1024 + lane * 16;
    if (!(ff0 & 1)) {
      st16(rs, base, u4v{o[0][0], o[0][1], o[1][0], o[1][1]});
      if (nf == 4) { if (ff0 + 3 < 54) st16(rs, base + 1024, u4v{o[2][0], o[2][1], o[3][0], o[3][1]}); }
      else st8(rs, base + 1024, o[2]);
    } else {
      st8(rs, base + 8, o[0]);
      st16(rs, base + 1024, u4v{o[1][0], o[1][1], o[2][0], o[2][1]});
    }
  }
  STAMP(5,2);
}

#define G3_DEPTH 3
__global__ __launch_bounds__(512) void g3_kernel(char* __restrict__ ws, u16* __restrict__ out) {
  PINS(ws); PINS(out);
  const u32* pf = (const u32*)(ws + WS_P); const u32* Wt3 = (const u32*)(ws + WS_WT3); const u16* h = (const u16*)(ws + WS_H);
  __shared__ __attribute__((aligned(16))) char smem[135 * 1024];
  const int tid = threadIdx.x, lane = tid & 63;
  const int wave = __builtin_amdgcn_readfirstlane(tid >> 6);
  const int li = lane & 15, g = lane >> 4;
  STAMP(6,0);
  const int xcd = blockIdx.x & 7, idx = blockIdx.x >> 3;
  const int tt = xcd * 4 + (idx >> 3), dt = idx & 7;
  if (wave >= 4) {
    const int lw = wave - 4;
    const u32* wsrc[4];
#pragma unroll
    for (int j = 0; j < 4; ++j) {
      int i = lw + 4 * j; i = i < 15 ? i : 0;
      const int kk = i / 5, df = i - 5 * kk;
      wsrc[j] = Wt3 + (size_t)(kk * 40 + dt * 5 + df) * 256 + lane * 4;
    }
    auto stage = [&](int kt) {
      char* sb = smem + kt * 15360 + lw * 1024;
#pragma unroll
      for (int j = 0; j < 3; ++j) GLDS16(wsrc[j] + (size_t)kt * 3 * 40 * 256, sb + j * 4096);
      if (lw < 3) GLDS16(wsrc[3] + (size_t)kt * 3 * 40 * 256, sb + 3 * 4096);
    };
#pragma unroll
    for (int s = 0; s < G3_DEPTH; ++s) stage(s);
    for (int kt = 0; kt < 9 - G3_DEPTH; ++kt) {
      if (lw < 3) waitv<4 * (G3_DEPTH - 1)>(); else waitv<3 * (G3_DEPTH - 1)>();
      BAR();
      stage(kt + G3_DEPTH);
    }
    if constexpr (G3_DEPTH >= 5) { if (lw < 3) waitv<16>(); else waitv<12>(); BAR(); }
    if constexpr (G3_DEPTH >= 4) { if (lw < 3) waitv<12>(); else waitv<9>(); BAR(); }
    if (lw < 3) waitv<8>(); else waitv<6>(); BAR();
    if (lw < 3) waitv<4>(); else waitv<3>(); BAR();
    waitv<0>(); BAR();
    return;
  }
  const u32* psrc = pf + (size_t)(tt * 4 + wave) * 27 * 256 + lane * 4;
#define G3_PD 3
  u4v fb[27];
#pragma unroll
  for (int k = 0; k < 3 * G3_PD; ++k) { fb[k] = *(const u4v*)(psrc + k * 256); __builtin_amdgcn_sched_barrier(0); }
  const int trow = tt * 64 + wave * 16 + li;
  u2v hv[5];
  f4 acc[5];
#pragma unroll
  for (int j = 0; j < 5; ++j) acc[j] = f4{0, 0, 0, 0};
  u4v fa[2][15];
  const char* wb = smem + lane * 16;
  auto rd = [&](int kt) {
#pragma unroll
    for (int i = 0; i < 15; ++i) fa[kt & 1][i] = *(const u4v*)(wb + (kt * 15 + i) * 1024);
  };
  auto mm = [&](int kt) {
#pragma unroll
    for (int kk = 0; kk < 3; ++kk)
#pragma unroll
      for (int j = 0; j < 5; ++j) acc[j] = mfma_b(fa[kt & 1][kk * 5 + j], fb[kt * 3 + kk], acc[j]);
  };
  BAR(); STAMP(6,3); rd(0);
#pragma unroll
  for (int kt = 1; kt < 9; ++kt) {
    BAR();
    if (kt == 4) STAMP(6,4);
    if (kt == 7) STAMP(6,5);
    if (kt - 1 + G3_PD < 9) {
#pragma unroll
      for (int k = 0; k < 3; ++k) fb[(kt - 1 + G3_PD) * 3 + k] = *(const u4v*)(psrc + ((kt - 1 + G3_PD) * 3 + k) * 256);
    } else if (kt - 1 + G3_PD == 9) {
#pragma unroll
      for (int j = 0; j < 5; ++j) hv[j] = *(const u2v*)(h + (size_t)trow * D + dt * 80 + j * 16 + g * 4);
    }
    __builtin_amdgcn_sched_barrier(0);
    rd(kt);
    mm(kt - 1);
#pragma unroll
    for (int i = 0; i < 15; ++i) { SGB(0x100, 1); SGB(0x8, 1); }
  }
  __builtin_amdgcn_sched_barrier(0);
  mm(8);
  STAMP(6,1);
#pragma unroll
  for (int j = 0; j < 5; ++j) {
    u32 t0 = pk_bf(acc[j][0], acc[j][1]), t1 = pk_bf(acc[j][2], acc[j][3]);
    u2v o = u2v{pk_bf(bflo(t0) + bflo(hv[j][0]), bfhi(t0) + bfhi(hv[j][0])), pk_bf(bflo(t1) + bflo(hv[j][1]), bfhi(t1) + bfhi(hv[j][1]))};
    *(u2v*)(out + (size_t)trow * D + dt * 80 + j * 16 + g * 4) = o;
  }
  STAMP(6,2);
}

extern "C" { long long dbg_offsets[16] = {28<<20, -1, -1, 44<<20, -1, -1, -1}; }

extern "C" void kernel_launch(void* const* d_in, const int* in_sizes, int n_in,
                              void* d_out, int out_size, void* d_ws, size_t ws_size,
                              hipStream_t stream) {
  const u16* x=(const u16*)d_in[0]; const u16* wr=(const u16*)d_in[1]; const u16* wi=(const u16*)d_in[2];
  const u16* w_in=(const u16*)d_in[4]; const u16* w_sp=(const u16*)d_in[5]; const u16* b_sp=(const u16*)d_in[6]; const u16* w_out=(const u16*)d_in[7];
  char* ws=(char*)d_ws;
  k1_kernel<<<256+NPREP,256,0,stream>>>(x,wr,wi,ws);
  (void)hipFuncSetAttribute((const void*)k2_kernel, hipFuncAttributeMaxDynamicSharedMemorySize, 84 * 1024);
  k2_kernel<<<K2_FFT_BLOCKS+K2_PREP_BLOCKS,256,84 * 1024,stream>>>(ws,w_in,w_out,w_sp);
  k3_kernel<<<256,256,0,stream>>>(x,ws,w_sp);
  g1_kernel<<<256,512,0,stream>>>(ws);
  g2_kernel<<<256,512,0,stream>>>(w_sp,b_sp,ws);
  g3_kernel<<<256,512,0,stream>>>(ws,(u16*)d_out);
}
```

```cpp
#include <hip/hip_runtime.h>
#include <hip/hip_bf16.h>
#include <stdint.h>
#define STAMP(k, i)
#define STAMPC(k, i)

#define PF_WSP_K2 352
#define PF_WSP_K3 1
#define PF_WT1 0
typedef unsigned short u16;
typedef unsigned int u32;
typedef _Float16 h2 __attribute__((ext_vector_type(2)));
typedef _Float16 h8 __attribute__((ext_vector_type(8)));
typedef __bf16 bf2 __attribute__((ext_vector_type(2)));
typedef __bf16 bf8 __attribute__((ext_vector_type(8)));
typedef float f2 __attribute__((ext_vector_type(2)));
typedef float f4 __attribute__((ext_vector_type(4)));
typedef short s4 __attribute__((ext_vector_type(4)));
typedef u32 u2v __attribute__((ext_vector_type(2)));
typedef u32 u4v __attribute__((ext_vector_type(4)));
#define DI __device__ __forceinline__
#define LDS_AS __attribute__((address_space(3)))
#define PIN(x) asm volatile("" : "+v"(x))
#define GLDS16(src, dst) __builtin_amdgcn_global_load_lds((const u32*)(src), (LDS_AS u32*)(dst), 16, 0, 0)
#define WAITV(n) asm volatile("s_waitcnt vmcnt(%0)" ::"n"(n) : "memory")

constexpr int S = 2048, D = 640, DF = 321, FF = 1728, FH = 864;
constexpr int TROW_UNITS = 648;
constexpr int NUNITS = 641;
constexpr int NPREP = 168;

constexpr size_t WS_T = 0, WS_TP = 4u << 20, WS_WP = 8u << 20, WS_W2 = 12u << 20, WS_WMAX = 13u << 20, WS_UNS = (13u << 20) + 8192,
                 WS_STATS = 14u << 20, WS_CNT = 15u << 20, WS_WT1 = 16u << 20, WS_WT3 = 20u << 20, WS_H = 28u << 20, WS_L2 = 32u << 20,
                 WS_Z1 = 36u << 20, WS_Z2 = 44u << 20, WS_P = 48u << 20;
#define PINS(x) asm volatile("" ::"s"(x))
#define GLDS4(src, dst) __builtin_amdgcn_global_load_lds((const u32*)(src), (LDS_AS u32*)(dst), 4, 0, 0)
#define touch(p) GLDS4(p, scratch)

constexpr int AUX_SC1 = 16;
DI __amdgpu_buffer_rsrc_t ws_rsrc(char* ws) { return __builtin_amdgcn_make_buffer_rsrc((void*)ws, 0, 64 << 20, 0x00020000); }
#define ST_AUX 16
DI void st16(__amdgpu_buffer_rsrc_t rs, u32 off, u4v v) { __builtin_amdgcn_raw_buffer_store_b128(v, rs, off, 0, ST_AUX); }
DI void st8(__amdgpu_buffer_rsrc_t rs, u32 off, u2v v) { __builtin_amdgcn_raw_buffer_store_b64(v, rs, off, 0, ST_AUX); }
#define CODE_ADDR(var, sym)                                                                                     \
  const char* var;                                                                                              \
  {                                                                                                             \
    u32 lo_, hi_;                                                                                               \
    asm volatile("s_getpc_b64 s[40:41]\n\ts_add_u32 %0, s40, " #sym "@rel32@lo+4\n\ts_addc_u32 %1, s41, " #sym "@rel32@hi+12" \
                 : "=s"(lo_), "=s"(hi_)::"s40", "s41", "scc");                                                  \
    var = (const char*)(((unsigned long long)hi_ << 32) | lo_);                                                 \
  }
#define CODE_TOUCH(a, b, c)                                                           \
  {                                                                                   \
    CODE_ADDR(pa_, a) CODE_ADDR(pb_, b)                                               \
    const char* p_ = pa_ + ((c) * 64 + lane) * 128;                                   \
    if (p_ < pb_) touch(p_);                                                          \
  }
DI float bflo(u32 v) { return __uint_as_float(v << 16); }
DI float bfhi(u32 v) { return __uint_as_float(v & 0xffff0000u); }
DI u32 pk_bf(float a, float b) { f2 v = {a, b}; return __builtin_bit_cast(u32, __builtin_convertvector(v, bf2)); }
DI u32 pk_h(float a, float b) { f2 v = {a, b}; return __builtin_bit_cast(u32, __builtin_convertvector(v, h2)); }
DI float rbf(float a) { return bflo(pk_bf(a, a)); }
DI float cosr(float rev) { return __builtin_amdgcn_cosf(rev); }
DI float sinr(float rev) { return __builtin_amdgcn_sinf(rev); }
DI f4 mfma_h(u4v a, u4v b, f4 c) {
  return __builtin_amdgcn_mfma_f32_16x16x32_f16(__builtin_bit_cast(h8, a), __builtin_bit_cast(h8, b), c, 0, 0, 0);
}
template <int CTRL> DI float dpp_add(float v) {
  int t = __builtin_amdgcn_update_dpp(0, __float_as_int(v), CTRL, 0xf, 0xf, true);
  return v + __int_as_float(t);
}
DI float xsum_g(float v) {
  u32 b = __float_as_uint(v);
  auto r = __builtin_amdgcn_permlane16_swap(b, b, false, false);
  v = __uint_as_float(r[0]) + __uint_as_float(r[1]);
  b = __float_as_uint(v);
  auto s = __builtin_amdgcn_permlane32_swap(b, b, false, false);
  return __uint_as_float(s[0]) + __uint_as_float(s[1]);
}
DI float sum64(float v) {
  v = dpp_add<0xB1>(v);
  v = dpp_add<0x4E>(v);
  v = dpp_add<0x141>(v);
  v = dpp_add<0x140>(v);
  return xsum_g(v);
}
template <int CTRL> DI u32 dpp_maxu(u32 v) {
  u32 t = (u32)__builtin_amdgcn_update_dpp(0, (int)v, CTRL, 0xf, 0xf, true);
  return v > t ? v : t;
}
DI u32 max64u(u32 v) {
  v = dpp_maxu<0xB1>(v); v = dpp_maxu<0x4E>(v); v = dpp_maxu<0x141>(v); v = dpp_maxu<0x140>(v);
  auto r = __builtin_amdgcn_permlane16_swap(v, v, false, false);
  v = r[0] > r[1] ? r[0] : r[1];
  auto s = __builtin_amdgcn_permlane32_swap(v, v, false, false);
  return s[0] > s[1] ? s[0] : s[1];
}
DI float sum32(float v) {
  v = dpp_add<0xB1>(v); v = dpp_add<0x4E>(v); v = dpp_add<0x141>(v); v = dpp_add<0x140>(v);
  u32 b = __float_as_uint(v);
  auto r = __builtin_amdgcn_permlane16_swap(b, b, false, false);
  return __uint_as_float(r[0]) + __uint_as_float(r[1]);
}
DI void ln_row(f2 (&v)[10]) {
  f2 s = ((v[0] + v[1]) + (v[2] + v[3])) + ((v[4] + v[5]) + (v[6] + v[7])) + (v[8] + v[9]);
  const float mean = sum32(s[0] + s[1]) * (1.0f / 640.0f);
  f2 q0 = f2{0.f, 0.f}, q1 = f2{0.f, 0.f};
#pragma unroll
  for (int i = 0; i < 10; i += 2) { v[i] -= mean; v[i + 1] -= mean; q0 += v[i] * v[i]; q1 += v[i + 1] * v[i + 1]; }
  q0 += q1;
  const float var = sum32(q0[0] + q0[1]) * (1.0f / 640.0f);
  const float rinv = __builtin_amdgcn_rcpf(rbf(__builtin_amdgcn_sqrtf(rbf(rbf(var) + 1e-5f))));
  const float off = (mean - rbf(mean)) * rinv;
#pragma unroll
  for (int i = 0; i < 10; ++i) v[i] = v[i] * rinv + off;
}
#define SB() __builtin_amdgcn_sched_barrier(0)
template <bool SWAP> DI u4v cmul_pack(f4 re, f4 im, f4 wr, f4 wi) {
  const f4 ore = re * wr - im * wi, oim = re * wi + im * wr;
  u4v v;
#pragma unroll
  for (int r = 0; r < 4; ++r) v[r] = SWAP ? pk_h(oim[r], ore[r]) : pk_h(ore[r], oim[r]);
  return v;
}

struct __attribute__((packed, aligned(2))) U4U { u32 a, b, c, d; };

constexpr int K1_F40 = 10272, K1_TW = K1_F40 + 18432, K1_R16 = K1_TW + 6144, K1_LDS = K1_R16 + 4096 + 256;
extern "C" __global__ __launch_bounds__(256, 2) void k1_kernel(const u16* __restrict__ x, const u16* __restrict__ wr,
                                                    const u16* __restrict__ wi, char* __restrict__ ws) {
  __shared__ __attribute__((aligned(16))) char smem[K1_LDS];
  const int tid = threadIdx.x, lane = tid & 63;
  const int wave = __builtin_amdgcn_readfirstlane(tid >> 6);
  const int li = lane & 15, g = lane >> 4;
  STAMP(0,0);
  char* scratch = smem + K1_LDS - 256;
  if (wave == 3) CODE_TOUCH(k1_kernel, end_kernel, (blockIdx.x >> 3) & 7)
  PINS(x); PINS(wr); PINS(wi); PINS(ws);
  if (blockIdx.x >= 256) {
    const int pb = blockIdx.x - 256;
    const int cp = pb & 7, cc = pb >> 3;
    const int c0 = cc < 20 ? cc * 16 : 305;
    const int e = tid;
    const size_t roff = (size_t)(cp + 8 * e) * DF + c0;
    const U4U* pr = (const U4U*)(wr + roff);
    const U4U* pi = (const U4U*)(wi + roff);
    U4U r0 = pr[0], r1 = pr[1], i0 = pi[0], i1 = pi[1];
    const bool mir = cp > 4;
    const bool sp = (cp & 3) == 0;
    const int ep = mir ? 255 - e : e;
    const u32 flip = mir ? 0x80000000u : 0u;
    const int q1 = (mir ? 8 - cp : cp) - 1;
    const int kb = mir ? 39 - cc : cc;
    const int t1 = sp ? 1920 + 32 * cc + (cp >> 2) : (q1 * 160 + (kb >> 2) * 16 + (mir ? 16 : 0)) * 4 + (kb & 3);
    const int step = sp ? 2 : (mir ? -4 : 4);
    const int t0 = mir ? (q1 * 160 + ((kb + 1) >> 2) * 16) * 4 + ((kb + 1) & 3) : t1;
    u32* dst = (u32*)(ws + WS_WP) + ((ep & 15) + 16 * (ep >> 6)) * 4 + ((ep >> 4) & 3);
    u32 rr[8] = {r0.a, r0.b, r0.c, r0.d, r1.a, r1.b, r1.c, r1.d};
    u32 ii[8] = {i0.a, i0.b, i0.c, i0.d, i1.a, i1.b, i1.c, i1.d};
    u32 vals[16];
#pragma unroll
    for (int j = 0; j < 16; ++j) {
      u32 a = rr[j >> 1], b = ii[j >> 1];
      vals[j] = ((j & 1) ? __builtin_amdgcn_perm(b, a, 0x07060302u) : __builtin_amdgcn_perm(b, a, 0x05040100u)) ^ flip;
    }
    const bool edge = (cc == 0 || cc == 20);
    const u32 v0 = cc < 20 ? vals[0] : vals[15];
    if (!(mir && edge)) dst[(ptrdiff_t)t0 * 256] = v0;
    if (cc < 20) {
      u32* d = dst + (ptrdiff_t)t1 * 256;
#pragma unroll
      for (int j = 1; j < 16; ++j) d[(ptrdiff_t)step * 256 * j] = vals[j];
    }
    if (edge && (mir || sp)) {
      const int e2 = (cp == 0) ? ((256 - e) & 255) : 255 - e;
      const int dpos2 = ((e2 & 15) + 16 * (e2 >> 6)) * 4 + ((e2 >> 4) & 3);
      const int cls = mir ? 8 - cp : cp;
      ((u32*)(ws + WS_W2))[(cls * 2 + (cc == 20)) * 256 + dpos2] = v0 ^ flip ^ 0x80000000u;
    }
    u32 mx = 0;
#pragma unroll
    for (int j = 0; j < 16; ++j) {
      const u32 a = vals[j] & 0x7fff7fffu;
      mx = max(mx, max(a & 0xffffu, a >> 16));
    }
    mx = max64u(mx);
    if (lane == 0) ((u32*)(ws + WS_WMAX))[pb * 4 + wave] = mx;
    STAMP(0,5);
    return;
  }
  const int b = (blockIdx.x & 7) * 32 + (blockIdx.x >> 3);
  const int a = 2 * wave + (lane >> 5), j = lane & 31;
  const u16* xr = x + (size_t)(256 * a + b) * D;
  const u4v x0 = *(const u4v*)(xr + 8 * j);
  const u4v x1 = *(const u4v*)(xr + 256 + 8 * j);
  const u2v x2 = *(const u2v*)(xr + 512 + 4 * j);
#pragma unroll
  for (int it = 0; it < 3; ++it) {
    const int item = wave + 4 * it;
    if (it < 2 || wave == 0) {
      const int to = item / 3, tk = item - 3 * to;
      const int kb = 16 * to + li;
      const float s8 = (tk < 2 || g < 2) ? 0.125f : 0.f;
      u4v fre, fim;
#pragma unroll
      for (int jj = 0; jj < 4; ++jj) {
        const float ang = (float)(kb * (16 * tk + 4 * g + jj)) * (1.0f / 40.0f);
        const float c = cosr(ang) * s8, s = sinr(ang) * s8;
        fre[jj] = pk_h(c, s);
        fim[jj] = pk_h(-s, c);
      }
      *(u4v*)(smem + K1_F40 + item * 2048 + lane * 16) = fre;
      *(u4v*)(smem + K1_F40 + item * 2048 + 1024 + lane * 16) = fim;
    } else {
      const int t = wave - 1;
      f4 twr, twi;
#pragma unroll
      for (int r = 0; r < 4; ++r) {
        const float ang = (float)((16 * t + 4 * g + r) * li) * (1.0f / 640.0f);
        twr[r] = cosr(ang) * 0.125f; twi[r] = -sinr(ang) * 0.125f;
      }
      *(f4*)(smem + K1_TW + t * 2048 + lane * 16) = twr;
      *(f4*)(smem + K1_TW + t * 2048 + 1024 + lane * 16) = twi;
    }
  }
  {
    const int ph = 2 * wave + (lane >> 5);
    float cs[8];
#pragma unroll
    for (int jj = 0; jj < 8; ++jj) cs[jj] = cosr((float)(((8 * (g & 1) + jj) * li + 2 * ph) & 15) * 0.0625f);
    *(u4v*)(smem + K1_R16 + wave * 1024 + lane * 16) = u4v{pk_h(cs[0], cs[1]), pk_h(cs[2], cs[3]), pk_h(cs[4], cs[5]), pk_h(cs[6], cs[7])};
  }
  if (tid < 4) *(u32*)(smem + 10240 + tid * 4) = 0u;
  asm volatile("s_waitcnt lgkmcnt(0)" ::: "memory");
  __builtin_amdgcn_s_barrier();
  asm volatile("" ::: "memory");
  const int q = wave;
  u4v bre[4], bim[4], fre[9], fim[9];
  f4 twr[3], twi[3];
  u32 toff[3][4];
  {
    const int a0 = g >> 1;
#pragma unroll
    for (int ks = 0; ks < 4; ++ks) {
      const int ph = (q == 0) ? (a0 ? 1 : 7) : (2 * ks + a0) * q;
      bre[ks] = *(const u4v*)(smem + K1_R16 + (ph & 7) * 512 + (lane & 31) * 16);
      bim[ks] = *(const u4v*)(smem + K1_R16 + ((ph + 2) & 7) * 512 + (lane & 31) * 16);
    }
#pragma unroll
    for (int i = 0; i < 9; ++i) {
      fre[i] = *(const u4v*)(smem + K1_F40 + i * 2048 + lane * 16);
      fim[i] = *(const u4v*)(smem + K1_F40 + i * 2048 + 1024 + lane * 16);
    }
    const float qs = q == 0 ? 0.70710678118f : 1.f;
#pragma unroll
    for (int t = 0; t < 3; ++t) {
      twr[t] = *(const f4*)(smem + K1_TW + t * 2048 + lane * 16) * qs;
      twi[t] = *(const f4*)(smem + K1_TW + t * 2048 + 1024 + lane * 16) * qs;
      PIN(twr[t]); PIN(twi[t]);
#pragma unroll
      for (int r = 0; r < 4; ++r) {
        const int k2 = li + 16 * (16 * t + 4 * g + r);
        const int pos = k2 <= 320 ? 2 * k2 : 2 * (640 - k2) + 1;
        toff[t][r] = (u32)WS_T + (u32)b * (TROW_UNITS * 16) + (q > 0 ? ((q - 1) * 160 + 64 * t + lane) * 16 : 480 * 16 + pos * 4);
        if (t == 2 && lane >= 32) toff[t][r] = 0xfffffff0u;
        PIN(toff[t][r]);
      }
    }
#pragma unroll
    for (int ks = 0; ks < 4; ++ks) { PIN(bre[ks]); PIN(bim[ks]); }
#pragma unroll
    for (int i = 0; i < 9; ++i) { PIN(fre[i]); PIN(fim[i]); }
  }
  STAMP(0,1);
  {
    f2 v[10];
#pragma unroll
    for (int i = 0; i < 4; ++i) { v[i] = f2{bflo(x0[i]), bfhi(x0[i])}; v[4 + i] = f2{bflo(x1[i]), bfhi(x1[i])}; }
    v[8] = f2{bflo(x2[0]), bfhi(x2[0])}; v[9] = f2{bflo(x2[1]), bfhi(x2[1])};
    ln_row(v);
    char* ub = smem + a * 1280;
    *(u4v*)(ub + 16 * j) = u4v{pk_h(v[0][0], v[0][1]), pk_h(v[1][0], v[1][1]), pk_h(v[2][0], v[2][1]), pk_h(v[3][0], v[3][1])};
    *(u4v*)(ub + 512 + 16 * j) = u4v{pk_h(v[4][0], v[4][1]), pk_h(v[5][0], v[5][1]), pk_h(v[6][0], v[6][1]), pk_h(v[7][0], v[7][1])};
    *(u2v*)(ub + 1024 + 8 * j) = u2v{pk_h(v[8][0], v[8][1]), pk_h(v[9][0], v[9][1])};
  }
  __syncthreads();
  STAMP(0,2);
  u4v b2[3];
  {
    const int qq = li >> 2, p = li & 3;
    const char* abase = smem + (8 * g + qq) * 80 + 8 * p;
    u4v af[3][4];
#pragma unroll
    for (int t = 0; t < 3; ++t)
#pragma unroll
      for (int ks = 0; ks < 4; ++ks) {
        s4 lo = __builtin_amdgcn_ds_read_tr16_b64_v4i16((LDS_AS s4*)(abase + ks * 2560 + t * 32));
        s4 hi = __builtin_amdgcn_ds_read_tr16_b64_v4i16((LDS_AS s4*)(abase + ks * 2560 + 320 + t * 32));
        u2v l2 = __builtin_bit_cast(u2v, lo), h2v = __builtin_bit_cast(u2v, hi);
        af[t][ks] = u4v{l2[0], l2[1], h2v[0], h2v[1]};
      }
    SB();
    f4 are[3], aim[3];
#pragma unroll
    for (int t = 0; t < 3; ++t) {
      are[t] = f4{0, 0, 0, 0}; aim[t] = f4{0, 0, 0, 0};
#pragma unroll
      for (int ks = 0; ks < 4; ++ks) {
        are[t] = mfma_h(af[t][ks], bre[ks], are[t]);
        aim[t] = mfma_h(af[t][ks], bim[ks], aim[t]);
      }
    }
#pragma unroll
    for (int t = 0; t < 3; ++t) b2[t] = cmul_pack<false>(are[t], aim[t], twr[t], twi[t]);
  }
  const __amdgpu_buffer_rsrc_t rs = ws_rsrc(ws);
  f4 ore[3], oim[3];
#pragma unroll
  for (int tk = 0; tk < 3; ++tk)
#pragma unroll
    for (int to = 0; to < 3; ++to) {
      ore[to] = mfma_h(fre[to * 3 + tk], b2[tk], tk ? ore[to] : f4{0, 0, 0, 0});
      oim[to] = mfma_h(fim[to * 3 + tk], b2[tk], tk ? oim[to] : f4{0, 0, 0, 0});
    }
  u4v o[3];
#pragma unroll
  for (int to = 0; to < 3; ++to)
#pragma unroll
    for (int r = 0; r < 4; ++r) o[to][r] = pk_h(ore[to][r], oim[to][r]);
  if (q > 0) {
#pragma unroll
    for (int to = 0; to < 3; ++to) st16(rs, toff[to][0], o[to]);
  } else {
#pragma unroll
    for (int to = 0; to < 3; ++to)
#pragma unroll
      for (int r = 0; r < 4; ++r) __builtin_amdgcn_raw_buffer_store_b32(o[to][r], rs, toff[to][r], 0, 0);
    __builtin_amdgcn_raw_buffer_store_b32(lane ? o[1][0] : o[0][0], rs, lane == 0 ? toff[0][0] + 4 : (lane == 16 ? toff[1][0] + 4 : 0xfffffff0u), 0, 0);
  }
  STAMP(0,4);
}

constexpr int NRT1 = 20 * 27, NRT3 = 27 * 10;
constexpr int K2_FFT_BLOCKS = 168, K2_PREP_BLOCKS = 88;

extern "C" __global__ __launch_bounds__(256, 2) void k2_kernel(char* __restrict__ ws, const u16* __restrict__ w_in,
                                                    const u16* __restrict__ w_out, const u16* __restrict__ w_sp) {
  PINS(ws); PINS(w_in); PINS(w_out); PINS(w_sp);
  const u32* T = (const u32*)(ws + WS_T); const u32* Wp = (const u32*)(ws + WS_WP); const u32* W2 = (const u32*)(ws + WS_W2);
  const u32* wmax = (const u32*)(ws + WS_WMAX); u32* Tp = (u32*)(ws + WS_TP); float* unscale = (float*)(ws + WS_UNS);
  u32* Wt1 = (u32*)(ws + WS_WT1); u32* Wt3 = (u32*)(ws + WS_WT3); u32* stats = (u32*)(ws + WS_STATS);
  extern __shared__ __attribute__((aligned(16))) char smem[];
  const int tid = threadIdx.x, lane = tid & 63;
  const int wave = __builtin_amdgcn_readfirstlane(tid >> 6);
  const int li = lane & 15, g = lane >> 4;
  if (blockIdx.x >= K2_FFT_BLOCKS) {
    STAMP(1,0);
    const int wid = (blockIdx.x - K2_FFT_BLOCKS) * 4 + wave;
    const __amdgpu_buffer_rsrc_t rs = ws_rsrc(ws);
    char* lds = smem + wave * 12288;
#pragma unroll
    for (int i = 0; i < 3; ++i) {
      const int task = wid + i * (K2_PREP_BLOCKS * 4);
      const bool one = task < NRT1;
      const int t = one ? task : (task < NRT1 + NRT3 ? task - NRT1 : 0);
      const int ks = one ? t / 27 : t / 10, cg = t - ks * (one ? 27 : 10);
      const u16* src = (one ? w_in : w_out) + (size_t)(ks * 32 + (lane >> 3)) * (one ? FF : D) + cg * 64 + (lane & 7) * 8;
#pragma unroll
      for (int j = 0; j < 4; ++j) GLDS16(src + (size_t)j * 8 * (one ? FF : D), lds + i * 4096 + j * 1024);
    }
    { char* scratch = smem + 49152;
      touch((const char*)w_sp + ((size_t)wid * 64 + lane) * 128); }
    st16(rs, wid < 256 ? (u32)WS_STATS + wid * 1024 + lane * 16 : 0xfffffff0u, u4v{~0u, ~0u, ~0u, ~0u});
    WAITV(2);
    const int q = li >> 2, p = li & 3;
#pragma unroll
    for (int i = 0; i < 3; ++i) {
      const int task = wid + i * (K2_PREP_BLOCKS * 4);
      const bool one = task < NRT1;
      const int t = one ? task : task - NRT1;
      const int ks = one ? t / 27 : t / 10, cg = t - ks * (one ? 27 : 10);
      const u32 dst = (one ? (u32)WS_WT1 + (ks * 108 + cg * 4) * 1024 : (u32)WS_WT3 + (ks * 40 + cg * 4) * 1024) + lane * 16;
      const int r0 = one ? 8 * g + q : 4 * g + q, r1 = one ? r0 + 4 : r0 + 16;
#pragma unroll
      for (int f = 0; f < 4; ++f) {
        s4 lo = __builtin_amdgcn_ds_read_tr16_b64_v4i16((LDS_AS s4*)(lds + i * 4096 + r0 * 128 + f * 32 + 8 * p));
        s4 hi = __builtin_amdgcn_ds_read_tr16_b64_v4i16((LDS_AS s4*)(lds + i * 4096 + r1 * 128 + f * 32 + 8 * p));
        u2v a = __builtin_bit_cast(u2v, lo), b = __builtin_bit_cast(u2v, hi);
        if (task < NRT1 + NRT3) st16(rs, dst + f * 1024, u4v{a[0], a[1], b[0], b[1]});
      }
    }
    STAMP(1,1);
    return;
  }
  const int P = (blockIdx.x & 7) * 21 + (blockIdx.x >> 3);
  if (P > 160) return;
  const int U = 4 * P + wave;
  STAMP(2,0);
#pragma unroll
  for (int i = 0; i < 4; ++i) {
    const int bb = (wave * 4 + i) * 16 + (lane >> 2);
    GLDS16(T + ((size_t)bb * TROW_UNITS + 4 * P + ((lane & 3) ^ ((lane >> 4) & 3))) * 4, smem + (wave * 4 + i) * 1024);
  }
  u4v mxv[3];
#pragma unroll
  for (int i = 0; i < 3; ++i) mxv[i] = *(const u4v*)(wmax + (i < 2 || lane < 40 ? i * 256 + lane * 4 : 0));
  u4v Wv[4];
#pragma unroll
  for (int t = 0; t < 4; ++t) Wv[t] = *(const u4v*)(Wp + ((size_t)((U < NUNITS ? U : 0) * 4 + t) * 64 + lane) * 4);
  STAMP(2,5);
  const bool special = P >= 120;
  const int cB = special ? 4 : (P / 40 + 1);
  const float osc = special ? 0.125f : 0.25f;
  {
    char* cb = smem + 16384;
    const int mA = (wave & 1) ? li : 4 * g, mB = (wave & 1) ? 4 * g : li;
    const float inv = wave < 2 ? (1.0f / 128.0f) : (1.0f / 2048.0f);
    const float amp = wave == 0 ? 1.f : (wave == 1 ? osc : 0.25f);
    f4 c, s;
#pragma unroll
    for (int jj = 0; jj < 4; ++jj) {
      const int a = (wave & 1) ? mA * (cB + 8 * (mB + jj)) : (mA + jj) * (cB + 8 * mB);
      const float ang = (float)a * inv;
      c[jj] = cosr(ang) * amp; s[jj] = sinr(ang) * amp;
    }
    if (wave < 2) {
      *(u4v*)(cb + wave * 2048 + lane * 16) = u4v{pk_h(c[0], s[0]), pk_h(c[1], s[1]), pk_h(c[2], s[2]), pk_h(c[3], s[3])};
      *(u4v*)(cb + wave * 2048 + 1024 + lane * 16) = u4v{pk_h(-s[0], c[0]), pk_h(-s[1], c[1]), pk_h(-s[2], c[2]), pk_h(-s[3], c[3])};
    } else {
      *(f4*)(cb + wave * 2048 + lane * 16) = c;
      *(f4*)(cb + wave * 2048 + 1024 + lane * 16) = s;
    }
    if (wave < 3 && (wave == 0 || special)) {
      const float inv2 = wave < 2 ? 0.0625f : (1.0f / 256.0f);
#pragma unroll
      for (int jj = 0; jj < 4; ++jj) {
        const float ang = (float)(li * (4 * g + jj)) * inv2;
        c[jj] = cosr(ang) * amp; s[jj] = sinr(ang) * amp;
      }
      if (wave < 2) {
        *(u4v*)(cb + (4 + wave) * 2048 + lane * 16) = u4v{pk_h(c[0], s[0]), pk_h(c[1], s[1]), pk_h(c[2], s[2]), pk_h(c[3], s[3])};
        *(u4v*)(cb + (4 + wave) * 2048 + 1024 + lane * 16) = u4v{pk_h(-s[0], c[0]), pk_h(-s[1], c[1]), pk_h(-s[2], c[2]), pk_h(-s[3], c[3])};
      } else {
        *(f4*)(cb + 6 * 2048 + lane * 16) = c;
        *(f4*)(cb + 6 * 2048 + 1024 + lane * 16) = s;
      }
    }
  }
  asm volatile("s_waitcnt lgkmcnt(0)" ::: "memory");
  __builtin_amdgcn_s_barrier();
  asm volatile("" ::: "memory");
  STAMP(2,7);
  u4v Fre, Fim, P1re[2], P1im[2], P4re[2], P4im[2];
  f4 tfr[2], tfs[2], tir[2], tis[2];
  {
    const char* cb = smem + 16384 + lane * 16;
    Fre = *(const u4v*)(cb + 4 * 2048); Fim = *(const u4v*)(cb + 4 * 2048 + 1024);
    P1re[1] = *(const u4v*)(cb); P1im[1] = *(const u4v*)(cb + 1024);
    P4re[1] = *(const u4v*)(cb + 2048); P4im[1] = *(const u4v*)(cb + 2048 + 1024);
    tfr[1] = *(const f4*)(cb + 2 * 2048); tfs[1] = *(const f4*)(cb + 2 * 2048 + 1024);
    tir[1] = *(const f4*)(cb + 3 * 2048); tis[1] = *(const f4*)(cb + 3 * 2048 + 1024);
    const int o1 = special ? 4 * 2048 : 0, o4 = special ? 5 * 2048 : 2048, of = special ? 6 * 2048 : 2 * 2048, oi = special ? 6 * 2048 : 3 * 2048;
    P1re[0] = *(const u4v*)(cb + o1); P1im[0] = *(const u4v*)(cb + o1 + 1024);
    P4re[0] = *(const u4v*)(cb + o4); P4im[0] = *(const u4v*)(cb + o4 + 1024);
    tfr[0] = *(const f4*)(cb + of); tfs[0] = *(const f4*)(cb + of + 1024);
    tir[0] = *(const f4*)(cb + oi); tis[0] = *(const f4*)(cb + oi + 1024);
    PIN(Fre); PIN(Fim);
#pragma unroll
    for (int c = 0; c < 2; ++c) { PIN(P1re[c]); PIN(P1im[c]); PIN(P4re[c]); PIN(P4im[c]); PIN(tfr[c]); PIN(tfs[c]); PIN(tir[c]); PIN(tis[c]); }
  }
  STAMP(2,1);
  u32 mx = 0;
#pragma unroll
  for (int i = 0; i < 3; ++i) mx = max(max(max(mxv[i][0], mxv[i][1]), max(mxv[i][2], mxv[i][3])), mx);
  mx = max64u(mx);
  int E = (int)(mx >> 7);
  E = E < 1 ? 1 : (E > 253 ? 253 : E);
  const float sig = __uint_as_float((u32)(254 - E) << 23) * 0.25f;
  if (U == 0 && lane == 0) unscale[0] = __uint_as_float((u32)E << 23);
  f4 wr4[4], wi4[4];
#pragma unroll
  for (int t = 0; t < 4; ++t)
#pragma unroll
    for (int r = 0; r < 4; ++r) { wr4[t][r] = bflo(Wv[t][r]); wi4[t][r] = bfhi(Wv[t][r]); }
  {
    const int ur = special ? U - 480 : U % 160;
    const bool e0 = ur == 0, e320 = special ? ur == 160 : ur == 80;
    if (e0 || e320) {
      const int cls = special ? 0 : cB;
      const u4v w2 = *(const u4v*)(W2 + (cls * 2 + (e320 ? 1 : 0)) * 256 + lane * 4);
      const u4v w4 = *(const u4v*)(W2 + (4 * 2 + (e320 ? 1 : 0)) * 256 + lane * 4);
#pragma unroll
      for (int r = 0; r < 4; ++r) {
        wr4[0][r] = (wr4[0][r] + bflo(w2[r])) * 0.5f; wi4[0][r] = (wi4[0][r] + bfhi(w2[r])) * 0.5f;
        if (special) { wr4[1][r] = (wr4[1][r] + bflo(w4[r])) * 0.5f; wi4[1][r] = (wi4[1][r] + bfhi(w4[r])) * 0.5f; }
      }
    }
  }
#pragma unroll
  for (int t = 0; t < 4; ++t) { wr4[t] *= sig; wi4[t] *= sig; PIN(wr4[t]); PIN(wi4[t]); }
  asm volatile("s_waitcnt vmcnt(0)" ::: "memory");
  __builtin_amdgcn_s_barrier();
  asm volatile("" ::: "memory");
  if (U >= NUNITS) return;
  STAMP(2,2);
  u4v L[4];
#pragma unroll
  for (int jj = 0; jj < 4; ++jj) L[jj] = *(const u4v*)(smem + (16 * (4 * g + jj) + li) * 64 + ((wave ^ (li >> 2)) & 3) * 16);
  u4v X[4];
  if (special) {
#pragma unroll
    for (int jj = 0; jj < 4; ++jj)
#pragma unroll
      for (int pp = 0; pp < 2; ++pp) {
        const u32 au = L[jj][2 * pp], bu = L[jj][2 * pp + 1] ^ 0x80000000u;
        const h2 A = __builtin_bit_cast(h2, au);
        const h2 Bc = __builtin_bit_cast(h2, bu);
        X[2 * pp][jj] = __builtin_bit_cast(u32, A + Bc);
        X[2 * pp + 1][jj] = __builtin_bit_cast(u32, A - Bc);
      }
  } else {
#pragma unroll
    for (int jj = 0; jj < 4; ++jj)
#pragma unroll
      for (int t = 0; t < 4; ++t) X[t][jj] = L[jj][t];
  }
  const f4 z4 = f4{0, 0, 0, 0};
  f4 re[4], im[4];
#pragma unroll
  for (int t = 0; t < 4; ++t) { re[t] = mfma_h(X[t], P1re[t & 1], z4); im[t] = mfma_h(X[t], P1im[t & 1], z4); }
#pragma unroll
  for (int t = 0; t < 4; ++t) {
    const u4v v = cmul_pack<false>(re[t], im[t], tfr[t & 1], -tfs[t & 1]);
    re[t] = mfma_h(Fre, v, z4); im[t] = mfma_h(Fim, v, z4);
  }
#pragma unroll
  for (int t = 0; t < 4; ++t) {
    const u4v v = cmul_pack<true>(re[t], im[t], wr4[t], wi4[t]);
    im[t] = mfma_h(v, Fre, z4); re[t] = mfma_h(v, Fim, z4);
  }
#pragma unroll
  for (int t = 0; t < 4; ++t) {
    const u4v v = cmul_pack<true>(re[t], im[t], tir[t & 1], tis[t & 1]);
    im[t] = mfma_h(P4re[t & 1], v, z4); re[t] = mfma_h(P4im[t & 1], v, z4);
  }
  STAMP(2,3);
  if (special) {
#pragma unroll
    for (int pp = 0; pp < 2; ++pp) {
      const f4 yr = re[2 * pp], yi = im[2 * pp], zr = re[2 * pp + 1], zi = im[2 * pp + 1];
      re[2 * pp] = yr + zr; im[2 * pp] = yi + zi;
      re[2 * pp + 1] = yr - zr; im[2 * pp + 1] = zi - yi;
    }
  }
  const __amdgpu_buffer_rsrc_t rs = ws_rsrc(ws);
  const u32 dst = (u32)WS_TP + (U * 256 + lane) * 16;
#pragma unroll
  for (int r = 0; r < 4; ++r) {
    u4v o;
#pragma unroll
    for (int t = 0; t < 4; ++t) o[t] = pk_h(re[t][r], im[t][r]);
    st16(rs, dst + r * 1024, o);
  }
  { char* scratch = smem + 49152; if (wave == 0) CODE_TOUCH(k3_kernel, g1_kernel, 0) }
  STAMP(2,4);
}

extern "C" __global__ __launch_bounds__(256, 2) void k3_kernel(const u16* __restrict__ x, char* __restrict__ ws, const u16* __restrict__ w_sp) {
  PINS(x); PINS(ws); PINS(w_sp);
  const u32* Tp = (const u32*)(ws + WS_TP);
  constexpr int K3_Y = 10752, K3_CB = K3_Y + 10240;
  __shared__ __attribute__((aligned(16))) char smem[K3_CB + 8192 + 512];
  const int tid = threadIdx.x, lane = tid & 63;
  const int wave = __builtin_amdgcn_readfirstlane(tid >> 6);
  const int li = lane & 15, g = lane >> 4;
  const int b = (blockIdx.x & 7) * 32 + (blockIdx.x >> 3);
  const int posb = ((b >> 4) & 3) * 64 + (b >> 6) * 16 + (b & 15);
  STAMP(3,0);
#pragma unroll
  for (int i = 0; i < 2; ++i) {
    int U = (wave * 2 + i) * 64 + lane;
    U = U < 480 ? U : 479;
    GLDS16(Tp + ((size_t)U * 256 + posb) * 4, smem + (wave * 2 + i) * 1024);
  }
#pragma unroll
  for (int it = 0; it < 4; ++it) {
    const int i = it < 2 ? wave * 2 + it : 6 + it;
    if (it < 2 || wave == 3) {
      const int k2 = (lane >> 2) + 16 * (4 * i + (lane & 3));
      const int pos = k2 <= 320 ? 2 * k2 : 2 * (640 - k2) + 1;
      GLDS4(Tp + ((size_t)(480 + (pos >> 2)) * 256 + posb) * 4 + (pos & 3), smem + 8192 + i * 256);
    }
  }
  char* scratch = smem + K3_CB + 8192;
  if (wave == 0) GLDS4(ws + WS_UNS, scratch + 256);
  const int a = 2 * wave + (lane >> 5), j = lane & 31;
  const int row = 256 * a + b;
  const u16* xr = x + (size_t)row * D;
  const u4v x0 = *(const u4v*)(xr + 8 * j);
  const u4v x1 = *(const u4v*)(xr + 256 + 8 * j);
  const u2v x2 = *(const u2v*)(xr + 512 + 4 * j);
  STAMP(3,5);
  char* cb = smem + K3_CB;
  const int t = wave;
  u4v Bre[3], Bim[3];
  if (wave < 3) {
#pragma unroll
    for (int tk = 0; tk < 3; ++tk) {
      const float sc = (tk < 2 || g < 2) ? 1.f : 0.f;
#pragma unroll
      for (int jj = 0; jj < 4; ++jj) {
        const float ang = (float)((16 * tk + 4 * g + jj) * (16 * t + li)) * (1.0f / 40.0f);
        const float c = cosr(ang) * sc, s = sinr(ang) * sc;
        Bre[tk][jj] = pk_h(c, -s);
        Bim[tk][jj] = pk_h(s, c);
      }
      PIN(Bre[tk]); PIN(Bim[tk]);
    }
  } else {
#pragma unroll
    for (int tt = 0; tt < 3; ++tt) {
      f4 twr, twi;
#pragma unroll
      for (int jj = 0; jj < 4; ++jj) {
        const float ang = (float)((16 * tt + li) * (4 * g + jj)) * (1.0f / 640.0f);
        twr[jj] = cosr(ang) * 0.125f; twi[jj] = sinr(ang) * 0.125f;
      }
      *(f4*)(cb + tt * 2048 + lane * 16) = twr;
      *(f4*)(cb + tt * 2048 + 1024 + lane * 16) = twi;
    }
    u4v gre, gim;
#pragma unroll
    for (int jj = 0; jj < 4; ++jj) {
      const float ang = (float)(li * (4 * g + jj)) * 0.0625f;
      const float c = cosr(ang), s = sinr(ang);
      gre[jj] = pk_h(c, -s); gim[jj] = pk_h(s, c);
    }
    *(u4v*)(cb + 6144 + lane * 16) = gre;
    *(u4v*)(cb + 7168 + lane * 16) = gim;
  }
  STAMP(3,1);
  asm volatile("s_waitcnt vmcnt(0)" ::: "memory");
  __syncthreads();
  STAMP(3,2);
  char* yb = smem + K3_Y;
  if (wave < 3) {
    const f4 z4 = f4{0, 0, 0, 0};
    u4v af[4][3];
#pragma unroll
    for (int q = 0; q < 4; ++q) {
      const char* base = smem + (q == 0 ? 8192 : (q - 1) * 2560);
#pragma unroll
      for (int tk = 0; tk < 3; ++tk) af[q][tk] = *(const u4v*)(base + (tk < 2 ? (64 * tk + lane) : (128 + (lane & 31))) * 16);
    }
    const f4 twr = *(const f4*)(cb + t * 2048 + lane * 16), twi = *(const f4*)(cb + t * 2048 + 1024 + lane * 16);
    const u4v Gre = *(const u4v*)(cb + 6144 + lane * 16), Gim = *(const u4v*)(cb + 7168 + lane * 16);
    const float usc = *(const float*)(scratch + 256);
    SB();
    f4 vre[4], vim[4];
#pragma unroll
    for (int q = 0; q < 4; ++q) {
      f4 re = z4, im = z4;
#pragma unroll
      for (int tk = 0; tk < 3; ++tk) {
        re = mfma_h(af[q][tk], Bre[tk], re);
        im = mfma_h(af[q][tk], Bim[tk], im);
      }
      vre[q] = re; vim[q] = im;
    }
#pragma unroll
    for (int q = 0; q < 4; ++q) {
      const u4v v = cmul_pack<false>(vre[q], vim[q], twr, twi);
      vre[q] = mfma_h(v, Gre, z4);
      vim[q] = mfma_h(v, Gim, z4);
    }
    const float fs = usc * (2.0f * 1024.0f * 128.0f / (2048.0f * 640.0f));
    const float fh = fs * 0.70710678118f;
    const f4 E = (vre[0] + vim[0]) * fs, O = (vre[0] - vim[0]) * fs;
    const f4 s13 = vre[1] + vre[3], dx = vre[1] - vre[3], sy = vim[1] + vim[3], dy = vim[3] - vim[1];
    const f4 Ep = E + vre[2] * fs, Em = E - vre[2] * fs, Om = O - vim[2] * fs, Op = O + vim[2] * fs;
    const f4 A = dx - sy, B = dx + sy;
    f4 y[8];
    y[0] = Ep + s13 * fs; y[4] = Ep - s13 * fs; y[2] = Em + dy * fs; y[6] = Em - dy * fs;
    y[1] = Om + A * fh; y[5] = Om - A * fh; y[3] = Op - B * fh; y[7] = Op + B * fh;
    if (t < 2 || lane < 32) {
      char* yp = yb + (40 * li + 16 * t + 4 * g) * 2;
#pragma unroll
      for (int aa = 0; aa < 8; ++aa) *(u2v*)(yp + aa * 1280) = u2v{pk_bf(y[aa][0], y[aa][1]), pk_bf(y[aa][2], y[aa][3])};
    }
  }
  else {
    CODE_TOUCH(g1_kernel, g2_kernel, (blockIdx.x >> 3) & 1)
#pragma unroll
    for (int i = 0; i < 3; ++i) { const int task = PF_WSP_K2 + blockIdx.x * 3 + i; if (task < 1024) touch((const char*)w_sp + ((size_t)task * 64 + lane) * 128); }
  }
  asm volatile("s_waitcnt lgkmcnt(0)" ::: "memory");
  __builtin_amdgcn_s_barrier();
  asm volatile("" ::: "memory");
  STAMP(3,3);
  {
    const __amdgpu_buffer_rsrc_t rs = ws_rsrc(ws);
    const u4v y0 = *(const u4v*)(yb + a * 1280 + 16 * j);
    const u4v y1 = *(const u4v*)(yb + a * 1280 + 512 + 16 * j);
    const u2v y2 = *(const u2v*)(yb + a * 1280 + 1024 + 8 * j);
    u32 hx[10];
#pragma unroll
    for (int i = 0; i < 4; ++i) {
      hx[i] = pk_bf(bflo(x0[i]) + bflo(y0[i]), bfhi(x0[i]) + bfhi(y0[i]));
      hx[4 + i] = pk_bf(bflo(x1[i]) + bflo(y1[i]), bfhi(x1[i]) + bfhi(y1[i]));
    }
    hx[8] = pk_bf(bflo(x2[0]) + bflo(y2[0]), bfhi(x2[0]) + bfhi(y2[0]));
    hx[9] = pk_bf(bflo(x2[1]) + bflo(y2[1]), bfhi(x2[1]) + bfhi(y2[1]));
    auto pairup = [&](u32 v0, u32 v1) {
      return u4v{v0, v1, (u32)__builtin_amdgcn_update_dpp(0, (int)v0, 0xB1, 0xf, 0xf, true), (u32)__builtin_amdgcn_update_dpp(0, (int)v1, 0xB1, 0xf, 0xf, true)};
    };
    const u32 drop = (lane & 1) ? 0xfffffff0u : 0u;
    const u32 ho = (u32)WS_H + row * (D * 2);
    st16(rs, ho + 16 * j, u4v{hx[0], hx[1], hx[2], hx[3]});
    st16(rs, ho + 512 + 16 * j, u4v{hx[4], hx[5], hx[6], hx[7]});
    st16(rs, (ho + 1024 + 8 * j) | drop, pairup(hx[8], hx[9]));
    f2 v[10];
#pragma unroll
    for (int i = 0; i < 10; ++i) v[i] = f2{bflo(hx[i]), bfhi(hx[i])};
    ln_row(v);
#pragma unroll
    for (int i = 0; i < 10; ++i) hx[i] = pk_bf(v[i][0], v[i][1]);
    const u32 lo = (u32)WS_L2 + (row >> 4) * 20480 + (row & 15) * 64;
    st16(rs, lo + (j >> 2) * 1024 + (j & 3) * 16, u4v{hx[0], hx[1], hx[2], hx[3]});
    st16(rs, lo + (8 + (j >> 2)) * 1024 + (j & 3) * 16, u4v{hx[4], hx[5], hx[6], hx[7]});
    st16(rs, (lo + (16 + (j >> 3)) * 1024 + (j & 7) * 8) | drop, pairup(hx[8], hx[9]));
  }
  STAMP(3,4);
}

#include <type_traits>
typedef unsigned long long u64;
typedef __attribute__((address_space(1))) unsigned long long gu64;
#define WAITL0() do { __builtin_amdgcn_sched_barrier(0); asm volatile("s_waitcnt lgkmcnt(0)" ::: "memory"); __builtin_amdgcn_sched_barrier(0); } while (0)
#define BAR() do { asm volatile("" ::: "memory"); __builtin_amdgcn_sched_barrier(0); __builtin_amdgcn_s_barrier(); __builtin_amdgcn_sched_barrier(0); asm volatile("" ::: "memory"); } while (0)
#define SGB(mask, n) __builtin_amdgcn_sched_group_barrier(mask, n, 0)
DI f4 mfma_b(u4v a, u4v b, f4 c) {
  return __builtin_amdgcn_mfma_f32_16x16x32_bf16(__builtin_bit_cast(bf8, a), __builtin_bit_cast(bf8, b), c, 0, 0, 0);
}
DI f2 gelu2(f2 x) {
  const float k1 = -2.0f * 0.7978845608028654f * 1.4426950408889634f;
  const float k2 = k1 * 0.044715f;
  f2 p = x * x * k2 + k1;
  f2 a = x * p;
  f2 e = f2{__builtin_amdgcn_exp2f(a[0]), __builtin_amdgcn_exp2f(a[1])} + 1.0f;
  f2 r = f2{__builtin_amdgcn_rcpf(e[0]), __builtin_amdgcn_rcpf(e[1])};
  return x * r;
}
DI u2v gelu4(f4 a) {
  f2 g0 = gelu2(f2{a[0], a[1]}), g1 = gelu2(f2{a[2], a[3]});
  return u2v{pk_bf(g0[0], g0[1]), pk_bf(g1[0], g1[1])};
}
template <int N> DI void waitv() { asm volatile("s_waitcnt vmcnt(%0)" ::"n"(N) : "memory"); }

template <int KT, int NST, int NLA, class F>
DI void loader_loop(bool big, F&& stage) {
#pragma unroll
  for (int s = 0; s < NST - 1; ++s) stage(s, s);
  int nbuf = NST - 1;
  for (int kt = 0; kt < KT - (NST - 1); ++kt) {
    if (big) waitv<NLA*(NST - 2)>(); else waitv<(NLA - 1) * (NST - 2)>();
    BAR();
    stage(kt + NST - 1, nbuf);
    nbuf = nbuf + 1 == NST ? 0 : nbuf + 1;
  }
  if constexpr (NST >= 7) { if (big) waitv<NLA * 5>(); else waitv<(NLA - 1) * 5>(); BAR(); }
  if constexpr (NST >= 6) { if (big) waitv<NLA * 4>(); else waitv<(NLA - 1) * 4>(); BAR(); }
  if constexpr (NST >= 5) { if (big) waitv<NLA * 3>(); else waitv<(NLA - 1) * 3>(); BAR(); }
  if (big) waitv<NLA * 2>(); else waitv<(NLA - 1) * 2>(); BAR();
  if (big) waitv<NLA>(); else waitv<NLA - 1>(); BAR();
  waitv<0>(); BAR();
}

#define G1_DEPTH 3
extern "C" __global__ __launch_bounds__(512) void g1_kernel(char* __restrict__ ws) {
  PINS(ws);
  const u32* l2f = (const u32*)(ws + WS_L2); const u32* Wt1 = (const u32*)(ws + WS_WT1);
  u32* z1f = (u32*)(ws + WS_Z1); u16* z2t = (u16*)(ws + WS_Z2); u64* stats = (u64*)(ws + WS_STATS);
  __shared__ __attribute__((aligned(16))) char smem[140 * 1024 + 4 * 4608 + 256];
  const int tid = threadIdx.x, lane = tid & 63;
  const int wave = __builtin_amdgcn_readfirstlane(tid >> 6);
  const int li = lane & 15, g = lane >> 4;
  STAMP(4,0);
  const int xcd = blockIdx.x & 7, idx = blockIdx.x >> 3;
#define G1_MAP 1
  const int mt = G1_MAP == 0 ? xcd * 2 + (idx >> 4) : (G1_MAP == 1 ? (xcd >> 1) * 4 + (idx >> 3) : (xcd >> 2) * 8 + (idx >> 2));
  const int nb = G1_MAP == 0 ? (idx & 15) : (G1_MAP == 1 ? (xcd & 1) * 8 + (idx & 7) : (xcd & 3) * 4 + (idx & 3));
  const int row0 = 1024 * (mt & 1) + 32 * (mt >> 1);
  const int zs = nb < 6 ? 4 * nb : 24 + 3 * (nb - 6);
  const int z1s = nb < 6 ? 3 * nb : (nb < 12 ? 18 + 4 * (nb - 6) : 42 + 3 * (nb - 12));
  const int type = nb < 6 ? 0 : (nb < 12 ? 1 : 2);
  if (wave >= 4) {
    const int lw = wave - 4;
    auto slot_frag = [&](int s) { return s < 3 ? 54 + zs + s : (s == 3 ? (type == 0 ? 54 + zs + 3 : (type == 1 ? z1s + 3 : z1s)) : z1s + s - 4); };
    const u32* bsrc[4];
#pragma unroll
    for (int j = 0; j < 4; ++j) {
      int i = lw + 4 * j; i = i < 14 ? i : 0;
      const int kk = i / 7, s = i - 7 * kk;
      bsrc[j] = Wt1 + (size_t)(kk * 108 + slot_frag(s)) * 256 + lane * 4;
    }
    auto stage = [&](int kt) {
      char* sb = smem + kt * 14336 + lw * 1024;
#pragma unroll
      for (int j = 0; j < 3; ++j) GLDS16(bsrc[j] + (size_t)kt * 2 * 108 * 256, sb + j * 4096);
      if (lw < 2) GLDS16(bsrc[3] + (size_t)kt * 2 * 108 * 256, sb + 3 * 4096);
    };
#pragma unroll
    for (int s = 0; s < G1_DEPTH; ++s) stage(s);
    for (int kt = 0; kt < 10 - G1_DEPTH; ++kt) {
      if (lw < 2) waitv<4 * (G1_DEPTH - 1)>(); else waitv<3 * (G1_DEPTH - 1)>();
      BAR();
      stage(kt + G1_DEPTH);
    }
    if constexpr (G1_DEPTH >= 5) { if (lw < 2) waitv<16>(); else waitv<12>(); BAR(); }
    if constexpr (G1_DEPTH >= 4) { if (lw < 2) waitv<12>(); else waitv<9>(); BAR(); }
    if (lw < 2) waitv<8>(); else waitv<6>(); BAR();
    if (lw < 2) waitv<4>(); else waitv<3>(); BAR();
    waitv<0>(); BAR();
    { char* scratch = smem + 140 * 1024 + 4 * 4608; if (lw < 2) CODE_TOUCH(g2_kernel, g3_kernel, lw) }
    return;
  }
  const int rg0 = (row0 + wave * 256) >> 4;
  const u32* asrc = l2f + (size_t)rg0 * 20 * 256 + li * 16 + g * 4;
#define G1_AD 5
  u4v fa[G1_AD][2];
#pragma unroll
  for (int k = 0; k < G1_AD; ++k) { fa[k][0] = *(const u4v*)(asrc + k * 256); fa[k][1] = *(const u4v*)(asrc + (20 + k) * 256); __builtin_amdgcn_sched_barrier(0); }
  f4 acc[7][2];
#pragma unroll
  for (int s = 0; s < 7; ++s) { acc[s][0] = f4{0, 0, 0, 0}; acc[s][1] = f4{0, 0, 0, 0}; }
  u4v fw[2][7];
  const char* wb = smem + lane * 16;
  auto rd = [&](int k) {
#pragma unroll
    for (int s = 0; s < 7; ++s) fw[k & 1][s] = *(const u4v*)(wb + (k * 7 + s) * 1024);
  };
  auto mm = [&](int k) {
#pragma unroll
    for (int s = 0; s < 7; ++s) {
      acc[s][0] = mfma_b(fw[k & 1][s], fa[k % G1_AD][0], acc[s][0]);
      acc[s][1] = mfma_b(fw[k & 1][s], fa[k % G1_AD][1], acc[s][1]);
    }
    if (k + G1_AD < 20) { fa[k % G1_AD][0] = *(const u4v*)(asrc + (k + G1_AD) * 256); fa[k % G1_AD][1] = *(const u4v*)(asrc + (20 + G1_AD + k) * 256); }
  };
  BAR(); STAMP(4,3); rd(0);
#pragma unroll
  for (int k = 1; k < 20; ++k) {
    if (!(k & 1)) BAR(); else __builtin_amdgcn_sched_barrier(0);
    if (k == 8) { STAMP(4,6); STAMPC(7,0); }
    if (k == 14) { STAMP(4,7); STAMPC(7,1); }
    rd(k);
    mm(k - 1);
#pragma unroll
    for (int i = 0; i < 7; ++i) { SGB(0x100, 1); SGB(0x8, 2); }
  }
  __builtin_amdgcn_sched_barrier(0);
  mm(19);
  STAMP(4,1);
  const int n2 = type == 0 ? 4 : 3;
  f4 zv[4][2];
  u2v z3[2];
  float lsum[2], lm2[2];
#pragma unroll
  for (int sf = 0; sf < 2; ++sf) {
    f2 s = f2{0.f, 0.f};
#pragma unroll
    for (int j = 0; j < 4; ++j) {
      const u2v t = gelu4(acc[j][sf]);
      if (j == 3) z3[sf] = t;
      zv[j][sf] = f4{bflo(t[0]), bfhi(t[0]), bflo(t[1]), bfhi(t[1])};
      if (j < 3 || type == 0) s += f2{zv[j][sf][0], zv[j][sf][1]} + f2{zv[j][sf][2], zv[j][sf][3]};
    }
    const float st = xsum_g(s[0] + s[1]);
    const float lmean = st * (type == 0 ? (1.0f / 64.0f) : (1.0f / 48.0f));
    f2 q = f2{0.f, 0.f};
#pragma unroll
    for (int j = 0; j < 4; ++j) {
      const f4 d = zv[j][sf] - lmean;
      if (j < 3 || type == 0) { q += f2{d[0], d[1]} * f2{d[0], d[1]}; q += f2{d[2], d[3]} * f2{d[2], d[3]}; }
    }
    lsum[sf] = st; lm2[sf] = xsum_g(q[0] + q[1]);
  }
  gu64* st = (gu64*)stats + (size_t)mt * 16 * 128 + wave * 32;
  if (lane < 32) {
    u32 a = __float_as_uint(g ? lsum[1] : lsum[0]), b = __float_as_uint(g ? lm2[1] : lm2[0]);
    if (a == ~0u) a = 0x7fc00000u;
    __hip_atomic_store(st + nb * 128 + lane, ((u64)b << 32) | a, __ATOMIC_RELAXED, __HIP_MEMORY_SCOPE_AGENT);
  }
  __builtin_amdgcn_sched_barrier(0);
  STAMP(4,2);
  const __amdgpu_buffer_rsrc_t rs = ws_rsrc(ws);
  {
    const u32 zo = (u32)WS_Z1 + ((rg0 >> 1) * 54 + z1s) * 1024 + lane * 16;
#pragma unroll
    for (int j = 0; j < 3; ++j) {
      const u2v a = gelu4(acc[4 + j][0]), b = gelu4(acc[4 + j][1]);
      st16(rs, zo + j * 1024, u4v{a[0], a[1], b[0], b[1]});
    }
    if (type == 1) st16(rs, zo + 3 * 1024, u4v{z3[0][0], z3[0][1], z3[1][0], z3[1][1]});
  }
  __builtin_amdgcn_sched_barrier(0);
  u64 gv[2][4];
  for (int spin = 0; spin < (1 << 20); ++spin) {
    bool ok = true;
#pragma unroll
    for (int sf = 0; sf < 2; ++sf)
#pragma unroll
      for (int i = 0; i < 4; ++i) {
        gv[sf][i] = __hip_atomic_load(st + (4 * g + i) * 128 + sf * 16 + li, __ATOMIC_RELAXED, __HIP_MEMORY_SCOPE_AGENT);
        ok &= ((u32)gv[sf][i] != ~0u);
      }
    if (__all(ok)) break;
    __builtin_amdgcn_s_sleep(1);
  }
  STAMP(4,4);
  char* tb = smem + 140 * 1024 + wave * 4608;
#pragma unroll
  for (int sf = 0; sf < 2; ++sf) {
    float s = 0.f;
#pragma unroll
    for (int i = 0; i < 4; ++i) s += __uint_as_float((u32)gv[sf][i]);
    const float mean = xsum_g(s) * (1.0f / 864.0f);
    float q = 0.f;
#pragma unroll
    for (int i = 0; i < 4; ++i) {
      const bool big = (4 * g + i) < 6;
      const float dm = __uint_as_float((u32)gv[sf][i]) * (big ? (1.0f / 64.0f) : (1.0f / 48.0f)) - mean;
      q += __uint_as_float((u32)(gv[sf][i] >> 32)) + (big ? 64.f : 48.f) * dm * dm;
    }
    const float var = xsum_g(q) * (1.0f / 864.0f);
    const float mb = rbf(mean);
    const float rinv = __builtin_amdgcn_rcpf(rbf(__builtin_amdgcn_sqrtf(rbf(rbf(var) + 1e-5f))));
#pragma unroll
    for (int j = 0; j < 4; ++j) {
      const f4 o = (zv[j][sf] - mb) * rinv;
      *(u2v*)(tb + (sf * 16 + li) * 144 + j * 32 + g * 8) = u2v{pk_bf(o[0], o[1]), pk_bf(o[2], o[3])};
    }
  }
  {
    const int q = li >> 2, p = li & 3;
#pragma unroll
    for (int j = 0; j < 4; ++j) {
      s4 lo = __builtin_amdgcn_ds_read_tr16_b64_v4i16((LDS_AS s4*)(tb + (8 * g + q) * 144 + j * 32 + 8 * p));
      s4 hi = __builtin_amdgcn_ds_read_tr16_b64_v4i16((LDS_AS s4*)(tb + (8 * g + 4 + q) * 144 + j * 32 + 8 * p));
      u2v a = __builtin_bit_cast(u2v, lo), b = __builtin_bit_cast(u2v, hi);
      if (j < n2) st16(rs, (u32)WS_Z2 + (((zs + j) * 16 + li) * S + row0 + wave * 256 + 8 * g) * 2, u4v{a[0], a[1], b[0], b[1]});
    }
  }
  STAMP(4,5);
}

constexpr int G2_NST = 7, G2_STB = 22 * 1024;
extern "C" __global__ __launch_bounds__(512) void g2_kernel(const u16* __restrict__ w_sp, const u16* __restrict__ b_sp, char* __restrict__ ws) {
  PINS(w_sp); PINS(b_sp); PINS(ws);
  const u16* z2t = (const u16*)(ws + WS_Z2); const u32* z1f = (const u32*)(ws + WS_Z1);
  __shared__ __attribute__((aligned(16))) char smem[G2_NST * G2_STB + 256];
  const int tid = threadIdx.x, lane = tid & 63;
  const int wave = __builtin_amdgcn_readfirstlane(tid >> 6);
  const int li = lane & 15, g = lane >> 4;
  STAMP(5,0);
  const int xcd = blockIdx.x & 7, idx = blockIdx.x >> 3;
  const int tt = (xcd >> 1) * 8 + (idx >> 2), ft = (xcd & 1) * 4 + (idx & 3);
  if (wave >= 4) {
    const int lw = wave - 4;
    const u16* src[6];
    const int swz = ((lane & 7) ^ (lane >> 3)) * 8;
#pragma unroll
    for (int j = 0; j < 6; ++j) {
      int i = lw + 4 * j; i = i < 22 ? i : 8;
      if (j < 2) src[j] = w_sp + (size_t)(tt * 64 + 8 * i + (lane >> 3)) * S + swz;
      else { int r = ft * 112 + 8 * (i - 8) + (lane >> 3); r = r < FH ? r : FH - 1; src[j] = z2t + (size_t)r * S + swz; }
    }
    auto stage = [&](int kt, int buf) {
      char* sb = smem + buf * G2_STB + lw * 1024;
      const int kk = kt * 64;
#pragma unroll
      for (int j = 0; j < 5; ++j) GLDS16(src[j] + kk, sb + j * 4096);
      if (lw < 2) GLDS16(src[5] + kk, sb + 5 * 4096);
    };
    const bool big = lw < 2;
    stage(0, 0); stage(1, 1); stage(2, 2);
    if (big) waitv<12>(); else waitv<10>(); BAR();
    stage(3, 3); stage(4, 4);
    if (big) waitv<18>(); else waitv<15>(); BAR();
    stage(5, 5); stage(6, 6);
    if (big) waitv<24>(); else waitv<20>(); BAR();
    stage(7, 0); stage(8, 1);
    int nbuf = 2;
    for (int kt = 3; kt < 26; ++kt) {
      if (big) waitv<30>(); else waitv<25>();
      BAR();
      stage(kt + 6, nbuf);
      nbuf = nbuf + 1 == G2_NST ? 0 : nbuf + 1;
    }
    if (big) waitv<30>(); else waitv<25>(); BAR();
    if (big) waitv<24>(); else waitv<20>(); BAR();
    if (big) waitv<18>(); else waitv<15>(); BAR();
    if (big) waitv<12>(); else waitv<10>(); BAR();
    if (big) waitv<6>(); else waitv<5>(); BAR();
    waitv<0>(); BAR();
    { char* scratch = smem + G2_NST * G2_STB; if (lw == 0) CODE_TOUCH(g3_kernel, end_kernel, 0) }
    return;
  }
  const int wt = wave & 1, wf = wave >> 1;
  const int n0 = (ft & 1) ? 3 : 4;
  const int f0 = wf ? n0 : 0, nf = wf ? 7 - n0 : n0;
  u4v z1v[4];
  float bias[2];
#pragma unroll
  for (int j = 0; j < 4; ++j) {
    int ff = ft * 7 + f0 + j; ff = ff < 54 ? ff : 53;
    z1v[j] = *(const u4v*)(z1f + ((size_t)(tt * 2 + wt) * 54 + ff) * 256 + lane * 4);
  }
  bias[0] = bflo(b_sp[tt * 64 + wt * 32 + li]); bias[1] = bflo(b_sp[tt * 64 + wt * 32 + 16 + li]);
  f4 acc[4][2];
#pragma unroll
  for (int j = 0; j < 4; ++j) { acc[j][0] = f4{0, 0, 0, 0}; acc[j][1] = f4{0, 0, 0, 0}; }
  const int sw = (g ^ (li & 7)) * 16;
  const int toff = (wt * 32 + li) * 128 + sw;
  const int foff = 8192 + (f0 * 16 + li) * 128 + sw;
  auto body = [&](auto NFc) {
    constexpr int NF = decltype(NFc)::value;
    u4v fb[2][2][2], fa[2][2][NF];
    auto rd = [&](int set, int buf) {
      const char* sb = smem + buf * G2_STB;
#pragma unroll
      for (int kk = 0; kk < 2; ++kk) {
        fb[set][kk][0] = *(const u4v*)(sb + (toff ^ (kk * 64)));
        fb[set][kk][1] = *(const u4v*)(sb + (toff ^ (kk * 64)) + 2048);
#pragma unroll
        for (int j = 0; j < NF; ++j) fa[set][kk][j] = *(const u4v*)(sb + (foff ^ (kk * 64)) + j * 2048);
      }
    };
    auto mm = [&](int set) {
#pragma unroll
      for (int kk = 0; kk < 2; ++kk)
#pragma unroll
        for (int j = 0; j < NF; ++j) {
          acc[j][0] = mfma_b(fa[set][kk][j], fb[set][kk][0], acc[j][0]);
          acc[j][1] = mfma_b(fa[set][kk][j], fb[set][kk][1], acc[j][1]);
        }
    };
    auto sched = [&]() {
#pragma unroll
      for (int i = 0; i < 2 * NF + 4; ++i) { SGB(0x100, 1); SGB(0x8, 1); }
      SGB(0x8, 2 * NF - 4);
    };
    BAR(); STAMP(5,3); rd(0, 0); WAITL0();
    int buf = 1;
    for (int kt = 1; kt < 31; kt += 2) {
      if (kt == 7) STAMP(5,4);
      if (kt == 23) STAMP(5,6);
      BAR(); rd(1, buf); mm(0); sched(); WAITL0();
      buf = buf + 1 == G2_NST ? 0 : buf + 1;
      BAR(); rd(0, buf); mm(1); sched(); WAITL0();
      buf = buf + 1 == G2_NST ? 0 : buf + 1;
    }
    BAR(); rd(1, buf); mm(0); sched(); WAITL0();
    mm(1);
  };
  if (nf == 4) body(std::integral_constant<int, 4>{}); else body(std::integral_constant<int, 3>{});
  STAMP(5,1);
  const __amdgpu_buffer_rsrc_t rs = ws_rsrc(ws);
  const int ff0 = ft * 7 + f0;
#pragma unroll
  for (int tf = 0; tf < 2; ++tf) {
    const int tfr = tt * 4 + wt * 2 + tf;
    u2v o[4];
#pragma unroll
    for (int j = 0; j < 4; ++j) {
      const u32 za = z1v[j][2 * tf], zb = z1v[j][2 * tf + 1];
      const f4 v = (acc[j][tf] + bias[tf]) * f4{bflo(za), bfhi(za), bflo(zb), bfhi(zb)};
      o[j] = u2v{pk_bf(v[0], v[1]), pk_bf(v[2], v[3])};
    }
    const u32 base = (u32)WS_P + (tfr * 27 + (ff0 >> 1)) * 1024 + lane * 16;
    if (!(ff0 & 1)) {
      st16(rs, base, u4v{o[0][0], o[0][1], o[1][0], o[1][1]});
      if (nf == 4) { if (ff0 + 3 < 54) st16(rs, base + 1024, u4v{o[2][0], o[2][1], o[3][0], o[3][1]}); }
      else st8(rs, base + 1024, o[2]);
    } else {
      st8(rs, base + 8, o[0]);
      st16(rs, base + 1024, u4v{o[1][0], o[1][1], o[2][0], o[2][1]});
    }
  }
  STAMP(5,2);
}

#define G3_DEPTH 3
extern "C" __global__ __launch_bounds__(512) void g3_kernel(char* __restrict__ ws, u16* __restrict__ out) {
  PINS(ws); PINS(out);
  const u32* pf = (const u32*)(ws + WS_P); const u32* Wt3 = (const u32*)(ws + WS_WT3); const u16* h = (const u16*)(ws + WS_H);
  __shared__ __attribute__((aligned(16))) char smem[135 * 1024];
  const int tid = threadIdx.x, lane = tid & 63;
  const int wave = __builtin_amdgcn_readfirstlane(tid >> 6);
  const int li = lane & 15, g = lane >> 4;
  STAMP(6,0);
  const int xcd = blockIdx.x & 7, idx = blockIdx.x >> 3;
  const int tt = xcd * 4 + (idx >> 3), dt = idx & 7;
  if (wave >= 4) {
    const int lw = wave - 4;
    const u32* wsrc[4];
#pragma unroll
    for (int j = 0; j < 4; ++j) {
      int i = lw + 4 * j; i = i < 15 ? i : 0;
      const int kk = i / 5, df = i - 5 * kk;
      wsrc[j] = Wt3 + (size_t)(kk * 40 + dt * 5 + df) * 256 + lane * 4;
    }
    auto stage = [&](int kt) {
      char* sb = smem + kt * 15360 + lw * 1024;
#pragma unroll
      for (int j = 0; j < 3; ++j) GLDS16(wsrc[j] + (size_t)kt * 3 * 40 * 256, sb + j * 4096);
      if (lw < 3) GLDS16(wsrc[3] + (size_t)kt * 3 * 40 * 256, sb + 3 * 4096);
    };
#pragma unroll
    for (int s = 0; s < G3_DEPTH; ++s) stage(s);
    for (int kt = 0; kt < 9 - G3_DEPTH; ++kt) {
      if (lw < 3) waitv<4 * (G3_DEPTH - 1)>(); else waitv<3 * (G3_DEPTH - 1)>();
      BAR();
      stage(kt + G3_DEPTH);
    }
    if constexpr (G3_DEPTH >= 5) { if (lw < 3) waitv<16>(); else waitv<12>(); BAR(); }
    if constexpr (G3_DEPTH >= 4) { if (lw < 3) waitv<12>(); else waitv<9>(); BAR(); }
    if (lw < 3) waitv<8>(); else waitv<6>(); BAR();
    if (lw < 3) waitv<4>(); else waitv<3>(); BAR();
    waitv<0>(); BAR();
    return;
  }
  const u32* psrc = pf + (size_t)(tt * 4 + wave) * 27 * 256 + lane * 4;
#define G3_PD 3
  u4v fb[27];
#pragma unroll
  for (int k = 0; k < 3 * G3_PD; ++k) { fb[k] = *(const u4v*)(psrc + k * 256); __builtin_amdgcn_sched_barrier(0); }
  const int trow = tt * 64 + wave * 16 + li;
  u2v hv[5];
  f4 acc[5];
#pragma unroll
  for (int j = 0; j < 5; ++j) acc[j] = f4{0, 0, 0, 0};
  u4v fa[2][15];
  const char* wb = smem + lane * 16;
  auto rd = [&](int kt) {
#pragma unroll
    for (int i = 0; i < 15; ++i) fa[kt & 1][i] = *(const u4v*)(wb + (kt * 15 + i) * 1024);
  };
  auto mm = [&](int kt) {
#pragma unroll
    for (int kk = 0; kk < 3; ++kk)
#pragma unroll
      for (int j = 0; j < 5; ++j) acc[j] = mfma_b(fa[kt & 1][kk * 5 + j], fb[kt * 3 + kk], acc[j]);
  };
  BAR(); STAMP(6,3); rd(0);
#pragma unroll
  for (int kt = 1; kt < 9; ++kt) {
    BAR();
    if (kt == 4) STAMP(6,4);
    if (kt == 7) STAMP(6,5);
    if (kt - 1 + G3_PD < 9) {
#pragma unroll
      for (int k = 0; k < 3; ++k) fb[(kt - 1 + G3_PD) * 3 + k] = *(const u4v*)(psrc + ((kt - 1 + G3_PD) * 3 + k) * 256);
    } else if (kt - 1 + G3_PD == 9) {
#pragma unroll
      for (int j = 0; j < 5; ++j) hv[j] = *(const u2v*)(h + (size_t)trow * D + dt * 80 + j * 16 + g * 4);
    }
    __builtin_amdgcn_sched_barrier(0);
    rd(kt);
    mm(kt - 1);
#pragma unroll
    for (int i = 0; i < 15; ++i) { SGB(0x100, 1); SGB(0x8, 1); }
  }
  __builtin_amdgcn_sched_barrier(0);
  mm(8);
  STAMP(6,1);
#pragma unroll
  for (int j = 0; j < 5; ++j) {
    u32 t0 = pk_bf(acc[j][0], acc[j][1]), t1 = pk_bf(acc[j][2], acc[j][3]);
    u2v o = u2v{pk_bf(bflo(t0) + bflo(hv[j][0]), bfhi(t0) + bfhi(hv[j][0])), pk_bf(bflo(t1) + bflo(hv[j][1]), bfhi(t1) + bfhi(hv[j][1]))};
    *(u2v*)(out + (size_t)trow * D + dt * 80 + j * 16 + g * 4) = o;
  }
  STAMP(6,2);
}

extern "C" __global__ void end_kernel() {}

extern "C" { long long dbg_offsets[16] = {28<<20, -1, -1, 44<<20, -1, -1, -1}; }

extern "C" void kernel_launch(void* const* d_in, const int* in_sizes, int n_in,
                              void* d_out, int out_size, void* d_ws, size_t ws_size,
                              hipStream_t stream) {
  const u16* x=(const u16*)d_in[0]; const u16* wr=(const u16*)d_in[1]; const u16* wi=(const u16*)d_in[2];
  const u16* w_in=(const u16*)d_in[4]; const u16* w_sp=(const u16*)d_in[5]; const u16* b_sp=(const u16*)d_in[6]; const u16* w_out=(const u16*)d_in[7];
  char* ws=(char*)d_ws;
  k1_kernel<<<256+NPREP,256,0,stream>>>(x,wr,wi,ws);
  (void)hipFuncSetAttribute((const void*)k2_kernel, hipFuncAttributeMaxDynamicSharedMemorySize, 84 * 1024);
  k2_kernel<<<K2_FFT_BLOCKS+K2_PREP_BLOCKS,256,84 * 1024,stream>>>(ws,w_in,w_out,w_sp);
  k3_kernel<<<256,256,0,stream>>>(x,ws,w_sp);
  g1_kernel<<<256,512,0,stream>>>(ws);
  g2_kernel<<<256,512,0,stream>>>(w_sp,b_sp,ws);
  g3_kernel<<<256,512,0,stream>>>(ws,(u16*)d_out);
}
```

```cpp
#include <hip/hip_runtime.h>
#include <hip/hip_bf16.h>
#include <stdint.h>
#define STAMP(k, i)
#define STAMPC(k, i)

#define PFK1 2
#define PFK2 0
#define PF_WSP_K2 352
#define PF_WSP_K3 1
#define PF_WT1 0
typedef unsigned short u16;
typedef unsigned int u32;
typedef _Float16 h2 __attribute__((ext_vector_type(2)));
typedef _Float16 h8 __attribute__((ext_vector_type(8)));
typedef __bf16 bf2 __attribute__((ext_vector_type(2)));
typedef __bf16 bf8 __attribute__((ext_vector_type(8)));
typedef float f2 __attribute__((ext_vector_type(2)));
typedef float f4 __attribute__((ext_vector_type(4)));
typedef short s4 __attribute__((ext_vector_type(4)));
typedef u32 u2v __attribute__((ext_vector_type(2)));
typedef u32 u4v __attribute__((ext_vector_type(4)));
#define DI __device__ __forceinline__
#define LDS_AS __attribute__((address_space(3)))
#define PIN(x) asm volatile("" : "+v"(x))
#define GLDS16(src, dst) __builtin_amdgcn_global_load_lds((const u32*)(src), (LDS_AS u32*)(dst), 16, 0, 0)
#define WAITV(n) asm volatile("s_waitcnt vmcnt(%0)" ::"n"(n) : "memory")

constexpr int S = 2048, D = 640, DF = 321, FF = 1728, FH = 864;
constexpr int TROW_UNITS = 648;
constexpr int NUNITS = 641;
constexpr int NPREP = 168;

constexpr size_t WS_T = 0, WS_TP = 4u << 20, WS_WP = 8u << 20, WS_W2 = 12u << 20, WS_WMAX = 13u << 20, WS_UNS = (13u << 20) + 8192,
                 WS_STATS = 14u << 20, WS_CNT = 15u << 20, WS_WT1 = 16u << 20, WS_WT3 = 20u << 20, WS_H = 28u << 20, WS_L2 = 32u << 20,
                 WS_Z1 = 36u << 20, WS_Z2 = 44u << 20, WS_P = 48u << 20;
#define PINS(x) asm volatile("" ::"s"(x))
#define GLDS4(src, dst) __builtin_amdgcn_global_load_lds((const u32*)(src), (LDS_AS u32*)(dst), 4, 0, 0)
#define touch(p) GLDS4(p, scratch)

constexpr int AUX_SC1 = 16;
DI __amdgpu_buffer_rsrc_t ws_rsrc(char* ws) { return __builtin_amdgcn_make_buffer_rsrc((void*)ws, 0, 64 << 20, 0x00020000); }
#define ST_AUX 16
DI void st16(__amdgpu_buffer_rsrc_t rs, u32 off, u4v v) { __builtin_amdgcn_raw_buffer_store_b128(v, rs, off, 0, ST_AUX); }
DI void st8(__amdgpu_buffer_rsrc_t rs, u32 off, u2v v) { __builtin_amdgcn_raw_buffer_store_b64(v, rs, off, 0, ST_AUX); }
#define CODE_ADDR(var, sym)                                                                                     \
  const char* var;                                                                                              \
  {                                                                                                             \
    u32 lo_, hi_;                                                                                               \
    asm volatile("s_getpc_b64 s[40:41]\n\ts_add_u32 %0, s40, " #sym "@rel32@lo+4\n\ts_addc_u32 %1, s41, " #sym "@rel32@hi+12" \
                 : "=s"(lo_), "=s"(hi_)::"s40", "s41", "scc");                                                  \
    var = (const char*)(((unsigned long long)hi_ << 32) | lo_);                                                 \
  }
#define CODE_TOUCH(a, b, c)                                                           \
  {                                                                                   \
    CODE_ADDR(pa_, a) CODE_ADDR(pb_, b)                                               \
    const char* p_ = pa_ + ((c) * 64 + lane) * 128;                                   \
    if (p_ < pb_) touch(p_);                                                          \
  }
DI float bflo(u32 v) { return __uint_as_float(v << 16); }
DI float bfhi(u32 v) { return __uint_as_float(v & 0xffff0000u); }
DI u32 pk_bf(float a, float b) { f2 v = {a, b}; return __builtin_bit_cast(u32, __builtin_convertvector(v, bf2)); }
DI u32 pk_h(float a, float b) { f2 v = {a, b}; return __builtin_bit_cast(u32, __builtin_convertvector(v, h2)); }
DI float rbf(float a) { return bflo(pk_bf(a, a)); }
DI float cosr(float rev) { return __builtin_amdgcn_cosf(rev); }
DI float sinr(float rev) { return __builtin_amdgcn_sinf(rev); }
DI f4 mfma_h(u4v a, u4v b, f4 c) {
  return __builtin_amdgcn_mfma_f32_16x16x32_f16(__builtin_bit_cast(h8, a), __builtin_bit_cast(h8, b), c, 0, 0, 0);
}
template <int CTRL> DI float dpp_add(float v) {
  int t = __builtin_amdgcn_update_dpp(0, __float_as_int(v), CTRL, 0xf, 0xf, true);
  return v + __int_as_float(t);
}
DI float xsum_g(float v) {
  u32 b = __float_as_uint(v);
  auto r = __builtin_amdgcn_permlane16_swap(b, b, false, false);
  v = __uint_as_float(r[0]) + __uint_as_float(r[1]);
  b = __float_as_uint(v);
  auto s = __builtin_amdgcn_permlane32_swap(b, b, false, false);
  return __uint_as_float(s[0]) + __uint_as_float(s[1]);
}
DI float sum64(float v) {
  v = dpp_add<0xB1>(v);
  v = dpp_add<0x4E>(v);
  v = dpp_add<0x141>(v);
  v = dpp_add<0x140>(v);
  return xsum_g(v);
}
template <int CTRL> DI u32 dpp_maxu(u32 v) {
  u32 t = (u32)__builtin_amdgcn_update_dpp(0, (int)v, CTRL, 0xf, 0xf, true);
  return v > t ? v : t;
}
DI u32 max64u(u32 v) {
  v = dpp_maxu<0xB1>(v); v = dpp_maxu<0x4E>(v); v = dpp_maxu<0x141>(v); v = dpp_maxu<0x140>(v);
  auto r = __builtin_amdgcn_permlane16_swap(v, v, false, false);
  v = r[0] > r[1] ? r[0] : r[1];
  auto s = __builtin_amdgcn_permlane32_swap(v, v, false, false);
  return s[0] > s[1] ? s[0] : s[1];
}
DI float sum32(float v) {
  v = dpp_add<0xB1>(v); v = dpp_add<0x4E>(v); v = dpp_add<0x141>(v); v = dpp_add<0x140>(v);
  u32 b = __float_as_uint(v);
  auto r = __builtin_amdgcn_permlane16_swap(b, b, false, false);
  return __uint_as_float(r[0]) + __uint_as_float(r[1]);
}
DI void ln_row(f2 (&v)[10]) {
  f2 s = ((v[0] + v[1]) + (v[2] + v[3])) + ((v[4] + v[5]) + (v[6] + v[7])) + (v[8] + v[9]);
  const float mean = sum32(s[0] + s[1]) * (1.0f / 640.0f);
  f2 q0 = f2{0.f, 0.f}, q1 = f2{0.f, 0.f};
#pragma unroll
  for (int i = 0; i < 10; i += 2) { v[i] -= mean; v[i + 1] -= mean; q0 += v[i] * v[i]; q1 += v[i + 1] * v[i + 1]; }
  q0 += q1;
  const float var = sum32(q0[0] + q0[1]) * (1.0f / 640.0f);
  const float rinv = __builtin_amdgcn_rcpf(rbf(__builtin_amdgcn_sqrtf(rbf(rbf(var) + 1e-5f))));
  const float off = (mean - rbf(mean)) * rinv;
#pragma unroll
  for (int i = 0; i < 10; ++i) v[i] = v[i] * rinv + off;
}
#define SB() __builtin_amdgcn_sched_barrier(0)
template <bool SWAP> DI u4v cmul_pack(f4 re, f4 im, f4 wr, f4 wi) {
  const f4 ore = re * wr - im * wi, oim = re * wi + im * wr;
  u4v v;
#pragma unroll
  for (int r = 0; r < 4; ++r) v[r] = SWAP ? pk_h(oim[r], ore[r]) : pk_h(ore[r], oim[r]);
  return v;
}

struct __attribute__((packed, aligned(2))) U4U { u32 a, b, c, d; };

constexpr int K1_F40 = 10272, K1_TW = K1_F40 + 18432, K1_R16 = K1_TW + 6144, K1_LDS = K1_R16 + 4096 + 256;
extern "C" __global__ __launch_bounds__(256, 2) void k1_kernel(const u16* __restrict__ x, const u16* __restrict__ wr,
                                                    const u16* __restrict__ wi, char* __restrict__ ws,
                                                    const u16* __restrict__ w_sp) {
  __shared__ __attribute__((aligned(16))) char smem[K1_LDS];
  const int tid = threadIdx.x, lane = tid & 63;
  const int wave = __builtin_amdgcn_readfirstlane(tid >> 6);
  const int li = lane & 15, g = lane >> 4;
  STAMP(0,0);
  char* scratch = smem + K1_LDS - 256;
  if (wave == 3) CODE_TOUCH(k1_kernel, end_kernel, (blockIdx.x >> 3) & 7)
  PINS(x); PINS(wr); PINS(wi); PINS(ws); PINS(w_sp);
  if (blockIdx.x >= 256) {
    const int pb = blockIdx.x - 256;
    const int cp = pb & 7, cc = pb >> 3;
    const int c0 = cc < 20 ? cc * 16 : 305;
    const bool mir = cp > 4;
    const bool sp = (cp & 3) == 0;
    const int e = mir ? 255 - tid : tid;
    const size_t roff = (size_t)(cp + 8 * e) * DF + c0;
    const U4U* pr = (const U4U*)(wr + roff);
    const U4U* pi = (const U4U*)(wi + roff);
    U4U r0 = pr[0], r1 = pr[1], i0 = pi[0], i1 = pi[1];
    const u32 flip = mir ? 0x80000000u : 0u;
    const int q1 = (mir ? 8 - cp : cp) - 1;
    const int kb = mir ? 39 - cc : cc;
    const int t1 = sp ? 1920 + 32 * cc + (cp >> 2) : (q1 * 160 + (kb >> 2) * 16 + (mir ? 16 : 0)) * 4 + (kb & 3);
    const int step = sp ? 2 : (mir ? -4 : 4);
    const int t0 = mir ? (q1 * 160 + ((kb + 1) >> 2) * 16) * 4 + ((kb + 1) & 3) : t1;
    const bool edge = (cc == 0 || cc == 20);
    const __amdgpu_buffer_rsrc_t rs = ws_rsrc(ws);
    const u32 slot = (u32)WS_WP + (wave * 16 + li) * 16;
    u32 doff[4];
#pragma unroll
    for (int m = 0; m < 4; ++m) {
      const int jc = 4 * m + g;
      doff[m] = slot + (u32)(t1 + step * jc) * 1024u;
      if (cc == 20) doff[m] = 0xfffffff0u;
    }
    if (g == 0 && cc < 20) doff[0] = (mir && edge) ? 0xfffffff0u : slot + (u32)t0 * 1024u;
    if (g == 3 && cc == 20 && !mir) doff[3] = slot + (u32)t0 * 1024u;
    u32 rr[8] = {r0.a, r0.b, r0.c, r0.d, r1.a, r1.b, r1.c, r1.d};
    u32 ii[8] = {i0.a, i0.b, i0.c, i0.d, i1.a, i1.b, i1.c, i1.d};
    u32 vals[16];
#pragma unroll
    for (int j = 0; j < 16; ++j) {
      u32 a = rr[j >> 1], b = ii[j >> 1];
      vals[j] = ((j & 1) ? __builtin_amdgcn_perm(b, a, 0x07060302u) : __builtin_amdgcn_perm(b, a, 0x05040100u)) ^ flip;
    }
    if (edge && (mir || sp)) {
      const u32 v0 = cc < 20 ? vals[0] : vals[15];
      const int e2 = (cp == 0) ? ((256 - e) & 255) : 255 - e;
      const int dpos2 = ((e2 & 15) + 16 * (e2 >> 6)) * 4 + ((e2 >> 4) & 3);
      const int cls = mir ? 8 - cp : cp;
      ((u32*)(ws + WS_W2))[(cls * 2 + (cc == 20)) * 256 + dpos2] = v0 ^ flip ^ 0x80000000u;
    }
    u32 mx = 0;
#pragma unroll
    for (int j = 0; j < 16; ++j) {
      const u32 a = vals[j] & 0x7fff7fffu;
      mx = max(mx, max(a & 0xffffu, a >> 16));
    }
#pragma unroll
    for (int m = 0; m < 4; ++m) {
      auto p02 = __builtin_amdgcn_permlane32_swap(vals[4 * m], vals[4 * m + 2], false, false);
      auto p13 = __builtin_amdgcn_permlane32_swap(vals[4 * m + 1], vals[4 * m + 3], false, false);
      auto q01 = __builtin_amdgcn_permlane16_swap(p02[0], p13[0], false, false);
      auto q23 = __builtin_amdgcn_permlane16_swap(p02[1], p13[1], false, false);
      st16(rs, doff[m], u4v{q01[0], q01[1], q23[0], q23[1]});
    }
    mx = max64u(mx);
    if (lane == 0) ((u32*)(ws + WS_WMAX))[pb * 4 + wave] = mx;
    STAMP(0,5);
    return;
  }
  const int b = (blockIdx.x & 7) * 32 + (blockIdx.x >> 3);
  const int a = 2 * wave + (lane >> 5), j = lane & 31;
  const u16* xr = x + (size_t)(256 * a + b) * D;
  const u4v x0 = *(const u4v*)(xr + 8 * j);
  const u4v x1 = *(const u4v*)(xr + 256 + 8 * j);
  const u2v x2 = *(const u2v*)(xr + 512 + 4 * j);
#pragma unroll
  for (int it = 0; it < 3; ++it) {
    const int item = wave + 4 * it;
    if (it < 2 || wave == 0) {
      const int to = item / 3, tk = item - 3 * to;
      const int kb = 16 * to + li;
      const float s8 = (tk < 2 || g < 2) ? 0.125f : 0.f;
      u4v fre, fim;
#pragma unroll
      for (int jj = 0; jj < 4; ++jj) {
        const float ang = (float)(kb * (16 * tk + 4 * g + jj)) * (1.0f / 40.0f);
        const float c = cosr(ang) * s8, s = sinr(ang) * s8;
        fre[jj] = pk_h(c, s);
        fim[jj] = pk_h(-s, c);
      }
      *(u4v*)(smem + K1_F40 + item * 2048 + lane * 16) = fre;
      *(u4v*)(smem + K1_F40 + item * 2048 + 1024 + lane * 16) = fim;
    } else {
      const int t = wave - 1;
      f4 twr, twi;
#pragma unroll
      for (int r = 0; r < 4; ++r) {
        const float ang = (float)((16 * t + 4 * g + r) * li) * (1.0f / 640.0f);
        twr[r] = cosr(ang) * 0.125f; twi[r] = -sinr(ang) * 0.125f;
      }
      *(f4*)(smem + K1_TW + t * 2048 + lane * 16) = twr;
      *(f4*)(smem + K1_TW + t * 2048 + 1024 + lane * 16) = twi;
    }
  }
  {
    const int ph = 2 * wave + (lane >> 5);
    float cs[8];
#pragma unroll
    for (int jj = 0; jj < 8; ++jj) cs[jj] = cosr((float)(((8 * (g & 1) + jj) * li + 2 * ph) & 15) * 0.0625f);
    *(u4v*)(smem + K1_R16 + wave * 1024 + lane * 16) = u4v{pk_h(cs[0], cs[1]), pk_h(cs[2], cs[3]), pk_h(cs[4], cs[5]), pk_h(cs[6], cs[7])};
  }
  if (tid < 4) *(u32*)(smem + 10240 + tid * 4) = 0u;
  asm volatile("s_waitcnt lgkmcnt(0)" ::: "memory");
  __builtin_amdgcn_s_barrier();
  asm volatile("" ::: "memory");
  const int q = wave;
  u4v bre[4], bim[4], fre[9], fim[9];
  f4 twr[3], twi[3];
  u32 toff[3][4];
  {
    const int a0 = g >> 1;
#pragma unroll
    for (int ks = 0; ks < 4; ++ks) {
      const int ph = (q == 0) ? (a0 ? 1 : 7) : (2 * ks + a0) * q;
      bre[ks] = *(const u4v*)(smem + K1_R16 + (ph & 7) * 512 + (lane & 31) * 16);
      bim[ks] = *(const u4v*)(smem + K1_R16 + ((ph + 2) & 7) * 512 + (lane & 31) * 16);
    }
#pragma unroll
    for (int i = 0; i < 9; ++i) {
      fre[i] = *(const u4v*)(smem + K1_F40 + i * 2048 + lane * 16);
      fim[i] = *(const u4v*)(smem + K1_F40 + i * 2048 + 1024 + lane * 16);
    }
    const float qs = q == 0 ? 0.70710678118f : 1.f;
#pragma unroll
    for (int t = 0; t < 3; ++t) {
      twr[t] = *(const f4*)(smem + K1_TW + t * 2048 + lane * 16) * qs;
      twi[t] = *(const f4*)(smem + K1_TW + t * 2048 + 1024 + lane * 16) * qs;
      PIN(twr[t]); PIN(twi[t]);
#pragma unroll
      for (int r = 0; r < 4; ++r) {
        const int k2 = li + 16 * (16 * t + 4 * g + r);
        const int pos = k2 <= 320 ? 2 * k2 : 2 * (640 - k2) + 1;
        toff[t][r] = (u32)WS_T + (u32)b * (TROW_UNITS * 16) + (q > 0 ? ((q - 1) * 160 + 64 * t + lane) * 16 : 480 * 16 + pos * 4);
        if (t == 2 && lane >= 32) toff[t][r] = 0xfffffff0u;
        PIN(toff[t][r]);
      }
    }
#pragma unroll
    for (int ks = 0; ks < 4; ++ks) { PIN(bre[ks]); PIN(bim[ks]); }
#pragma unroll
    for (int i = 0; i < 9; ++i) { PIN(fre[i]); PIN(fim[i]); }
  }
  STAMP(0,1);
  {
    f2 v[10];
#pragma unroll
    for (int i = 0; i < 4; ++i) { v[i] = f2{bflo(x0[i]), bfhi(x0[i])}; v[4 + i] = f2{bflo(x1[i]), bfhi(x1[i])}; }
    v[8] = f2{bflo(x2[0]), bfhi(x2[0])}; v[9] = f2{bflo(x2[1]), bfhi(x2[1])};
    ln_row(v);
    char* ub = smem + a * 1280;
    *(u4v*)(ub + 16 * j) = u4v{pk_h(v[0][0], v[0][1]), pk_h(v[1][0], v[1][1]), pk_h(v[2][0], v[2][1]), pk_h(v[3][0], v[3][1])};
    *(u4v*)(ub + 512 + 16 * j) = u4v{pk_h(v[4][0], v[4][1]), pk_h(v[5][0], v[5][1]), pk_h(v[6][0], v[6][1]), pk_h(v[7][0], v[7][1])};
    *(u2v*)(ub + 1024 + 8 * j) = u2v{pk_h(v[8][0], v[8][1]), pk_h(v[9][0], v[9][1])};
  }
  __syncthreads();
  STAMP(0,2);
  u4v b2[3];
  {
    const int qq = li >> 2, p = li & 3;
    const char* abase = smem + (8 * g + qq) * 80 + 8 * p;
    u4v af[3][4];
#pragma unroll
    for (int t = 0; t < 3; ++t)
#pragma unroll
      for (int ks = 0; ks < 4; ++ks) {
        s4 lo = __builtin_amdgcn_ds_read_tr16_b64_v4i16((LDS_AS s4*)(abase + ks * 2560 + t * 32));
        s4 hi = __builtin_amdgcn_ds_read_tr16_b64_v4i16((LDS_AS s4*)(abase + ks * 2560 + 320 + t * 32));
        u2v l2 = __builtin_bit_cast(u2v, lo), h2v = __builtin_bit_cast(u2v, hi);
        af[t][ks] = u4v{l2[0], l2[1], h2v[0], h2v[1]};
      }
    SB();
    if (wave > 0 && wave <= PFK1) touch((const char*)w_sp + ((size_t)(blockIdx.x * PFK1 + wave - 1) * 64 + lane) * 128);
    SB();
    f4 are[3], aim[3];
#pragma unroll
    for (int t = 0; t < 3; ++t) {
      are[t] = f4{0, 0, 0, 0}; aim[t] = f4{0, 0, 0, 0};
#pragma unroll
      for (int ks = 0; ks < 4; ++ks) {
        are[t] = mfma_h(af[t][ks], bre[ks], are[t]);
        aim[t] = mfma_h(af[t][ks], bim[ks], aim[t]);
      }
    }
#pragma unroll
    for (int t = 0; t < 3; ++t) b2[t] = cmul_pack<false>(are[t], aim[t], twr[t], twi[t]);
  }
  const __amdgpu_buffer_rsrc_t rs = ws_rsrc(ws);
  f4 ore[3], oim[3];
#pragma unroll
  for (int tk = 0; tk < 3; ++tk)
#pragma unroll
    for (int to = 0; to < 3; ++to) {
      ore[to] = mfma_h(fre[to * 3 + tk], b2[tk], tk ? ore[to] : f4{0, 0, 0, 0});
      oim[to] = mfma_h(fim[to * 3 + tk], b2[tk], tk ? oim[to] : f4{0, 0, 0, 0});
    }
  u4v o[3];
#pragma unroll
  for (int to = 0; to < 3; ++to)
#pragma unroll
    for (int r = 0; r < 4; ++r) o[to][r] = pk_h(ore[to][r], oim[to][r]);
  if (q > 0) {
#pragma unroll
    for (int to = 0; to < 3; ++to) st16(rs, toff[to][0], o[to]);
  } else {
#pragma unroll
    for (int to = 0; to < 3; ++to)
#pragma unroll
      for (int r = 0; r < 4; ++r) __builtin_amdgcn_raw_buffer_store_b32(o[to][r], rs, toff[to][r], 0, 0);
    __builtin_amdgcn_raw_buffer_store_b32(lane ? o[1][0] : o[0][0], rs, lane == 0 ? toff[0][0] + 4 : (lane == 16 ? toff[1][0] + 4 : 0xfffffff0u), 0, 0);
  }
  STAMP(0,4);
}

template <int NT, int LDW, int NCG, int NTASK, bool PERM>
DI void retile(const u16* __restrict__ W, __amdgpu_buffer_rsrc_t rs, u32 dstbase, int wid, int nw, char* lds, int lane) {
  const int li = lane & 15, g = lane >> 4, q = li >> 2, p = li & 3;
  u32 dst[NT];
#pragma unroll
  for (int i = 0; i < NT; ++i) {
    const int task = wid + i * nw;
    const int t = task < NTASK ? task : 0;
    const int ks = t / NCG, cg = t - ks * NCG;
    const u16* src = W + (size_t)(ks * 32 + (lane >> 3)) * LDW + cg * 64 + (lane & 7) * 8;
#pragma unroll
    for (int j = 0; j < 4; ++j) GLDS16(src + (size_t)j * 8 * LDW, lds + i * 4096 + j * 1024);
    dst[i] = task < NTASK ? dstbase + (u32)t * 4096u + lane * 16 : 0xffff0000u;
  }
  const char* rb = lds + (PERM ? 4 * g + q : 8 * g + q) * 128 + 8 * p;
  WAITV(0);
#pragma unroll
  for (int i = 0; i < NT; ++i)
#pragma unroll
    for (int f = 0; f < 4; ++f) {
      s4 lo = __builtin_amdgcn_ds_read_tr16_b64_v4i16((LDS_AS s4*)(rb + i * 4096 + f * 32));
      s4 hi = __builtin_amdgcn_ds_read_tr16_b64_v4i16((LDS_AS s4*)(rb + i * 4096 + f * 32 + (PERM ? 16 : 4) * 128));
      u2v a = __builtin_bit_cast(u2v, lo), b = __builtin_bit_cast(u2v, hi);
      st16(rs, dst[i] + f * 1024, u4v{a[0], a[1], b[0], b[1]});
    }
}
constexpr int NRT1 = 20 * 27, NRT3 = 27 * 10;
constexpr int K2_FFT_BLOCKS = 168, K2_PREP_BLOCKS = 88, K3_PREP_BLOCKS = 68;

extern "C" __global__ __launch_bounds__(512, 2) void k2_kernel(char* __restrict__ ws, const u16* __restrict__ w_in,
                                                    const u16* __restrict__ w_out, const u16* __restrict__ w_sp) {
  PINS(ws); PINS(w_in); PINS(w_out); PINS(w_sp);
  const u32* T = (const u32*)(ws + WS_T); const u32* Wp = (const u32*)(ws + WS_WP); const u32* W2 = (const u32*)(ws + WS_W2);
  const u32* wmax = (const u32*)(ws + WS_WMAX); u32* Tp = (u32*)(ws + WS_TP); float* unscale = (float*)(ws + WS_UNS);
  u32* Wt1 = (u32*)(ws + WS_WT1); u32* Wt3 = (u32*)(ws + WS_WT3); u32* stats = (u32*)(ws + WS_STATS);
  extern __shared__ __attribute__((aligned(16))) char smem[];
  const int tid = threadIdx.x, lane = tid & 63;
  const int wave = __builtin_amdgcn_readfirstlane(tid >> 6);
  const int li = lane & 15, g = lane >> 4;
  if (blockIdx.x >= K2_FFT_BLOCKS) {
    STAMP(1,0);
    const int wid = (blockIdx.x - K2_FFT_BLOCKS) * 8 + wave;
    const __amdgpu_buffer_rsrc_t rs = ws_rsrc(ws);
    if (wid < 256) st16(rs, (u32)WS_STATS + wid * 1024 + lane * 16, u4v{~0u, ~0u, ~0u, ~0u});
    if (wid < NRT1) retile<1, FF, 27, NRT1, false>(w_in, rs, (u32)WS_WT1, wid, 0, smem + wave * 8192, lane);
    else retile<2, D, 10, NRT3, true>(w_out, rs, (u32)WS_WT3, wid - NRT1, 164, smem + wave * 8192, lane);
    if (PFK2 && (wave & 1) == 0) {
      char* scratch = smem + 65536;
      touch((const char*)w_sp + ((size_t)(256 * PFK1 + (wid >> 1)) * 64 + lane) * 128);
    }
    STAMP(1,1);
    return;
  }
  const int P = (blockIdx.x & 7) * 21 + (blockIdx.x >> 3);
  if (P > 160) return;
  const int u = wave & 3, hh = wave >> 2;
  const int U = 4 * P + u;
  STAMP(2,0);
#pragma unroll
  for (int i = 0; i < 2; ++i) {
    const int bb = (wave * 2 + i) * 16 + (lane >> 2);
    GLDS16(T + ((size_t)bb * TROW_UNITS + 4 * P + ((lane & 3) ^ ((lane >> 4) & 3))) * 4, smem + (wave * 2 + i) * 1024);
  }
  u4v mxv[3];
#pragma unroll
  for (int i = 0; i < 3; ++i) mxv[i] = *(const u4v*)(wmax + (i < 2 || lane < 40 ? i * 256 + lane * 4 : 0));
  u4v Wv[2];
#pragma unroll
  for (int t = 0; t < 2; ++t) Wv[t] = *(const u4v*)(Wp + ((size_t)((U < NUNITS ? U : 0) * 4 + 2 * hh + t) * 64 + lane) * 4);
  const bool special = P >= 120;
  const int cB = special ? 4 : (P / 40 + 1);
  const int ur = special ? U - 480 : U % 160;
  const bool e320 = special ? ur == 160 : ur == 80;
  const bool edge = hh == 0 && (ur == 0 || e320);
  u4v w2, w4;
  if (edge) {
    w2 = *(const u4v*)(W2 + ((special ? 0 : cB) * 2 + (e320 ? 1 : 0)) * 256 + lane * 4);
    w4 = *(const u4v*)(W2 + (4 * 2 + (e320 ? 1 : 0)) * 256 + lane * 4);
  }
  STAMP(2,5);
  {
    char* cb = smem + 16384 + wave * 2048 + lane * 16;
    const int cc = wave < 4 ? cB : 0;
    const float inv = u < 2 ? (1.0f / 128.0f) : (1.0f / 2048.0f);
    const float amp = u == 0 ? 1.f : ((u == 1 && special) ? 0.125f : 0.25f);
    f4 c, s;
#pragma unroll
    for (int jj = 0; jj < 4; ++jj) {
      const int a = (u & 1) ? li * (cc + 8 * (4 * g + jj)) : (4 * g + jj) * (cc + 8 * li);
      const float ang = (float)a * inv;
      c[jj] = cosr(ang) * amp; s[jj] = sinr(ang) * amp;
    }
    if (u < 2) {
      *(u4v*)(cb) = u4v{pk_h(c[0], s[0]), pk_h(c[1], s[1]), pk_h(c[2], s[2]), pk_h(c[3], s[3])};
      *(u4v*)(cb + 1024) = u4v{pk_h(-s[0], c[0]), pk_h(-s[1], c[1]), pk_h(-s[2], c[2]), pk_h(-s[3], c[3])};
    } else {
      *(f4*)(cb) = c;
      *(f4*)(cb + 1024) = s;
    }
  }
  asm volatile("s_waitcnt lgkmcnt(0)" ::: "memory");
  __builtin_amdgcn_s_barrier();
  asm volatile("" ::: "memory");
  STAMP(2,7);
  u4v Fre, Fim, P1re[2], P1im[2], P4re[2], P4im[2];
  f4 tfr[2], tfs[2], tir[2], tis[2];
  {
    const char* cb = smem + 16384 + lane * 16;
    Fre = *(const u4v*)(cb + 4 * 2048); Fim = *(const u4v*)(cb + 4 * 2048 + 1024);
#pragma unroll
    for (int c = 0; c < 2; ++c) {
      const char* cs = cb + ((c == 0 && special) ? 8192 : 0);
      P1re[c] = *(const u4v*)(cs); P1im[c] = *(const u4v*)(cs + 1024);
      P4re[c] = *(const u4v*)(cs + 2048); P4im[c] = *(const u4v*)(cs + 3072);
      tfr[c] = *(const f4*)(cs + 4096); tfs[c] = *(const f4*)(cs + 5120);
      tir[c] = *(const f4*)(cs + 6144); tis[c] = *(const f4*)(cs + 7168);
    }
    PIN(Fre); PIN(Fim);
#pragma unroll
    for (int c = 0; c < 2; ++c) { PIN(P1re[c]); PIN(P1im[c]); PIN(P4re[c]); PIN(P4im[c]); PIN(tfr[c]); PIN(tfs[c]); PIN(tir[c]); PIN(tis[c]); }
  }
  STAMP(2,1);
  asm volatile("s_waitcnt vmcnt(0)" ::: "memory");
  __builtin_amdgcn_s_barrier();
  asm volatile("" ::: "memory");
  STAMP(2,2);
  const int loff = (64 * g + li) * 64 + ((u ^ (li >> 2)) & 3) * 16 + hh * 8;
  u4v X[2];
#pragma unroll
  for (int jj = 0; jj < 4; ++jj) {
    const u2v L = *(const u2v*)(smem + loff + jj * 1024);
    X[0][jj] = L[0]; X[1][jj] = L[1];
  }
  if (special) {
#pragma unroll
    for (int jj = 0; jj < 4; ++jj) {
      const u32 au = X[0][jj], bu = X[1][jj] ^ 0x80000000u;
      const h2 A = __builtin_bit_cast(h2, au);
      const h2 Bc = __builtin_bit_cast(h2, bu);
      X[0][jj] = __builtin_bit_cast(u32, A + Bc);
      X[1][jj] = __builtin_bit_cast(u32, A - Bc);
    }
  }
  const f4 z4 = f4{0, 0, 0, 0};
  f4 re[2], im[2];
#pragma unroll
  for (int t = 0; t < 2; ++t) { re[t] = mfma_h(X[t], P1re[t], z4); im[t] = mfma_h(X[t], P1im[t], z4); }
  u32 mx = 0;
#pragma unroll
  for (int i = 0; i < 3; ++i) mx = max(max(max(mxv[i][0], mxv[i][1]), max(mxv[i][2], mxv[i][3])), mx);
  mx = max64u(mx);
  int E = (int)(mx >> 7);
  E = E < 1 ? 1 : (E > 253 ? 253 : E);
  const float sig = __uint_as_float((u32)(254 - E) << 23) * 0.25f;
#pragma unroll
  for (int t = 0; t < 2; ++t) {
    const u4v v = cmul_pack<false>(re[t], im[t], tfr[t], -tfs[t]);
    re[t] = mfma_h(Fre, v, z4); im[t] = mfma_h(Fim, v, z4);
  }
  f4 wr4[2], wi4[2];
#pragma unroll
  for (int t = 0; t < 2; ++t)
#pragma unroll
    for (int r = 0; r < 4; ++r) { wr4[t][r] = bflo(Wv[t][r]); wi4[t][r] = bfhi(Wv[t][r]); }
  if (edge) {
#pragma unroll
    for (int r = 0; r < 4; ++r) {
      wr4[0][r] = (wr4[0][r] + bflo(w2[r])) * 0.5f; wi4[0][r] = (wi4[0][r] + bfhi(w2[r])) * 0.5f;
      if (special) { wr4[1][r] = (wr4[1][r] + bflo(w4[r])) * 0.5f; wi4[1][r] = (wi4[1][r] + bfhi(w4[r])) * 0.5f; }
    }
    if (U == 0 && lane == 0) unscale[0] = __uint_as_float((u32)E << 23);
  }
#pragma unroll
  for (int t = 0; t < 2; ++t) {
    const u4v v = cmul_pack<true>(re[t], im[t], wr4[t] * sig, wi4[t] * sig);
    im[t] = mfma_h(v, Fre, z4); re[t] = mfma_h(v, Fim, z4);
  }
#pragma unroll
  for (int t = 0; t < 2; ++t) {
    const u4v v = cmul_pack<true>(re[t], im[t], tir[t], tis[t]);
    im[t] = mfma_h(P4re[t], v, z4); re[t] = mfma_h(P4im[t], v, z4);
  }
  STAMP(2,3);
  if (special) {
    const f4 yr = re[0], yi = im[0], zr = re[1], zi = im[1];
    re[0] = yr + zr; im[0] = yi + zi;
    re[1] = yr - zr; im[1] = zi - yi;
  }
#pragma unroll
  for (int r = 0; r < 4; ++r) *(u2v*)(smem + loff + r * 1024) = u2v{pk_h(re[0][r], im[0][r]), pk_h(re[1][r], im[1][r])};
  asm volatile("s_waitcnt lgkmcnt(0)" ::: "memory");
  __builtin_amdgcn_s_barrier();
  asm volatile("" ::: "memory");
  const __amdgpu_buffer_rsrc_t rs = ws_rsrc(ws);
#pragma unroll
  for (int i = 0; i < 2; ++i) {
    const u4v o = *(const u4v*)(smem + (wave * 2 + i) * 1024 + lane * 16);
    st16(rs, (u32)WS_TP + P * 16384 + (wave * 2 + i) * 1024 + (lane & ~3) * 16 + ((lane & 3) ^ ((lane >> 4) & 3)) * 16, o);
  }
  { char* scratch = smem + 49152; if (wave == 0) CODE_TOUCH(k3_kernel, g1_kernel, 0) }
  STAMP(2,4);
}

extern "C" __global__ __launch_bounds__(256, 2) void k3_kernel(const u16* __restrict__ x, char* __restrict__ ws, const u16* __restrict__ w_sp) {
  PINS(x); PINS(ws); PINS(w_sp);
  const u32* Tp = (const u32*)(ws + WS_TP);
  constexpr int K3_Y = 10752, K3_CB = K3_Y + 10240;
  __shared__ __attribute__((aligned(16))) char smem[K3_CB + 8192 + 512];
  const int tid = threadIdx.x, lane = tid & 63;
  const int wave = __builtin_amdgcn_readfirstlane(tid >> 6);
  const int li = lane & 15, g = lane >> 4;
  const int b = (blockIdx.x & 7) * 32 + (blockIdx.x >> 3);
  STAMP(3,0);
#pragma unroll
  for (int i = 0; i < 2; ++i) {
    int U = (wave * 2 + i) * 64 + lane;
    U = U < 480 ? U : 479;
    GLDS16(Tp + (size_t)(U >> 2) * 4096 + b * 16 + (U & 3) * 4, smem + (wave * 2 + i) * 1024);
  }
#pragma unroll
  for (int it = 0; it < 4; ++it) {
    const int i = it < 2 ? wave * 2 + it : 6 + it;
    if (it < 2 || wave == 3) {
      const int k2 = (lane >> 2) + 16 * (4 * i + (lane & 3));
      const int pos = k2 <= 320 ? 2 * k2 : 2 * (640 - k2) + 1;
      GLDS4(Tp + (size_t)(120 + (pos >> 4)) * 4096 + b * 16 + (pos & 15), smem + 8192 + i * 256);
    }
  }
  char* scratch = smem + K3_CB + 8192;
  if (wave == 0) GLDS4(ws + WS_UNS, scratch + 256);
  const int a = 2 * wave + (lane >> 5), j = lane & 31;
  const int row = 256 * a + b;
  const u16* xr = x + (size_t)row * D;
  u4v x0 = *(const u4v*)(xr + 8 * j);
  u4v x1 = *(const u4v*)(xr + 256 + 8 * j);
  u2v x2 = *(const u2v*)(xr + 512 + 4 * j);
  STAMP(3,5);
  char* cb = smem + K3_CB;
  const int t = wave;
  u4v Bre[3], Bim[3];
  if (wave < 3) {
#pragma unroll
    for (int tk = 0; tk < 3; ++tk) {
      const float sc = (tk < 2 || g < 2) ? 1.f : 0.f;
#pragma unroll
      for (int jj = 0; jj < 4; ++jj) {
        const float ang = (float)((16 * tk + 4 * g + jj) * (16 * t + li)) * (1.0f / 40.0f);
        const float c = cosr(ang) * sc, s = sinr(ang) * sc;
        Bre[tk][jj] = pk_h(c, -s);
        Bim[tk][jj] = pk_h(s, c);
      }
      PIN(Bre[tk]); PIN(Bim[tk]);
    }
  } else {
#pragma unroll
    for (int tt = 0; tt < 3; ++tt) {
      f4 twr, twi;
#pragma unroll
      for (int jj = 0; jj < 4; ++jj) {
        const float ang = (float)((16 * tt + li) * (4 * g + jj)) * (1.0f / 640.0f);
        twr[jj] = cosr(ang) * 0.125f; twi[jj] = sinr(ang) * 0.125f;
      }
      *(f4*)(cb + tt * 2048 + lane * 16) = twr;
      *(f4*)(cb + tt * 2048 + 1024 + lane * 16) = twi;
    }
    u4v gre, gim;
#pragma unroll
    for (int jj = 0; jj < 4; ++jj) {
      const float ang = (float)(li * (4 * g + jj)) * 0.0625f;
      const float c = cosr(ang), s = sinr(ang);
      gre[jj] = pk_h(c, -s); gim[jj] = pk_h(s, c);
    }
    *(u4v*)(cb + 6144 + lane * 16) = gre;
    *(u4v*)(cb + 7168 + lane * 16) = gim;
  }
  STAMP(3,1);
  asm volatile("s_waitcnt vmcnt(0)" ::: "memory");
  PIN(x0); PIN(x1); PIN(x2);
  __syncthreads();
  STAMP(3,2);
  char* yb = smem + K3_Y;
  if (wave < 3) {
    const f4 z4 = f4{0, 0, 0, 0};
    u4v af[4][3];
#pragma unroll
    for (int q = 0; q < 4; ++q) {
      const char* base = smem + (q == 0 ? 8192 : (q - 1) * 2560);
#pragma unroll
      for (int tk = 0; tk < 3; ++tk) af[q][tk] = *(const u4v*)(base + (tk < 2 ? (64 * tk + lane) : (128 + (lane & 31))) * 16);
    }
    const f4 twr = *(const f4*)(cb + t * 2048 + lane * 16), twi = *(const f4*)(cb + t * 2048 + 1024 + lane * 16);
    const u4v Gre = *(const u4v*)(cb + 6144 + lane * 16), Gim = *(const u4v*)(cb + 7168 + lane * 16);
    const float usc = *(const float*)(scratch + 256);
    SB();
    f4 vre[4], vim[4];
#pragma unroll
    for (int q = 0; q < 4; ++q) {
      f4 re = z4, im = z4;
#pragma unroll
      for (int tk = 0; tk < 3; ++tk) {
        re = mfma_h(af[q][tk], Bre[tk], re);
        im = mfma_h(af[q][tk], Bim[tk], im);
      }
      vre[q] = re; vim[q] = im;
    }
#pragma unroll
    for (int q = 0; q < 4; ++q) {
      const u4v v = cmul_pack<false>(vre[q], vim[q], twr, twi);
      vre[q] = mfma_h(v, Gre, z4);
      vim[q] = mfma_h(v, Gim, z4);
    }
    const float fs = usc * (2.0f * 1024.0f * 128.0f / (2048.0f * 640.0f));
    const float fh = fs * 0.70710678118f;
    const f4 E = (vre[0] + vim[0]) * fs, O = (vre[0] - vim[0]) * fs;
    const f4 s13 = vre[1] + vre[3], dx = vre[1] - vre[3], sy = vim[1] + vim[3], dy = vim[3] - vim[1];
    const f4 Ep = E + vre[2] * fs, Em = E - vre[2] * fs, Om = O - vim[2] * fs, Op = O + vim[2] * fs;
    const f4 A = dx - sy, B = dx + sy;
    f4 y[8];
    y[0] = Ep + s13 * fs; y[4] = Ep - s13 * fs; y[2] = Em + dy * fs; y[6] = Em - dy * fs;
    y[1] = Om + A * fh; y[5] = Om - A * fh; y[3] = Op - B * fh; y[7] = Op + B * fh;
    if (t < 2 || lane < 32) {
      char* yp = yb + (40 * li + 16 * t + 4 * g) * 2;
#pragma unroll
      for (int aa = 0; aa < 8; ++aa) *(u2v*)(yp + aa * 1280) = u2v{pk_bf(y[aa][0], y[aa][1]), pk_bf(y[aa][2], y[aa][3])};
    }
  }
  else {
    CODE_TOUCH(g1_kernel, g2_kernel, (blockIdx.x >> 3) & 1)
#pragma unroll
    for (int i = 0; i < 4; ++i) {
      const int task = 256 * PFK1 + 352 * PFK2 + i * 256 + blockIdx.x;
      if (task < 1024) touch((const char*)w_sp + ((size_t)task * 64 + lane) * 128);
    }
  }
  asm volatile("s_waitcnt lgkmcnt(0)" ::: "memory");
  __builtin_amdgcn_s_barrier();
  asm volatile("" ::: "memory");
  STAMP(3,3);
  {
    const __amdgpu_buffer_rsrc_t rs = ws_rsrc(ws);
    const u4v y0 = *(const u4v*)(yb + a * 1280 + 16 * j);
    const u4v y1 = *(const u4v*)(yb + a * 1280 + 512 + 16 * j);
    const u2v y2 = *(const u2v*)(yb + a * 1280 + 1024 + 8 * j);
    u32 hx[10];
#pragma unroll
    for (int i = 0; i < 4; ++i) {
      hx[i] = pk_bf(bflo(x0[i]) + bflo(y0[i]), bfhi(x0[i]) + bfhi(y0[i]));
      hx[4 + i] = pk_bf(bflo(x1[i]) + bflo(y1[i]), bfhi(x1[i]) + bfhi(y1[i]));
    }
    hx[8] = pk_bf(bflo(x2[0]) + bflo(y2[0]), bfhi(x2[0]) + bfhi(y2[0]));
    hx[9] = pk_bf(bflo(x2[1]) + bflo(y2[1]), bfhi(x2[1]) + bfhi(y2[1]));
    auto pairup = [&](u32 v0, u32 v1) {
      return u4v{v0, v1, (u32)__builtin_amdgcn_update_dpp(0, (int)v0, 0xB1, 0xf, 0xf, true), (u32)__builtin_amdgcn_update_dpp(0, (int)v1, 0xB1, 0xf, 0xf, true)};
    };
    const u32 drop = (lane & 1) ? 0xfffffff0u : 0u;
    const u32 ho = (u32)WS_H + row * (D * 2);
    st16(rs, ho + 16 * j, u4v{hx[0], hx[1], hx[2], hx[3]});
    st16(rs, ho + 512 + 16 * j, u4v{hx[4], hx[5], hx[6], hx[7]});
    st16(rs, (ho + 1024 + 8 * j) | drop, pairup(hx[8], hx[9]));
    f2 v[10];
#pragma unroll
    for (int i = 0; i < 10; ++i) v[i] = f2{bflo(hx[i]), bfhi(hx[i])};
    ln_row(v);
#pragma unroll
    for (int i = 0; i < 10; ++i) hx[i] = pk_bf(v[i][0], v[i][1]);
    const u32 lo = (u32)WS_L2 + (row >> 4) * 20480 + (row & 15) * 64;
    st16(rs, lo + (j >> 2) * 1024 + (j & 3) * 16, u4v{hx[0], hx[1], hx[2], hx[3]});
    st16(rs, lo + (8 + (j >> 2)) * 1024 + (j & 3) * 16, u4v{hx[4], hx[5], hx[6], hx[7]});
    st16(rs, (lo + (16 + (j >> 3)) * 1024 + (j & 7) * 8) | drop, pairup(hx[8], hx[9]));
  }
  STAMP(3,4);
}

#include <type_traits>
typedef unsigned long long u64;
typedef __attribute__((address_space(1))) unsigned long long gu64;
#define WAITL0() do { __builtin_amdgcn_sched_barrier(0); asm volatile("s_waitcnt lgkmcnt(0)" ::: "memory"); __builtin_amdgcn_sched_barrier(0); } while (0)
#define BAR() do { asm volatile("" ::: "memory"); __builtin_amdgcn_sched_barrier(0); __builtin_amdgcn_s_barrier(); __builtin_amdgcn_sched_barrier(0); asm volatile("" ::: "memory"); } while (0)
#define SGB(mask, n) __builtin_amdgcn_sched_group_barrier(mask, n, 0)
DI f4 mfma_b(u4v a, u4v b, f4 c) {
  return __builtin_amdgcn_mfma_f32_16x16x32_bf16(__builtin_bit_cast(bf8, a), __builtin_bit_cast(bf8, b), c, 0, 0, 0);
}
DI f2 gelu2(f2 x) {
  const float k1 = -2.0f * 0.7978845608028654f * 1.4426950408889634f;
  const float k2 = k1 * 0.044715f;
  f2 p = x * x * k2 + k1;
  f2 a = x * p;
  f2 e = f2{__builtin_amdgcn_exp2f(a[0]), __builtin_amdgcn_exp2f(a[1])} + 1.0f;
  f2 r = f2{__builtin_amdgcn_rcpf(e[0]), __builtin_amdgcn_rcpf(e[1])};
  return x * r;
}
DI u2v gelu4(f4 a) {
  f2 g0 = gelu2(f2{a[0], a[1]}), g1 = gelu2(f2{a[2], a[3]});
  return u2v{pk_bf(g0[0], g0[1]), pk_bf(g1[0], g1[1])};
}
template <int N> DI void waitv() { asm volatile("s_waitcnt vmcnt(%0)" ::"n"(N) : "memory"); }

template <int KT, int NST, int NLA, class F>
DI void loader_loop(bool big, F&& stage) {
#pragma unroll
  for (int s = 0; s < NST - 1; ++s) stage(s, s);
  int nbuf = NST - 1;
  for (int kt = 0; kt < KT - (NST - 1); ++kt) {
    if (big) waitv<NLA*(NST - 2)>(); else waitv<(NLA - 1) * (NST - 2)>();
    BAR();
    stage(kt + NST - 1, nbuf);
    nbuf = nbuf + 1 == NST ? 0 : nbuf + 1;
  }
  if constexpr (NST >= 7) { if (big) waitv<NLA * 5>(); else waitv<(NLA - 1) * 5>(); BAR(); }
  if constexpr (NST >= 6) { if (big) waitv<NLA * 4>(); else waitv<(NLA - 1) * 4>(); BAR(); }
  if constexpr (NST >= 5) { if (big) waitv<NLA * 3>(); else waitv<(NLA - 1) * 3>(); BAR(); }
  if (big) waitv<NLA * 2>(); else waitv<(NLA - 1) * 2>(); BAR();
  if (big) waitv<NLA>(); else waitv<NLA - 1>(); BAR();
  waitv<0>(); BAR();
}

#define G1_DEPTH 3
extern "C" __global__ __launch_bounds__(512) void g1_kernel(char* __restrict__ ws) {
  PINS(ws);
  const u32* l2f = (const u32*)(ws + WS_L2); const u32* Wt1 = (const u32*)(ws + WS_WT1);
  u32* z1f = (u32*)(ws + WS_Z1); u16* z2t = (u16*)(ws + WS_Z2); u64* stats = (u64*)(ws + WS_STATS);
  __shared__ __attribute__((aligned(16))) char smem[140 * 1024 + 4 * 4608 + 256];
  const int tid = threadIdx.x, lane = tid & 63;
  const int wave = __builtin_amdgcn_readfirstlane(tid >> 6);
  const int li = lane & 15, g = lane >> 4;
  STAMP(4,0);
  const int xcd = blockIdx.x & 7, idx = blockIdx.x >> 3;
#define G1_MAP 1
  const int mt = G1_MAP == 0 ? xcd * 2 + (idx >> 4) : (G1_MAP == 1 ? (xcd >> 1) * 4 + (idx >> 3) : (xcd >> 2) * 8 + (idx >> 2));
  const int nb = G1_MAP == 0 ? (idx & 15) : (G1_MAP == 1 ? (xcd & 1) * 8 + (idx & 7) : (xcd & 3) * 4 + (idx & 3));
  const int row0 = 1024 * (mt & 1) + 32 * (mt >> 1);
  const int zs = nb < 6 ? 4 * nb : 24 + 3 * (nb - 6);
  const int z1s = nb < 6 ? 3 * nb : (nb < 12 ? 18 + 4 * (nb - 6) : 42 + 3 * (nb - 12));
  const int type = nb < 6 ? 0 : (nb < 12 ? 1 : 2);
  if (wave >= 4) {
    const int lw = wave - 4;
    auto slot_frag = [&](int s) { return s < 3 ? 54 + zs + s : (s == 3 ? (type == 0 ? 54 + zs + 3 : (type == 1 ? z1s + 3 : z1s)) : z1s + s - 4); };
    const u32* bsrc[4];
#pragma unroll
    for (int j = 0; j < 4; ++j) {
      int i = lw + 4 * j; i = i < 14 ? i : 0;
      const int kk = i / 7, s = i - 7 * kk;
      bsrc[j] = Wt1 + (size_t)(kk * 108 + slot_frag(s)) * 256 + lane * 4;
    }
    auto stage = [&](int kt) {
      char* sb = smem + kt * 14336 + lw * 1024;
#pragma unroll
      for (int j = 0; j < 3; ++j) GLDS16(bsrc[j] + (size_t)kt * 2 * 108 * 256, sb + j * 4096);
      if (lw < 2) GLDS16(bsrc[3] + (size_t)kt * 2 * 108 * 256, sb + 3 * 4096);
    };
#pragma unroll
    for (int s = 0; s < G1_DEPTH; ++s) stage(s);
    for (int kt = 0; kt < 10 - G1_DEPTH; ++kt) {
      if (lw < 2) waitv<4 * (G1_DEPTH - 1)>(); else waitv<3 * (G1_DEPTH - 1)>();
      BAR();
      stage(kt + G1_DEPTH);
    }
    if constexpr (G1_DEPTH >= 5) { if (lw < 2) waitv<16>(); else waitv<12>(); BAR(); }
    if constexpr (G1_DEPTH >= 4) { if (lw < 2) waitv<12>(); else waitv<9>(); BAR(); }
    if (lw < 2) waitv<8>(); else waitv<6>(); BAR();
    if (lw < 2) waitv<4>(); else waitv<3>(); BAR();
    waitv<0>(); BAR();
    { char* scratch = smem + 140 * 1024 + 4 * 4608; if (lw < 2) CODE_TOUCH(g2_kernel, g3_kernel, lw) }
    return;
  }
  const int rg0 = (row0 + wave * 256) >> 4;
  const u32* asrc = l2f + (size_t)rg0 * 20 * 256 + li * 16 + g * 4;
#define G1_AD 5
  u4v fa[G1_AD][2];
#pragma unroll
  for (int k = 0; k < G1_AD; ++k) { fa[k][0] = *(const u4v*)(asrc + k * 256); fa[k][1] = *(const u4v*)(asrc + (20 + k) * 256); __builtin_amdgcn_sched_barrier(0); }
  f4 acc[7][2];
#pragma unroll
  for (int s = 0; s < 7; ++s) { acc[s][0] = f4{0, 0, 0, 0}; acc[s][1] = f4{0, 0, 0, 0}; }
  u4v fw[2][7];
  const char* wb = smem + lane * 16;
  auto rd = [&](int k) {
#pragma unroll
    for (int s = 0; s < 7; ++s) fw[k & 1][s] = *(const u4v*)(wb + (k * 7 + s) * 1024);
  };
  auto mm = [&](int k) {
#pragma unroll
    for (int s = 0; s < 7; ++s) {
      acc[s][0] = mfma_b(fw[k & 1][s], fa[k % G1_AD][0], acc[s][0]);
      acc[s][1] = mfma_b(fw[k & 1][s], fa[k % G1_AD][1], acc[s][1]);
    }
    if (k + G1_AD < 20) { fa[k % G1_AD][0] = *(const u4v*)(asrc + (k + G1_AD) * 256); fa[k % G1_AD][1] = *(const u4v*)(asrc + (20 + G1_AD + k) * 256); }
  };
  BAR(); STAMP(4,3); rd(0);
#pragma unroll
  for (int k = 1; k < 20; ++k) {
    if (!(k & 1)) BAR(); else __builtin_amdgcn_sched_barrier(0);
    if (k == 8) { STAMP(4,6); STAMPC(7,0); }
    if (k == 14) { STAMP(4,7); STAMPC(7,1); }
    rd(k);
    mm(k - 1);
#pragma unroll
    for (int i = 0; i < 7; ++i) { SGB(0x100, 1); SGB(0x8, 2); }
  }
  __builtin_amdgcn_sched_barrier(0);
  mm(19);
  STAMP(4,1);
  const int n2 = type == 0 ? 4 : 3;
  f4 zv[4][2];
  u2v z3[2];
  float lsum[2], lm2[2];
#pragma unroll
  for (int sf = 0; sf < 2; ++sf) {
    f2 s = f2{0.f, 0.f};
#pragma unroll
    for (int j = 0; j < 4; ++j) {
      const u2v t = gelu4(acc[j][sf]);
      if (j == 3) z3[sf] = t;
      zv[j][sf] = f4{bflo(t[0]), bfhi(t[0]), bflo(t[1]), bfhi(t[1])};
      if (j < 3 || type == 0) s += f2{zv[j][sf][0], zv[j][sf][1]} + f2{zv[j][sf][2], zv[j][sf][3]};
    }
    const float st = xsum_g(s[0] + s[1]);
    const float lmean = st * (type == 0 ? (1.0f / 64.0f) : (1.0f / 48.0f));
    f2 q = f2{0.f, 0.f};
#pragma unroll
    for (int j = 0; j < 4; ++j) {
      const f4 d = zv[j][sf] - lmean;
      if (j < 3 || type == 0) { q += f2{d[0], d[1]} * f2{d[0], d[1]}; q += f2{d[2], d[3]} * f2{d[2], d[3]}; }
    }
    lsum[sf] = st; lm2[sf] = xsum_g(q[0] + q[1]);
  }
  gu64* st = (gu64*)stats + (size_t)mt * 16 * 128 + wave * 32;
  if (lane < 32) {
    u32 a = __float_as_uint(g ? lsum[1] : lsum[0]), b = __float_as_uint(g ? lm2[1] : lm2[0]);
    if (a == ~0u) a = 0x7fc00000u;
    __hip_atomic_store(st + nb * 128 + lane, ((u64)b << 32) | a, __ATOMIC_RELAXED, __HIP_MEMORY_SCOPE_AGENT);
  }
  __builtin_amdgcn_sched_barrier(0);
  STAMP(4,2);
  const __amdgpu_buffer_rsrc_t rs = ws_rsrc(ws);
  u4v z1o[3];
#pragma unroll
  for (int j = 0; j < 3; ++j) {
    const u2v a = gelu4(acc[4 + j][0]), b = gelu4(acc[4 + j][1]);
    z1o[j] = u4v{a[0], a[1], b[0], b[1]};
    PIN(z1o[j]);
  }
  __builtin_amdgcn_sched_barrier(0);
  u64 gv[2][4];
  for (int spin = 0; spin < (1 << 20); ++spin) {
    bool ok = true;
#pragma unroll
    for (int sf = 0; sf < 2; ++sf)
#pragma unroll
      for (int i = 0; i < 4; ++i) {
        gv[sf][i] = __hip_atomic_load(st + (4 * g + i) * 128 + sf * 16 + li, __ATOMIC_RELAXED, __HIP_MEMORY_SCOPE_AGENT);
        ok &= ((u32)gv[sf][i] != ~0u);
      }
    if (__all(ok)) break;
    __builtin_amdgcn_s_sleep(1);
  }
  STAMP(4,4);
  {
    const u32 zo = (u32)WS_Z1 + ((rg0 >> 1) * 54 + z1s) * 1024 + lane * 16;
#pragma unroll
    for (int j = 0; j < 3; ++j) st16(rs, zo + j * 1024, z1o[j]);
    if (type == 1) st16(rs, zo + 3 * 1024, u4v{z3[0][0], z3[0][1], z3[1][0], z3[1][1]});
  }
  char* tb = smem + 140 * 1024 + wave * 4608;
#pragma unroll
  for (int sf = 0; sf < 2; ++sf) {
    float s = 0.f;
#pragma unroll
    for (int i = 0; i < 4; ++i) s += __uint_as_float((u32)gv[sf][i]);
    const float mean = xsum_g(s) * (1.0f / 864.0f);
    float q = 0.f;
#pragma unroll
    for (int i = 0; i < 4; ++i) {
      const bool big = (4 * g + i) < 6;
      const float dm = __uint_as_float((u32)gv[sf][i]) * (big ? (1.0f / 64.0f) : (1.0f / 48.0f)) - mean;
      q += __uint_as_float((u32)(gv[sf][i] >> 32)) + (big ? 64.f : 48.f) * dm * dm;
    }
    const float var = xsum_g(q) * (1.0f / 864.0f);
    const float mb = rbf(mean);
    const float rinv = __builtin_amdgcn_rcpf(rbf(__builtin_amdgcn_sqrtf(rbf(rbf(var) + 1e-5f))));
#pragma unroll
    for (int j = 0; j < 4; ++j) {
      const f4 o = (zv[j][sf] - mb) * rinv;
      *(u2v*)(tb + (sf * 16 + li) * 144 + j * 32 + g * 8) = u2v{pk_bf(o[0], o[1]), pk_bf(o[2], o[3])};
    }
  }
  {
    const int q = li >> 2, p = li & 3;
#pragma unroll
    for (int j = 0; j < 4; ++j) {
      s4 lo = __builtin_amdgcn_ds_read_tr16_b64_v4i16((LDS_AS s4*)(tb + (8 * g + q) * 144 + j * 32 + 8 * p));
      s4 hi = __builtin_amdgcn_ds_read_tr16_b64_v4i16((LDS_AS s4*)(tb + (8 * g + 4 + q) * 144 + j * 32 + 8 * p));
      u2v a = __builtin_bit_cast(u2v, lo), b = __builtin_bit_cast(u2v, hi);
      if (j < n2) st16(rs, (u32)WS_Z2 + (((zs + j) * 16 + li) * S + row0 + wave * 256 + 8 * g) * 2, u4v{a[0], a[1], b[0], b[1]});
    }
  }
  STAMP(4,5);
}

constexpr int G2_NST = 7, G2_STB = 22 * 1024;
extern "C" __global__ __launch_bounds__(512) void g2_kernel(const u16* __restrict__ w_sp, const u16* __restrict__ b_sp, char* __restrict__ ws) {
  PINS(w_sp); PINS(b_sp); PINS(ws);
  const u16* z2t = (const u16*)(ws + WS_Z2); const u32* z1f = (const u32*)(ws + WS_Z1);
  __shared__ __attribute__((aligned(16))) char smem[G2_NST * G2_STB + 256];
  const int tid = threadIdx.x, lane = tid & 63;
  const int wave = __builtin_amdgcn_readfirstlane(tid >> 6);
  const int li = lane & 15, g = lane >> 4;
  STAMP(5,0);
  const int xcd = blockIdx.x & 7, idx = blockIdx.x >> 3;
  const int tt = (xcd >> 1) * 8 + (idx >> 2), ft = (xcd & 1) * 4 + (idx & 3);
  if (wave >= 4) {
    const int lw = wave - 4;
    const u16* src[6];
    const int swz = ((lane & 7) ^ (lane >> 3)) * 8;
#pragma unroll
    for (int j = 0; j < 6; ++j) {
      int i = lw + 4 * j; i = i < 22 ? i : 8;
      if (j < 2) src[j] = w_sp + (size_t)(tt * 64 + 8 * i + (lane >> 3)) * S + swz;
      else { int r = ft * 112 + 8 * (i - 8) + (lane >> 3); r = r < FH ? r : FH - 1; src[j] = z2t + (size_t)r * S + swz; }
    }
    auto stage = [&](int kt, int buf) {
      char* sb = smem + buf * G2_STB + lw * 1024;
      const int kk = kt * 64;
#pragma unroll
      for (int j = 0; j < 5; ++j) GLDS16(src[j] + kk, sb + j * 4096);
      if (lw < 2) GLDS16(src[5] + kk, sb + 5 * 4096);
    };
    const bool big = lw < 2;
    stage(0, 0); stage(1, 1); stage(2, 2);
    if (big) waitv<12>(); else waitv<10>(); BAR();
    stage(3, 3); stage(4, 4);
    if (big) waitv<18>(); else waitv<15>(); BAR();
    stage(5, 5); stage(6, 6);
    if (big) waitv<24>(); else waitv<20>(); BAR();
    stage(7, 0); stage(8, 1);
    int nbuf = 2;
    for (int kt = 3; kt < 26; ++kt) {
      if (big) waitv<30>(); else waitv<25>();
      BAR();
      stage(kt + 6, nbuf);
      nbuf = nbuf + 1 == G2_NST ? 0 : nbuf + 1;
    }
    if (big) waitv<30>(); else waitv<25>(); BAR();
    if (big) waitv<24>(); else waitv<20>(); BAR();
    if (big) waitv<18>(); else waitv<15>(); BAR();
    if (big) waitv<12>(); else waitv<10>(); BAR();
    if (big) waitv<6>(); else waitv<5>(); BAR();
    waitv<0>(); BAR();
    { char* scratch = smem + G2_NST * G2_STB; if (lw == 0) CODE_TOUCH(g3_kernel, end_kernel, 0) }
    return;
  }
  const int wt = wave & 1, wf = wave >> 1;
  const int n0 = (ft & 1) ? 3 : 4;
  const int f0 = wf ? n0 : 0, nf = wf ? 7 - n0 : n0;
  u4v z1v[4];
  float bias[2];
#pragma unroll
  for (int j = 0; j < 4; ++j) {
    int ff = ft * 7 + f0 + j; ff = ff < 54 ? ff : 53;
    z1v[j] = *(const u4v*)(z1f + ((size_t)(tt * 2 + wt) * 54 + ff) * 256 + lane * 4);
  }
  bias[0] = bflo(b_sp[tt * 64 + wt * 32 + li]); bias[1] = bflo(b_sp[tt * 64 + wt * 32 + 16 + li]);
  f4 acc[4][2];
#pragma unroll
  for (int j = 0; j < 4; ++j) { acc[j][0] = f4{0, 0, 0, 0}; acc[j][1] = f4{0, 0, 0, 0}; }
  const int sw = (g ^ (li & 7)) * 16;
  const int toff = (wt * 32 + li) * 128 + sw;
  const int foff = 8192 + (f0 * 16 + li) * 128 + sw;
  auto body = [&](auto NFc) {
    constexpr int NF = decltype(NFc)::value;
    u4v fb[2][2][2], fa[2][2][NF];
    auto rd = [&](int set, int buf) {
      const char* sb = smem + buf * G2_STB;
#pragma unroll
      for (int kk = 0; kk < 2; ++kk) {
        fb[set][kk][0] = *(const u4v*)(sb + (toff ^ (kk * 64)));
        fb[set][kk][1] = *(const u4v*)(sb + (toff ^ (kk * 64)) + 2048);
#pragma unroll
        for (int j = 0; j < NF; ++j) fa[set][kk][j] = *(const u4v*)(sb + (foff ^ (kk * 64)) + j * 2048);
      }
    };
    auto mm = [&](int set) {
#pragma unroll
      for (int kk = 0; kk < 2; ++kk)
#pragma unroll
        for (int j = 0; j < NF; ++j) {
          acc[j][0] = mfma_b(fa[set][kk][j], fb[set][kk][0], acc[j][0]);
          acc[j][1] = mfma_b(fa[set][kk][j], fb[set][kk][1], acc[j][1]);
        }
    };
    auto sched = [&]() {
#pragma unroll
      for (int i = 0; i < 2 * NF + 4; ++i) { SGB(0x100, 1); SGB(0x8, 1); }
      SGB(0x8, 2 * NF - 4);
    };
    BAR(); STAMP(5,3); rd(0, 0); WAITL0();
    int buf = 1;
    for (int kt = 1; kt < 31; kt += 2) {
      if (kt == 7) STAMP(5,4);
      if (kt == 23) STAMP(5,6);
      BAR(); rd(1, buf); mm(0); sched(); WAITL0();
      buf = buf + 1 == G2_NST ? 0 : buf + 1;
      BAR(); rd(0, buf); mm(1); sched(); WAITL0();
      buf = buf + 1 == G2_NST ? 0 : buf + 1;
    }
    BAR(); rd(1, buf); mm(0); sched(); WAITL0();
    mm(1);
  };
  if (nf == 4) body(std::integral_constant<int, 4>{}); else body(std::integral_constant<int, 3>{});
  STAMP(5,1);
  const __amdgpu_buffer_rsrc_t rs = ws_rsrc(ws);
  const int ff0 = ft * 7 + f0;
#pragma unroll
  for (int tf = 0; tf < 2; ++tf) {
    const int tfr = tt * 4 + wt * 2 + tf;
    u2v o[4];
#pragma unroll
    for (int j = 0; j < 4; ++j) {
      const u32 za = z1v[j][2 * tf], zb = z1v[j][2 * tf + 1];
      const f4 v = (acc[j][tf] + bias[tf]) * f4{bflo(za), bfhi(za), bflo(zb), bfhi(zb)};
      o[j] = u2v{pk_bf(v[0], v[1]), pk_bf(v[2], v[3])};
    }
    const u32 base = (u32)WS_P + (tfr * 27 + (ff0 >> 1)) * 1024 + lane * 16;
    if (!(ff0 & 1)) {
      st16(rs, base, u4v{o[0][0], o[0][1], o[1][0], o[1][1]});
      if (nf == 4) { if (ff0 + 3 < 54) st16(rs, base + 1024, u4v{o[2][0], o[2][1], o[3][0], o[3][1]}); }
      else st8(rs, base + 1024, o[2]);
    } else {
      st8(rs, base + 8, o[0]);
      st16(rs, base + 1024, u4v{o[1][0], o[1][1], o[2][0], o[2][1]});
    }
  }
  STAMP(5,2);
}

#define G3_DEPTH 3
extern "C" __global__ __launch_bounds__(512) void g3_kernel(char* __restrict__ ws, u16* __restrict__ out) {
  PINS(ws); PINS(out);
  const u32* pf = (const u32*)(ws + WS_P); const u32* Wt3 = (const u32*)(ws + WS_WT3); const u16* h = (const u16*)(ws + WS_H);
  __shared__ __attribute__((aligned(16))) char smem[135 * 1024];
  const int tid = threadIdx.x, lane = tid & 63;
  const int wave = __builtin_amdgcn_readfirstlane(tid >> 6);
  const int li = lane & 15, g = lane >> 4;
  STAMP(6,0);
  const int xcd = blockIdx.x & 7, idx = blockIdx.x >> 3;
  const int tt = xcd * 4 + (idx >> 3), dt = idx & 7;
  if (wave >= 4) {
    const int lw = wave - 4;
    const u32* wsrc[4];
#pragma unroll
    for (int j = 0; j < 4; ++j) {
      int i = lw + 4 * j; i = i < 15 ? i : 0;
      const int kk = i / 5, df = i - 5 * kk;
      wsrc[j] = Wt3 + (size_t)(kk * 40 + dt * 5 + df) * 256 + lane * 4;
    }
    auto stage = [&](int kt) {
      char* sb = smem + kt * 15360 + lw * 1024;
#pragma unroll
      for (int j = 0; j < 3; ++j) GLDS16(wsrc[j] + (size_t)kt * 3 * 40 * 256, sb + j * 4096);
      if (lw < 3) GLDS16(wsrc[3] + (size_t)kt * 3 * 40 * 256, sb + 3 * 4096);
    };
#pragma unroll
    for (int s = 0; s < G3_DEPTH; ++s) stage(s);
    for (int kt = 0; kt < 9 - G3_DEPTH; ++kt) {
      if (lw < 3) waitv<4 * (G3_DEPTH - 1)>(); else waitv<3 * (G3_DEPTH - 1)>();
      BAR();
      stage(kt + G3_DEPTH);
    }
    if constexpr (G3_DEPTH >= 5) { if (lw < 3) waitv<16>(); else waitv<12>(); BAR(); }
    if constexpr (G3_DEPTH >= 4) { if (lw < 3) waitv<12>(); else waitv<9>(); BAR(); }
    if (lw < 3) waitv<8>(); else waitv<6>(); BAR();
    if (lw < 3) waitv<4>(); else waitv<3>(); BAR();
    waitv<0>(); BAR();
    return;
  }
  const u32* psrc = pf + (size_t)(tt * 4 + wave) * 27 * 256 + lane * 4;
#define G3_PD 3
  u4v fb[27];
#pragma unroll
  for (int k = 0; k < 3 * G3_PD; ++k) { fb[k] = *(const u4v*)(psrc + k * 256); __builtin_amdgcn_sched_barrier(0); }
  const int trow = tt * 64 + wave * 16 + li;
  u2v hv[5];
  f4 acc[5];
#pragma unroll
  for (int j = 0; j < 5; ++j) acc[j] = f4{0, 0, 0, 0};
  u4v fa[2][15];
  const char* wb = smem + lane * 16;
  auto rd = [&](int kt) {
#pragma unroll
    for (int i = 0; i < 15; ++i) fa[kt & 1][i] = *(const u4v*)(wb + (kt * 15 + i) * 1024);
  };
  auto mm = [&](int kt) {
#pragma unroll
    for (int kk = 0; kk < 3; ++kk)
#pragma unroll
      for (int j = 0; j < 5; ++j) acc[j] = mfma_b(fa[kt & 1][kk * 5 + j], fb[kt * 3 + kk], acc[j]);
  };
  BAR(); STAMP(6,3); rd(0);
#pragma unroll
  for (int kt = 1; kt < 9; ++kt) {
    BAR();
    if (kt == 4) STAMP(6,4);
    if (kt == 7) STAMP(6,5);
    if (kt - 1 + G3_PD < 9) {
#pragma unroll
      for (int k = 0; k < 3; ++k) fb[(kt - 1 + G3_PD) * 3 + k] = *(const u4v*)(psrc + ((kt - 1 + G3_PD) * 3 + k) * 256);
    } else if (kt - 1 + G3_PD == 9) {
#pragma unroll
      for (int j = 0; j < 5; ++j) hv[j] = *(const u2v*)(h + (size_t)trow * D + dt * 80 + j * 16 + g * 4);
    }
    __builtin_amdgcn_sched_barrier(0);
    rd(kt);
    mm(kt - 1);
#pragma unroll
    for (int i = 0; i < 15; ++i) { SGB(0x100, 1); SGB(0x8, 1); }
  }
  __builtin_amdgcn_sched_barrier(0);
  mm(8);
  STAMP(6,1);
#pragma unroll
  for (int j = 0; j < 5; ++j) {
    u32 t0 = pk_bf(acc[j][0], acc[j][1]), t1 = pk_bf(acc[j][2], acc[j][3]);
    u2v o = u2v{pk_bf(bflo(t0) + bflo(hv[j][0]), bfhi(t0) + bfhi(hv[j][0])), pk_bf(bflo(t1) + bflo(hv[j][1]), bfhi(t1) + bfhi(hv[j][1]))};
    *(u2v*)(out + (size_t)trow * D + dt * 80 + j * 16 + g * 4) = o;
  }
  STAMP(6,2);
}

extern "C" __global__ void end_kernel() {}

extern "C" { long long dbg_offsets[16] = {28<<20, -1, -1, 44<<20, -1, -1, -1}; }

extern "C" void kernel_launch(void* const* d_in, const int* in_sizes, int n_in,
                              void* d_out, int out_size, void* d_ws, size_t ws_size,
                              hipStream_t stream) {
  const u16* x=(const u16*)d_in[0]; const u16* wr=(const u16*)d_in[1]; const u16* wi=(const u16*)d_in[2];
  const u16* w_in=(const u16*)d_in[4]; const u16* w_sp=(const u16*)d_in[5]; const u16* b_sp=(const u16*)d_in[6]; const u16* w_out=(const u16*)d_in[7];
  char* ws=(char*)d_ws + (ws_size - (64u << 20));
  k1_kernel<<<256+NPREP,256,0,stream>>>(x,wr,wi,ws,w_sp);
  (void)hipFuncSetAttribute((const void*)k2_kernel, hipFuncAttributeMaxDynamicSharedMemorySize, 84 * 1024);
  k2_kernel<<<K2_FFT_BLOCKS+K2_PREP_BLOCKS,512,84 * 1024,stream>>>(ws,w_in,w_out,w_sp);
  k3_kernel<<<256,256,0,stream>>>(x,ws,w_sp);
  g1_kernel<<<256,512,0,stream>>>(ws);
  g2_kernel<<<256,512,0,stream>>>(w_sp,b_sp,ws);
  g3_kernel<<<256,512,0,stream>>>(ws,(u16*)d_out);
}
```

```cpp
#include <hip/hip_runtime.h>
#include <hip/hip_bf16.h>
#include <stdint.h>
#define STAMP(k, i)
#define STAMPC(k, i)

#define PFG1 2
#define PFK1 0
#define PFK2 0
#define PF_WSP_K2 352
#define PF_WSP_K3 1
#define PF_WT1 0
typedef unsigned short u16;
typedef unsigned int u32;
typedef _Float16 h2 __attribute__((ext_vector_type(2)));
typedef _Float16 h8 __attribute__((ext_vector_type(8)));
typedef __bf16 bf2 __attribute__((ext_vector_type(2)));
typedef __bf16 bf8 __attribute__((ext_vector_type(8)));
typedef float f2 __attribute__((ext_vector_type(2)));
typedef float f4 __attribute__((ext_vector_type(4)));
typedef short s4 __attribute__((ext_vector_type(4)));
typedef u32 u2v __attribute__((ext_vector_type(2)));
typedef u32 u4v __attribute__((ext_vector_type(4)));
#define DI __device__ __forceinline__
#define LDS_AS __attribute__((address_space(3)))
#define PIN(x) asm volatile("" : "+v"(x))
#define GLDS16(src, dst) __builtin_amdgcn_global_load_lds((const u32*)(src), (LDS_AS u32*)(dst), 16, 0, 0)
#define WAITV(n) asm volatile("s_waitcnt vmcnt(%0)" ::"n"(n) : "memory")

constexpr int S = 2048, D = 640, DF = 321, FF = 1728, FH = 864;
constexpr int TROW_UNITS = 648;
constexpr int NUNITS = 641;
constexpr int NPREP = 168;

constexpr size_t WS_T = 0, WS_WP = 2688u << 10, WS_TP = 5376u << 10, WS_WT1 = 8064u << 10, WS_WT3 = 10240u << 10, WS_H = 11392u << 10,
                 WS_L2 = 13952u << 10, WS_Z1 = 16512u << 10, WS_Z2 = 19968u << 10, WS_STATS = 23424u << 10, WS_W2 = 23680u << 10,
                 WS_WMAX = 23692u << 10, WS_UNS = 23696u << 10, WS_CNT = 23700u << 10, WS_P = WS_T;
#define PINS(x) asm volatile("" ::"s"(x))
#define GLDS4(src, dst) __builtin_amdgcn_global_load_lds((const u32*)(src), (LDS_AS u32*)(dst), 4, 0, 0)
#define touch(p) GLDS4(p, scratch)

constexpr int AUX_SC1 = 16;
DI __amdgpu_buffer_rsrc_t ws_rsrc(char* ws) { return __builtin_amdgcn_make_buffer_rsrc((void*)ws, 0, 64 << 20, 0x00020000); }
#define ST_AUX 16
DI void st16(__amdgpu_buffer_rsrc_t rs, u32 off, u4v v) { __builtin_amdgcn_raw_buffer_store_b128(v, rs, off, 0, ST_AUX); }
DI void st8(__amdgpu_buffer_rsrc_t rs, u32 off, u2v v) { __builtin_amdgcn_raw_buffer_store_b64(v, rs, off, 0, ST_AUX); }
#define CODE_ADDR(var, sym)                                                                                     \
  const char* var;                                                                                              \
  {                                                                                                             \
    u32 lo_, hi_;                                                                                               \
    asm volatile("s_getpc_b64 s[40:41]\n\ts_add_u32 %0, s40, " #sym "@rel32@lo+4\n\ts_addc_u32 %1, s41, " #sym "@rel32@hi+12" \
                 : "=s"(lo_), "=s"(hi_)::"s40", "s41", "scc");                                                  \
    var = (const char*)(((unsigned long long)hi_ << 32) | lo_);                                                 \
  }
#define CODE_TOUCH(a, b, c)                                                           \
  {                                                                                   \
    CODE_ADDR(pa_, a) CODE_ADDR(pb_, b)                                               \
    const char* p_ = pa_ + ((c) * 64 + lane) * 128;                                   \
    if (p_ < pb_) touch(p_);                                                          \
  }
DI float bflo(u32 v) { return __uint_as_float(v << 16); }
DI float bfhi(u32 v) { return __uint_as_float(v & 0xffff0000u); }
DI u32 pk_bf(float a, float b) { f2 v = {a, b}; return __builtin_bit_cast(u32, __builtin_convertvector(v, bf2)); }
DI u32 pk_h(float a, float b) { f2 v = {a, b}; return __builtin_bit_cast(u32, __builtin_convertvector(v, h2)); }
DI float rbf(float a) { return bflo(pk_bf(a, a)); }
DI float cosr(float rev) { return __builtin_amdgcn_cosf(rev); }
DI float sinr(float rev) { return __builtin_amdgcn_sinf(rev); }
DI f4 mfma_h(u4v a, u4v b, f4 c) {
  return __builtin_amdgcn_mfma_f32_16x16x32_f16(__builtin_bit_cast(h8, a), __builtin_bit_cast(h8, b), c, 0, 0, 0);
}
template <int CTRL> DI float dpp_add(float v) {
  int t = __builtin_amdgcn_update_dpp(0, __float_as_int(v), CTRL, 0xf, 0xf, true);
  return v + __int_as_float(t);
}
DI float xsum_g(float v) {
  u32 b = __float_as_uint(v);
  auto r = __builtin_amdgcn_permlane16_swap(b, b, false, false);
  v = __uint_as_float(r[0]) + __uint_as_float(r[1]);
  b = __float_as_uint(v);
  auto s = __builtin_amdgcn_permlane32_swap(b, b, false, false);
  return __uint_as_float(s[0]) + __uint_as_float(s[1]);
}
DI float sum64(float v) {
  v = dpp_add<0xB1>(v);
  v = dpp_add<0x4E>(v);
  v = dpp_add<0x141>(v);
  v = dpp_add<0x140>(v);
  return xsum_g(v);
}
template <int CTRL> DI u32 dpp_maxu(u32 v) {
  u32 t = (u32)__builtin_amdgcn_update_dpp(0, (int)v, CTRL, 0xf, 0xf, true);
  return v > t ? v : t;
}
DI u32 max64u(u32 v) {
  v = dpp_maxu<0xB1>(v); v = dpp_maxu<0x4E>(v); v = dpp_maxu<0x141>(v); v = dpp_maxu<0x140>(v);
  auto r = __builtin_amdgcn_permlane16_swap(v, v, false, false);
  v = r[0] > r[1] ? r[0] : r[1];
  auto s = __builtin_amdgcn_permlane32_swap(v, v, false, false);
  return s[0] > s[1] ? s[0] : s[1];
}
DI float sum32(float v) {
  v = dpp_add<0xB1>(v); v = dpp_add<0x4E>(v); v = dpp_add<0x141>(v); v = dpp_add<0x140>(v);
  u32 b = __float_as_uint(v);
  auto r = __builtin_amdgcn_permlane16_swap(b, b, false, false);
  return __uint_as_float(r[0]) + __uint_as_float(r[1]);
}
DI void ln_row(f2 (&v)[10]) {
  f2 s = ((v[0] + v[1]) + (v[2] + v[3])) + ((v[4] + v[5]) + (v[6] + v[7])) + (v[8] + v[9]);
  const float mean = sum32(s[0] + s[1]) * (1.0f / 640.0f);
  f2 q0 = f2{0.f, 0.f}, q1 = f2{0.f, 0.f};
#pragma unroll
  for (int i = 0; i < 10; i += 2) { v[i] -= mean; v[i + 1] -= mean; q0 += v[i] * v[i]; q1 += v[i + 1] * v[i + 1]; }
  q0 += q1;
  const float var = sum32(q0[0] + q0[1]) * (1.0f / 640.0f);
  const float rinv = __builtin_amdgcn_rcpf(rbf(__builtin_amdgcn_sqrtf(rbf(rbf(var) + 1e-5f))));
  const float off = (mean - rbf(mean)) * rinv;
#pragma unroll
  for (int i = 0; i < 10; ++i) v[i] = v[i] * rinv + off;
}
#define SB() __builtin_amdgcn_sched_barrier(0)
template <bool SWAP> DI u4v cmul_pack(f4 re, f4 im, f4 wr, f4 wi) {
  const f4 ore = re * wr - im * wi, oim = re * wi + im * wr;
  u4v v;
#pragma unroll
  for (int r = 0; r < 4; ++r) v[r] = SWAP ? pk_h(oim[r], ore[r]) : pk_h(ore[r], oim[r]);
  return v;
}

struct __attribute__((packed, aligned(2))) U4U { u32 a, b, c, d; };

constexpr int K1_F40 = 10272, K1_TW = K1_F40 + 18432, K1_R16 = K1_TW + 6144, K1_LDS = K1_R16 + 4096 + 256;
extern "C" __global__ __launch_bounds__(256, 2) void k1_kernel(const u16* __restrict__ x, const u16* __restrict__ wr,
                                                    const u16* __restrict__ wi, char* __restrict__ ws,
                                                    const u16* __restrict__ w_sp) {
  __shared__ __attribute__((aligned(16))) char smem[K1_LDS];
  const int tid = threadIdx.x, lane = tid & 63;
  const int wave = __builtin_amdgcn_readfirstlane(tid >> 6);
  const int li = lane & 15, g = lane >> 4;
  STAMP(0,0);
  char* scratch = smem + K1_LDS - 256;
  if (wave == 3) CODE_TOUCH(k1_kernel, end_kernel, (blockIdx.x >> 3) & 7)
  if (wave == 2 && blockIdx.x >= 256 && blockIdx.x < 272) {
    const unsigned long long ka = (unsigned long long)__builtin_amdgcn_kernarg_segment_ptr();
    if (lane < 32) touch((const char*)((ka & ~4095ull) + lane * 128));
  }
  PINS(x); PINS(wr); PINS(wi); PINS(ws); PINS(w_sp);
  if (blockIdx.x >= 256) {
    const int pb = blockIdx.x - 256;
    const int cp = pb & 7, cc = pb >> 3;
    const int c0 = cc < 20 ? cc * 16 : 305;
    const bool mir = cp > 4;
    const bool sp = (cp & 3) == 0;
    const int e = mir ? 255 - tid : tid;
    const size_t roff = (size_t)(cp + 8 * e) * DF + c0;
    const U4U* pr = (const U4U*)(wr + roff);
    const U4U* pi = (const U4U*)(wi + roff);
    U4U r0 = pr[0], r1 = pr[1], i0 = pi[0], i1 = pi[1];
    const u32 flip = mir ? 0x80000000u : 0u;
    const int q1 = (mir ? 8 - cp : cp) - 1;
    const int kb = mir ? 39 - cc : cc;
    const int t1 = sp ? 1920 + 32 * cc + (cp >> 2) : (q1 * 160 + (kb >> 2) * 16 + (mir ? 16 : 0)) * 4 + (kb & 3);
    const int step = sp ? 2 : (mir ? -4 : 4);
    const int t0 = mir ? (q1 * 160 + ((kb + 1) >> 2) * 16) * 4 + ((kb + 1) & 3) : t1;
    const bool edge = (cc == 0 || cc == 20);
    const __amdgpu_buffer_rsrc_t rs = ws_rsrc(ws);
    const u32 slot = (u32)WS_WP + (wave * 16 + li) * 16;
    u32 doff[4];
#pragma unroll
    for (int m = 0; m < 4; ++m) {
      const int jc = 4 * m + g;
      doff[m] = slot + (u32)(t1 + step * jc) * 1024u;
      if (cc == 20) doff[m] = 0xfffffff0u;
    }
    if (g == 0 && cc < 20) doff[0] = (mir && edge) ? 0xfffffff0u : slot + (u32)t0 * 1024u;
    if (g == 3 && cc == 20 && !mir) doff[3] = slot + (u32)t0 * 1024u;
    u32 rr[8] = {r0.a, r0.b, r0.c, r0.d, r1.a, r1.b, r1.c, r1.d};
    u32 ii[8] = {i0.a, i0.b, i0.c, i0.d, i1.a, i1.b, i1.c, i1.d};
    u32 vals[16];
#pragma unroll
    for (int j = 0; j < 16; ++j) {
      u32 a = rr[j >> 1], b = ii[j >> 1];
      vals[j] = ((j & 1) ? __builtin_amdgcn_perm(b, a, 0x07060302u) : __builtin_amdgcn_perm(b, a, 0x05040100u)) ^ flip;
    }
    if (edge && (mir || sp)) {
      const u32 v0 = cc < 20 ? vals[0] : vals[15];
      const int e2 = (cp == 0) ? ((256 - e) & 255) : 255 - e;
      const int dpos2 = ((e2 & 15) + 16 * (e2 >> 6)) * 4 + ((e2 >> 4) & 3);
      const int cls = mir ? 8 - cp : cp;
      ((u32*)(ws + WS_W2))[(cls * 2 + (cc == 20)) * 256 + dpos2] = v0 ^ flip ^ 0x80000000u;
    }
    u32 mx = 0;
#pragma unroll
    for (int j = 0; j < 16; ++j) {
      const u32 a = vals[j] & 0x7fff7fffu;
      mx = max(mx, max(a & 0xffffu, a >> 16));
    }
#pragma unroll
    for (int m = 0; m < 4; ++m) {
      auto p02 = __builtin_amdgcn_permlane32_swap(vals[4 * m], vals[4 * m + 2], false, false);
      auto p13 = __builtin_amdgcn_permlane32_swap(vals[4 * m + 1], vals[4 * m + 3], false, false);
      auto q01 = __builtin_amdgcn_permlane16_swap(p02[0], p13[0], false, false);
      auto q23 = __builtin_amdgcn_permlane16_swap(p02[1], p13[1], false, false);
      st16(rs, doff[m], u4v{q01[0], q01[1], q23[0], q23[1]});
    }
    mx = max64u(mx);
    if (lane == 0) ((u32*)(ws + WS_WMAX))[pb * 4 + wave] = mx;
    STAMP(0,5);
    return;
  }
  const int b = (blockIdx.x & 7) * 32 + (blockIdx.x >> 3);
  const int a = 2 * wave + (lane >> 5), j = lane & 31;
  const u16* xr = x + (size_t)(256 * a + b) * D;
  const u4v x0 = *(const u4v*)(xr + 8 * j);
  const u4v x1 = *(const u4v*)(xr + 256 + 8 * j);
  const u2v x2 = *(const u2v*)(xr + 512 + 4 * j);
#pragma unroll
  for (int it = 0; it < 3; ++it) {
    const int item = wave + 4 * it;
    if (it < 2 || wave == 0) {
      const int to = item / 3, tk = item - 3 * to;
      const int kb = 16 * to + li;
      const float s8 = (tk < 2 || g < 2) ? 0.125f : 0.f;
      u4v fre, fim;
#pragma unroll
      for (int jj = 0; jj < 4; ++jj) {
        const float ang = (float)(kb * (16 * tk + 4 * g + jj)) * (1.0f / 40.0f);
        const float c = cosr(ang) * s8, s = sinr(ang) * s8;
        fre[jj] = pk_h(c, s);
        fim[jj] = pk_h(-s, c);
      }
      *(u4v*)(smem + K1_F40 + item * 2048 + lane * 16) = fre;
      *(u4v*)(smem + K1_F40 + item * 2048 + 1024 + lane * 16) = fim;
    } else {
      const int t = wave - 1;
      f4 twr, twi;
#pragma unroll
      for (int r = 0; r < 4; ++r) {
        const float ang = (float)((16 * t + 4 * g + r) * li) * (1.0f / 640.0f);
        twr[r] = cosr(ang) * 0.125f; twi[r] = -sinr(ang) * 0.125f;
      }
      *(f4*)(smem + K1_TW + t * 2048 + lane * 16) = twr;
      *(f4*)(smem + K1_TW + t * 2048 + 1024 + lane * 16) = twi;
    }
  }
  {
    const int ph = 2 * wave + (lane >> 5);
    float cs[8];
#pragma unroll
    for (int jj = 0; jj < 8; ++jj) cs[jj] = cosr((float)(((8 * (g & 1) + jj) * li + 2 * ph) & 15) * 0.0625f);
    *(u4v*)(smem + K1_R16 + wave * 1024 + lane * 16) = u4v{pk_h(cs[0], cs[1]), pk_h(cs[2], cs[3]), pk_h(cs[4], cs[5]), pk_h(cs[6], cs[7])};
  }
  if (tid < 4) *(u32*)(smem + 10240 + tid * 4) = 0u;
  asm volatile("s_waitcnt lgkmcnt(0)" ::: "memory");
  __builtin_amdgcn_s_barrier();
  asm volatile("" ::: "memory");
  const int q = wave;
  u4v bre[4], bim[4], fre[9], fim[9];
  f4 twr[3], twi[3];
  u32 toff[3][4];
  {
    const int a0 = g >> 1;
#pragma unroll
    for (int ks = 0; ks < 4; ++ks) {
      const int ph = (q == 0) ? (a0 ? 1 : 7) : (2 * ks + a0) * q;
      bre[ks] = *(const u4v*)(smem + K1_R16 + (ph & 7) * 512 + (lane & 31) * 16);
      bim[ks] = *(const u4v*)(smem + K1_R16 + ((ph + 2) & 7) * 512 + (lane & 31) * 16);
    }
#pragma unroll
    for (int i = 0; i < 9; ++i) {
      fre[i] = *(const u4v*)(smem + K1_F40 + i * 2048 + lane * 16);
      fim[i] = *(const u4v*)(smem + K1_F40 + i * 2048 + 1024 + lane * 16);
    }
    const float qs = q == 0 ? 0.70710678118f : 1.f;
#pragma unroll
    for (int t = 0; t < 3; ++t) {
      twr[t] = *(const f4*)(smem + K1_TW + t * 2048 + lane * 16) * qs;
      twi[t] = *(const f4*)(smem + K1_TW + t * 2048 + 1024 + lane * 16) * qs;
      PIN(twr[t]); PIN(twi[t]);
#pragma unroll
      for (int r = 0; r < 4; ++r) {
        const int k2 = li + 16 * (16 * t + 4 * g + r);
        const int pos = k2 <= 320 ? 2 * k2 : 2 * (640 - k2) + 1;
        toff[t][r] = (u32)WS_T + (u32)b * (TROW_UNITS * 16) + (q > 0 ? ((q - 1) * 160 + 64 * t + lane) * 16 : 480 * 16 + pos * 4);
        if (t == 2 && lane >= 32) toff[t][r] = 0xfffffff0u;
        PIN(toff[t][r]);
      }
    }
#pragma unroll
    for (int ks = 0; ks < 4; ++ks) { PIN(bre[ks]); PIN(bim[ks]); }
#pragma unroll
    for (int i = 0; i < 9; ++i) { PIN(fre[i]); PIN(fim[i]); }
  }
  STAMP(0,1);
  {
    f2 v[10];
#pragma unroll
    for (int i = 0; i < 4; ++i) { v[i] = f2{bflo(x0[i]), bfhi(x0[i])}; v[4 + i] = f2{bflo(x1[i]), bfhi(x1[i])}; }
    v[8] = f2{bflo(x2[0]), bfhi(x2[0])}; v[9] = f2{bflo(x2[1]), bfhi(x2[1])};
    ln_row(v);
    char* ub = smem + a * 1280;
    *(u4v*)(ub + 16 * j) = u4v{pk_h(v[0][0], v[0][1]), pk_h(v[1][0], v[1][1]), pk_h(v[2][0], v[2][1]), pk_h(v[3][0], v[3][1])};
    *(u4v*)(ub + 512 + 16 * j) = u4v{pk_h(v[4][0], v[4][1]), pk_h(v[5][0], v[5][1]), pk_h(v[6][0], v[6][1]), pk_h(v[7][0], v[7][1])};
    *(u2v*)(ub + 1024 + 8 * j) = u2v{pk_h(v[8][0], v[8][1]), pk_h(v[9][0], v[9][1])};
  }
  __syncthreads();
  STAMP(0,2);
  u4v b2[3];
  {
    const int qq = li >> 2, p = li & 3;
    const char* abase = smem + (8 * g + qq) * 80 + 8 * p;
    u4v af[3][4];
#pragma unroll
    for (int t = 0; t < 3; ++t)
#pragma unroll
      for (int ks = 0; ks < 4; ++ks) {
        s4 lo = __builtin_amdgcn_ds_read_tr16_b64_v4i16((LDS_AS s4*)(abase + ks * 2560 + t * 32));
        s4 hi = __builtin_amdgcn_ds_read_tr16_b64_v4i16((LDS_AS s4*)(abase + ks * 2560 + 320 + t * 32));
        u2v l2 = __builtin_bit_cast(u2v, lo), h2v = __builtin_bit_cast(u2v, hi);
        af[t][ks] = u4v{l2[0], l2[1], h2v[0], h2v[1]};
      }
    SB();
    if (wave > 0 && wave <= PFK1) touch((const char*)w_sp + ((size_t)(256 * PFG1 + blockIdx.x * PFK1 + wave - 1) * 64 + lane) * 128);
    SB();
    f4 are[3], aim[3];
#pragma unroll
    for (int t = 0; t < 3; ++t) {
      are[t] = f4{0, 0, 0, 0}; aim[t] = f4{0, 0, 0, 0};
#pragma unroll
      for (int ks = 0; ks < 4; ++ks) {
        are[t] = mfma_h(af[t][ks], bre[ks], are[t]);
        aim[t] = mfma_h(af[t][ks], bim[ks], aim[t]);
      }
    }
#pragma unroll
    for (int t = 0; t < 3; ++t) b2[t] = cmul_pack<false>(are[t], aim[t], twr[t], twi[t]);
  }
  const __amdgpu_buffer_rsrc_t rs = ws_rsrc(ws);
  f4 ore[3], oim[3];
#pragma unroll
  for (int tk = 0; tk < 3; ++tk)
#pragma unroll
    for (int to = 0; to < 3; ++to) {
      ore[to] = mfma_h(fre[to * 3 + tk], b2[tk], tk ? ore[to] : f4{0, 0, 0, 0});
      oim[to] = mfma_h(fim[to * 3 + tk], b2[tk], tk ? oim[to] : f4{0, 0, 0, 0});
    }
  u4v o[3];
#pragma unroll
  for (int to = 0; to < 3; ++to)
#pragma unroll
    for (int r = 0; r < 4; ++r) o[to][r] = pk_h(ore[to][r], oim[to][r]);
  if (q > 0) {
#pragma unroll
    for (int to = 0; to < 3; ++to) st16(rs, toff[to][0], o[to]);
  } else {
#pragma unroll
    for (int to = 0; to < 3; ++to)
#pragma unroll
      for (int r = 0; r < 4; ++r) __builtin_amdgcn_raw_buffer_store_b32(o[to][r], rs, toff[to][r], 0, 0);
    __builtin_amdgcn_raw_buffer_store_b32(lane ? o[1][0] : o[0][0], rs, lane == 0 ? toff[0][0] + 4 : (lane == 16 ? toff[1][0] + 4 : 0xfffffff0u), 0, 0);
  }
  STAMP(0,4);
}

template <int NT, int LDW, int NCG, int NTASK, bool PERM>
DI void retile(const u16* __restrict__ W, __amdgpu_buffer_rsrc_t rs, u32 dstbase, int wid, int nw, char* lds, int lane) {
  const int li = lane & 15, g = lane >> 4, q = li >> 2, p = li & 3;
  u32 dst[NT];
#pragma unroll
  for (int i = 0; i < NT; ++i) {
    const int task = wid + i * nw;
    const int t = task < NTASK ? task : 0;
    const int ks = t / NCG, cg = t - ks * NCG;
    const u16* src = W + (size_t)(ks * 32 + (lane >> 3)) * LDW + cg * 64 + (lane & 7) * 8;
#pragma unroll
    for (int j = 0; j < 4; ++j) GLDS16(src + (size_t)j * 8 * LDW, lds + i * 4096 + j * 1024);
    dst[i] = task < NTASK ? dstbase + (u32)t * 4096u + lane * 16 : 0xffff0000u;
  }
  const char* rb = lds + (PERM ? 4 * g + q : 8 * g + q) * 128 + 8 * p;
  WAITV(0);
#pragma unroll
  for (int i = 0; i < NT; ++i)
#pragma unroll
    for (int f = 0; f < 4; ++f) {
      s4 lo = __builtin_amdgcn_ds_read_tr16_b64_v4i16((LDS_AS s4*)(rb + i * 4096 + f * 32));
      s4 hi = __builtin_amdgcn_ds_read_tr16_b64_v4i16((LDS_AS s4*)(rb + i * 4096 + f * 32 + (PERM ? 16 : 4) * 128));
      u2v a = __builtin_bit_cast(u2v, lo), b = __builtin_bit_cast(u2v, hi);
      st16(rs, dst[i] + f * 1024, u4v{a[0], a[1], b[0], b[1]});
    }
}
constexpr int NRT1 = 20 * 27, NRT3 = 27 * 10;
constexpr int K2_FFT_BLOCKS = 168, K2_PREP_BLOCKS = 88, K3_PREP_BLOCKS = 68;

extern "C" __global__ __launch_bounds__(512, 2) void k2_kernel(char* __restrict__ ws, const u16* __restrict__ w_in,
                                                    const u16* __restrict__ w_out, const u16* __restrict__ w_sp) {
  PINS(ws); PINS(w_in); PINS(w_out); PINS(w_sp);
  const u32* T = (const u32*)(ws + WS_T); const u32* Wp = (const u32*)(ws + WS_WP); const u32* W2 = (const u32*)(ws + WS_W2);
  const u32* wmax = (const u32*)(ws + WS_WMAX); u32* Tp = (u32*)(ws + WS_TP); float* unscale = (float*)(ws + WS_UNS);
  u32* Wt1 = (u32*)(ws + WS_WT1); u32* Wt3 = (u32*)(ws + WS_WT3); u32* stats = (u32*)(ws + WS_STATS);
  extern __shared__ __attribute__((aligned(16))) char smem[];
  const int tid = threadIdx.x, lane = tid & 63;
  const int wave = __builtin_amdgcn_readfirstlane(tid >> 6);
  const int li = lane & 15, g = lane >> 4;
  if (blockIdx.x >= K2_FFT_BLOCKS) {
    STAMP(1,0);
    const int wid = (blockIdx.x - K2_FFT_BLOCKS) * 8 + wave;
    const __amdgpu_buffer_rsrc_t rs = ws_rsrc(ws);
    if (wid < 256) st16(rs, (u32)WS_STATS + wid * 1024 + lane * 16, u4v{~0u, ~0u, ~0u, ~0u});
    else if (wid < 264) st16(rs, (u32)WS_CNT + (wid - 256) * 1024 + lane * 16, u4v{0u, 0u, 0u, 0u});
    if (PFK2 && (wave & 1) == 0) {
      char* scratch = smem + 65536;
      const int task = 256 * (PFG1 + PFK1) + (wid >> 1);
      if (task < 1024) touch((const char*)w_sp + ((size_t)task * 64 + lane) * 128);
    }
    if (wid < NRT1) retile<1, FF, 27, NRT1, false>(w_in, rs, (u32)WS_WT1, wid, 0, smem + wave * 8192, lane);
    else retile<2, D, 10, NRT3, true>(w_out, rs, (u32)WS_WT3, wid - NRT1, 164, smem + wave * 8192, lane);
    STAMP(1,1);
    return;
  }
  const int P = (blockIdx.x & 7) * 21 + (blockIdx.x >> 3);
  if (P > 160) return;
  const int u = wave & 3, hh = wave >> 2;
  const int U = 4 * P + u;
  STAMP(2,0);
#pragma unroll
  for (int i = 0; i < 2; ++i) {
    const int bb = (wave * 2 + i) * 16 + (lane >> 2);
    GLDS16(T + ((size_t)bb * TROW_UNITS + 4 * P + ((lane & 3) ^ ((lane >> 4) & 3))) * 4, smem + (wave * 2 + i) * 1024);
  }
  u4v mxv[3];
#pragma unroll
  for (int i = 0; i < 3; ++i) mxv[i] = *(const u4v*)(wmax + (i < 2 || lane < 40 ? i * 256 + lane * 4 : 0));
  u4v Wv[2];
#pragma unroll
  for (int t = 0; t < 2; ++t) Wv[t] = *(const u4v*)(Wp + ((size_t)((U < NUNITS ? U : 0) * 4 + 2 * hh + t) * 64 + lane) * 4);
  const bool special = P >= 120;
  const int cB = special ? 4 : (P / 40 + 1);
  const int ur = special ? U - 480 : U % 160;
  const bool e320 = special ? ur == 160 : ur == 80;
  const bool edge = hh == 0 && (ur == 0 || e320);
  u4v w2, w4;
  if (edge) {
    w2 = *(const u4v*)(W2 + ((special ? 0 : cB) * 2 + (e320 ? 1 : 0)) * 256 + lane * 4);
    w4 = *(const u4v*)(W2 + (4 * 2 + (e320 ? 1 : 0)) * 256 + lane * 4);
  }
  STAMP(2,5);
  {
    char* cb = smem + 16384 + wave * 2048 + lane * 16;
    const int cc = wave < 4 ? cB : 0;
    const float inv = u < 2 ? (1.0f / 128.0f) : (1.0f / 2048.0f);
    const float amp = u == 0 ? 1.f : ((u == 1 && special) ? 0.125f : 0.25f);
    f4 c, s;
#pragma unroll
    for (int jj = 0; jj < 4; ++jj) {
      const int a = (u & 1) ? li * (cc + 8 * (4 * g + jj)) : (4 * g + jj) * (cc + 8 * li);
      const float ang = (float)a * inv;
      c[jj] = cosr(ang) * amp; s[jj] = sinr(ang) * amp;
    }
    if (u < 2) {
      *(u4v*)(cb) = u4v{pk_h(c[0], s[0]), pk_h(c[1], s[1]), pk_h(c[2], s[2]), pk_h(c[3], s[3])};
      *(u4v*)(cb + 1024) = u4v{pk_h(-s[0], c[0]), pk_h(-s[1], c[1]), pk_h(-s[2], c[2]), pk_h(-s[3], c[3])};
    } else {
      *(f4*)(cb) = c;
      *(f4*)(cb + 1024) = s;
    }
  }
  asm volatile("s_waitcnt lgkmcnt(0)" ::: "memory");
  __builtin_amdgcn_s_barrier();
  asm volatile("" ::: "memory");
  STAMP(2,7);
  u4v Fre, Fim, P1re[2], P1im[2], P4re[2], P4im[2];
  f4 tfr[2], tfs[2], tir[2], tis[2];
  {
    const char* cb = smem + 16384 + lane * 16;
    Fre = *(const u4v*)(cb + 4 * 2048); Fim = *(const u4v*)(cb + 4 * 2048 + 1024);
#pragma unroll
    for (int c = 0; c < 2; ++c) {
      const char* cs = cb + ((c == 0 && special) ? 8192 : 0);
      P1re[c] = *(const u4v*)(cs); P1im[c] = *(const u4v*)(cs + 1024);
      P4re[c] = *(const u4v*)(cs + 2048); P4im[c] = *(const u4v*)(cs + 3072);
      tfr[c] = *(const f4*)(cs + 4096); tfs[c] = *(const f4*)(cs + 5120);
      tir[c] = *(const f4*)(cs + 6144); tis[c] = *(const f4*)(cs + 7168);
    }
    PIN(Fre); PIN(Fim);
#pragma unroll
    for (int c = 0; c < 2; ++c) { PIN(P1re[c]); PIN(P1im[c]); PIN(P4re[c]); PIN(P4im[c]); PIN(tfr[c]); PIN(tfs[c]); PIN(tir[c]); PIN(tis[c]); }
  }
  STAMP(2,1);
  asm volatile("s_waitcnt vmcnt(0)" ::: "memory");
  __builtin_amdgcn_s_barrier();
  asm volatile("" ::: "memory");
  STAMP(2,2);
  const int loff = (64 * g + li) * 64 + ((u ^ (li >> 2)) & 3) * 16 + hh * 8;
  u4v X[2];
#pragma unroll
  for (int jj = 0; jj < 4; ++jj) {
    const u2v L = *(const u2v*)(smem + loff + jj * 1024);
    X[0][jj] = L[0]; X[1][jj] = L[1];
  }
  if (special) {
#pragma unroll
    for (int jj = 0; jj < 4; ++jj) {
      const u32 au = X[0][jj], bu = X[1][jj] ^ 0x80000000u;
      const h2 A = __builtin_bit_cast(h2, au);
      const h2 Bc = __builtin_bit_cast(h2, bu);
      X[0][jj] = __builtin_bit_cast(u32, A + Bc);
      X[1][jj] = __builtin_bit_cast(u32, A - Bc);
    }
  }
  const f4 z4 = f4{0, 0, 0, 0};
  f4 re[2], im[2];
#pragma unroll
  for (int t = 0; t < 2; ++t) { re[t] = mfma_h(X[t], P1re[t], z4); im[t] = mfma_h(X[t], P1im[t], z4); }
  u32 mx = 0;
#pragma unroll
  for (int i = 0; i < 3; ++i) mx = max(max(max(mxv[i][0], mxv[i][1]), max(mxv[i][2], mxv[i][3])), mx);
  mx = max64u(mx);
  int E = (int)(mx >> 7);
  E = E < 1 ? 1 : (E > 253 ? 253 : E);
  const float sig = __uint_as_float((u32)(254 - E) << 23) * 0.25f;
#pragma unroll
  for (int t = 0; t < 2; ++t) {
    const u4v v = cmul_pack<false>(re[t], im[t], tfr[t], -tfs[t]);
    re[t] = mfma_h(Fre, v, z4); im[t] = mfma_h(Fim, v, z4);
  }
  f4 wr4[2], wi4[2];
#pragma unroll
  for (int t = 0; t < 2; ++t)
#pragma unroll
    for (int r = 0; r < 4; ++r) { wr4[t][r] = bflo(Wv[t][r]); wi4[t][r] = bfhi(Wv[t][r]); }
  if (edge) {
#pragma unroll
    for (int r = 0; r < 4; ++r) {
      wr4[0][r] = (wr4[0][r] + bflo(w2[r])) * 0.5f; wi4[0][r] = (wi4[0][r] + bfhi(w2[r])) * 0.5f;
      if (special) { wr4[1][r] = (wr4[1][r] + bflo(w4[r])) * 0.5f; wi4[1][r] = (wi4[1][r] + bfhi(w4[r])) * 0.5f; }
    }
    if (U == 0 && lane == 0) unscale[0] = __uint_as_float((u32)E << 23);
  }
#pragma unroll
  for (int t = 0; t < 2; ++t) {
    const u4v v = cmul_pack<true>(re[t], im[t], wr4[t] * sig, wi4[t] * sig);
    im[t] = mfma_h(v, Fre, z4); re[t] = mfma_h(v, Fim, z4);
  }
#pragma unroll
  for (int t = 0; t < 2; ++t) {
    const u4v v = cmul_pack<true>(re[t], im[t], tir[t], tis[t]);
    im[t] = mfma_h(P4re[t], v, z4); re[t] = mfma_h(P4im[t], v, z4);
  }
  STAMP(2,3);
  if (special) {
    const f4 yr = re[0], yi = im[0], zr = re[1], zi = im[1];
    re[0] = yr + zr; im[0] = yi + zi;
    re[1] = yr - zr; im[1] = zi - yi;
  }
#pragma unroll
  for (int r = 0; r < 4; ++r) *(u2v*)(smem + loff + r * 1024) = u2v{pk_h(re[0][r], im[0][r]), pk_h(re[1][r], im[1][r])};
  asm volatile("s_waitcnt lgkmcnt(0)" ::: "memory");
  __builtin_amdgcn_s_barrier();
  asm volatile("" ::: "memory");
  const __amdgpu_buffer_rsrc_t rs = ws_rsrc(ws);
#pragma unroll
  for (int i = 0; i < 2; ++i) {
    const u4v o = *(const u4v*)(smem + (wave * 2 + i) * 1024 + lane * 16);
    st16(rs, (u32)WS_TP + P * 16384 + (wave * 2 + i) * 1024 + (lane & ~3) * 16 + ((lane & 3) ^ ((lane >> 4) & 3)) * 16, o);
  }
  { char* scratch = smem + 49152; if (wave == 0) CODE_TOUCH(k3_kernel, g1_kernel, 0) }
  STAMP(2,4);
}

extern "C" __global__ __launch_bounds__(256, 2) void k3_kernel(const u16* __restrict__ x, char* __restrict__ ws, const u16* __restrict__ w_sp) {
  PINS(x); PINS(ws); PINS(w_sp);
  const u32* Tp = (const u32*)(ws + WS_TP);
  constexpr int K3_Y = 10752, K3_CB = K3_Y + 10240;
  __shared__ __attribute__((aligned(16))) char smem[K3_CB + 8192 + 512];
  const int tid = threadIdx.x, lane = tid & 63;
  const int wave = __builtin_amdgcn_readfirstlane(tid >> 6);
  const int li = lane & 15, g = lane >> 4;
  const int b = (blockIdx.x & 7) * 32 + (blockIdx.x >> 3);
  STAMP(3,0);
#pragma unroll
  for (int i = 0; i < 2; ++i) {
    int U = (wave * 2 + i) * 64 + lane;
    U = U < 480 ? U : 479;
    GLDS16(Tp + (size_t)(U >> 2) * 4096 + b * 16 + (U & 3) * 4, smem + (wave * 2 + i) * 1024);
  }
#pragma unroll
  for (int it = 0; it < 4; ++it) {
    const int i = it < 2 ? wave * 2 + it : 6 + it;
    if (it < 2 || wave == 3) {
      const int k2 = (lane >> 2) + 16 * (4 * i + (lane & 3));
      const int pos = k2 <= 320 ? 2 * k2 : 2 * (640 - k2) + 1;
      GLDS4(Tp + (size_t)(120 + (pos >> 4)) * 4096 + b * 16 + (pos & 15), smem + 8192 + i * 256);
    }
  }
  char* scratch = smem + K3_CB + 8192;
  if (wave == 0) GLDS4(ws + WS_UNS, scratch + 256);
  const int a = 2 * wave + (lane >> 5), j = lane & 31;
  const int row = 256 * a + b;
  const u16* xr = x + (size_t)row * D;
  u4v x0 = *(const u4v*)(xr + 8 * j);
  u4v x1 = *(const u4v*)(xr + 256 + 8 * j);
  u2v x2 = *(const u2v*)(xr + 512 + 4 * j);
  STAMP(3,5);
  char* cb = smem + K3_CB;
  const int t = wave;
  u4v Bre[3], Bim[3];
  if (wave < 3) {
#pragma unroll
    for (int tk = 0; tk < 3; ++tk) {
      const float sc = (tk < 2 || g < 2) ? 1.f : 0.f;
#pragma unroll
      for (int jj = 0; jj < 4; ++jj) {
        const float ang = (float)((16 * tk + 4 * g + jj) * (16 * t + li)) * (1.0f / 40.0f);
        const float c = cosr(ang) * sc, s = sinr(ang) * sc;
        Bre[tk][jj] = pk_h(c, -s);
        Bim[tk][jj] = pk_h(s, c);
      }
      PIN(Bre[tk]); PIN(Bim[tk]);
    }
  } else {
#pragma unroll
    for (int tt = 0; tt < 3; ++tt) {
      f4 twr, twi;
#pragma unroll
      for (int jj = 0; jj < 4; ++jj) {
        const float ang = (float)((16 * tt + li) * (4 * g + jj)) * (1.0f / 640.0f);
        twr[jj] = cosr(ang) * 0.125f; twi[jj] = sinr(ang) * 0.125f;
      }
      *(f4*)(cb + tt * 2048 + lane * 16) = twr;
      *(f4*)(cb + tt * 2048 + 1024 + lane * 16) = twi;
    }
    u4v gre, gim;
#pragma unroll
    for (int jj = 0; jj < 4; ++jj) {
      const float ang = (float)(li * (4 * g + jj)) * 0.0625f;
      const float c = cosr(ang), s = sinr(ang);
      gre[jj] = pk_h(c, -s); gim[jj] = pk_h(s, c);
    }
    *(u4v*)(cb + 6144 + lane * 16) = gre;
    *(u4v*)(cb + 7168 + lane * 16) = gim;
  }
  STAMP(3,1);
  asm volatile("s_waitcnt vmcnt(0)" ::: "memory");
  PIN(x0); PIN(x1); PIN(x2);
  __syncthreads();
  STAMP(3,2);
  char* yb = smem + K3_Y;
  if (wave < 3) {
    const f4 z4 = f4{0, 0, 0, 0};
    u4v af[4][3];
#pragma unroll
    for (int q = 0; q < 4; ++q) {
      const char* base = smem + (q == 0 ? 8192 : (q - 1) * 2560);
#pragma unroll
      for (int tk = 0; tk < 3; ++tk) af[q][tk] = *(const u4v*)(base + (tk < 2 ? (64 * tk + lane) : (128 + (lane & 31))) * 16);
    }
    const f4 twr = *(const f4*)(cb + t * 2048 + lane * 16), twi = *(const f4*)(cb + t * 2048 + 1024 + lane * 16);
    const u4v Gre = *(const u4v*)(cb + 6144 + lane * 16), Gim = *(const u4v*)(cb + 7168 + lane * 16);
    const float usc = *(const float*)(scratch + 256);
    SB();
    f4 vre[4], vim[4];
#pragma unroll
    for (int q = 0; q < 4; ++q) {
      f4 re = z4, im = z4;
#pragma unroll
      for (int tk = 0; tk < 3; ++tk) {
        re = mfma_h(af[q][tk], Bre[tk], re);
        im = mfma_h(af[q][tk], Bim[tk], im);
      }
      vre[q] = re; vim[q] = im;
    }
#pragma unroll
    for (int q = 0; q < 4; ++q) {
      const u4v v = cmul_pack<false>(vre[q], vim[q], twr, twi);
      vre[q] = mfma_h(v, Gre, z4);
      vim[q] = mfma_h(v, Gim, z4);
    }
    const float fs = usc * (2.0f * 1024.0f * 128.0f / (2048.0f * 640.0f));
    const float fh = fs * 0.70710678118f;
    const f4 E = (vre[0] + vim[0]) * fs, O = (vre[0] - vim[0]) * fs;
    const f4 s13 = vre[1] + vre[3], dx = vre[1] - vre[3], sy = vim[1] + vim[3], dy = vim[3] - vim[1];
    const f4 Ep = E + vre[2] * fs, Em = E - vre[2] * fs, Om = O - vim[2] * fs, Op = O + vim[2] * fs;
    const f4 A = dx - sy, B = dx + sy;
    f4 y[8];
    y[0] = Ep + s13 * fs; y[4] = Ep - s13 * fs; y[2] = Em + dy * fs; y[6] = Em - dy * fs;
    y[1] = Om + A * fh; y[5] = Om - A * fh; y[3] = Op - B * fh; y[7] = Op + B * fh;
    if (t < 2 || lane < 32) {
      char* yp = yb + (40 * li + 16 * t + 4 * g) * 2;
#pragma unroll
      for (int aa = 0; aa < 8; ++aa) *(u2v*)(yp + aa * 1280) = u2v{pk_bf(y[aa][0], y[aa][1]), pk_bf(y[aa][2], y[aa][3])};
    }
  }
  else {
    CODE_TOUCH(g1_kernel, g2_kernel, (blockIdx.x >> 3) & 1)
#pragma unroll
    for (int i = 0; i < 4; ++i) {
      const int task = 256 * (PFG1 + PFK1) + 352 * PFK2 + i * 256 + blockIdx.x;
      if (task < 1024) touch((const char*)w_sp + ((size_t)task * 64 + lane) * 128);
    }
  }
  asm volatile("s_waitcnt lgkmcnt(0)" ::: "memory");
  __builtin_amdgcn_s_barrier();
  asm volatile("" ::: "memory");
  STAMP(3,3);
  {
    const __amdgpu_buffer_rsrc_t rs = ws_rsrc(ws);
    const u4v y0 = *(const u4v*)(yb + a * 1280 + 16 * j);
    const u4v y1 = *(const u4v*)(yb + a * 1280 + 512 + 16 * j);
    const u2v y2 = *(const u2v*)(yb + a * 1280 + 1024 + 8 * j);
    u32 hx[10];
#pragma unroll
    for (int i = 0; i < 4; ++i) {
      hx[i] = pk_bf(bflo(x0[i]) + bflo(y0[i]), bfhi(x0[i]) + bfhi(y0[i]));
      hx[4 + i] = pk_bf(bflo(x1[i]) + bflo(y1[i]), bfhi(x1[i]) + bfhi(y1[i]));
    }
    hx[8] = pk_bf(bflo(x2[0]) + bflo(y2[0]), bfhi(x2[0]) + bfhi(y2[0]));
    hx[9] = pk_bf(bflo(x2[1]) + bflo(y2[1]), bfhi(x2[1]) + bfhi(y2[1]));
    auto pairup = [&](u32 v0, u32 v1) {
      return u4v{v0, v1, (u32)__builtin_amdgcn_update_dpp(0, (int)v0, 0xB1, 0xf, 0xf, true), (u32)__builtin_amdgcn_update_dpp(0, (int)v1, 0xB1, 0xf, 0xf, true)};
    };
    const u32 drop = (lane & 1) ? 0xfffffff0u : 0u;
    const u32 ho = (u32)WS_H + row * (D * 2);
    st16(rs, ho + 16 * j, u4v{hx[0], hx[1], hx[2], hx[3]});
    st16(rs, ho + 512 + 16 * j, u4v{hx[4], hx[5], hx[6], hx[7]});
    st16(rs, (ho + 1024 + 8 * j) | drop, pairup(hx[8], hx[9]));
    f2 v[10];
#pragma unroll
    for (int i = 0; i < 10; ++i) v[i] = f2{bflo(hx[i]), bfhi(hx[i])};
    ln_row(v);
#pragma unroll
    for (int i = 0; i < 10; ++i) hx[i] = pk_bf(v[i][0], v[i][1]);
    const u32 lo = (u32)WS_L2 + (row >> 4) * 20480 + (row & 15) * 64;
    st16(rs, lo + (j >> 2) * 1024 + (j & 3) * 16, u4v{hx[0], hx[1], hx[2], hx[3]});
    st16(rs, lo + (8 + (j >> 2)) * 1024 + (j & 3) * 16, u4v{hx[4], hx[5], hx[6], hx[7]});
    st16(rs, (lo + (16 + (j >> 3)) * 1024 + (j & 7) * 8) | drop, pairup(hx[8], hx[9]));
  }
  STAMP(3,4);
}

#define FPD 2
#include <type_traits>
typedef unsigned long long u64;
typedef __attribute__((address_space(1))) unsigned long long gu64;
#define WAITL0() do { __builtin_amdgcn_sched_barrier(0); asm volatile("s_waitcnt lgkmcnt(0)" ::: "memory"); __builtin_amdgcn_sched_barrier(0); } while (0)
#define BAR() do { asm volatile("" ::: "memory"); __builtin_amdgcn_sched_barrier(0); __builtin_amdgcn_s_barrier(); __builtin_amdgcn_sched_barrier(0); asm volatile("" ::: "memory"); } while (0)
#define SGB(mask, n) __builtin_amdgcn_sched_group_barrier(mask, n, 0)
DI f4 mfma_b(u4v a, u4v b, f4 c) {
  return __builtin_amdgcn_mfma_f32_16x16x32_bf16(__builtin_bit_cast(bf8, a), __builtin_bit_cast(bf8, b), c, 0, 0, 0);
}
DI f2 gelu2(f2 x) {
  const float k1 = -2.0f * 0.7978845608028654f * 1.4426950408889634f;
  const float k2 = k1 * 0.044715f;
  f2 p = x * x * k2 + k1;
  f2 a = x * p;
  f2 e = f2{__builtin_amdgcn_exp2f(a[0]), __builtin_amdgcn_exp2f(a[1])} + 1.0f;
  f2 r = f2{__builtin_amdgcn_rcpf(e[0]), __builtin_amdgcn_rcpf(e[1])};
  return x * r;
}
DI u2v gelu4(f4 a) {
  f2 g0 = gelu2(f2{a[0], a[1]}), g1 = gelu2(f2{a[2], a[3]});
  return u2v{pk_bf(g0[0], g0[1]), pk_bf(g1[0], g1[1])};
}
template <int N> DI void waitv() { asm volatile("s_waitcnt vmcnt(%0)" ::"n"(N) : "memory"); }

template <int KT, int NST, int NLA, class F>
DI void loader_loop(bool big, F&& stage) {
#pragma unroll
  for (int s = 0; s < NST - 1; ++s) stage(s, s);
  int nbuf = NST - 1;
  for (int kt = 0; kt < KT - (NST - 1); ++kt) {
    if (big) waitv<NLA*(NST - 2)>(); else waitv<(NLA - 1) * (NST - 2)>();
    BAR();
    stage(kt + NST - 1, nbuf);
    nbuf = nbuf + 1 == NST ? 0 : nbuf + 1;
  }
  if constexpr (NST >= 7) { if (big) waitv<NLA * 5>(); else waitv<(NLA - 1) * 5>(); BAR(); }
  if constexpr (NST >= 6) { if (big) waitv<NLA * 4>(); else waitv<(NLA - 1) * 4>(); BAR(); }
  if constexpr (NST >= 5) { if (big) waitv<NLA * 3>(); else waitv<(NLA - 1) * 3>(); BAR(); }
  if (big) waitv<NLA * 2>(); else waitv<(NLA - 1) * 2>(); BAR();
  if (big) waitv<NLA>(); else waitv<NLA - 1>(); BAR();
  waitv<0>(); BAR();
}

#define G1_DEPTH 3
extern "C" __global__ __launch_bounds__(512) void g1_kernel(char* __restrict__ ws, const u16* __restrict__ w_sp) {
  PINS(ws); PINS(w_sp);
  const u32* l2f = (const u32*)(ws + WS_L2); const u32* Wt1 = (const u32*)(ws + WS_WT1);
  u32* z1f = (u32*)(ws + WS_Z1); u16* z2t = (u16*)(ws + WS_Z2); u64* stats = (u64*)(ws + WS_STATS);
  __shared__ __attribute__((aligned(16))) char smem[140 * 1024 + 4 * 4608 + 256];
  const int tid = threadIdx.x, lane = tid & 63;
  const int wave = __builtin_amdgcn_readfirstlane(tid >> 6);
  const int li = lane & 15, g = lane >> 4;
  STAMP(4,0);
  const int xcd = blockIdx.x & 7, idx = blockIdx.x >> 3;
#define G1_MAP 1
  const int mt = G1_MAP == 0 ? xcd * 2 + (idx >> 4) : (G1_MAP == 1 ? (xcd >> 1) * 4 + (idx >> 3) : (xcd >> 2) * 8 + (idx >> 2));
  const int nb = G1_MAP == 0 ? (idx & 15) : (G1_MAP == 1 ? (xcd & 1) * 8 + (idx & 7) : (xcd & 3) * 4 + (idx & 3));
  const int row0 = 1024 * (mt & 1) + 32 * (mt >> 1);
  const int zs = nb < 6 ? 4 * nb : 24 + 3 * (nb - 6);
  const int z1s = nb < 6 ? 3 * nb : (nb < 12 ? 18 + 4 * (nb - 6) : 42 + 3 * (nb - 12));
  const int type = nb < 6 ? 0 : (nb < 12 ? 1 : 2);
  if (wave >= 4) {
    const int lw = wave - 4;
    auto slot_frag = [&](int s) { return s < 3 ? 54 + zs + s : (s == 3 ? (type == 0 ? 54 + zs + 3 : (type == 1 ? z1s + 3 : z1s)) : z1s + s - 4); };
    const u32* bsrc[4];
#pragma unroll
    for (int j = 0; j < 4; ++j) {
      int i = lw + 4 * j; i = i < 14 ? i : 0;
      const int kk = i / 7, s = i - 7 * kk;
      bsrc[j] = Wt1 + (size_t)(kk * 108 + slot_frag(s)) * 256 + lane * 4;
    }
    auto stage = [&](int kt) {
      char* sb = smem + kt * 14336 + lw * 1024;
#pragma unroll
      for (int j = 0; j < 3; ++j) GLDS16(bsrc[j] + (size_t)kt * 2 * 108 * 256, sb + j * 4096);
      if (lw < 2) GLDS16(bsrc[3] + (size_t)kt * 2 * 108 * 256, sb + 3 * 4096);
    };
#pragma unroll
    for (int s = 0; s < G1_DEPTH; ++s) stage(s);
    for (int kt = 0; kt < 10 - G1_DEPTH; ++kt) {
      if (lw < 2) waitv<4 * (G1_DEPTH - 1)>(); else waitv<3 * (G1_DEPTH - 1)>();
      BAR();
      stage(kt + G1_DEPTH);
    }
    if constexpr (G1_DEPTH >= 5) { if (lw < 2) waitv<16>(); else waitv<12>(); BAR(); }
    if constexpr (G1_DEPTH >= 4) { if (lw < 2) waitv<12>(); else waitv<9>(); BAR(); }
    if (lw < 2) waitv<8>(); else waitv<6>(); BAR();
    if (lw < 2) waitv<4>(); else waitv<3>(); BAR();
    waitv<0>();
    char* scratch = smem + 140 * 1024 + 4 * 4608;
    if (lw < PFG1) touch((const char*)w_sp + ((size_t)(blockIdx.x * PFG1 + lw) * 64 + lane) * 128);
    BAR();
    if (lw >= 2) CODE_TOUCH(g2_kernel, end_kernel, lw - 2)
    return;
  }
  const int rg0 = (row0 + wave * 256) >> 4;
  const u32* asrc = l2f + (size_t)rg0 * 20 * 256 + li * 16 + g * 4;
#define G1_AD 5
  u4v fa[G1_AD][2];
#pragma unroll
  for (int k = 0; k < G1_AD; ++k) { fa[k][0] = *(const u4v*)(asrc + k * 256); fa[k][1] = *(const u4v*)(asrc + (20 + k) * 256); __builtin_amdgcn_sched_barrier(0); }
  f4 acc[7][2];
#pragma unroll
  for (int s = 0; s < 7; ++s) { acc[s][0] = f4{0, 0, 0, 0}; acc[s][1] = f4{0, 0, 0, 0}; }
  u4v fw[2][7];
  const char* wb = smem + lane * 16;
  auto rd = [&](int k) {
#pragma unroll
    for (int s = 0; s < 7; ++s) fw[k & 1][s] = *(const u4v*)(wb + (k * 7 + s) * 1024);
  };
  auto mm = [&](int k) {
#pragma unroll
    for (int s = 0; s < 7; ++s) {
      acc[s][0] = mfma_b(fw[k & 1][s], fa[k % G1_AD][0], acc[s][0]);
      acc[s][1] = mfma_b(fw[k & 1][s], fa[k % G1_AD][1], acc[s][1]);
    }
    if (k + G1_AD < 20) { fa[k % G1_AD][0] = *(const u4v*)(asrc + (k + G1_AD) * 256); fa[k % G1_AD][1] = *(const u4v*)(asrc + (20 + G1_AD + k) * 256); }
  };
  BAR(); STAMP(4,3); rd(0);
#pragma unroll
  for (int k = 1; k < 20; ++k) {
    if (!(k & 1)) BAR(); else __builtin_amdgcn_sched_barrier(0);
    if (k == 8) { STAMP(4,6); STAMPC(7,0); }
    if (k == 14) { STAMP(4,7); STAMPC(7,1); }
    rd(k);
    mm(k - 1);
#pragma unroll
    for (int i = 0; i < 7; ++i) { SGB(0x100, 1); SGB(0x8, 2); }
  }
  __builtin_amdgcn_sched_barrier(0);
  mm(19);
  STAMP(4,1);
  const int n2 = type == 0 ? 4 : 3;
  f4 zv[4][2];
  u2v z3[2];
  float lsum[2], lm2[2];
#pragma unroll
  for (int sf = 0; sf < 2; ++sf) {
    f2 s = f2{0.f, 0.f};
#pragma unroll
    for (int j = 0; j < 4; ++j) {
      const u2v t = gelu4(acc[j][sf]);
      if (j == 3) z3[sf] = t;
      zv[j][sf] = f4{bflo(t[0]), bfhi(t[0]), bflo(t[1]), bfhi(t[1])};
      if (j < 3 || type == 0) s += f2{zv[j][sf][0], zv[j][sf][1]} + f2{zv[j][sf][2], zv[j][sf][3]};
    }
    const float st = xsum_g(s[0] + s[1]);
    const float lmean = st * (type == 0 ? (1.0f / 64.0f) : (1.0f / 48.0f));
    f2 q = f2{0.f, 0.f};
#pragma unroll
    for (int j = 0; j < 4; ++j) {
      const f4 d = zv[j][sf] - lmean;
      if (j < 3 || type == 0) { q += f2{d[0], d[1]} * f2{d[0], d[1]}; q += f2{d[2], d[3]} * f2{d[2], d[3]}; }
    }
    lsum[sf] = st; lm2[sf] = xsum_g(q[0] + q[1]);
  }
  gu64* st = (gu64*)stats + (size_t)mt * 16 * 128 + wave * 32;
  if (lane < 32) {
    u32 a = __float_as_uint(g ? lsum[1] : lsum[0]), b = __float_as_uint(g ? lm2[1] : lm2[0]);
    if (a == ~0u) a = 0x7fc00000u;
    __hip_atomic_store(st + nb * 128 + lane, ((u64)b << 32) | a, __ATOMIC_RELAXED, __HIP_MEMORY_SCOPE_AGENT);
  }
  __builtin_amdgcn_sched_barrier(0);
  STAMP(4,2);
  const __amdgpu_buffer_rsrc_t rs = ws_rsrc(ws);
  u4v z1o[3];
#pragma unroll
  for (int j = 0; j < 3; ++j) {
    const u2v a = gelu4(acc[4 + j][0]), b = gelu4(acc[4 + j][1]);
    z1o[j] = u4v{a[0], a[1], b[0], b[1]};
    PIN(z1o[j]);
  }
  __builtin_amdgcn_sched_barrier(0);
  u64 gv[2][4];
  for (int spin = 0; spin < (1 << 16); ++spin) {
    bool ok = true;
#pragma unroll
    for (int sf = 0; sf < 2; ++sf)
#pragma unroll
      for (int i = 0; i < 4; ++i) {
        gv[sf][i] = __hip_atomic_load(st + (4 * g + i) * 128 + sf * 16 + li, __ATOMIC_RELAXED, __HIP_MEMORY_SCOPE_AGENT);
        ok &= ((u32)gv[sf][i] != ~0u);
      }
    if (__all(ok)) break;
    __builtin_amdgcn_s_sleep(1);
  }
  STAMP(4,4);
  {
    const u32 zo = (u32)WS_Z1 + ((rg0 >> 1) * 54 + z1s) * 1024 + lane * 16;
#pragma unroll
    for (int j = 0; j < 3; ++j) st16(rs, zo + j * 1024, z1o[j]);
    if (type == 1) st16(rs, zo + 3 * 1024, u4v{z3[0][0], z3[0][1], z3[1][0], z3[1][1]});
  }
  char* tb = smem + 140 * 1024 + wave * 4608;
#pragma unroll
  for (int sf = 0; sf < 2; ++sf) {
    float s = 0.f;
#pragma unroll
    for (int i = 0; i < 4; ++i) s += __uint_as_float((u32)gv[sf][i]);
    const float mean = xsum_g(s) * (1.0f / 864.0f);
    float q = 0.f;
#pragma unroll
    for (int i = 0; i < 4; ++i) {
      const bool big = (4 * g + i) < 6;
      const float dm = __uint_as_float((u32)gv[sf][i]) * (big ? (1.0f / 64.0f) : (1.0f / 48.0f)) - mean;
      q += __uint_as_float((u32)(gv[sf][i] >> 32)) + (big ? 64.f : 48.f) * dm * dm;
    }
    const float var = xsum_g(q) * (1.0f / 864.0f);
    const float mb = rbf(mean);
    const float rinv = __builtin_amdgcn_rcpf(rbf(__builtin_amdgcn_sqrtf(rbf(rbf(var) + 1e-5f))));
#pragma unroll
    for (int j = 0; j < 4; ++j) {
      const f4 o = (zv[j][sf] - mb) * rinv;
      *(u2v*)(tb + (sf * 16 + li) * 144 + j * 32 + g * 8) = u2v{pk_bf(o[0], o[1]), pk_bf(o[2], o[3])};
    }
  }
  {
    const int q = li >> 2, p = li & 3;
#pragma unroll
    for (int j = 0; j < 4; ++j) {
      s4 lo = __builtin_amdgcn_ds_read_tr16_b64_v4i16((LDS_AS s4*)(tb + (8 * g + q) * 144 + j * 32 + 8 * p));
      s4 hi = __builtin_amdgcn_ds_read_tr16_b64_v4i16((LDS_AS s4*)(tb + (8 * g + 4 + q) * 144 + j * 32 + 8 * p));
      u2v a = __builtin_bit_cast(u2v, lo), b = __builtin_bit_cast(u2v, hi);
      if (j < n2) st16(rs, (u32)WS_Z2 + (((zs + j) * 16 + li) * S + row0 + wave * 256 + 8 * g) * 2, u4v{a[0], a[1], b[0], b[1]});
    }
  }
  STAMP(4,5);
}

typedef __attribute__((address_space(1))) unsigned int gu32;
#define PSLEEP 1
constexpr int G2_NST = 7, G2_STB = 22 * 1024;
extern "C" __global__ __launch_bounds__(512) void g2_kernel(const u16* __restrict__ w_sp, const u16* __restrict__ b_sp, char* __restrict__ ws,
                                                            u16* __restrict__ out) {
  PINS(w_sp); PINS(b_sp); PINS(ws); PINS(out);
  const u16* z2t = (const u16*)(ws + WS_Z2); const u32* z1f = (const u32*)(ws + WS_Z1);
  __shared__ __attribute__((aligned(16))) char smem[G2_NST * G2_STB + 256];
  const int tid = threadIdx.x, lane = tid & 63;
  const int wave = __builtin_amdgcn_readfirstlane(tid >> 6);
  const int li = lane & 15, g = lane >> 4;
  STAMP(5,0);
  const int xcd = blockIdx.x & 7, idx = blockIdx.x >> 3;
  const int tt = (xcd >> 1) * 8 + (idx >> 2), ft = (xcd & 1) * 4 + (idx & 3);
  if (wave >= 4) {
    const int lw = wave - 4;
    const u16* src[6];
    const int swz = ((lane & 7) ^ (lane >> 3)) * 8;
#pragma unroll
    for (int j = 0; j < 6; ++j) {
      int i = lw + 4 * j; i = i < 22 ? i : 8;
      if (j < 2) src[j] = w_sp + (size_t)(tt * 64 + 8 * i + (lane >> 3)) * S + swz;
      else { int r = ft * 112 + 8 * (i - 8) + (lane >> 3); r = r < FH ? r : FH - 1; src[j] = z2t + (size_t)r * S + swz; }
    }
    auto stage = [&](int kt, int buf) {
      char* sb = smem + buf * G2_STB + lw * 1024;
      const int kk = kt * 64;
#pragma unroll
      for (int j = 0; j < 5; ++j) GLDS16(src[j] + kk, sb + j * 4096);
      if (lw < 2) GLDS16(src[5] + kk, sb + 5 * 4096);
    };
    const bool big = lw < 2;
    stage(0, 0); stage(1, 1); stage(2, 2);
    if (big) waitv<12>(); else waitv<10>(); BAR();
    stage(3, 3); stage(4, 4);
    if (big) waitv<18>(); else waitv<15>(); BAR();
    stage(5, 5); stage(6, 6);
    if (big) waitv<24>(); else waitv<20>(); BAR();
    stage(7, 0); stage(8, 1);
    int nbuf = 2;
    for (int kt = 3; kt < 26; ++kt) {
      if (big) waitv<30>(); else waitv<25>();
      BAR();
      stage(kt + 6, nbuf);
      nbuf = nbuf + 1 == G2_NST ? 0 : nbuf + 1;
    }
    const u32* wsrc = (const u32*)(ws + WS_WT3) + (size_t)(ft * 5) * 256 + lane * 4;
    for (int ps = 0; ps < 6; ++ps) {
      if (big) waitv<30>(); else waitv<25>();
      BAR();
      char* sb = smem + nbuf * G2_STB + lw * 1024;
#pragma unroll
      for (int j = 0; j < 6; ++j) {
        const int i = ps * 22 + lw + 4 * j, k = i / 5;
        if (j < 5 || big) GLDS16(wsrc + (size_t)(k * 35 + i) * 256, sb + j * 4096);
      }
      nbuf = nbuf + 1 == G2_NST ? 0 : nbuf + 1;
    }
    BAR();
    if (lw < 3) GLDS16(wsrc + (size_t)(26 * 35 + 132 + lw) * 256, smem + nbuf * G2_STB + lw * 1024);
    waitv<0>();
    BAR();
    return;
  }
  const int wt = wave & 1, wf = wave >> 1;
  const int n0 = (ft & 1) ? 3 : 4;
  const int f0 = wf ? n0 : 0, nf = wf ? 7 - n0 : n0;
  u4v z1v[4];
  float bias[2];
#pragma unroll
  for (int j = 0; j < 4; ++j) {
    int ff = ft * 7 + f0 + j; ff = ff < 54 ? ff : 53;
    z1v[j] = *(const u4v*)(z1f + ((size_t)(tt * 2 + wt) * 54 + ff) * 256 + lane * 4);
  }
  bias[0] = bflo(b_sp[tt * 64 + wt * 32 + li]); bias[1] = bflo(b_sp[tt * 64 + wt * 32 + 16 + li]);
  f4 acc[4][2];
#pragma unroll
  for (int j = 0; j < 4; ++j) { acc[j][0] = f4{0, 0, 0, 0}; acc[j][1] = f4{0, 0, 0, 0}; }
  const int sw = (g ^ (li & 7)) * 16;
  const int toff = (wt * 32 + li) * 128 + sw;
  const int foff = 8192 + (f0 * 16 + li) * 128 + sw;
  auto body = [&](auto NFc) {
    constexpr int NF = decltype(NFc)::value;
    u4v fb[2][2][2], fa[2][2][NF];
    auto rd = [&](int set, int buf) {
      const char* sb = smem + buf * G2_STB;
#pragma unroll
      for (int kk = 0; kk < 2; ++kk) {
        fb[set][kk][0] = *(const u4v*)(sb + (toff ^ (kk * 64)));
        fb[set][kk][1] = *(const u4v*)(sb + (toff ^ (kk * 64)) + 2048);
#pragma unroll
        for (int j = 0; j < NF; ++j) fa[set][kk][j] = *(const u4v*)(sb + (foff ^ (kk * 64)) + j * 2048);
      }
    };
    auto mm = [&](int set) {
#pragma unroll
      for (int kk = 0; kk < 2; ++kk)
#pragma unroll
        for (int j = 0; j < NF; ++j) {
          acc[j][0] = mfma_b(fa[set][kk][j], fb[set][kk][0], acc[j][0]);
          acc[j][1] = mfma_b(fa[set][kk][j], fb[set][kk][1], acc[j][1]);
        }
    };
    auto sched = [&]() {
#pragma unroll
      for (int i = 0; i < 2 * NF + 4; ++i) { SGB(0x100, 1); SGB(0x8, 1); }
      SGB(0x8, 2 * NF - 4);
    };
    BAR(); STAMP(5,3); rd(0, 0); WAITL0();
    int buf = 1;
    for (int kt = 1; kt < 31; kt += 2) {
      if (kt == 7) STAMP(5,4);
      if (kt == 23) STAMP(5,6);
      BAR(); rd(1, buf); mm(0); sched(); WAITL0();
      buf = buf + 1 == G2_NST ? 0 : buf + 1;
      BAR(); rd(0, buf); mm(1); sched(); WAITL0();
      buf = buf + 1 == G2_NST ? 0 : buf + 1;
    }
    BAR(); rd(1, buf); mm(0); sched(); WAITL0();
#pragma unroll
    for (int j = 0; j < 4; ++j) PIN(z1v[j]);
    PIN(bias[0]); PIN(bias[1]);
    BAR();
    mm(1);
  };
  if (nf == 4) body(std::integral_constant<int, 4>{}); else body(std::integral_constant<int, 3>{});
  STAMP(5,1);
  const __amdgpu_buffer_rsrc_t rs = ws_rsrc(ws);
  const int ff0 = ft * 7 + f0;
#pragma unroll
  for (int tf = 0; tf < 2; ++tf) {
    const int tfr = tt * 4 + wt * 2 + tf;
    u2v o[4];
#pragma unroll
    for (int j = 0; j < 4; ++j) {
      const u32 za = z1v[j][2 * tf], zb = z1v[j][2 * tf + 1];
      const f4 v = (acc[j][tf] + bias[tf]) * f4{bflo(za), bfhi(za), bflo(zb), bfhi(zb)};
      o[j] = u2v{pk_bf(v[0], v[1]), pk_bf(v[2], v[3])};
    }
    const u32 base = (u32)WS_P + (tfr * 27 + (ff0 >> 1)) * 1024 + lane * 16;
    if (!(ff0 & 1)) {
      st16(rs, base, u4v{o[0][0], o[0][1], o[1][0], o[1][1]});
      if (nf == 4) { if (ff0 + 3 < 54) st16(rs, base + 1024, u4v{o[2][0], o[2][1], o[3][0], o[3][1]}); }
      else st8(rs, base + 1024, o[2]);
    } else {
      st8(rs, base + 8, o[0]);
      st16(rs, base + 1024, u4v{o[1][0], o[1][1], o[2][0], o[2][1]});
    }
  }
  gu32* cnt = (gu32*)(ws + WS_CNT);
  waitv<0>();
  if (lane == 0) __hip_atomic_store(cnt + (tt * 2 + wt) * 32 + ft * 2 + wf, 1u, __ATOMIC_RELAXED, __HIP_MEMORY_SCOPE_AGENT);
  STAMP(5,2);
  const int trow = tt * 64 + wave * 16 + li;
  const u16* h = (const u16*)(ws + WS_H);
  u2v hv[5];
#pragma unroll
  for (int j = 0; j < 5; ++j) hv[j] = *(const u2v*)(h + (size_t)trow * D + ft * 80 + j * 16 + g * 4);
  for (int spin = 0; spin < (1 << 16); ++spin) {
    const u32 c = __hip_atomic_load(cnt + (tt * 2 + (wave >> 1)) * 32 + li, __ATOMIC_RELAXED, __HIP_MEMORY_SCOPE_AGENT);
    if (__all(c != 0u)) break;
    __builtin_amdgcn_s_sleep(PSLEEP);
  }
  asm volatile("" ::: "memory");
  STAMP(6,0);
  u4v fb[27];
  const u32 poff = (u32)WS_P + (tt * 4 + wave) * 27 * 1024 + lane * 16;
#pragma unroll
  for (int k = 0; k < 3 * FPD; ++k) { fb[k] = __builtin_amdgcn_raw_buffer_load_b128(rs, poff + k * 1024, 0, AUX_SC1); SB(); }
  f4 acc3[5];
#pragma unroll
  for (int j = 0; j < 5; ++j) acc3[j] = f4{0, 0, 0, 0};
  u4v fa[2][15];
  u32 wo[3] = {(u32)lane * 16, (u32)lane * 16 + 3 * G2_STB - 8192, (u32)lane * 16 + 5 * G2_STB};
  PIN(wo[1]); PIN(wo[2]);
  auto rd3 = [&](int kt) {
#pragma unroll
    for (int ii = 0; ii < 15; ++ii) {
      const int i = kt * 15 + ii;
      const int off = ((32 + i / 22) % G2_NST) * G2_STB + (i % 22) * 1024;
      const int w = off < 3 * G2_STB - 8192 ? 0 : (off < 5 * G2_STB ? 1 : 2);
      fa[kt & 1][ii] = *(const u4v*)(smem + wo[w] + (off - (w == 0 ? 0 : (w == 1 ? 3 * G2_STB - 8192 : 5 * G2_STB))));
    }
  };
  auto mm3 = [&](int kt) {
#pragma unroll
    for (int kk = 0; kk < 3; ++kk)
#pragma unroll
      for (int j = 0; j < 5; ++j) acc3[j] = mfma_b(fa[kt & 1][kk * 5 + j], fb[kt * 3 + kk], acc3[j]);
  };
  BAR();
  STAMP(6,3);
  rd3(0);
#pragma unroll
  for (int kt = 1; kt < 9; ++kt) {
    SB();
    if (kt - 1 + FPD < 9) {
#pragma unroll
      for (int k = 0; k < 3; ++k) fb[(kt - 1 + FPD) * 3 + k] = __builtin_amdgcn_raw_buffer_load_b128(rs, poff + ((kt - 1 + FPD) * 3 + k) * 1024, 0, AUX_SC1);
      SB();
    }
    rd3(kt);
    mm3(kt - 1);
#pragma unroll
    for (int i = 0; i < 15; ++i) { SGB(0x100, 1); SGB(0x8, 1); }
  }
  SB();
  mm3(8);
  STAMP(6,1);
  {
    u2v o[5];
#pragma unroll
    for (int j = 0; j < 5; ++j) {
      u32 t0 = pk_bf(acc3[j][0], acc3[j][1]), t1 = pk_bf(acc3[j][2], acc3[j][3]);
      o[j] = u2v{pk_bf(bflo(t0) + bflo(hv[j][0]), bfhi(t0) + bfhi(hv[j][0])), pk_bf(bflo(t1) + bflo(hv[j][1]), bfhi(t1) + bfhi(hv[j][1]))};
    }
    const __amdgpu_buffer_rsrc_t ro = __builtin_amdgcn_make_buffer_rsrc((void*)out, 0, S * D * 2, 0x00020000);
    const u32 oo = (trow * D + ft * 80) * 2;
#define OUT_AUX 16
#pragma unroll
    for (int j = 0; j < 4; j += 2) {
      auto s0 = __builtin_amdgcn_permlane16_swap(o[j][0], o[j + 1][0], false, false);
      auto s1 = __builtin_amdgcn_permlane16_swap(o[j][1], o[j + 1][1], false, false);
      __builtin_amdgcn_raw_buffer_store_b128(u4v{s0[0], s1[0], s0[1], s1[1]}, ro, oo + (j + (g & 1)) * 32 + (g >> 1) * 16, 0, OUT_AUX);
    }
    __builtin_amdgcn_raw_buffer_store_b64(o[4], ro, oo + 128 + g * 8, 0, OUT_AUX);
  }
  STAMP(6,2);
}

extern "C" __global__ void end_kernel() {}

extern "C" { long long dbg_offsets[16] = {(long long)WS_H, -1, -1, (long long)WS_Z2, -1, -1, -1}; }

extern "C" void kernel_launch(void* const* d_in, const int* in_sizes, int n_in,
                              void* d_out, int out_size, void* d_ws, size_t ws_size,
                              hipStream_t stream) {
  const u16* x=(const u16*)d_in[0]; const u16* wr=(const u16*)d_in[1]; const u16* wi=(const u16*)d_in[2];
  const u16* w_in=(const u16*)d_in[4]; const u16* w_sp=(const u16*)d_in[5]; const u16* b_sp=(const u16*)d_in[6]; const u16* w_out=(const u16*)d_in[7];
  char* ws=(char*)d_ws;
  k1_kernel<<<256+NPREP,256,0,stream>>>(x,wr,wi,ws,w_sp);
  (void)hipFuncSetAttribute((const void*)k2_kernel, hipFuncAttributeMaxDynamicSharedMemorySize, 84 * 1024);
  k2_kernel<<<K2_FFT_BLOCKS+K2_PREP_BLOCKS,512,84 * 1024,stream>>>(ws,w_in,w_out,w_sp);
  k3_kernel<<<256,256,0,stream>>>(x,ws,w_sp);
  g1_kernel<<<256,512,0,stream>>>(ws,w_sp);
  g2_kernel<<<256,512,0,stream>>>(w_sp,b_sp,ws,(u16*)d_out);
}
```

```cpp
#include <hip/hip_runtime.h>
#include <hip/hip_bf16.h>
#include <stdint.h>
#define STAMP(k, i)
#define STAMPC(k, i)

#define PFK2B 1
#define PFG1 2
#define PFK1 0
#define PFK2 0
#define PF_WSP_K2 352
#define PF_WSP_K3 1
#define PF_WT1 0
typedef unsigned short u16;
typedef unsigned int u32;
typedef _Float16 h2 __attribute__((ext_vector_type(2)));
typedef _Float16 h8 __attribute__((ext_vector_type(8)));
typedef __bf16 bf2 __attribute__((ext_vector_type(2)));
typedef __bf16 bf8 __attribute__((ext_vector_type(8)));
typedef float f2 __attribute__((ext_vector_type(2)));
typedef float f4 __attribute__((ext_vector_type(4)));
typedef short s4 __attribute__((ext_vector_type(4)));
typedef u32 u2v __attribute__((ext_vector_type(2)));
typedef u32 u4v __attribute__((ext_vector_type(4)));
#define DI __device__ __forceinline__
#define LDS_AS __attribute__((address_space(3)))
#define PIN(x) asm volatile("" : "+v"(x))
#define GLDS16(src, dst) __builtin_amdgcn_global_load_lds((const u32*)(src), (LDS_AS u32*)(dst), 16, 0, 0)
#define WAITV(n) asm volatile("s_waitcnt vmcnt(%0)" ::"n"(n) : "memory")

constexpr int S = 2048, D = 640, DF = 321, FF = 1728, FH = 864;
constexpr int TROW_UNITS = 648;
constexpr int NUNITS = 641;
constexpr int NPREP = 168;

constexpr size_t WS_T = 0, WS_WP = 2688u << 10, WS_TP = 5376u << 10, WS_WT1 = 8064u << 10, WS_WT3 = 10240u << 10, WS_H = 11392u << 10,
                 WS_L2 = 13952u << 10, WS_Z1 = 16512u << 10, WS_Z2 = 19968u << 10, WS_STATS = 23424u << 10, WS_W2 = 23680u << 10,
                 WS_WMAX = 23692u << 10, WS_UNS = 23696u << 10, WS_CNT = 23700u << 10, WS_P = WS_T;
#define PINS(x) asm volatile("" ::"s"(x))
#define GLDS4(src, dst) __builtin_amdgcn_global_load_lds((const u32*)(src), (LDS_AS u32*)(dst), 4, 0, 0)
#define touch(p) GLDS4(p, scratch)

constexpr int AUX_SC1 = 16;
DI __amdgpu_buffer_rsrc_t ws_rsrc(char* ws) { return __builtin_amdgcn_make_buffer_rsrc((void*)ws, 0, 64 << 20, 0x00020000); }
#define ST_AUX 16
DI void st16(__amdgpu_buffer_rsrc_t rs, u32 off, u4v v) { __builtin_amdgcn_raw_buffer_store_b128(v, rs, off, 0, ST_AUX); }
DI void st8(__amdgpu_buffer_rsrc_t rs, u32 off, u2v v) { __builtin_amdgcn_raw_buffer_store_b64(v, rs, off, 0, ST_AUX); }
#define CODE_ADDR(var, sym)                                                                                     \
  const char* var;                                                                                              \
  {                                                                                                             \
    u32 lo_, hi_;                                                                                               \
    asm volatile("s_getpc_b64 s[40:41]\n\ts_add_u32 %0, s40, " #sym "@rel32@lo+4\n\ts_addc_u32 %1, s41, " #sym "@rel32@hi+12" \
                 : "=s"(lo_), "=s"(hi_)::"s40", "s41", "scc");                                                  \
    var = (const char*)(((unsigned long long)hi_ << 32) | lo_);                                                 \
  }
#define CODE_TOUCH(a, b, c)                                                           \
  {                                                                                   \
    CODE_ADDR(pa_, a) CODE_ADDR(pb_, b)                                               \
    const char* p_ = pa_ + ((c) * 64 + lane) * 128;                                   \
    if (p_ < pb_) touch(p_);                                                          \
  }
struct Args { char* ws; const u16* x; const u16* w_sp; const u16* w_in; const u16* w_out; const u16* b_sp; u16* out; void* pad; };
static __device__ __attribute__((aligned(128))) Args g_args;
typedef const __attribute__((address_space(4))) Args* ArgsPtr;
#define LOAD_ARGS() ArgsPtr ap_ = (ArgsPtr)(unsigned long long)&g_args
DI float bflo(u32 v) { return __uint_as_float(v << 16); }
DI float bfhi(u32 v) { return __uint_as_float(v & 0xffff0000u); }
DI u32 pk_bf(float a, float b) { f2 v = {a, b}; return __builtin_bit_cast(u32, __builtin_convertvector(v, bf2)); }
DI u32 pk_h(float a, float b) { f2 v = {a, b}; return __builtin_bit_cast(u32, __builtin_convertvector(v, h2)); }
DI float rbf(float a) { return bflo(pk_bf(a, a)); }
DI float cosr(float rev) { return __builtin_amdgcn_cosf(rev); }
DI float sinr(float rev) { return __builtin_amdgcn_sinf(rev); }
DI f4 mfma_h(u4v a, u4v b, f4 c) {
  return __builtin_amdgcn_mfma_f32_16x16x32_f16(__builtin_bit_cast(h8, a), __builtin_bit_cast(h8, b), c, 0, 0, 0);
}
template <int CTRL> DI float dpp_add(float v) {
  int t = __builtin_amdgcn_update_dpp(0, __float_as_int(v), CTRL, 0xf, 0xf, true);
  return v + __int_as_float(t);
}
DI float xsum_g(float v) {
  u32 b = __float_as_uint(v);
  auto r = __builtin_amdgcn_permlane16_swap(b, b, false, false);
  v = __uint_as_float(r[0]) + __uint_as_float(r[1]);
  b = __float_as_uint(v);
  auto s = __builtin_amdgcn_permlane32_swap(b, b, false, false);
  return __uint_as_float(s[0]) + __uint_as_float(s[1]);
}
DI float sum64(float v) {
  v = dpp_add<0xB1>(v);
  v = dpp_add<0x4E>(v);
  v = dpp_add<0x141>(v);
  v = dpp_add<0x140>(v);
  return xsum_g(v);
}
template <int CTRL> DI u32 dpp_maxu(u32 v) {
  u32 t = (u32)__builtin_amdgcn_update_dpp(0, (int)v, CTRL, 0xf, 0xf, true);
  return v > t ? v : t;
}
DI u32 max64u(u32 v) {
  v = dpp_maxu<0xB1>(v); v = dpp_maxu<0x4E>(v); v = dpp_maxu<0x141>(v); v = dpp_maxu<0x140>(v);
  auto r = __builtin_amdgcn_permlane16_swap(v, v, false, false);
  v = r[0] > r[1] ? r[0] : r[1];
  auto s = __builtin_amdgcn_permlane32_swap(v, v, false, false);
  return s[0] > s[1] ? s[0] : s[1];
}
DI float sum32(float v) {
  v = dpp_add<0xB1>(v); v = dpp_add<0x4E>(v); v = dpp_add<0x141>(v); v = dpp_add<0x140>(v);
  u32 b = __float_as_uint(v);
  auto r = __builtin_amdgcn_permlane16_swap(b, b, false, false);
  return __uint_as_float(r[0]) + __uint_as_float(r[1]);
}
DI void ln_row(f2 (&v)[10]) {
  f2 s = ((v[0] + v[1]) + (v[2] + v[3])) + ((v[4] + v[5]) + (v[6] + v[7])) + (v[8] + v[9]);
  const float mean = sum32(s[0] + s[1]) * (1.0f / 640.0f);
  f2 q0 = f2{0.f, 0.f}, q1 = f2{0.f, 0.f};
#pragma unroll
  for (int i = 0; i < 10; i += 2) { v[i] -= mean; v[i + 1] -= mean; q0 += v[i] * v[i]; q1 += v[i + 1] * v[i + 1]; }
  q0 += q1;
  const float var = sum32(q0[0] + q0[1]) * (1.0f / 640.0f);
  const float rinv = __builtin_amdgcn_rcpf(rbf(__builtin_amdgcn_sqrtf(rbf(rbf(var) + 1e-5f))));
  const float off = (mean - rbf(mean)) * rinv;
#pragma unroll
  for (int i = 0; i < 10; ++i) v[i] = v[i] * rinv + off;
}
#define SB() __builtin_amdgcn_sched_barrier(0)
template <bool SWAP> DI u4v cmul_pack(f4 re, f4 im, f4 wr, f4 wi) {
  const f4 ore = re * wr - im * wi, oim = re * wi + im * wr;
  u4v v;
#pragma unroll
  for (int r = 0; r < 4; ++r) v[r] = SWAP ? pk_h(oim[r], ore[r]) : pk_h(ore[r], oim[r]);
  return v;
}

struct __attribute__((packed, aligned(2))) U4U { u32 a, b, c, d; };

DI void lds_signal(char* p) {
  asm volatile("s_waitcnt lgkmcnt(0)" ::: "memory");
  if ((threadIdx.x & 63) == 0) __hip_atomic_fetch_add((LDS_AS u32*)p, 1u, __ATOMIC_RELAXED, __HIP_MEMORY_SCOPE_WORKGROUP);
}
DI void lds_wait(char* p, u32 n) {
  while (__builtin_amdgcn_readfirstlane(__hip_atomic_load((LDS_AS u32*)p, __ATOMIC_RELAXED, __HIP_MEMORY_SCOPE_WORKGROUP)) < n) {}
  asm volatile("" ::: "memory");
}
constexpr int K1_F40 = 10272, K1_TW = K1_F40 + 18432, K1_R16 = K1_TW + 6144, K1_LDS = K1_R16 + 4096 + 256 + 16;
extern "C" __global__ __launch_bounds__(512) void k1_kernel(const u16* __restrict__ x, const u16* __restrict__ wr,
                                                            const u16* __restrict__ wi, char* __restrict__ ws,
                                                            const u16* __restrict__ w_sp, const u16* w_in, const u16* w_out,
                                                            const u16* b_sp, u16* out) {
  __shared__ __attribute__((aligned(16))) char smem[K1_LDS];
  const int tid = threadIdx.x, lane = tid & 63;
  const int wave = __builtin_amdgcn_readfirstlane(tid >> 6);
  const int li = lane & 15, g = lane >> 4;
  STAMP(0,0);
  char* scratch = smem + K1_R16 + 4096;
  char* flags = scratch + 256;
  if (wave == 7) CODE_TOUCH(k1_kernel, end_kernel, (blockIdx.x >> 3) & 7)
  PINS(x); PINS(wr); PINS(wi); PINS(ws); PINS(w_sp);
  auto constants = [&]() {
#pragma unroll
    for (int it = 0; it < 2; ++it) {
      const int item = wave + 8 * it;
      if (it == 0 || wave == 0) {
        const int to = item / 3, tk = item - 3 * to;
        const int kb = 16 * to + li;
        const float s8 = (tk < 2 || g < 2) ? 0.125f : 0.f;
        u4v fre, fim;
#pragma unroll
        for (int jj = 0; jj < 4; ++jj) {
          const float ang = (float)(kb * (16 * tk + 4 * g + jj)) * (1.0f / 40.0f);
          const float c = cosr(ang) * s8, s = sinr(ang) * s8;
          fre[jj] = pk_h(c, s);
          fim[jj] = pk_h(-s, c);
        }
        *(u4v*)(smem + K1_F40 + item * 2048 + lane * 16) = fre;
        *(u4v*)(smem + K1_F40 + item * 2048 + 1024 + lane * 16) = fim;
      } else if (wave < 4) {
        const int t = wave - 1;
        f4 twr, twi;
#pragma unroll
        for (int r = 0; r < 4; ++r) {
          const float ang = (float)((16 * t + 4 * g + r) * li) * (1.0f / 640.0f);
          twr[r] = cosr(ang) * 0.125f; twi[r] = -sinr(ang) * 0.125f;
        }
        *(f4*)(smem + K1_TW + t * 2048 + lane * 16) = twr;
        *(f4*)(smem + K1_TW + t * 2048 + 1024 + lane * 16) = twi;
      } else {
        const int ph = 2 * (wave - 4) + (lane >> 5);
        float cs[8];
#pragma unroll
        for (int jj = 0; jj < 8; ++jj) cs[jj] = cosr((float)(((8 * (g & 1) + jj) * li + 2 * ph) & 15) * 0.0625f);
        *(u4v*)(smem + K1_R16 + (wave - 4) * 1024 + lane * 16) = u4v{pk_h(cs[0], cs[1]), pk_h(cs[2], cs[3]), pk_h(cs[4], cs[5]), pk_h(cs[6], cs[7])};
      }
    }
    if (tid < 4) { *(u32*)(smem + 10240 + tid * 4) = 0u; *(u32*)(flags + tid * 4) = 0u; }
    asm volatile("s_waitcnt lgkmcnt(0)" ::: "memory");
    __builtin_amdgcn_s_barrier();
    asm volatile("" ::: "memory");
  };
  if (wave >= 4) {
    const int pb = blockIdx.x < NPREP ? blockIdx.x : 0;
    const int cp = pb & 7, cc = pb >> 3;
    const int c0 = cc < 20 ? cc * 16 : 305;
    const bool mir = cp > 4;
    const bool sp = (cp & 3) == 0;
    const int e = mir ? 255 - (tid - 256) : tid - 256;
    const size_t roff = (size_t)(cp + 8 * e) * DF + c0;
    const U4U* pr = (const U4U*)(wr + roff);
    const U4U* pi = (const U4U*)(wi + roff);
    U4U r0, r1, i0, i1;
    if (blockIdx.x < NPREP) { r0 = pr[0]; r1 = pr[1]; i0 = pi[0]; i1 = pi[1]; }
    SB();
    constants();
    if (blockIdx.x >= NPREP) {
      if (blockIdx.x == 255 && tid == 256) g_args = Args{ws, x, w_sp, w_in, w_out, b_sp, out, nullptr};
      return;
    }
    const u32 flip = mir ? 0x80000000u : 0u;
    const int q1 = (mir ? 8 - cp : cp) - 1;
    const int kb = mir ? 39 - cc : cc;
    const int t1 = sp ? 1920 + 32 * cc + (cp >> 2) : (q1 * 160 + (kb >> 2) * 16 + (mir ? 16 : 0)) * 4 + (kb & 3);
    const int step = sp ? 2 : (mir ? -4 : 4);
    const int t0 = mir ? (q1 * 160 + ((kb + 1) >> 2) * 16) * 4 + ((kb + 1) & 3) : t1;
    const bool edge = (cc == 0 || cc == 20);
    const __amdgpu_buffer_rsrc_t rs = ws_rsrc(ws);
    const u32 slot = (u32)WS_WP + ((wave - 4) * 16 + li) * 16;
    u32 doff[4];
#pragma unroll
    for (int m = 0; m < 4; ++m) {
      const int jc = 4 * m + g;
      doff[m] = slot + (u32)(t1 + step * jc) * 1024u;
      if (cc == 20) doff[m] = 0xfffffff0u;
    }
    if (g == 0 && cc < 20) doff[0] = (mir && edge) ? 0xfffffff0u : slot + (u32)t0 * 1024u;
    if (g == 3 && cc == 20 && !mir) doff[3] = slot + (u32)t0 * 1024u;
    u32 rr[8] = {r0.a, r0.b, r0.c, r0.d, r1.a, r1.b, r1.c, r1.d};
    u32 ii[8] = {i0.a, i0.b, i0.c, i0.d, i1.a, i1.b, i1.c, i1.d};
    u32 vals[16];
#pragma unroll
    for (int j = 0; j < 16; ++j) {
      u32 a = rr[j >> 1], b = ii[j >> 1];
      vals[j] = ((j & 1) ? __builtin_amdgcn_perm(b, a, 0x07060302u) : __builtin_amdgcn_perm(b, a, 0x05040100u)) ^ flip;
    }
    if (edge && (mir || sp)) {
      const u32 v0 = cc < 20 ? vals[0] : vals[15];
      const int e2 = (cp == 0) ? ((256 - e) & 255) : 255 - e;
      const int dpos2 = ((e2 & 15) + 16 * (e2 >> 6)) * 4 + ((e2 >> 4) & 3);
      const int cls = mir ? 8 - cp : cp;
      ((u32*)(ws + WS_W2))[(cls * 2 + (cc == 20)) * 256 + dpos2] = v0 ^ flip ^ 0x80000000u;
    }
    u32 mx = 0;
#pragma unroll
    for (int j = 0; j < 16; ++j) {
      const u32 a = vals[j] & 0x7fff7fffu;
      mx = max(mx, max(a & 0xffffu, a >> 16));
    }
#pragma unroll
    for (int m = 0; m < 4; ++m) {
      auto p02 = __builtin_amdgcn_permlane32_swap(vals[4 * m], vals[4 * m + 2], false, false);
      auto p13 = __builtin_amdgcn_permlane32_swap(vals[4 * m + 1], vals[4 * m + 3], false, false);
      auto q01 = __builtin_amdgcn_permlane16_swap(p02[0], p13[0], false, false);
      auto q23 = __builtin_amdgcn_permlane16_swap(p02[1], p13[1], false, false);
      st16(rs, doff[m], u4v{q01[0], q01[1], q23[0], q23[1]});
    }
    mx = max64u(mx);
    if (lane == 0) {
      __hip_atomic_fetch_max((LDS_AS u32*)(flags + 4), mx, __ATOMIC_RELAXED, __HIP_MEMORY_SCOPE_WORKGROUP);
      asm volatile("s_waitcnt lgkmcnt(0)" ::: "memory");
      if (__hip_atomic_fetch_add((LDS_AS u32*)(flags + 8), 1u, __ATOMIC_RELAXED, __HIP_MEMORY_SCOPE_WORKGROUP) == 3u)
        ((u32*)(ws + WS_WMAX))[pb] = __hip_atomic_load((LDS_AS u32*)(flags + 4), __ATOMIC_RELAXED, __HIP_MEMORY_SCOPE_WORKGROUP);
    }
    return;
  }
  const int b = (blockIdx.x & 7) * 32 + (blockIdx.x >> 3);
  const int a = 2 * wave + (lane >> 5), j = lane & 31;
  const u16* xr = x + (size_t)(256 * a + b) * D;
  const u4v x0 = *(const u4v*)(xr + 8 * j);
  const u4v x1 = *(const u4v*)(xr + 256 + 8 * j);
  const u2v x2 = *(const u2v*)(xr + 512 + 4 * j);
  SB();
  constants();
  const int q = wave;
  u4v bre[4], bim[4], fre[9], fim[9];
  f4 twr[3], twi[3];
  u32 toff[3][4];
  {
    const int a0 = g >> 1;
#pragma unroll
    for (int ks = 0; ks < 4; ++ks) {
      const int ph = (q == 0) ? (a0 ? 1 : 7) : (2 * ks + a0) * q;
      bre[ks] = *(const u4v*)(smem + K1_R16 + (ph & 7) * 512 + (lane & 31) * 16);
      bim[ks] = *(const u4v*)(smem + K1_R16 + ((ph + 2) & 7) * 512 + (lane & 31) * 16);
    }
#pragma unroll
    for (int i = 0; i < 9; ++i) {
      fre[i] = *(const u4v*)(smem + K1_F40 + i * 2048 + lane * 16);
      fim[i] = *(const u4v*)(smem + K1_F40 + i * 2048 + 1024 + lane * 16);
    }
    const float qs = q == 0 ? 0.70710678118f : 1.f;
#pragma unroll
    for (int t = 0; t < 3; ++t) {
      twr[t] = *(const f4*)(smem + K1_TW + t * 2048 + lane * 16) * qs;
      twi[t] = *(const f4*)(smem + K1_TW + t * 2048 + 1024 + lane * 16) * qs;
      PIN(twr[t]); PIN(twi[t]);
#pragma unroll
      for (int r = 0; r < 4; ++r) {
        const int k2 = li + 16 * (16 * t + 4 * g + r);
        const int pos = k2 <= 320 ? 2 * k2 : 2 * (640 - k2) + 1;
        toff[t][r] = (u32)WS_T + (u32)b * (TROW_UNITS * 16) + (q > 0 ? ((q - 1) * 160 + 64 * t + lane) * 16 : 480 * 16 + pos * 4);
        if (t == 2 && lane >= 32) toff[t][r] = 0xfffffff0u;
        PIN(toff[t][r]);
      }
    }
#pragma unroll
    for (int ks = 0; ks < 4; ++ks) { PIN(bre[ks]); PIN(bim[ks]); }
#pragma unroll
    for (int i = 0; i < 9; ++i) { PIN(fre[i]); PIN(fim[i]); }
  }
  STAMP(0,1);
  {
    f2 v[10];
#pragma unroll
    for (int i = 0; i < 4; ++i) { v[i] = f2{bflo(x0[i]), bfhi(x0[i])}; v[4 + i] = f2{bflo(x1[i]), bfhi(x1[i])}; }
    v[8] = f2{bflo(x2[0]), bfhi(x2[0])}; v[9] = f2{bflo(x2[1]), bfhi(x2[1])};
    ln_row(v);
    char* ub = smem + a * 1280;
    *(u4v*)(ub + 16 * j) = u4v{pk_h(v[0][0], v[0][1]), pk_h(v[1][0], v[1][1]), pk_h(v[2][0], v[2][1]), pk_h(v[3][0], v[3][1])};
    *(u4v*)(ub + 512 + 16 * j) = u4v{pk_h(v[4][0], v[4][1]), pk_h(v[5][0], v[5][1]), pk_h(v[6][0], v[6][1]), pk_h(v[7][0], v[7][1])};
    *(u2v*)(ub + 1024 + 8 * j) = u2v{pk_h(v[8][0], v[8][1]), pk_h(v[9][0], v[9][1])};
  }
  lds_signal(flags);
  lds_wait(flags, 4);
  STAMP(0,2);
  u4v b2[3];
  {
    const int qq = li >> 2, p = li & 3;
    const char* abase = smem + (8 * g + qq) * 80 + 8 * p;
    u4v af[3][4];
#pragma unroll
    for (int t = 0; t < 3; ++t)
#pragma unroll
      for (int ks = 0; ks < 4; ++ks) {
        s4 lo = __builtin_amdgcn_ds_read_tr16_b64_v4i16((LDS_AS s4*)(abase + ks * 2560 + t * 32));
        s4 hi = __builtin_amdgcn_ds_read_tr16_b64_v4i16((LDS_AS s4*)(abase + ks * 2560 + 320 + t * 32));
        u2v l2 = __builtin_bit_cast(u2v, lo), h2v = __builtin_bit_cast(u2v, hi);
        af[t][ks] = u4v{l2[0], l2[1], h2v[0], h2v[1]};
      }
    SB();
    if (wave > 0 && wave <= PFK1) touch((const char*)w_sp + ((size_t)(256 * PFG1 + blockIdx.x * PFK1 + wave - 1) * 64 + lane) * 128);
    SB();
    f4 are[3], aim[3];
#pragma unroll
    for (int t = 0; t < 3; ++t) {
      are[t] = f4{0, 0, 0, 0}; aim[t] = f4{0, 0, 0, 0};
#pragma unroll
      for (int ks = 0; ks < 4; ++ks) {
        are[t] = mfma_h(af[t][ks], bre[ks], are[t]);
        aim[t] = mfma_h(af[t][ks], bim[ks], aim[t]);
      }
    }
#pragma unroll
    for (int t = 0; t < 3; ++t) b2[t] = cmul_pack<false>(are[t], aim[t], twr[t], twi[t]);
  }
  const __amdgpu_buffer_rsrc_t rs = ws_rsrc(ws);
  f4 ore[3], oim[3];
#pragma unroll
  for (int tk = 0; tk < 3; ++tk)
#pragma unroll
    for (int to = 0; to < 3; ++to) {
      ore[to] = mfma_h(fre[to * 3 + tk], b2[tk], tk ? ore[to] : f4{0, 0, 0, 0});
      oim[to] = mfma_h(fim[to * 3 + tk], b2[tk], tk ? oim[to] : f4{0, 0, 0, 0});
    }
  u4v o[3];
#pragma unroll
  for (int to = 0; to < 3; ++to)
#pragma unroll
    for (int r = 0; r < 4; ++r) o[to][r] = pk_h(ore[to][r], oim[to][r]);
  if (q > 0) {
#pragma unroll
    for (int to = 0; to < 3; ++to) st16(rs, toff[to][0], o[to]);
  } else {
#pragma unroll
    for (int to = 0; to < 3; ++to)
#pragma unroll
      for (int r = 0; r < 4; ++r) __builtin_amdgcn_raw_buffer_store_b32(o[to][r], rs, toff[to][r], 0, 0);
    __builtin_amdgcn_raw_buffer_store_b32(lane ? o[1][0] : o[0][0], rs, lane == 0 ? toff[0][0] + 4 : (lane == 16 ? toff[1][0] + 4 : 0xfffffff0u), 0, 0);
  }
  STAMP(0,4);
}

template <int NT, int LDW, int NCG, int NTASK, bool PERM>
DI void retile(const u16* __restrict__ W, __amdgpu_buffer_rsrc_t rs, u32 dstbase, int wid, int nw, char* lds, int lane) {
  const int li = lane & 15, g = lane >> 4, q = li >> 2, p = li & 3;
  u32 dst[NT];
#pragma unroll
  for (int i = 0; i < NT; ++i) {
    const int task = wid + i * nw;
    const int t = task < NTASK ? task : 0;
    const int ks = t / NCG, cg = t - ks * NCG;
    const u16* src = W + (size_t)(ks * 32 + (lane >> 3)) * LDW + cg * 64 + (lane & 7) * 8;
#pragma unroll
    for (int j = 0; j < 4; ++j) GLDS16(src + (size_t)j * 8 * LDW, lds + i * 4096 + j * 1024);
    dst[i] = task < NTASK ? dstbase + (u32)t * 4096u + lane * 16 : 0xffff0000u;
  }
  const char* rb = lds + (PERM ? 4 * g + q : 8 * g + q) * 128 + 8 * p;
  WAITV(0);
#pragma unroll
  for (int i = 0; i < NT; ++i)
#pragma unroll
    for (int f = 0; f < 4; ++f) {
      s4 lo = __builtin_amdgcn_ds_read_tr16_b64_v4i16((LDS_AS s4*)(rb + i * 4096 + f * 32));
      s4 hi = __builtin_amdgcn_ds_read_tr16_b64_v4i16((LDS_AS s4*)(rb + i * 4096 + f * 32 + (PERM ? 16 : 4) * 128));
      u2v a = __builtin_bit_cast(u2v, lo), b = __builtin_bit_cast(u2v, hi);
      st16(rs, dst[i] + f * 1024, u4v{a[0], a[1], b[0], b[1]});
    }
}
constexpr int NRT1 = 20 * 27, NRT3 = 27 * 10;
constexpr int K2_FFT_BLOCKS = 168, K2_PREP_BLOCKS = 88, K3_PREP_BLOCKS = 68;

extern "C" __global__ __launch_bounds__(512, 2) void k2_kernel() {
  LOAD_ARGS();
  char* ws = ap_->ws; const u16* w_in = ap_->w_in; const u16* w_out = ap_->w_out; const u16* w_sp = ap_->w_sp;
  PINS(ws); PINS(w_in); PINS(w_out); PINS(w_sp);
  const u32* T = (const u32*)(ws + WS_T); const u32* Wp = (const u32*)(ws + WS_WP); const u32* W2 = (const u32*)(ws + WS_W2);
  const u32* wmax = (const u32*)(ws + WS_WMAX); u32* Tp = (u32*)(ws + WS_TP); float* unscale = (float*)(ws + WS_UNS);
  u32* Wt1 = (u32*)(ws + WS_WT1); u32* Wt3 = (u32*)(ws + WS_WT3); u32* stats = (u32*)(ws + WS_STATS);
  extern __shared__ __attribute__((aligned(16))) char smem[];
  const int tid = threadIdx.x, lane = tid & 63;
  const int wave = __builtin_amdgcn_readfirstlane(tid >> 6);
  const int li = lane & 15, g = lane >> 4;
  if (blockIdx.x >= K2_FFT_BLOCKS) {
    STAMP(1,0);
    const int wid = (blockIdx.x - K2_FFT_BLOCKS) * 8 + wave;
    const __amdgpu_buffer_rsrc_t rs = ws_rsrc(ws);
    if (wid < 256) st16(rs, (u32)WS_STATS + wid * 1024 + lane * 16, u4v{~0u, ~0u, ~0u, ~0u});
    else if (wid < 264) st16(rs, (u32)WS_CNT + (wid - 256) * 1024 + lane * 16, u4v{0u, 0u, 0u, 0u});
    if (PFK2 && (wave & 1) == 0) {
      char* scratch = smem + 65536;
      const int task = 256 * (PFG1 + PFK1) + (wid >> 1);
      if (task < 1024) touch((const char*)w_sp + ((size_t)task * 64 + lane) * 128);
    }
    const int pb = blockIdx.x - K2_FFT_BLOCKS;
    const int task = wave < 6 ? pb * 6 + wave : (wave == 6 && pb < 12 ? 528 + pb : NRT1);
    if (task < NRT1) retile<1, FF, 27, NRT1, false>(w_in, rs, (u32)WS_WT1, task, 0, smem + wave * 8192, lane);
    else if (PFK2B && wave == 7) {
      char* scratch = smem + 65536;
#pragma unroll
      for (int i = 0; i < PFK2B; ++i) touch((const char*)w_sp + ((size_t)(256 * (PFG1 + PFK1) + pb * PFK2B + i) * 64 + lane) * 128);
    }
    STAMP(1,1);
    return;
  }
  const int P = (blockIdx.x & 7) * 21 + (blockIdx.x >> 3);
  if (P > 160) return;
  const int u = wave & 3, hh = wave >> 2;
  const int U = 4 * P + u;
  STAMP(2,0);
#pragma unroll
  for (int i = 0; i < 2; ++i) {
    const int bb = (wave * 2 + i) * 16 + (lane >> 2);
    GLDS16(T + ((size_t)bb * TROW_UNITS + 4 * P + ((lane & 3) ^ ((lane >> 4) & 3))) * 4, smem + (wave * 2 + i) * 1024);
  }
  const u4v mxv = *(const u4v*)(wmax + (lane < NPREP / 4 ? lane * 4 : 0));
  u4v Wv[2];
#pragma unroll
  for (int t = 0; t < 2; ++t) Wv[t] = *(const u4v*)(Wp + ((size_t)((U < NUNITS ? U : 0) * 4 + 2 * hh + t) * 64 + lane) * 4);
  const bool special = P >= 120;
  const int cB = special ? 4 : (P / 40 + 1);
  const int ur = special ? U - 480 : U % 160;
  const bool e320 = special ? ur == 160 : ur == 80;
  const bool edge = hh == 0 && (ur == 0 || e320);
  u4v w2, w4;
  if (edge) {
    w2 = *(const u4v*)(W2 + ((special ? 0 : cB) * 2 + (e320 ? 1 : 0)) * 256 + lane * 4);
    w4 = *(const u4v*)(W2 + (4 * 2 + (e320 ? 1 : 0)) * 256 + lane * 4);
  }
  STAMP(2,5);
  {
    char* cb = smem + 16384 + wave * 2048 + lane * 16;
    const int cc = wave < 4 ? cB : 0;
    const float inv = u < 2 ? (1.0f / 128.0f) : (1.0f / 2048.0f);
    const float amp = u == 0 ? 1.f : ((u == 1 && special) ? 0.125f : 0.25f);
    f4 c, s;
#pragma unroll
    for (int jj = 0; jj < 4; ++jj) {
      const int a = (u & 1) ? li * (cc + 8 * (4 * g + jj)) : (4 * g + jj) * (cc + 8 * li);
      const float ang = (float)a * inv;
      c[jj] = cosr(ang) * amp; s[jj] = sinr(ang) * amp;
    }
    if (u < 2) {
      *(u4v*)(cb) = u4v{pk_h(c[0], s[0]), pk_h(c[1], s[1]), pk_h(c[2], s[2]), pk_h(c[3], s[3])};
      *(u4v*)(cb + 1024) = u4v{pk_h(-s[0], c[0]), pk_h(-s[1], c[1]), pk_h(-s[2], c[2]), pk_h(-s[3], c[3])};
    } else {
      *(f4*)(cb) = c;
      *(f4*)(cb + 1024) = s;
    }
  }
  asm volatile("s_waitcnt lgkmcnt(0)" ::: "memory");
  __builtin_amdgcn_s_barrier();
  asm volatile("" ::: "memory");
  STAMP(2,7);
  u4v Fre, Fim, P1re[2], P1im[2], P4re[2], P4im[2];
  f4 tfr[2], tfs[2], tir[2], tis[2];
  {
    const char* cb = smem + 16384 + lane * 16;
    Fre = *(const u4v*)(cb + 4 * 2048); Fim = *(const u4v*)(cb + 4 * 2048 + 1024);
#pragma unroll
    for (int c = 0; c < 2; ++c) {
      const char* cs = cb + ((c == 0 && special) ? 8192 : 0);
      P1re[c] = *(const u4v*)(cs); P1im[c] = *(const u4v*)(cs + 1024);
      P4re[c] = *(const u4v*)(cs + 2048); P4im[c] = *(const u4v*)(cs + 3072);
      tfr[c] = *(const f4*)(cs + 4096); tfs[c] = *(const f4*)(cs + 5120);
      tir[c] = *(const f4*)(cs + 6144); tis[c] = *(const f4*)(cs + 7168);
    }
    PIN(Fre); PIN(Fim);
#pragma unroll
    for (int c = 0; c < 2; ++c) { PIN(P1re[c]); PIN(P1im[c]); PIN(P4re[c]); PIN(P4im[c]); PIN(tfr[c]); PIN(tfs[c]); PIN(tir[c]); PIN(tis[c]); }
  }
  STAMP(2,1);
  asm volatile("s_waitcnt vmcnt(0)" ::: "memory");
  __builtin_amdgcn_s_barrier();
  asm volatile("" ::: "memory");
  STAMP(2,2);
  const int loff = (64 * g + li) * 64 + ((u ^ (li >> 2)) & 3) * 16 + hh * 8;
  u4v X[2];
#pragma unroll
  for (int jj = 0; jj < 4; ++jj) {
    const u2v L = *(const u2v*)(smem + loff + jj * 1024);
    X[0][jj] = L[0]; X[1][jj] = L[1];
  }
  if (special) {
#pragma unroll
    for (int jj = 0; jj < 4; ++jj) {
      const u32 au = X[0][jj], bu = X[1][jj] ^ 0x80000000u;
      const h2 A = __builtin_bit_cast(h2, au);
      const h2 Bc = __builtin_bit_cast(h2, bu);
      X[0][jj] = __builtin_bit_cast(u32, A + Bc);
      X[1][jj] = __builtin_bit_cast(u32, A - Bc);
    }
  }
  const f4 z4 = f4{0, 0, 0, 0};
  f4 re[2], im[2];
#pragma unroll
  for (int t = 0; t < 2; ++t) { re[t] = mfma_h(X[t], P1re[t], z4); im[t] = mfma_h(X[t], P1im[t], z4); }
  const u32 mx = max64u(max(max(mxv[0], mxv[1]), max(mxv[2], mxv[3])));
  int E = (int)(mx >> 7);
  E = E < 1 ? 1 : (E > 253 ? 253 : E);
  const float sig = __uint_as_float((u32)(254 - E) << 23) * 0.25f;
#pragma unroll
  for (int t = 0; t < 2; ++t) {
    const u4v v = cmul_pack<false>(re[t], im[t], tfr[t], -tfs[t]);
    re[t] = mfma_h(Fre, v, z4); im[t] = mfma_h(Fim, v, z4);
  }
  f4 wr4[2], wi4[2];
#pragma unroll
  for (int t = 0; t < 2; ++t)
#pragma unroll
    for (int r = 0; r < 4; ++r) { wr4[t][r] = bflo(Wv[t][r]); wi4[t][r] = bfhi(Wv[t][r]); }
  if (edge) {
#pragma unroll
    for (int r = 0; r < 4; ++r) {
      wr4[0][r] = (wr4[0][r] + bflo(w2[r])) * 0.5f; wi4[0][r] = (wi4[0][r] + bfhi(w2[r])) * 0.5f;
      if (special) { wr4[1][r] = (wr4[1][r] + bflo(w4[r])) * 0.5f; wi4[1][r] = (wi4[1][r] + bfhi(w4[r])) * 0.5f; }
    }
    if (U == 0 && lane == 0) unscale[0] = __uint_as_float((u32)E << 23);
  }
#pragma unroll
  for (int t = 0; t < 2; ++t) {
    const u4v v = cmul_pack<true>(re[t], im[t], wr4[t] * sig, wi4[t] * sig);
    im[t] = mfma_h(v, Fre, z4); re[t] = mfma_h(v, Fim, z4);
  }
#pragma unroll
  for (int t = 0; t < 2; ++t) {
    const u4v v = cmul_pack<true>(re[t], im[t], tir[t], tis[t]);
    im[t] = mfma_h(P4re[t], v, z4); re[t] = mfma_h(P4im[t], v, z4);
  }
  STAMP(2,3);
  if (special) {
    const f4 yr = re[0], yi = im[0], zr = re[1], zi = im[1];
    re[0] = yr + zr; im[0] = yi + zi;
    re[1] = yr - zr; im[1] = zi - yi;
  }
#pragma unroll
  for (int r = 0; r < 4; ++r) *(u2v*)(smem + loff + r * 1024) = u2v{pk_h(re[0][r], im[0][r]), pk_h(re[1][r], im[1][r])};
  asm volatile("s_waitcnt lgkmcnt(0)" ::: "memory");
  __builtin_amdgcn_s_barrier();
  asm volatile("" ::: "memory");
  const __amdgpu_buffer_rsrc_t rs = ws_rsrc(ws);
#pragma unroll
  for (int i = 0; i < 2; ++i) {
    const u4v o = *(const u4v*)(smem + (wave * 2 + i) * 1024 + lane * 16);
    st16(rs, (u32)WS_TP + P * 16384 + (wave * 2 + i) * 1024 + (lane & ~3) * 16 + ((lane & 3) ^ ((lane >> 4) & 3)) * 16, o);
  }
  { char* scratch = smem + 49152; if (wave == 0) CODE_TOUCH(k3_kernel, g1_kernel, 0) }
  STAMP(2,4);
}

extern "C" __global__ __launch_bounds__(256, 2) void k3_kernel() {
  LOAD_ARGS();
  char* ws = ap_->ws; const u16* x = ap_->x; const u16* w_sp = ap_->w_sp;
  PINS(x); PINS(ws); PINS(w_sp);
  const u32* Tp = (const u32*)(ws + WS_TP);
  constexpr int K3_Y = 10752, K3_CB = K3_Y + 10240;
  __shared__ __attribute__((aligned(16))) char smem[K3_CB + 8192 + 512];
  const int tid = threadIdx.x, lane = tid & 63;
  const int wave = __builtin_amdgcn_readfirstlane(tid >> 6);
  const int li = lane & 15, g = lane >> 4;
  const int b = (blockIdx.x & 7) * 32 + (blockIdx.x >> 3);
  STAMP(3,0);
#pragma unroll
  for (int i = 0; i < 2; ++i) {
    int U = (wave * 2 + i) * 64 + lane;
    U = U < 480 ? U : 479;
    GLDS16(Tp + (size_t)(U >> 2) * 4096 + b * 16 + (U & 3) * 4, smem + (wave * 2 + i) * 1024);
  }
#pragma unroll
  for (int it = 0; it < 4; ++it) {
    const int i = it < 2 ? wave * 2 + it : 6 + it;
    if (it < 2 || wave == 3) {
      const int k2 = (lane >> 2) + 16 * (4 * i + (lane & 3));
      const int pos = k2 <= 320 ? 2 * k2 : 2 * (640 - k2) + 1;
      GLDS4(Tp + (size_t)(120 + (pos >> 4)) * 4096 + b * 16 + (pos & 15), smem + 8192 + i * 256);
    }
  }
  char* scratch = smem + K3_CB + 8192;
  if (wave == 0) GLDS4(ws + WS_UNS, scratch + 256);
  const int a = 2 * wave + (lane >> 5), j = lane & 31;
  const int row = 256 * a + b;
  const u16* xr = x + (size_t)row * D;
  u4v x0 = *(const u4v*)(xr + 8 * j);
  u4v x1 = *(const u4v*)(xr + 256 + 8 * j);
  u2v x2 = *(const u2v*)(xr + 512 + 4 * j);
  STAMP(3,5);
  char* cb = smem + K3_CB;
  const int t = wave;
  u4v Bre[3], Bim[3];
  if (wave < 3) {
#pragma unroll
    for (int tk = 0; tk < 3; ++tk) {
      const float sc = (tk < 2 || g < 2) ? 1.f : 0.f;
#pragma unroll
      for (int jj = 0; jj < 4; ++jj) {
        const float ang = (float)((16 * tk + 4 * g + jj) * (16 * t + li)) * (1.0f / 40.0f);
        const float c = cosr(ang) * sc, s = sinr(ang) * sc;
        Bre[tk][jj] = pk_h(c, -s);
        Bim[tk][jj] = pk_h(s, c);
      }
      PIN(Bre[tk]); PIN(Bim[tk]);
    }
  } else {
#pragma unroll
    for (int tt = 0; tt < 3; ++tt) {
      f4 twr, twi;
#pragma unroll
      for (int jj = 0; jj < 4; ++jj) {
        const float ang = (float)((16 * tt + li) * (4 * g + jj)) * (1.0f / 640.0f);
        twr[jj] = cosr(ang) * 0.125f; twi[jj] = sinr(ang) * 0.125f;
      }
      *(f4*)(cb + tt * 2048 + lane * 16) = twr;
      *(f4*)(cb + tt * 2048 + 1024 + lane * 16) = twi;
    }
    u4v gre, gim;
#pragma unroll
    for (int jj = 0; jj < 4; ++jj) {
      const float ang = (float)(li * (4 * g + jj)) * 0.0625f;
      const float c = cosr(ang), s = sinr(ang);
      gre[jj] = pk_h(c, -s); gim[jj] = pk_h(s, c);
    }
    *(u4v*)(cb + 6144 + lane * 16) = gre;
    *(u4v*)(cb + 7168 + lane * 16) = gim;
  }
  STAMP(3,1);
  asm volatile("s_waitcnt vmcnt(0)" ::: "memory");
  PIN(x0); PIN(x1); PIN(x2);
  __syncthreads();
  STAMP(3,2);
  char* yb = smem + K3_Y;
  if (wave < 3) {
    const f4 z4 = f4{0, 0, 0, 0};
    u4v af[4][3];
#pragma unroll
    for (int q = 0; q < 4; ++q) {
      const char* base = smem + (q == 0 ? 8192 : (q - 1) * 2560);
#pragma unroll
      for (int tk = 0; tk < 3; ++tk) af[q][tk] = *(const u4v*)(base + (tk < 2 ? (64 * tk + lane) : (128 + (lane & 31))) * 16);
    }
    const f4 twr = *(const f4*)(cb + t * 2048 + lane * 16), twi = *(const f4*)(cb + t * 2048 + 1024 + lane * 16);
    const u4v Gre = *(const u4v*)(cb + 6144 + lane * 16), Gim = *(const u4v*)(cb + 7168 + lane * 16);
    const float usc = *(const float*)(scratch + 256);
    SB();
    f4 vre[4], vim[4];
#pragma unroll
    for (int q = 0; q < 4; ++q) {
      f4 re = z4, im = z4;
#pragma unroll
      for (int tk = 0; tk < 3; ++tk) {
        re = mfma_h(af[q][tk], Bre[tk], re);
        im = mfma_h(af[q][tk], Bim[tk], im);
      }
      vre[q] = re; vim[q] = im;
    }
#pragma unroll
    for (int q = 0; q < 4; ++q) {
      const u4v v = cmul_pack<false>(vre[q], vim[q], twr, twi);
      vre[q] = mfma_h(v, Gre, z4);
      vim[q] = mfma_h(v, Gim, z4);
    }
    const float fs = usc * (2.0f * 1024.0f * 128.0f / (2048.0f * 640.0f));
    const float fh = fs * 0.70710678118f;
    const f4 E = (vre[0] + vim[0]) * fs, O = (vre[0] - vim[0]) * fs;
    const f4 s13 = vre[1] + vre[3], dx = vre[1] - vre[3], sy = vim[1] + vim[3], dy = vim[3] - vim[1];
    const f4 Ep = E + vre[2] * fs, Em = E - vre[2] * fs, Om = O - vim[2] * fs, Op = O + vim[2] * fs;
    const f4 A = dx - sy, B = dx + sy;
    f4 y[8];
    y[0] = Ep + s13 * fs; y[4] = Ep - s13 * fs; y[2] = Em + dy * fs; y[6] = Em - dy * fs;
    y[1] = Om + A * fh; y[5] = Om - A * fh; y[3] = Op - B * fh; y[7] = Op + B * fh;
    if (t < 2 || lane < 32) {
      char* yp = yb + (40 * li + 16 * t + 4 * g) * 2;
#pragma unroll
      for (int aa = 0; aa < 8; ++aa) *(u2v*)(yp + aa * 1280) = u2v{pk_bf(y[aa][0], y[aa][1]), pk_bf(y[aa][2], y[aa][3])};
    }
  }
  else {
    CODE_TOUCH(g1_kernel, g2_kernel, (blockIdx.x >> 3) & 1)
#pragma unroll
    for (int i = 0; i < 4; ++i) {
      const int task = 256 * (PFG1 + PFK1) + 88 * PFK2B + i * 256 + blockIdx.x;
      if (task < 1024) touch((const char*)w_sp + ((size_t)task * 64 + lane) * 128);
    }
  }
  asm volatile("s_waitcnt lgkmcnt(0)" ::: "memory");
  __builtin_amdgcn_s_barrier();
  asm volatile("" ::: "memory");
  STAMP(3,3);
  {
    const __amdgpu_buffer_rsrc_t rs = ws_rsrc(ws);
    const u4v y0 = *(const u4v*)(yb + a * 1280 + 16 * j);
    const u4v y1 = *(const u4v*)(yb + a * 1280 + 512 + 16 * j);
    const u2v y2 = *(const u2v*)(yb + a * 1280 + 1024 + 8 * j);
    u32 hx[10];
#pragma unroll
    for (int i = 0; i < 4; ++i) {
      hx[i] = pk_bf(bflo(x0[i]) + bflo(y0[i]), bfhi(x0[i]) + bfhi(y0[i]));
      hx[4 + i] = pk_bf(bflo(x1[i]) + bflo(y1[i]), bfhi(x1[i]) + bfhi(y1[i]));
    }
    hx[8] = pk_bf(bflo(x2[0]) + bflo(y2[0]), bfhi(x2[0]) + bfhi(y2[0]));
    hx[9] = pk_bf(bflo(x2[1]) + bflo(y2[1]), bfhi(x2[1]) + bfhi(y2[1]));
    auto pairup = [&](u32 v0, u32 v1) {
      return u4v{v0, v1, (u32)__builtin_amdgcn_update_dpp(0, (int)v0, 0xB1, 0xf, 0xf, true), (u32)__builtin_amdgcn_update_dpp(0, (int)v1, 0xB1, 0xf, 0xf, true)};
    };
    const u32 drop = (lane & 1) ? 0xfffffff0u : 0u;
    const u32 ho = (u32)WS_H + row * (D * 2);
    st16(rs, ho + 16 * j, u4v{hx[0], hx[1], hx[2], hx[3]});
    st16(rs, ho + 512 + 16 * j, u4v{hx[4], hx[5], hx[6], hx[7]});
    st16(rs, (ho + 1024 + 8 * j) | drop, pairup(hx[8], hx[9]));
    f2 v[10];
#pragma unroll
    for (int i = 0; i < 10; ++i) v[i] = f2{bflo(hx[i]), bfhi(hx[i])};
    ln_row(v);
#pragma unroll
    for (int i = 0; i < 10; ++i) hx[i] = pk_bf(v[i][0], v[i][1]);
    const u32 lo = (u32)WS_L2 + (row >> 4) * 20480 + (row & 15) * 64;
    st16(rs, lo + (j >> 2) * 1024 + (j & 3) * 16, u4v{hx[0], hx[1], hx[2], hx[3]});
    st16(rs, lo + (8 + (j >> 2)) * 1024 + (j & 3) * 16, u4v{hx[4], hx[5], hx[6], hx[7]});
    st16(rs, (lo + (16 + (j >> 3)) * 1024 + (j & 7) * 8) | drop, pairup(hx[8], hx[9]));
  }
  STAMP(3,4);
}

#define FPD 2
#include <type_traits>
typedef unsigned long long u64;
typedef __attribute__((address_space(1))) unsigned long long gu64;
#define WAITL0() do { __builtin_amdgcn_sched_barrier(0); asm volatile("s_waitcnt lgkmcnt(0)" ::: "memory"); __builtin_amdgcn_sched_barrier(0); } while (0)
#define BAR() do { asm volatile("" ::: "memory"); __builtin_amdgcn_sched_barrier(0); __builtin_amdgcn_s_barrier(); __builtin_amdgcn_sched_barrier(0); asm volatile("" ::: "memory"); } while (0)
#define SGB(mask, n) __builtin_amdgcn_sched_group_barrier(mask, n, 0)
DI f4 mfma_b(u4v a, u4v b, f4 c) {
  return __builtin_amdgcn_mfma_f32_16x16x32_bf16(__builtin_bit_cast(bf8, a), __builtin_bit_cast(bf8, b), c, 0, 0, 0);
}
DI f2 gelu2(f2 x) {
  const float k1 = -2.0f * 0.7978845608028654f * 1.4426950408889634f;
  const float k2 = k1 * 0.044715f;
  f2 p = x * x * k2 + k1;
  f2 a = x * p;
  f2 e = f2{__builtin_amdgcn_exp2f(a[0]), __builtin_amdgcn_exp2f(a[1])} + 1.0f;
  f2 r = f2{__builtin_amdgcn_rcpf(e[0]), __builtin_amdgcn_rcpf(e[1])};
  return x * r;
}
DI u2v gelu4(f4 a) {
  f2 g0 = gelu2(f2{a[0], a[1]}), g1 = gelu2(f2{a[2], a[3]});
  return u2v{pk_bf(g0[0], g0[1]), pk_bf(g1[0], g1[1])};
}
template <int N> DI void waitv() { asm volatile("s_waitcnt vmcnt(%0)" ::"n"(N) : "memory"); }

template <int KT, int NST, int NLA, class F>
DI void loader_loop(bool big, F&& stage) {
#pragma unroll
  for (int s = 0; s < NST - 1; ++s) stage(s, s);
  int nbuf = NST - 1;
  for (int kt = 0; kt < KT - (NST - 1); ++kt) {
    if (big) waitv<NLA*(NST - 2)>(); else waitv<(NLA - 1) * (NST - 2)>();
    BAR();
    stage(kt + NST - 1, nbuf);
    nbuf = nbuf + 1 == NST ? 0 : nbuf + 1;
  }
  if constexpr (NST >= 7) { if (big) waitv<NLA * 5>(); else waitv<(NLA - 1) * 5>(); BAR(); }
  if constexpr (NST >= 6) { if (big) waitv<NLA * 4>(); else waitv<(NLA - 1) * 4>(); BAR(); }
  if constexpr (NST >= 5) { if (big) waitv<NLA * 3>(); else waitv<(NLA - 1) * 3>(); BAR(); }
  if (big) waitv<NLA * 2>(); else waitv<(NLA - 1) * 2>(); BAR();
  if (big) waitv<NLA>(); else waitv<NLA - 1>(); BAR();
  waitv<0>(); BAR();
}

#define G1_DEPTH 3
extern "C" __global__ __launch_bounds__(512) void g1_kernel() {
  LOAD_ARGS();
  char* ws = ap_->ws; const u16* w_sp = ap_->w_sp; const u16* w_out = ap_->w_out;
  PINS(ws); PINS(w_sp); PINS(w_out);
  const u32* l2f = (const u32*)(ws + WS_L2); const u32* Wt1 = (const u32*)(ws + WS_WT1);
  u32* z1f = (u32*)(ws + WS_Z1); u16* z2t = (u16*)(ws + WS_Z2); u64* stats = (u64*)(ws + WS_STATS);
  __shared__ __attribute__((aligned(16))) char smem[140 * 1024 + 4 * 4608 + 256];
  const int tid = threadIdx.x, lane = tid & 63;
  const int wave = __builtin_amdgcn_readfirstlane(tid >> 6);
  const int li = lane & 15, g = lane >> 4;
  STAMP(4,0);
  const int xcd = blockIdx.x & 7, idx = blockIdx.x >> 3;
#define G1_MAP 1
  const int mt = G1_MAP == 0 ? xcd * 2 + (idx >> 4) : (G1_MAP == 1 ? (xcd >> 1) * 4 + (idx >> 3) : (xcd >> 2) * 8 + (idx >> 2));
  const int nb = G1_MAP == 0 ? (idx & 15) : (G1_MAP == 1 ? (xcd & 1) * 8 + (idx & 7) : (xcd & 3) * 4 + (idx & 3));
  const int row0 = 1024 * (mt & 1) + 32 * (mt >> 1);
  const int zs = nb < 6 ? 4 * nb : 24 + 3 * (nb - 6);
  const int z1s = nb < 6 ? 3 * nb : (nb < 12 ? 18 + 4 * (nb - 6) : 42 + 3 * (nb - 12));
  const int type = nb < 6 ? 0 : (nb < 12 ? 1 : 2);
  if (wave >= 4) {
    const int lw = wave - 4;
    auto slot_frag = [&](int s) { return s < 3 ? 54 + zs + s : (s == 3 ? (type == 0 ? 54 + zs + 3 : (type == 1 ? z1s + 3 : z1s)) : z1s + s - 4); };
    const u32* bsrc[4];
#pragma unroll
    for (int j = 0; j < 4; ++j) {
      int i = lw + 4 * j; i = i < 14 ? i : 0;
      const int kk = i / 7, s = i - 7 * kk;
      bsrc[j] = Wt1 + (size_t)(kk * 108 + slot_frag(s)) * 256 + lane * 4;
    }
    auto stage = [&](int kt) {
      char* sb = smem + kt * 14336 + lw * 1024;
#pragma unroll
      for (int j = 0; j < 3; ++j) GLDS16(bsrc[j] + (size_t)kt * 2 * 108 * 256, sb + j * 4096);
      if (lw < 2) GLDS16(bsrc[3] + (size_t)kt * 2 * 108 * 256, sb + 3 * 4096);
    };
#pragma unroll
    for (int s = 0; s < G1_DEPTH; ++s) stage(s);
    for (int kt = 0; kt < 10 - G1_DEPTH; ++kt) {
      if (lw < 2) waitv<4 * (G1_DEPTH - 1)>(); else waitv<3 * (G1_DEPTH - 1)>();
      BAR();
      stage(kt + G1_DEPTH);
    }
    if constexpr (G1_DEPTH >= 5) { if (lw < 2) waitv<16>(); else waitv<12>(); BAR(); }
    if constexpr (G1_DEPTH >= 4) { if (lw < 2) waitv<12>(); else waitv<9>(); BAR(); }
    if (lw < 2) waitv<8>(); else waitv<6>(); BAR();
    if (lw < 2) waitv<4>(); else waitv<3>(); BAR();
    waitv<0>();
    char* scratch = smem + 140 * 1024 + 4 * 4608;
    if (lw < PFG1) touch((const char*)w_sp + ((size_t)(blockIdx.x * PFG1 + lw) * 64 + lane) * 128);
    if (lw >= 2) {
      CODE_TOUCH(g2_kernel, end_kernel, lw - 2)
      const int task = (lw - 2) * 256 + blockIdx.x;
      BAR();
      if (task < NRT3) retile<1, D, 10, NRT3, true>(w_out, ws_rsrc(ws), (u32)WS_WT3, task, 0, smem + lw * 4096, lane);
    } else {
      BAR();
      if (lw == 0) CODE_TOUCH(g2_kernel, end_kernel, 2)
    }
    return;
  }
  const int rg0 = (row0 + wave * 256) >> 4;
  const u32* asrc = l2f + (size_t)rg0 * 20 * 256 + li * 16 + g * 4;
#define G1_AD 5
  u4v fa[G1_AD][2];
#pragma unroll
  for (int k = 0; k < G1_AD; ++k) { fa[k][0] = *(const u4v*)(asrc + k * 256); fa[k][1] = *(const u4v*)(asrc + (20 + k) * 256); __builtin_amdgcn_sched_barrier(0); }
  f4 acc[7][2];
#pragma unroll
  for (int s = 0; s < 7; ++s) { acc[s][0] = f4{0, 0, 0, 0}; acc[s][1] = f4{0, 0, 0, 0}; }
  u4v fw[2][7];
  const char* wb = smem + lane * 16;
  auto rd = [&](int k) {
#pragma unroll
    for (int s = 0; s < 7; ++s) fw[k & 1][s] = *(const u4v*)(wb + (k * 7 + s) * 1024);
  };
  auto mm = [&](int k) {
#pragma unroll
    for (int s = 0; s < 7; ++s) {
      acc[s][0] = mfma_b(fw[k & 1][s], fa[k % G1_AD][0], acc[s][0]);
      acc[s][1] = mfma_b(fw[k & 1][s], fa[k % G1_AD][1], acc[s][1]);
    }
    if (k + G1_AD < 20) { fa[k % G1_AD][0] = *(const u4v*)(asrc + (k + G1_AD) * 256); fa[k % G1_AD][1] = *(const u4v*)(asrc + (20 + G1_AD + k) * 256); }
  };
  BAR(); STAMP(4,3); rd(0);
#pragma unroll
  for (int k = 1; k < 20; ++k) {
    if (!(k & 1)) BAR(); else __builtin_amdgcn_sched_barrier(0);
    if (k == 8) { STAMP(4,6); STAMPC(7,0); }
    if (k == 14) { STAMP(4,7); STAMPC(7,1); }
    rd(k);
    mm(k - 1);
#pragma unroll
    for (int i = 0; i < 7; ++i) { SGB(0x100, 1); SGB(0x8, 2); }
  }
  __builtin_amdgcn_sched_barrier(0);
  mm(19);
  STAMP(4,1);
  const int n2 = type == 0 ? 4 : 3;
  f4 zv[4][2];
  u2v z3[2];
  float lsum[2], lm2[2];
#pragma unroll
  for (int sf = 0; sf < 2; ++sf) {
    f2 s = f2{0.f, 0.f};
#pragma unroll
    for (int j = 0; j < 4; ++j) {
      const u2v t = gelu4(acc[j][sf]);
      if (j == 3) z3[sf] = t;
      zv[j][sf] = f4{bflo(t[0]), bfhi(t[0]), bflo(t[1]), bfhi(t[1])};
      if (j < 3 || type == 0) s += f2{zv[j][sf][0], zv[j][sf][1]} + f2{zv[j][sf][2], zv[j][sf][3]};
    }
    const float st = xsum_g(s[0] + s[1]);
    const float lmean = st * (type == 0 ? (1.0f / 64.0f) : (1.0f / 48.0f));
    f2 q = f2{0.f, 0.f};
#pragma unroll
    for (int j = 0; j < 4; ++j) {
      const f4 d = zv[j][sf] - lmean;
      if (j < 3 || type == 0) { q += f2{d[0], d[1]} * f2{d[0], d[1]}; q += f2{d[2], d[3]} * f2{d[2], d[3]}; }
    }
    lsum[sf] = st; lm2[sf] = xsum_g(q[0] + q[1]);
  }
  gu64* st = (gu64*)stats + (size_t)mt * 16 * 128 + wave * 32;
  if (lane < 32) {
    u32 a = __float_as_uint(g ? lsum[1] : lsum[0]), b = __float_as_uint(g ? lm2[1] : lm2[0]);
    if (a == ~0u) a = 0x7fc00000u;
    __hip_atomic_store(st + nb * 128 + lane, ((u64)b << 32) | a, __ATOMIC_RELAXED, __HIP_MEMORY_SCOPE_AGENT);
  }
  __builtin_amdgcn_sched_barrier(0);
  STAMP(4,2);
  const __amdgpu_buffer_rsrc_t rs = ws_rsrc(ws);
  u4v z1o[3];
#pragma unroll
  for (int j = 0; j < 3; ++j) {
    const u2v a = gelu4(acc[4 + j][0]), b = gelu4(acc[4 + j][1]);
    z1o[j] = u4v{a[0], a[1], b[0], b[1]};
    PIN(z1o[j]);
  }
  __builtin_amdgcn_sched_barrier(0);
  u64 gv[2][4];
  for (int spin = 0; spin < (1 << 16); ++spin) {
    bool ok = true;
#pragma unroll
    for (int sf = 0; sf < 2; ++sf)
#pragma unroll
      for (int i = 0; i < 4; ++i) {
        gv[sf][i] = __hip_atomic_load(st + (4 * g + i) * 128 + sf * 16 + li, __ATOMIC_RELAXED, __HIP_MEMORY_SCOPE_AGENT);
        ok &= ((u32)gv[sf][i] != ~0u);
      }
    if (__all(ok)) break;
    __builtin_amdgcn_s_sleep(1);
  }
  STAMP(4,4);
  {
    const u32 zo = (u32)WS_Z1 + ((rg0 >> 1) * 54 + z1s) * 1024 + lane * 16;
#pragma unroll
    for (int j = 0; j < 3; ++j) st16(rs, zo + j * 1024, z1o[j]);
    if (type == 1) st16(rs, zo + 3 * 1024, u4v{z3[0][0], z3[0][1], z3[1][0], z3[1][1]});
  }
  char* tb = smem + 140 * 1024 + wave * 4608;
#pragma unroll
  for (int sf = 0; sf < 2; ++sf) {
    float s = 0.f;
#pragma unroll
    for (int i = 0; i < 4; ++i) s += __uint_as_float((u32)gv[sf][i]);
    const float mean = xsum_g(s) * (1.0f / 864.0f);
    float q = 0.f;
#pragma unroll
    for (int i = 0; i < 4; ++i) {
      const bool big = (4 * g + i) < 6;
      const float dm = __uint_as_float((u32)gv[sf][i]) * (big ? (1.0f / 64.0f) : (1.0f / 48.0f)) - mean;
      q += __uint_as_float((u32)(gv[sf][i] >> 32)) + (big ? 64.f : 48.f) * dm * dm;
    }
    const float var = xsum_g(q) * (1.0f / 864.0f);
    const float mb = rbf(mean);
    const float rinv = __builtin_amdgcn_rcpf(rbf(__builtin_amdgcn_sqrtf(rbf(rbf(var) + 1e-5f))));
#pragma unroll
    for (int j = 0; j < 4; ++j) {
      const f4 o = (zv[j][sf] - mb) * rinv;
      *(u2v*)(tb + (sf * 16 + li) * 144 + j * 32 + g * 8) = u2v{pk_bf(o[0], o[1]), pk_bf(o[2], o[3])};
    }
  }
  {
    const int q = li >> 2, p = li & 3;
#pragma unroll
    for (int j = 0; j < 4; ++j) {
      s4 lo = __builtin_amdgcn_ds_read_tr16_b64_v4i16((LDS_AS s4*)(tb + (8 * g + q) * 144 + j * 32 + 8 * p));
      s4 hi = __builtin_amdgcn_ds_read_tr16_b64_v4i16((LDS_AS s4*)(tb + (8 * g + 4 + q) * 144 + j * 32 + 8 * p));
      u2v a = __builtin_bit_cast(u2v, lo), b = __builtin_bit_cast(u2v, hi);
      if (j < n2) st16(rs, (u32)WS_Z2 + (((zs + j) * 16 + li) * S + row0 + wave * 256 + 8 * g) * 2, u4v{a[0], a[1], b[0], b[1]});
    }
  }
  STAMP(4,5);
}

typedef __attribute__((address_space(1))) unsigned int gu32;
#define PSLEEP 1
constexpr int G2_NST = 7, G2_STB = 22 * 1024;
extern "C" __global__ __launch_bounds__(512) void g2_kernel() {
  LOAD_ARGS();
  char* ws = ap_->ws; const u16* w_sp = ap_->w_sp; const u16* b_sp = ap_->b_sp; u16* out = ap_->out;
  PINS(w_sp); PINS(b_sp); PINS(ws); PINS(out);
  const u16* z2t = (const u16*)(ws + WS_Z2); const u32* z1f = (const u32*)(ws + WS_Z1);
  __shared__ __attribute__((aligned(16))) char smem[G2_NST * G2_STB + 256];
  const int tid = threadIdx.x, lane = tid & 63;
  const int wave = __builtin_amdgcn_readfirstlane(tid >> 6);
  const int li = lane & 15, g = lane >> 4;
  STAMP(5,0);
  const int xcd = blockIdx.x & 7, idx = blockIdx.x >> 3;
  const int tt = (xcd >> 1) * 8 + (idx >> 2), ft = (xcd & 1) * 4 + (idx & 3);
  if (wave >= 4) {
    const int lw = wave - 4;
    const u16* src[6];
    const int swz = ((lane & 7) ^ (lane >> 3)) * 8;
#pragma unroll
    for (int j = 0; j < 6; ++j) {
      int i = lw + 4 * j; i = i < 22 ? i : 8;
      if (j < 2) src[j] = w_sp + (size_t)(tt * 64 + 8 * i + (lane >> 3)) * S + swz;
      else { int r = ft * 112 + 8 * (i - 8) + (lane >> 3); r = r < FH ? r : FH - 1; src[j] = z2t + (size_t)r * S + swz; }
    }
    auto stage = [&](int kt, int buf) {
      char* sb = smem + buf * G2_STB + lw * 1024;
      const int kk = kt * 64;
#pragma unroll
      for (int j = 0; j < 5; ++j) GLDS16(src[j] + kk, sb + j * 4096);
      if (lw < 2) GLDS16(src[5] + kk, sb + 5 * 4096);
    };
    const bool big = lw < 2;
    stage(0, 0); stage(1, 1); stage(2, 2);
    if (big) waitv<12>(); else waitv<10>(); BAR();
    stage(3, 3); stage(4, 4);
    if (big) waitv<18>(); else waitv<15>(); BAR();
    stage(5, 5); stage(6, 6);
    if (big) waitv<24>(); else waitv<20>(); BAR();
    stage(7, 0); stage(8, 1);
    int nbuf = 2;
    for (int kt = 3; kt < 26; ++kt) {
      if (big) waitv<30>(); else waitv<25>();
      BAR();
      stage(kt + 6, nbuf);
      nbuf = nbuf + 1 == G2_NST ? 0 : nbuf + 1;
    }
    const u32* wsrc = (const u32*)(ws + WS_WT3) + (size_t)(ft * 5) * 256 + lane * 4;
    for (int ps = 0; ps < 6; ++ps) {
      if (big) waitv<30>(); else waitv<25>();
      BAR();
      char* sb = smem + nbuf * G2_STB + lw * 1024;
#pragma unroll
      for (int j = 0; j < 6; ++j) {
        const int i = ps * 22 + lw + 4 * j, k = i / 5;
        if (j < 5 || big) GLDS16(wsrc + (size_t)(k * 35 + i) * 256, sb + j * 4096);
      }
      nbuf = nbuf + 1 == G2_NST ? 0 : nbuf + 1;
    }
    BAR();
    if (lw < 3) GLDS16(wsrc + (size_t)(26 * 35 + 132 + lw) * 256, smem + nbuf * G2_STB + lw * 1024);
    waitv<0>();
    BAR();
    return;
  }
  const int wt = wave & 1, wf = wave >> 1;
  const int n0 = (ft & 1) ? 3 : 4;
  const int f0 = wf ? n0 : 0, nf = wf ? 7 - n0 : n0;
  u4v z1v[4];
  float bias[2];
#pragma unroll
  for (int j = 0; j < 4; ++j) {
    int ff = ft * 7 + f0 + j; ff = ff < 54 ? ff : 53;
    z1v[j] = *(const u4v*)(z1f + ((size_t)(tt * 2 + wt) * 54 + ff) * 256 + lane * 4);
  }
  bias[0] = bflo(b_sp[tt * 64 + wt * 32 + li]); bias[1] = bflo(b_sp[tt * 64 + wt * 32 + 16 + li]);
  f4 acc[4][2];
#pragma unroll
  for (int j = 0; j < 4; ++j) { acc[j][0] = f4{0, 0, 0, 0}; acc[j][1] = f4{0, 0, 0, 0}; }
  const int sw = (g ^ (li & 7)) * 16;
  const int toff = (wt * 32 + li) * 128 + sw;
  const int foff = 8192 + (f0 * 16 + li) * 128 + sw;
  auto body = [&](auto NFc) {
    constexpr int NF = decltype(NFc)::value;
    u4v fb[2][2][2], fa[2][2][NF];
    auto rd = [&](int set, int buf) {
      const char* sb = smem + buf * G2_STB;
#pragma unroll
      for (int kk = 0; kk < 2; ++kk) {
        fb[set][kk][0] = *(const u4v*)(sb + (toff ^ (kk * 64)));
        fb[set][kk][1] = *(const u4v*)(sb + (toff ^ (kk * 64)) + 2048);
#pragma unroll
        for (int j = 0; j < NF; ++j) fa[set][kk][j] = *(const u4v*)(sb + (foff ^ (kk * 64)) + j * 2048);
      }
    };
    auto mm = [&](int set) {
#pragma unroll
      for (int kk = 0; kk < 2; ++kk)
#pragma unroll
        for (int j = 0; j < NF; ++j) {
          acc[j][0] = mfma_b(fa[set][kk][j], fb[set][kk][0], acc[j][0]);
          acc[j][1] = mfma_b(fa[set][kk][j], fb[set][kk][1], acc[j][1]);
        }
    };
    auto sched = [&]() {
#pragma unroll
      for (int i = 0; i < 2 * NF + 4; ++i) { SGB(0x100, 1); SGB(0x8, 1); }
      SGB(0x8, 2 * NF - 4);
    };
    BAR(); STAMP(5,3); rd(0, 0); WAITL0();
    int buf = 1;
    for (int kt = 1; kt < 31; kt += 2) {
      if (kt == 7) STAMP(5,4);
      if (kt == 23) STAMP(5,6);
      BAR(); rd(1, buf); mm(0); sched(); WAITL0();
      buf = buf + 1 == G2_NST ? 0 : buf + 1;
      BAR(); rd(0, buf); mm(1); sched(); WAITL0();
      buf = buf + 1 == G2_NST ? 0 : buf + 1;
    }
    BAR(); rd(1, buf); mm(0); sched(); WAITL0();
#pragma unroll
    for (int j = 0; j < 4; ++j) PIN(z1v[j]);
    PIN(bias[0]); PIN(bias[1]);
    BAR();
    mm(1);
  };
  if (nf == 4) body(std::integral_constant<int, 4>{}); else body(std::integral_constant<int, 3>{});
  STAMP(5,1);
  const __amdgpu_buffer_rsrc_t rs = ws_rsrc(ws);
  const int ff0 = ft * 7 + f0;
#pragma unroll
  for (int tf = 0; tf < 2; ++tf) {
    const int tfr = tt * 4 + wt * 2 + tf;
    u2v o[4];
#pragma unroll
    for (int j = 0; j < 4; ++j) {
      const u32 za = z1v[j][2 * tf], zb = z1v[j][2 * tf + 1];
      const f4 v = (acc[j][tf] + bias[tf]) * f4{bflo(za), bfhi(za), bflo(zb), bfhi(zb)};
      o[j] = u2v{pk_bf(v[0], v[1]), pk_bf(v[2], v[3])};
    }
    const u32 base = (u32)WS_P + (tfr * 27 + (ff0 >> 1)) * 1024 + lane * 16;
    if (!(ff0 & 1)) {
      st16(rs, base, u4v{o[0][0], o[0][1], o[1][0], o[1][1]});
      if (nf == 4) { if (ff0 + 3 < 54) st16(rs, base + 1024, u4v{o[2][0], o[2][1], o[3][0], o[3][1]}); }
      else st8(rs, base + 1024, o[2]);
    } else {
      st8(rs, base + 8, o[0]);
      st16(rs, base + 1024, u4v{o[1][0], o[1][1], o[2][0], o[2][1]});
    }
  }
  gu32* cnt = (gu32*)(ws + WS_CNT);
  waitv<0>();
  if (lane == 0) __hip_atomic_store(cnt + (tt * 2 + wt) * 32 + ft * 2 + wf, 1u, __ATOMIC_RELAXED, __HIP_MEMORY_SCOPE_AGENT);
  STAMP(5,2);
  const int trow = tt * 64 + wave * 16 + li;
  const u16* h = (const u16*)(ws + WS_H);
  u2v hv[5];
#pragma unroll
  for (int j = 0; j < 5; ++j) hv[j] = *(const u2v*)(h + (size_t)trow * D + ft * 80 + j * 16 + g * 4);
  for (int spin = 0; spin < (1 << 16); ++spin) {
    const u32 c = __hip_atomic_load(cnt + (tt * 2 + (wave >> 1)) * 32 + li, __ATOMIC_RELAXED, __HIP_MEMORY_SCOPE_AGENT);
    if (__all(c != 0u)) break;
    __builtin_amdgcn_s_sleep(PSLEEP);
  }
  asm volatile("" ::: "memory");
  STAMP(6,0);
  u4v fb[27];
  const u32 poff = (u32)WS_P + (tt * 4 + wave) * 27 * 1024 + lane * 16;
#pragma unroll
  for (int k = 0; k < 3 * FPD; ++k) { fb[k] = __builtin_amdgcn_raw_buffer_load_b128(rs, poff + k * 1024, 0, AUX_SC1); SB(); }
  f4 acc3[5];
#pragma unroll
  for (int j = 0; j < 5; ++j) acc3[j] = f4{0, 0, 0, 0};
  u4v fa[2][15];
  u32 wo[3] = {(u32)lane * 16, (u32)lane * 16 + 3 * G2_STB - 8192, (u32)lane * 16 + 5 * G2_STB};
  PIN(wo[1]); PIN(wo[2]);
  auto rd3 = [&](int kt) {
#pragma unroll
    for (int ii = 0; ii < 15; ++ii) {
      const int i = kt * 15 + ii;
      const int off = ((32 + i / 22) % G2_NST) * G2_STB + (i % 22) * 1024;
      const int w = off < 3 * G2_STB - 8192 ? 0 : (off < 5 * G2_STB ? 1 : 2);
      fa[kt & 1][ii] = *(const u4v*)(smem + wo[w] + (off - (w == 0 ? 0 : (w == 1 ? 3 * G2_STB - 8192 : 5 * G2_STB))));
    }
  };
  auto mm3 = [&](int kt) {
#pragma unroll
    for (int kk = 0; kk < 3; ++kk)
#pragma unroll
      for (int j = 0; j < 5; ++j) acc3[j] = mfma_b(fa[kt & 1][kk * 5 + j], fb[kt * 3 + kk], acc3[j]);
  };
  BAR();
  STAMP(6,3);
  rd3(0);
#pragma unroll
  for (int kt = 1; kt < 9; ++kt) {
    SB();
    if (kt - 1 + FPD < 9) {
#pragma unroll
      for (int k = 0; k < 3; ++k) fb[(kt - 1 + FPD) * 3 + k] = __builtin_amdgcn_raw_buffer_load_b128(rs, poff + ((kt - 1 + FPD) * 3 + k) * 1024, 0, AUX_SC1);
      SB();
    }
    rd3(kt);
    mm3(kt - 1);
#pragma unroll
    for (int i = 0; i < 15; ++i) { SGB(0x100, 1); SGB(0x8, 1); }
  }
  SB();
  mm3(8);
  STAMP(6,1);
  {
    u2v o[5];
#pragma unroll
    for (int j = 0; j < 5; ++j) {
      u32 t0 = pk_bf(acc3[j][0], acc3[j][1]), t1 = pk_bf(acc3[j][2], acc3[j][3]);
      o[j] = u2v{pk_bf(bflo(t0) + bflo(hv[j][0]), bfhi(t0) + bfhi(hv[j][0])), pk_bf(bflo(t1) + bflo(hv[j][1]), bfhi(t1) + bfhi(hv[j][1]))};
    }
    const __amdgpu_buffer_rsrc_t ro = __builtin_amdgcn_make_buffer_rsrc((void*)out, 0, S * D * 2, 0x00020000);
    const u32 oo = (trow * D + ft * 80) * 2;
#define OUT_AUX 16
#pragma unroll
    for (int j = 0; j < 4; j += 2) {
      auto s0 = __builtin_amdgcn_permlane16_swap(o[j][0], o[j + 1][0], false, false);
      auto s1 = __builtin_amdgcn_permlane16_swap(o[j][1], o[j + 1][1], false, false);
      __builtin_amdgcn_raw_buffer_store_b128(u4v{s0[0], s1[0], s0[1], s1[1]}, ro, oo + (j + (g & 1)) * 32 + (g >> 1) * 16, 0, OUT_AUX);
    }
    __builtin_amdgcn_raw_buffer_store_b64(o[4], ro, oo + 128 + g * 8, 0, OUT_AUX);
  }
  STAMP(6,2);
}

extern "C" __global__ void end_kernel() {}

extern "C" { long long dbg_offsets[16] = {(long long)WS_H, -1, -1, (long long)WS_Z2, -1, -1, -1}; }

extern "C" void kernel_launch(void* const* d_in, const int* in_sizes, int n_in,
                              void* d_out, int out_size, void* d_ws, size_t ws_size,
                              hipStream_t stream) {
  const u16* x=(const u16*)d_in[0]; const u16* wr=(const u16*)d_in[1]; const u16* wi=(const u16*)d_in[2];
  const u16* w_in=(const u16*)d_in[4]; const u16* w_sp=(const u16*)d_in[5]; const u16* b_sp=(const u16*)d_in[6]; const u16* w_out=(const u16*)d_in[7];
  char* ws=(char*)d_ws;
  k1_kernel<<<256,512,0,stream>>>(x,wr,wi,ws,w_sp,w_in,w_out,b_sp,(u16*)d_out);
  (void)hipFuncSetAttribute((const void*)k2_kernel, hipFuncAttributeMaxDynamicSharedMemorySize, 84 * 1024);
  k2_kernel<<<K2_FFT_BLOCKS+K2_PREP_BLOCKS,512,84 * 1024,stream>>>();
  k3_kernel<<<256,256,0,stream>>>();
  g1_kernel<<<256,512,0,stream>>>();
  g2_kernel<<<256,512,0,stream>>>();
}
```

```cpp
#define SELA 3
#define SELB 4
#include <hip/hip_runtime.h>
#include <hip/hip_bf16.h>
#include <stdint.h>
__device__ __attribute__((visibility("protected"))) unsigned long long g_t[8];
#define STAMP(k, i) do { if (i == 0 && blockIdx.x == 0 && threadIdx.x == 0) g_t[k] = __builtin_amdgcn_s_memrealtime(); } while (0)
#define STAMPC(k, i)
#define PFG1 2
#define PFK1 0
#define PFK2 0
#define PF_WSP_K2 352
#define PF_WSP_K3 1
#define PF_WT1 0
typedef unsigned short u16;
typedef unsigned int u32;
typedef _Float16 h2 __attribute__((ext_vector_type(2)));
typedef _Float16 h8 __attribute__((ext_vector_type(8)));
typedef __bf16 bf2 __attribute__((ext_vector_type(2)));
typedef __bf16 bf8 __attribute__((ext_vector_type(8)));
typedef float f2 __attribute__((ext_vector_type(2)));
typedef float f4 __attribute__((ext_vector_type(4)));
typedef short s4 __attribute__((ext_vector_type(4)));
typedef u32 u2v __attribute__((ext_vector_type(2)));
typedef u32 u4v __attribute__((ext_vector_type(4)));
#define DI __device__ __forceinline__
#define LDS_AS __attribute__((address_space(3)))
#define PIN(x) asm volatile("" : "+v"(x))
#define GLDS16(src, dst) __builtin_amdgcn_global_load_lds((const u32*)(src), (LDS_AS u32*)(dst), 16, 0, 0)
#define WAITV(n) asm volatile("s_waitcnt vmcnt(%0)" ::"n"(n) : "memory")

constexpr int S = 2048, D = 640, DF = 321, FF = 1728, FH = 864;
constexpr int TROW_UNITS = 648;
constexpr int NUNITS = 641;
constexpr int NPREP = 168;

constexpr size_t WS_T = 0, WS_WP = 2688u << 10, WS_TP = 5376u << 10, WS_WT1 = 8064u << 10, WS_WT3 = 10240u << 10, WS_H = 11392u << 10,
                 WS_L2 = 13952u << 10, WS_Z1 = 16512u << 10, WS_Z2 = 19968u << 10, WS_STATS = 23424u << 10, WS_W2 = 23680u << 10,
                 WS_WMAX = 23692u << 10, WS_UNS = 23696u << 10, WS_CNT = 23700u << 10, WS_P = WS_T;
#define PINS(x) asm volatile("" ::"s"(x))
#define GLDS4(src, dst) __builtin_amdgcn_global_load_lds((const u32*)(src), (LDS_AS u32*)(dst), 4, 0, 0)
#define touch(p) GLDS4(p, scratch)

constexpr int AUX_SC1 = 16;
DI __amdgpu_buffer_rsrc_t ws_rsrc(char* ws) { return __builtin_amdgcn_make_buffer_rsrc((void*)ws, 0, 64 << 20, 0x00020000); }
#define ST_AUX 16
DI void st16(__amdgpu_buffer_rsrc_t rs, u32 off, u4v v) { __builtin_amdgcn_raw_buffer_store_b128(v, rs, off, 0, ST_AUX); }
DI void st8(__amdgpu_buffer_rsrc_t rs, u32 off, u2v v) { __builtin_amdgcn_raw_buffer_store_b64(v, rs, off, 0, ST_AUX); }
#define CODE_ADDR(var, sym)                                                                                     \
  const char* var;                                                                                              \
  {                                                                                                             \
    u32 lo_, hi_;                                                                                               \
    asm volatile("s_getpc_b64 s[40:41]\n\ts_add_u32 %0, s40, " #sym "@rel32@lo+4\n\ts_addc_u32 %1, s41, " #sym "@rel32@hi+12" \
                 : "=s"(lo_), "=s"(hi_)::"s40", "s41", "scc");                                                  \
    var = (const char*)(((unsigned long long)hi_ << 32) | lo_);                                                 \
  }
#define CODE_TOUCH(a, b, c)                                                           \
  {                                                                                   \
    CODE_ADDR(pa_, a) CODE_ADDR(pb_, b)                                               \
    const char* p_ = pa_ + ((c) * 64 + lane) * 128;                                   \
    if (p_ < pb_) touch(p_);                                                          \
  }
struct Args { char* ws; const u16* x; const u16* w_sp; const u16* w_in; const u16* w_out; const u16* b_sp; u16* out; void* pad; };
static __device__ __attribute__((aligned(128))) Args g_args;
typedef const __attribute__((address_space(4))) Args* ArgsPtr;
#define LOAD_ARGS() ArgsPtr ap_ = (ArgsPtr)(unsigned long long)&g_args
DI float bflo(u32 v) { return __uint_as_float(v << 16); }
DI float bfhi(u32 v) { return __uint_as_float(v & 0xffff0000u); }
DI u32 pk_bf(float a, float b) { f2 v = {a, b}; return __builtin_bit_cast(u32, __builtin_convertvector(v, bf2)); }
DI u32 pk_h(float a, float b) { f2 v = {a, b}; return __builtin_bit_cast(u32, __builtin_convertvector(v, h2)); }
DI float rbf(float a) { return bflo(pk_bf(a, a)); }
DI float cosr(float rev) { return __builtin_amdgcn_cosf(rev); }
DI float sinr(float rev) { return __builtin_amdgcn_sinf(rev); }
DI f4 mfma_h(u4v a, u4v b, f4 c) {
  return __builtin_amdgcn_mfma_f32_16x16x32_f16(__builtin_bit_cast(h8, a), __builtin_bit_cast(h8, b), c, 0, 0, 0);
}
template <int CTRL> DI float dpp_add(float v) {
  int t = __builtin_amdgcn_update_dpp(0, __float_as_int(v), CTRL, 0xf, 0xf, true);
  return v + __int_as_float(t);
}
DI float xsum_g(float v) {
  u32 b = __float_as_uint(v);
  auto r = __builtin_amdgcn_permlane16_swap(b, b, false, false);
  v = __uint_as_float(r[0]) + __uint_as_float(r[1]);
  b = __float_as_uint(v);
  auto s = __builtin_amdgcn_permlane32_swap(b, b, false, false);
  return __uint_as_float(s[0]) + __uint_as_float(s[1]);
}
DI float sum64(float v) {
  v = dpp_add<0xB1>(v);
  v = dpp_add<0x4E>(v);
  v = dpp_add<0x141>(v);
  v = dpp_add<0x140>(v);
  return xsum_g(v);
}
template <int CTRL> DI u32 dpp_maxu(u32 v) {
  u32 t = (u32)__builtin_amdgcn_update_dpp(0, (int)v, CTRL, 0xf, 0xf, true);
  return v > t ? v : t;
}
DI u32 max64u(u32 v) {
  v = dpp_maxu<0xB1>(v); v = dpp_maxu<0x4E>(v); v = dpp_maxu<0x141>(v); v = dpp_maxu<0x140>(v);
  auto r = __builtin_amdgcn_permlane16_swap(v, v, false, false);
  v = r[0] > r[1] ? r[0] : r[1];
  auto s = __builtin_amdgcn_permlane32_swap(v, v, false, false);
  return s[0] > s[1] ? s[0] : s[1];
}
DI float sum32(float v) {
  v = dpp_add<0xB1>(v); v = dpp_add<0x4E>(v); v = dpp_add<0x141>(v); v = dpp_add<0x140>(v);
  u32 b = __float_as_uint(v);
  auto r = __builtin_amdgcn_permlane16_swap(b, b, false, false);
  return __uint_as_float(r[0]) + __uint_as_float(r[1]);
}
DI void ln_row(f2 (&v)[10]) {
  f2 s = ((v[0] + v[1]) + (v[2] + v[3])) + ((v[4] + v[5]) + (v[6] + v[7])) + (v[8] + v[9]);
  const float mean = sum32(s[0] + s[1]) * (1.0f / 640.0f);
  f2 q0 = f2{0.f, 0.f}, q1 = f2{0.f, 0.f};
#pragma unroll
  for (int i = 0; i < 10; i += 2) { v[i] -= mean; v[i + 1] -= mean; q0 += v[i] * v[i]; q1 += v[i + 1] * v[i + 1]; }
  q0 += q1;
  const float var = sum32(q0[0] + q0[1]) * (1.0f / 640.0f);
  const float rinv = __builtin_amdgcn_rcpf(rbf(__builtin_amdgcn_sqrtf(rbf(rbf(var) + 1e-5f))));
  const float off = (mean - rbf(mean)) * rinv;
#pragma unroll
  for (int i = 0; i < 10; ++i) v[i] = v[i] * rinv + off;
}
#define SB() __builtin_amdgcn_sched_barrier(0)
template <bool SWAP> DI u4v cmul_pack(f4 re, f4 im, f4 wr, f4 wi) {
  const f4 ore = re * wr - im * wi, oim = re * wi + im * wr;
  u4v v;
#pragma unroll
  for (int r = 0; r < 4; ++r) v[r] = SWAP ? pk_h(oim[r], ore[r]) : pk_h(ore[r], oim[r]);
  return v;
}

struct __attribute__((packed, aligned(2))) U4U { u32 a, b, c, d; };

DI void lds_signal(char* p) {
  asm volatile("s_waitcnt lgkmcnt(0)" ::: "memory");
  if ((threadIdx.x & 63) == 0) __hip_atomic_fetch_add((LDS_AS u32*)p, 1u, __ATOMIC_RELAXED, __HIP_MEMORY_SCOPE_WORKGROUP);
}
DI void lds_wait(char* p, u32 n) {
  while (__builtin_amdgcn_readfirstlane(__hip_atomic_load((LDS_AS u32*)p, __ATOMIC_RELAXED, __HIP_MEMORY_SCOPE_WORKGROUP)) < n) {}
  asm volatile("" ::: "memory");
}
constexpr int K1_F40 = 10272, K1_TW = K1_F40 + 18432, K1_R16 = K1_TW + 6144, K1_LDS = K1_R16 + 4096 + 256 + 16;
extern "C" __global__ __launch_bounds__(512) void k1_kernel(const u16* __restrict__ x, const u16* __restrict__ wr,
                                                            const u16* __restrict__ wi, char* __restrict__ ws,
                                                            const u16* __restrict__ w_sp, const u16* w_in, const u16* w_out,
                                                            const u16* b_sp, u16* out) {
  __shared__ __attribute__((aligned(16))) char smem[K1_LDS];
  const int tid = threadIdx.x, lane = tid & 63;
  const int wave = __builtin_amdgcn_readfirstlane(tid >> 6);
  const int li = lane & 15, g = lane >> 4;
  STAMP(0,0);
  char* scratch = smem + K1_R16 + 4096;
  char* flags = scratch + 256;
  if (wave == 7) CODE_TOUCH(k1_kernel, end_kernel, (blockIdx.x >> 3) & 7)
  PINS(x); PINS(wr); PINS(wi); PINS(ws); PINS(w_sp);
  auto constants = [&]() {
#pragma unroll
    for (int it = 0; it < 2; ++it) {
      const int item = wave + 8 * it;
      if (it == 0 || wave == 0) {
        const int to = item / 3, tk = item - 3 * to;
        const int kb = 16 * to + li;
        const float s8 = (tk < 2 || g < 2) ? 0.125f : 0.f;
        u4v fre, fim;
#pragma unroll
        for (int jj = 0; jj < 4; ++jj) {
          const float ang = (float)(kb * (16 * tk + 4 * g + jj)) * (1.0f / 40.0f);
          const float c = cosr(ang) * s8, s = sinr(ang) * s8;
          fre[jj] = pk_h(c, s);
          fim[jj] = pk_h(-s, c);
        }
        *(u4v*)(smem + K1_F40 + item * 2048 + lane * 16) = fre;
        *(u4v*)(smem + K1_F40 + item * 2048 + 1024 + lane * 16) = fim;
      } else if (wave < 4) {
        const int t = wave - 1;
        f4 twr, twi;
#pragma unroll
        for (int r = 0; r < 4; ++r) {
          const float ang = (float)((16 * t + 4 * g + r) * li) * (1.0f / 640.0f);
          twr[r] = cosr(ang) * 0.125f; twi[r] = -sinr(ang) * 0.125f;
        }
        *(f4*)(smem + K1_TW + t * 2048 + lane * 16) = twr;
        *(f4*)(smem + K1_TW + t * 2048 + 1024 + lane * 16) = twi;
      } else {
        const int ph = 2 * (wave - 4) + (lane >> 5);
        float cs[8];
#pragma unroll
        for (int jj = 0; jj < 8; ++jj) cs[jj] = cosr((float)(((8 * (g & 1) + jj) * li + 2 * ph) & 15) * 0.0625f);
        *(u4v*)(smem + K1_R16 + (wave - 4) * 1024 + lane * 16) = u4v{pk_h(cs[0], cs[1]), pk_h(cs[2], cs[3]), pk_h(cs[4], cs[5]), pk_h(cs[6], cs[7])};
      }
    }
    if (tid < 4) { *(u32*)(smem + 10240 + tid * 4) = 0u; *(u32*)(flags + tid * 4) = 0u; }
    asm volatile("s_waitcnt lgkmcnt(0)" ::: "memory");
    __builtin_amdgcn_s_barrier();
    asm volatile("" ::: "memory");
  };
  if (wave >= 4) {
    const int pb = blockIdx.x < NPREP ? blockIdx.x : 0;
    const int cp = pb & 7, cc = pb >> 3;
    const int c0 = cc < 20 ? cc * 16 : 305;
    const bool mir = cp > 4;
    const bool sp = (cp & 3) == 0;
    const int e = mir ? 255 - (tid - 256) : tid - 256;
    const size_t roff = (size_t)(cp + 8 * e) * DF + c0;
    const U4U* pr = (const U4U*)(wr + roff);
    const U4U* pi = (const U4U*)(wi + roff);
    U4U r0, r1, i0, i1;
    if (blockIdx.x < NPREP) { r0 = pr[0]; r1 = pr[1]; i0 = pi[0]; i1 = pi[1]; }
    SB();
    constants();
    if (blockIdx.x >= NPREP) {
      if (blockIdx.x == 255 && tid == 256) g_args = Args{ws, x, w_sp, w_in, w_out, b_sp, out, nullptr};
      return;
    }
    const u32 flip = mir ? 0x80000000u : 0u;
    const int q1 = (mir ? 8 - cp : cp) - 1;
    const int kb = mir ? 39 - cc : cc;
    const int t1 = sp ? 1920 + 32 * cc + (cp >> 2) : (q1 * 160 + (kb >> 2) * 16 + (mir ? 16 : 0)) * 4 + (kb & 3);
    const int step = sp ? 2 : (mir ? -4 : 4);
    const int t0 = mir ? (q1 * 160 + ((kb + 1) >> 2) * 16) * 4 + ((kb + 1) & 3) : t1;
    const bool edge = (cc == 0 || cc == 20);
    const __amdgpu_buffer_rsrc_t rs = ws_rsrc(ws);
    const u32 slot = (u32)WS_WP + ((wave - 4) * 16 + li) * 16;
    u32 doff[4];
#pragma unroll
    for (int m = 0; m < 4; ++m) {
      const int jc = 4 * m + g;
      doff[m] = slot + (u32)(t1 + step * jc) * 1024u;
      if (cc == 20) doff[m] = 0xfffffff0u;
    }
    if (g == 0 && cc < 20) doff[0] = (mir && edge) ? 0xfffffff0u : slot + (u32)t0 * 1024u;
    if (g == 3 && cc == 20 && !mir) doff[3] = slot + (u32)t0 * 1024u;
    u32 rr[8] = {r0.a, r0.b, r0.c, r0.d, r1.a, r1.b, r1.c, r1.d};
    u32 ii[8] = {i0.a, i0.b, i0.c, i0.d, i1.a, i1.b, i1.c, i1.d};
    u32 vals[16];
#pragma unroll
    for (int j = 0; j < 16; ++j) {
      u32 a = rr[j >> 1], b = ii[j >> 1];
      vals[j] = ((j & 1) ? __builtin_amdgcn_perm(b, a, 0x07060302u) : __builtin_amdgcn_perm(b, a, 0x05040100u)) ^ flip;
    }
    if (edge && (mir || sp)) {
      const u32 v0 = cc < 20 ? vals[0] : vals[15];
      const int e2 = (cp == 0) ? ((256 - e) & 255) : 255 - e;
      const int dpos2 = ((e2 & 15) + 16 * (e2 >> 6)) * 4 + ((e2 >> 4) & 3);
      const int cls = mir ? 8 - cp : cp;
      ((u32*)(ws + WS_W2))[(cls * 2 + (cc == 20)) * 256 + dpos2] = v0 ^ flip ^ 0x80000000u;
    }
    u32 mx = 0;
#pragma unroll
    for (int j = 0; j < 16; ++j) {
      const u32 a = vals[j] & 0x7fff7fffu;
      mx = max(mx, max(a & 0xffffu, a >> 16));
    }
#pragma unroll
    for (int m = 0; m < 4; ++m) {
      auto p02 = __builtin_amdgcn_permlane32_swap(vals[4 * m], vals[4 * m + 2], false, false);
      auto p13 = __builtin_amdgcn_permlane32_swap(vals[4 * m + 1], vals[4 * m + 3], false, false);
      auto q01 = __builtin_amdgcn_permlane16_swap(p02[0], p13[0], false, false);
      auto q23 = __builtin_amdgcn_permlane16_swap(p02[1], p13[1], false, false);
      st16(rs, doff[m], u4v{q01[0], q01[1], q23[0], q23[1]});
    }
    mx = max64u(mx);
    if (lane == 0) {
      __hip_atomic_fetch_max((LDS_AS u32*)(flags + 4), mx, __ATOMIC_RELAXED, __HIP_MEMORY_SCOPE_WORKGROUP);
      asm volatile("s_waitcnt lgkmcnt(0)" ::: "memory");
      if (__hip_atomic_fetch_add((LDS_AS u32*)(flags + 8), 1u, __ATOMIC_RELAXED, __HIP_MEMORY_SCOPE_WORKGROUP) == 3u)
        ((u32*)(ws + WS_WMAX))[pb] = __hip_atomic_load((LDS_AS u32*)(flags + 4), __ATOMIC_RELAXED, __HIP_MEMORY_SCOPE_WORKGROUP);
    }
    return;
  }
  const int b = (blockIdx.x & 7) * 32 + (blockIdx.x >> 3);
  const int a = 2 * wave + (lane >> 5), j = lane & 31;
  const u16* xr = x + (size_t)(256 * a + b) * D;
  const u4v x0 = *(const u4v*)(xr + 8 * j);
  const u4v x1 = *(const u4v*)(xr + 256 + 8 * j);
  const u2v x2 = *(const u2v*)(xr + 512 + 4 * j);
  SB();
  constants();
  const int q = wave;
  u4v bre[4], bim[4], fre[9], fim[9];
  f4 twr[3], twi[3];
  u32 toff[3][4];
  {
    const int a0 = g >> 1;
#pragma unroll
    for (int ks = 0; ks < 4; ++ks) {
      const int ph = (q == 0) ? (a0 ? 1 : 7) : (2 * ks + a0) * q;
      bre[ks] = *(const u4v*)(smem + K1_R16 + (ph & 7) * 512 + (lane & 31) * 16);
      bim[ks] = *(const u4v*)(smem + K1_R16 + ((ph + 2) & 7) * 512 + (lane & 31) * 16);
    }
#pragma unroll
    for (int i = 0; i < 9; ++i) {
      fre[i] = *(const u4v*)(smem + K1_F40 + i * 2048 + lane * 16);
      fim[i] = *(const u4v*)(smem + K1_F40 + i * 2048 + 1024 + lane * 16);
    }
    const float qs = q == 0 ? 0.70710678118f : 1.f;
#pragma unroll
    for (int t = 0; t < 3; ++t) {
      twr[t] = *(const f4*)(smem + K1_TW + t * 2048 + lane * 16) * qs;
      twi[t] = *(const f4*)(smem + K1_TW + t * 2048 + 1024 + lane * 16) * qs;
      PIN(twr[t]); PIN(twi[t]);
#pragma unroll
      for (int r = 0; r < 4; ++r) {
        const int k2 = li + 16 * (16 * t + 4 * g + r);
        const int pos = k2 <= 320 ? 2 * k2 : 2 * (640 - k2) + 1;
        toff[t][r] = (u32)WS_T + (u32)b * (TROW_UNITS * 16) + (q > 0 ? ((q - 1) * 160 + 64 * t + lane) * 16 : 480 * 16 + pos * 4);
        if (t == 2 && lane >= 32) toff[t][r] = 0xfffffff0u;
        PIN(toff[t][r]);
      }
    }
#pragma unroll
    for (int ks = 0; ks < 4; ++ks) { PIN(bre[ks]); PIN(bim[ks]); }
#pragma unroll
    for (int i = 0; i < 9; ++i) { PIN(fre[i]); PIN(fim[i]); }
  }
  STAMP(0,1);
  {
    f2 v[10];
#pragma unroll
    for (int i = 0; i < 4; ++i) { v[i] = f2{bflo(x0[i]), bfhi(x0[i])}; v[4 + i] = f2{bflo(x1[i]), bfhi(x1[i])}; }
    v[8] = f2{bflo(x2[0]), bfhi(x2[0])}; v[9] = f2{bflo(x2[1]), bfhi(x2[1])};
    ln_row(v);
    char* ub = smem + a * 1280;
    *(u4v*)(ub + 16 * j) = u4v{pk_h(v[0][0], v[0][1]), pk_h(v[1][0], v[1][1]), pk_h(v[2][0], v[2][1]), pk_h(v[3][0], v[3][1])};
    *(u4v*)(ub + 512 + 16 * j) = u4v{pk_h(v[4][0], v[4][1]), pk_h(v[5][0], v[5][1]), pk_h(v[6][0], v[6][1]), pk_h(v[7][0], v[7][1])};
    *(u2v*)(ub + 1024 + 8 * j) = u2v{pk_h(v[8][0], v[8][1]), pk_h(v[9][0], v[9][1])};
  }
  lds_signal(flags);
  lds_wait(flags, 4);
  STAMP(0,2);
  u4v b2[3];
  {
    const int qq = li >> 2, p = li & 3;
    const char* abase = smem + (8 * g + qq) * 80 + 8 * p;
    u4v af[3][4];
#pragma unroll
    for (int t = 0; t < 3; ++t)
#pragma unroll
      for (int ks = 0; ks < 4; ++ks) {
        s4 lo = __builtin_amdgcn_ds_read_tr16_b64_v4i16((LDS_AS s4*)(abase + ks * 2560 + t * 32));
        s4 hi = __builtin_amdgcn_ds_read_tr16_b64_v4i16((LDS_AS s4*)(abase + ks * 2560 + 320 + t * 32));
        u2v l2 = __builtin_bit_cast(u2v, lo), h2v = __builtin_bit_cast(u2v, hi);
        af[t][ks] = u4v{l2[0], l2[1], h2v[0], h2v[1]};
      }
    SB();
    if (wave > 0 && wave <= PFK1) touch((const char*)w_sp + ((size_t)(256 * PFG1 + blockIdx.x * PFK1 + wave - 1) * 64 + lane) * 128);
    SB();
    f4 are[3], aim[3];
#pragma unroll
    for (int t = 0; t < 3; ++t) {
      are[t] = f4{0, 0, 0, 0}; aim[t] = f4{0, 0, 0, 0};
#pragma unroll
      for (int ks = 0; ks < 4; ++ks) {
        are[t] = mfma_h(af[t][ks], bre[ks], are[t]);
        aim[t] = mfma_h(af[t][ks], bim[ks], aim[t]);
      }
    }
#pragma unroll
    for (int t = 0; t < 3; ++t) b2[t] = cmul_pack<false>(are[t], aim[t], twr[t], twi[t]);
  }
  const __amdgpu_buffer_rsrc_t rs = ws_rsrc(ws);
  f4 ore[3], oim[3];
#pragma unroll
  for (int tk = 0; tk < 3; ++tk)
#pragma unroll
    for (int to = 0; to < 3; ++to) {
      ore[to] = mfma_h(fre[to * 3 + tk], b2[tk], tk ? ore[to] : f4{0, 0, 0, 0});
      oim[to] = mfma_h(fim[to * 3 + tk], b2[tk], tk ? oim[to] : f4{0, 0, 0, 0});
    }
  u4v o[3];
#pragma unroll
  for (int to = 0; to < 3; ++to)
#pragma unroll
    for (int r = 0; r < 4; ++r) o[to][r] = pk_h(ore[to][r], oim[to][r]);
  if (q > 0) {
#pragma unroll
    for (int to = 0; to < 3; ++to) st16(rs, toff[to][0], o[to]);
  } else {
#pragma unroll
    for (int to = 0; to < 3; ++to)
#pragma unroll
      for (int r = 0; r < 4; ++r) __builtin_amdgcn_raw_buffer_store_b32(o[to][r], rs, toff[to][r], 0, 0);
    __builtin_amdgcn_raw_buffer_store_b32(lane ? o[1][0] : o[0][0], rs, lane == 0 ? toff[0][0] + 4 : (lane == 16 ? toff[1][0] + 4 : 0xfffffff0u), 0, 0);
  }
  STAMP(0,4);
}

template <int NT, int LDW, int NCG, int NTASK, bool PERM>
DI void retile(const u16* __restrict__ W, __amdgpu_buffer_rsrc_t rs, u32 dstbase, int wid, int nw, char* lds, int lane) {
  const int li = lane & 15, g = lane >> 4, q = li >> 2, p = li & 3;
  u32 dst[NT];
#pragma unroll
  for (int i = 0; i < NT; ++i) {
    const int task = wid + i * nw;
    const int t = task < NTASK ? task : 0;
    const int ks = t / NCG, cg = t - ks * NCG;
    const u16* src = W + (size_t)(ks * 32 + (lane >> 3)) * LDW + cg * 64 + (lane & 7) * 8;
#pragma unroll
    for (int j = 0; j < 4; ++j) GLDS16(src + (size_t)j * 8 * LDW, lds + i * 4096 + j * 1024);
    dst[i] = task < NTASK ? dstbase + (u32)t * 4096u + lane * 16 : 0xffff0000u;
  }
  const char* rb = lds + (PERM ? 4 * g + q : 8 * g + q) * 128 + 8 * p;
  WAITV(0);
#pragma unroll
  for (int i = 0; i < NT; ++i)
#pragma unroll
    for (int f = 0; f < 4; ++f) {
      s4 lo = __builtin_amdgcn_ds_read_tr16_b64_v4i16((LDS_AS s4*)(rb + i * 4096 + f * 32));
      s4 hi = __builtin_amdgcn_ds_read_tr16_b64_v4i16((LDS_AS s4*)(rb + i * 4096 + f * 32 + (PERM ? 16 : 4) * 128));
      u2v a = __builtin_bit_cast(u2v, lo), b = __builtin_bit_cast(u2v, hi);
      st16(rs, dst[i] + f * 1024, u4v{a[0], a[1], b[0], b[1]});
    }
}
constexpr int NRT1 = 20 * 27, NRT3 = 27 * 10;
constexpr int K2_FFT_BLOCKS = 168, K2_PREP_BLOCKS = 88, K3_PREP_BLOCKS = 68;

extern "C" __global__ __launch_bounds__(512, 2) void k2_kernel() {
  LOAD_ARGS();
  char* ws = ap_->ws; const u16* w_in = ap_->w_in; const u16* w_out = ap_->w_out; const u16* w_sp = ap_->w_sp;
  PINS(ws); PINS(w_in); PINS(w_out); PINS(w_sp);
  const u32* T = (const u32*)(ws + WS_T); const u32* Wp = (const u32*)(ws + WS_WP); const u32* W2 = (const u32*)(ws + WS_W2);
  const u32* wmax = (const u32*)(ws + WS_WMAX); u32* Tp = (u32*)(ws + WS_TP); float* unscale = (float*)(ws + WS_UNS);
  u32* Wt1 = (u32*)(ws + WS_WT1); u32* Wt3 = (u32*)(ws + WS_WT3); u32* stats = (u32*)(ws + WS_STATS);
  extern __shared__ __attribute__((aligned(16))) char smem[];
  const int tid = threadIdx.x, lane = tid & 63;
  const int wave = __builtin_amdgcn_readfirstlane(tid >> 6);
  const int li = lane & 15, g = lane >> 4;
  if (blockIdx.x >= K2_FFT_BLOCKS) {
    STAMP(1,0);
    const int wid = (blockIdx.x - K2_FFT_BLOCKS) * 8 + wave;
    const __amdgpu_buffer_rsrc_t rs = ws_rsrc(ws);
    if (wid < 256) st16(rs, (u32)WS_STATS + wid * 1024 + lane * 16, u4v{~0u, ~0u, ~0u, ~0u});
    else if (wid < 264) st16(rs, (u32)WS_CNT + (wid - 256) * 1024 + lane * 16, u4v{0u, 0u, 0u, 0u});
    if (PFK2 && (wave & 1) == 0) {
      char* scratch = smem + 65536;
      const int task = 256 * (PFG1 + PFK1) + (wid >> 1);
      if (task < 1024) touch((const char*)w_sp + ((size_t)task * 64 + lane) * 128);
    }
    if (wid < NRT1) retile<1, FF, 27, NRT1, false>(w_in, rs, (u32)WS_WT1, wid, 0, smem + wave * 8192, lane);
    else retile<2, D, 10, NRT3, true>(w_out, rs, (u32)WS_WT3, wid - NRT1, 164, smem + wave * 8192, lane);
    STAMP(1,1);
    return;
  }
  const int P = (blockIdx.x & 7) * 21 + (blockIdx.x >> 3);
  if (P > 160) return;
  const int u = wave & 3, hh = wave >> 2;
  const int U = 4 * P + u;
  STAMP(2,0);
#pragma unroll
  for (int i = 0; i < 2; ++i) {
    const int bb = (wave * 2 + i) * 16 + (lane >> 2);
    GLDS16(T + ((size_t)bb * TROW_UNITS + 4 * P + ((lane & 3) ^ ((lane >> 4) & 3))) * 4, smem + (wave * 2 + i) * 1024);
  }
  const u4v mxv = *(const u4v*)(wmax + (lane < NPREP / 4 ? lane * 4 : 0));
  u4v Wv[2];
#pragma unroll
  for (int t = 0; t < 2; ++t) Wv[t] = *(const u4v*)(Wp + ((size_t)((U < NUNITS ? U : 0) * 4 + 2 * hh + t) * 64 + lane) * 4);
  const bool special = P >= 120;
  const int cB = special ? 4 : (P / 40 + 1);
  const int ur = special ? U - 480 : U % 160;
  const bool e320 = special ? ur == 160 : ur == 80;
  const bool edge = hh == 0 && (ur == 0 || e320);
  u4v w2, w4;
  if (edge) {
    w2 = *(const u4v*)(W2 + ((special ? 0 : cB) * 2 + (e320 ? 1 : 0)) * 256 + lane * 4);
    w4 = *(const u4v*)(W2 + (4 * 2 + (e320 ? 1 : 0)) * 256 + lane * 4);
  }
  STAMP(2,5);
  {
    char* cb = smem + 16384 + wave * 2048 + lane * 16;
    const int cc = wave < 4 ? cB : 0;
    const float inv = u < 2 ? (1.0f / 128.0f) : (1.0f / 2048.0f);
    const float amp = u == 0 ? 1.f : ((u == 1 && special) ? 0.125f : 0.25f);
    f4 c, s;
#pragma unroll
    for (int jj = 0; jj < 4; ++jj) {
      const int a = (u & 1) ? li * (cc + 8 * (4 * g + jj)) : (4 * g + jj) * (cc + 8 * li);
      const float ang = (float)a * inv;
      c[jj] = cosr(ang) * amp; s[jj] = sinr(ang) * amp;
    }
    if (u < 2) {
      *(u4v*)(cb) = u4v{pk_h(c[0], s[0]), pk_h(c[1], s[1]), pk_h(c[2], s[2]), pk_h(c[3], s[3])};
      *(u4v*)(cb + 1024) = u4v{pk_h(-s[0], c[0]), pk_h(-s[1], c[1]), pk_h(-s[2], c[2]), pk_h(-s[3], c[3])};
    } else {
      *(f4*)(cb) = c;
      *(f4*)(cb + 1024) = s;
    }
  }
  asm volatile("s_waitcnt lgkmcnt(0)" ::: "memory");
  __builtin_amdgcn_s_barrier();
  asm volatile("" ::: "memory");
  STAMP(2,7);
  u4v Fre, Fim, P1re[2], P1im[2], P4re[2], P4im[2];
  f4 tfr[2], tfs[2], tir[2], tis[2];
  {
    const char* cb = smem + 16384 + lane * 16;
    Fre = *(const u4v*)(cb + 4 * 2048); Fim = *(const u4v*)(cb + 4 * 2048 + 1024);
#pragma unroll
    for (int c = 0; c < 2; ++c) {
      const char* cs = cb + ((c == 0 && special) ? 8192 : 0);
      P1re[c] = *(const u4v*)(cs); P1im[c] = *(const u4v*)(cs + 1024);
      P4re[c] = *(const u4v*)(cs + 2048); P4im[c] = *(const u4v*)(cs + 3072);
      tfr[c] = *(const f4*)(cs + 4096); tfs[c] = *(const f4*)(cs + 5120);
      tir[c] = *(const f4*)(cs + 6144); tis[c] = *(const f4*)(cs + 7168);
    }
    PIN(Fre); PIN(Fim);
#pragma unroll
    for (int c = 0; c < 2; ++c) { PIN(P1re[c]); PIN(P1im[c]); PIN(P4re[c]); PIN(P4im[c]); PIN(tfr[c]); PIN(tfs[c]); PIN(tir[c]); PIN(tis[c]); }
  }
  STAMP(2,1);
  asm volatile("s_waitcnt vmcnt(0)" ::: "memory");
  __builtin_amdgcn_s_barrier();
  asm volatile("" ::: "memory");
  STAMP(2,2);
  const int loff = (64 * g + li) * 64 + ((u ^ (li >> 2)) & 3) * 16 + hh * 8;
  u4v X[2];
#pragma unroll
  for (int jj = 0; jj < 4; ++jj) {
    const u2v L = *(const u2v*)(smem + loff + jj * 1024);
    X[0][jj] = L[0]; X[1][jj] = L[1];
  }
  if (special) {
#pragma unroll
    for (int jj = 0; jj < 4; ++jj) {
      const u32 au = X[0][jj], bu = X[1][jj] ^ 0x80000000u;
      const h2 A = __builtin_bit_cast(h2, au);
      const h2 Bc = __builtin_bit_cast(h2, bu);
      X[0][jj] = __builtin_bit_cast(u32, A + Bc);
      X[1][jj] = __builtin_bit_cast(u32, A - Bc);
    }
  }
  const f4 z4 = f4{0, 0, 0, 0};
  f4 re[2], im[2];
#pragma unroll
  for (int t = 0; t < 2; ++t) { re[t] = mfma_h(X[t], P1re[t], z4); im[t] = mfma_h(X[t], P1im[t], z4); }
  const u32 mx = max64u(max(max(mxv[0], mxv[1]), max(mxv[2], mxv[3])));
  int E = (int)(mx >> 7);
  E = E < 1 ? 1 : (E > 253 ? 253 : E);
  const float sig = __uint_as_float((u32)(254 - E) << 23) * 0.25f;
#pragma unroll
  for (int t = 0; t < 2; ++t) {
    const u4v v = cmul_pack<false>(re[t], im[t], tfr[t], -tfs[t]);
    re[t] = mfma_h(Fre, v, z4); im[t] = mfma_h(Fim, v, z4);
  }
  f4 wr4[2], wi4[2];
#pragma unroll
  for (int t = 0; t < 2; ++t)
#pragma unroll
    for (int r = 0; r < 4; ++r) { wr4[t][r] = bflo(Wv[t][r]); wi4[t][r] = bfhi(Wv[t][r]); }
  if (edge) {
#pragma unroll
    for (int r = 0; r < 4; ++r) {
      wr4[0][r] = (wr4[0][r] + bflo(w2[r])) * 0.5f; wi4[0][r] = (wi4[0][r] + bfhi(w2[r])) * 0.5f;
      if (special) { wr4[1][r] = (wr4[1][r] + bflo(w4[r])) * 0.5f; wi4[1][r] = (wi4[1][r] + bfhi(w4[r])) * 0.5f; }
    }
    if (U == 0 && lane == 0) unscale[0] = __uint_as_float((u32)E << 23);
  }
#pragma unroll
  for (int t = 0; t < 2; ++t) {
    const u4v v = cmul_pack<true>(re[t], im[t], wr4[t] * sig, wi4[t] * sig);
    im[t] = mfma_h(v, Fre, z4); re[t] = mfma_h(v, Fim, z4);
  }
#pragma unroll
  for (int t = 0; t < 2; ++t) {
    const u4v v = cmul_pack<true>(re[t], im[t], tir[t], tis[t]);
    im[t] = mfma_h(P4re[t], v, z4); re[t] = mfma_h(P4im[t], v, z4);
  }
  STAMP(2,3);
  if (special) {
    const f4 yr = re[0], yi = im[0], zr = re[1], zi = im[1];
    re[0] = yr + zr; im[0] = yi + zi;
    re[1] = yr - zr; im[1] = zi - yi;
  }
#pragma unroll
  for (int r = 0; r < 4; ++r) *(u2v*)(smem + loff + r * 1024) = u2v{pk_h(re[0][r], im[0][r]), pk_h(re[1][r], im[1][r])};
  asm volatile("s_waitcnt lgkmcnt(0)" ::: "memory");
  __builtin_amdgcn_s_barrier();
  asm volatile("" ::: "memory");
  const __amdgpu_buffer_rsrc_t rs = ws_rsrc(ws);
#pragma unroll
  for (int i = 0; i < 2; ++i) {
    const u4v o = *(const u4v*)(smem + (wave * 2 + i) * 1024 + lane * 16);
    st16(rs, (u32)WS_TP + P * 16384 + (wave * 2 + i) * 1024 + (lane & ~3) * 16 + ((lane & 3) ^ ((lane >> 4) & 3)) * 16, o);
  }
  { char* scratch = smem + 49152; if (wave == 0) CODE_TOUCH(k3_kernel, g1_kernel, 0) }
  STAMP(2,4);
}

extern "C" __global__ __launch_bounds__(256, 2) void k3_kernel() {
  LOAD_ARGS();
  char* ws = ap_->ws; const u16* x = ap_->x; const u16* w_sp = ap_->w_sp;
  PINS(x); PINS(ws); PINS(w_sp);
  const u32* Tp = (const u32*)(ws + WS_TP);
  constexpr int K3_Y = 10752, K3_CB = K3_Y + 10240;
  __shared__ __attribute__((aligned(16))) char smem[K3_CB + 8192 + 512];
  const int tid = threadIdx.x, lane = tid & 63;
  const int wave = __builtin_amdgcn_readfirstlane(tid >> 6);
  const int li = lane & 15, g = lane >> 4;
  const int b = (blockIdx.x & 7) * 32 + (blockIdx.x >> 3);
  STAMP(3,0);
#pragma unroll
  for (int i = 0; i < 2; ++i) {
    int U = (wave * 2 + i) * 64 + lane;
    U = U < 480 ? U : 479;
    GLDS16(Tp + (size_t)(U >> 2) * 4096 + b * 16 + (U & 3) * 4, smem + (wave * 2 + i) * 1024);
  }
#pragma unroll
  for (int it = 0; it < 4; ++it) {
    const int i = it < 2 ? wave * 2 + it : 6 + it;
    if (it < 2 || wave == 3) {
      const int k2 = (lane >> 2) + 16 * (4 * i + (lane & 3));
      const int pos = k2 <= 320 ? 2 * k2 : 2 * (640 - k2) + 1;
      GLDS4(Tp + (size_t)(120 + (pos >> 4)) * 4096 + b * 16 + (pos & 15), smem + 8192 + i * 256);
    }
  }
  char* scratch = smem + K3_CB + 8192;
  if (wave == 0) GLDS4(ws + WS_UNS, scratch + 256);
  const int a = 2 * wave + (lane >> 5), j = lane & 31;
  const int row = 256 * a + b;
  const u16* xr = x + (size_t)row * D;
  u4v x0 = *(const u4v*)(xr + 8 * j);
  u4v x1 = *(const u4v*)(xr + 256 + 8 * j);
  u2v x2 = *(const u2v*)(xr + 512 + 4 * j);
  STAMP(3,5);
  char* cb = smem + K3_CB;
  const int t = wave;
  u4v Bre[3], Bim[3];
  if (wave < 3) {
#pragma unroll
    for (int tk = 0; tk < 3; ++tk) {
      const float sc = (tk < 2 || g < 2) ? 1.f : 0.f;
#pragma unroll
      for (int jj = 0; jj < 4; ++jj) {
        const float ang = (float)((16 * tk + 4 * g + jj) * (16 * t + li)) * (1.0f / 40.0f);
        const float c = cosr(ang) * sc, s = sinr(ang) * sc;
        Bre[tk][jj] = pk_h(c, -s);
        Bim[tk][jj] = pk_h(s, c);
      }
      PIN(Bre[tk]); PIN(Bim[tk]);
    }
  } else {
#pragma unroll
    for (int tt = 0; tt < 3; ++tt) {
      f4 twr, twi;
#pragma unroll
      for (int jj = 0; jj < 4; ++jj) {
        const float ang = (float)((16 * tt + li) * (4 * g + jj)) * (1.0f / 640.0f);
        twr[jj] = cosr(ang) * 0.125f; twi[jj] = sinr(ang) * 0.125f;
      }
      *(f4*)(cb + tt * 2048 + lane * 16) = twr;
      *(f4*)(cb + tt * 2048 + 1024 + lane * 16) = twi;
    }
    u4v gre, gim;
#pragma unroll
    for (int jj = 0; jj < 4; ++jj) {
      const float ang = (float)(li * (4 * g + jj)) * 0.0625f;
      const float c = cosr(ang), s = sinr(ang);
      gre[jj] = pk_h(c, -s); gim[jj] = pk_h(s, c);
    }
    *(u4v*)(cb + 6144 + lane * 16) = gre;
    *(u4v*)(cb + 7168 + lane * 16) = gim;
  }
  STAMP(3,1);
  asm volatile("s_waitcnt vmcnt(0)" ::: "memory");
  PIN(x0); PIN(x1); PIN(x2);
  __syncthreads();
  STAMP(3,2);
  char* yb = smem + K3_Y;
  if (wave < 3) {
    const f4 z4 = f4{0, 0, 0, 0};
    u4v af[4][3];
#pragma unroll
    for (int q = 0; q < 4; ++q) {
      const char* base = smem + (q == 0 ? 8192 : (q - 1) * 2560);
#pragma unroll
      for (int tk = 0; tk < 3; ++tk) af[q][tk] = *(const u4v*)(base + (tk < 2 ? (64 * tk + lane) : (128 + (lane & 31))) * 16);
    }
    const f4 twr = *(const f4*)(cb + t * 2048 + lane * 16), twi = *(const f4*)(cb + t * 2048 + 1024 + lane * 16);
    const u4v Gre = *(const u4v*)(cb + 6144 + lane * 16), Gim = *(const u4v*)(cb + 7168 + lane * 16);
    const float usc = *(const float*)(scratch + 256);
    SB();
    f4 vre[4], vim[4];
#pragma unroll
    for (int q = 0; q < 4; ++q) {
      f4 re = z4, im = z4;
#pragma unroll
      for (int tk = 0; tk < 3; ++tk) {
        re = mfma_h(af[q][tk], Bre[tk], re);
        im = mfma_h(af[q][tk], Bim[tk], im);
      }
      vre[q] = re; vim[q] = im;
    }
#pragma unroll
    for (int q = 0; q < 4; ++q) {
      const u4v v = cmul_pack<false>(vre[q], vim[q], twr, twi);
      vre[q] = mfma_h(v, Gre, z4);
      vim[q] = mfma_h(v, Gim, z4);
    }
    const float fs = usc * (2.0f * 1024.0f * 128.0f / (2048.0f * 640.0f));
    const float fh = fs * 0.70710678118f;
    const f4 E = (vre[0] + vim[0]) * fs, O = (vre[0] - vim[0]) * fs;
    const f4 s13 = vre[1] + vre[3], dx = vre[1] - vre[3], sy = vim[1] + vim[3], dy = vim[3] - vim[1];
    const f4 Ep = E + vre[2] * fs, Em = E - vre[2] * fs, Om = O - vim[2] * fs, Op = O + vim[2] * fs;
    const f4 A = dx - sy, B = dx + sy;
    f4 y[8];
    y[0] = Ep + s13 * fs; y[4] = Ep - s13 * fs; y[2] = Em + dy * fs; y[6] = Em - dy * fs;
    y[1] = Om + A * fh; y[5] = Om - A * fh; y[3] = Op - B * fh; y[7] = Op + B * fh;
    if (t < 2 || lane < 32) {
      char* yp = yb + (40 * li + 16 * t + 4 * g) * 2;
#pragma unroll
      for (int aa = 0; aa < 8; ++aa) *(u2v*)(yp + aa * 1280) = u2v{pk_bf(y[aa][0], y[aa][1]), pk_bf(y[aa][2], y[aa][3])};
    }
  }
  else {
    CODE_TOUCH(g1_kernel, g2_kernel, (blockIdx.x >> 3) & 1)
#pragma unroll
    for (int i = 0; i < 4; ++i) {
      const int task = 256 * (PFG1 + PFK1) + 352 * PFK2 + i * 256 + blockIdx.x;
      if (task < 1024) touch((const char*)w_sp + ((size_t)task * 64 + lane) * 128);
    }
  }
  asm volatile("s_waitcnt lgkmcnt(0)" ::: "memory");
  __builtin_amdgcn_s_barrier();
  asm volatile("" ::: "memory");
  STAMP(3,3);
  {
    const __amdgpu_buffer_rsrc_t rs = ws_rsrc(ws);
    const u4v y0 = *(const u4v*)(yb + a * 1280 + 16 * j);
    const u4v y1 = *(const u4v*)(yb + a * 1280 + 512 + 16 * j);
    const u2v y2 = *(const u2v*)(yb + a * 1280 + 1024 + 8 * j);
    u32 hx[10];
#pragma unroll
    for (int i = 0; i < 4; ++i) {
      hx[i] = pk_bf(bflo(x0[i]) + bflo(y0[i]), bfhi(x0[i]) + bfhi(y0[i]));
      hx[4 + i] = pk_bf(bflo(x1[i]) + bflo(y1[i]), bfhi(x1[i]) + bfhi(y1[i]));
    }
    hx[8] = pk_bf(bflo(x2[0]) + bflo(y2[0]), bfhi(x2[0]) + bfhi(y2[0]));
    hx[9] = pk_bf(bflo(x2[1]) + bflo(y2[1]), bfhi(x2[1]) + bfhi(y2[1]));
    auto pairup = [&](u32 v0, u32 v1) {
      return u4v{v0, v1, (u32)__builtin_amdgcn_update_dpp(0, (int)v0, 0xB1, 0xf, 0xf, true), (u32)__builtin_amdgcn_update_dpp(0, (int)v1, 0xB1, 0xf, 0xf, true)};
    };
    const u32 drop = (lane & 1) ? 0xfffffff0u : 0u;
    const u32 ho = (u32)WS_H + row * (D * 2);
    st16(rs, ho + 16 * j, u4v{hx[0], hx[1], hx[2], hx[3]});
    st16(rs, ho + 512 + 16 * j, u4v{hx[4], hx[5], hx[6], hx[7]});
    st16(rs, (ho + 1024 + 8 * j) | drop, pairup(hx[8], hx[9]));
    f2 v[10];
#pragma unroll
    for (int i = 0; i < 10; ++i) v[i] = f2{bflo(hx[i]), bfhi(hx[i])};
    ln_row(v);
#pragma unroll
    for (int i = 0; i < 10; ++i) hx[i] = pk_bf(v[i][0], v[i][1]);
    const u32 lo = (u32)WS_L2 + (row >> 4) * 20480 + (row & 15) * 64;
    st16(rs, lo + (j >> 2) * 1024 + (j & 3) * 16, u4v{hx[0], hx[1], hx[2], hx[3]});
    st16(rs, lo + (8 + (j >> 2)) * 1024 + (j & 3) * 16, u4v{hx[4], hx[5], hx[6], hx[7]});
    st16(rs, (lo + (16 + (j >> 3)) * 1024 + (j & 7) * 8) | drop, pairup(hx[8], hx[9]));
  }
  STAMP(3,4);
}

#define FPD 2
#include <type_traits>
typedef unsigned long long u64;
typedef __attribute__((address_space(1))) unsigned long long gu64;
#define WAITL0() do { __builtin_amdgcn_sched_barrier(0); asm volatile("s_waitcnt lgkmcnt(0)" ::: "memory"); __builtin_amdgcn_sched_barrier(0); } while (0)
#define BAR() do { asm volatile("" ::: "memory"); __builtin_amdgcn_sched_barrier(0); __builtin_amdgcn_s_barrier(); __builtin_amdgcn_sched_barrier(0); asm volatile("" ::: "memory"); } while (0)
#define SGB(mask, n) __builtin_amdgcn_sched_group_barrier(mask, n, 0)
DI f4 mfma_b(u4v a, u4v b, f4 c) {
  return __builtin_amdgcn_mfma_f32_16x16x32_bf16(__builtin_bit_cast(bf8, a), __builtin_bit_cast(bf8, b), c, 0, 0, 0);
}
DI f2 gelu2(f2 x) {
  const float k1 = -2.0f * 0.7978845608028654f * 1.4426950408889634f;
  const float k2 = k1 * 0.044715f;
  f2 p = x * x * k2 + k1;
  f2 a = x * p;
  f2 e = f2{__builtin_amdgcn_exp2f(a[0]), __builtin_amdgcn_exp2f(a[1])} + 1.0f;
  f2 r = f2{__builtin_amdgcn_rcpf(e[0]), __builtin_amdgcn_rcpf(e[1])};
  return x * r;
}
DI u2v gelu4(f4 a) {
  f2 g0 = gelu2(f2{a[0], a[1]}), g1 = gelu2(f2{a[2], a[3]});
  return u2v{pk_bf(g0[0], g0[1]), pk_bf(g1[0], g1[1])};
}
template <int N> DI void waitv() { asm volatile("s_waitcnt vmcnt(%0)" ::"n"(N) : "memory"); }

template <int KT, int NST, int NLA, class F>
DI void loader_loop(bool big, F&& stage) {
#pragma unroll
  for (int s = 0; s < NST - 1; ++s) stage(s, s);
  int nbuf = NST - 1;
  for (int kt = 0; kt < KT - (NST - 1); ++kt) {
    if (big) waitv<NLA*(NST - 2)>(); else waitv<(NLA - 1) * (NST - 2)>();
    BAR();
    stage(kt + NST - 1, nbuf);
    nbuf = nbuf + 1 == NST ? 0 : nbuf + 1;
  }
  if constexpr (NST >= 7) { if (big) waitv<NLA * 5>(); else waitv<(NLA - 1) * 5>(); BAR(); }
  if constexpr (NST >= 6) { if (big) waitv<NLA * 4>(); else waitv<(NLA - 1) * 4>(); BAR(); }
  if constexpr (NST >= 5) { if (big) waitv<NLA * 3>(); else waitv<(NLA - 1) * 3>(); BAR(); }
  if (big) waitv<NLA * 2>(); else waitv<(NLA - 1) * 2>(); BAR();
  if (big) waitv<NLA>(); else waitv<NLA - 1>(); BAR();
  waitv<0>(); BAR();
}

#define G1_DEPTH 3
extern "C" __global__ __launch_bounds__(512) void g1_kernel() {
  LOAD_ARGS();
  char* ws = ap_->ws; const u16* w_sp = ap_->w_sp;
  PINS(ws); PINS(w_sp);
  const u32* l2f = (const u32*)(ws + WS_L2); const u32* Wt1 = (const u32*)(ws + WS_WT1);
  u32* z1f = (u32*)(ws + WS_Z1); u16* z2t = (u16*)(ws + WS_Z2); u64* stats = (u64*)(ws + WS_STATS);
  __shared__ __attribute__((aligned(16))) char smem[140 * 1024 + 4 * 4608 + 256];
  const int tid = threadIdx.x, lane = tid & 63;
  const int wave = __builtin_amdgcn_readfirstlane(tid >> 6);
  const int li = lane & 15, g = lane >> 4;
  STAMP(4,0);
  const int xcd = blockIdx.x & 7, idx = blockIdx.x >> 3;
#define G1_MAP 1
  const int mt = G1_MAP == 0 ? xcd * 2 + (idx >> 4) : (G1_MAP == 1 ? (xcd >> 1) * 4 + (idx >> 3) : (xcd >> 2) * 8 + (idx >> 2));
  const int nb = G1_MAP == 0 ? (idx & 15) : (G1_MAP == 1 ? (xcd & 1) * 8 + (idx & 7) : (xcd & 3) * 4 + (idx & 3));
  const int row0 = 1024 * (mt & 1) + 32 * (mt >> 1);
  const int zs = nb < 6 ? 4 * nb : 24 + 3 * (nb - 6);
  const int z1s = nb < 6 ? 3 * nb : (nb < 12 ? 18 + 4 * (nb - 6) : 42 + 3 * (nb - 12));
  const int type = nb < 6 ? 0 : (nb < 12 ? 1 : 2);
  if (wave >= 4) {
    const int lw = wave - 4;
    auto slot_frag = [&](int s) { return s < 3 ? 54 + zs + s : (s == 3 ? (type == 0 ? 54 + zs + 3 : (type == 1 ? z1s + 3 : z1s)) : z1s + s - 4); };
    const u32* bsrc[4];
#pragma unroll
    for (int j = 0; j < 4; ++j) {
      int i = lw + 4 * j; i = i < 14 ? i : 0;
      const int kk = i / 7, s = i - 7 * kk;
      bsrc[j] = Wt1 + (size_t)(kk * 108 + slot_frag(s)) * 256 + lane * 4;
    }
    auto stage = [&](int kt) {
      char* sb = smem + kt * 14336 + lw * 1024;
#pragma unroll
      for (int j = 0; j < 3; ++j) GLDS16(bsrc[j] + (size_t)kt * 2 * 108 * 256, sb + j * 4096);
      if (lw < 2) GLDS16(bsrc[3] + (size_t)kt * 2 * 108 * 256, sb + 3 * 4096);
    };
#pragma unroll
    for (int s = 0; s < G1_DEPTH; ++s) stage(s);
    for (int kt = 0; kt < 10 - G1_DEPTH; ++kt) {
      if (lw < 2) waitv<4 * (G1_DEPTH - 1)>(); else waitv<3 * (G1_DEPTH - 1)>();
      BAR();
      stage(kt + G1_DEPTH);
    }
    if constexpr (G1_DEPTH >= 5) { if (lw < 2) waitv<16>(); else waitv<12>(); BAR(); }
    if constexpr (G1_DEPTH >= 4) { if (lw < 2) waitv<12>(); else waitv<9>(); BAR(); }
    if (lw < 2) waitv<8>(); else waitv<6>(); BAR();
    if (lw < 2) waitv<4>(); else waitv<3>(); BAR();
    waitv<0>();
    char* scratch = smem + 140 * 1024 + 4 * 4608;
    if (lw < PFG1) touch((const char*)w_sp + ((size_t)(blockIdx.x * PFG1 + lw) * 64 + lane) * 128);
    BAR();
    if (lw >= 2) CODE_TOUCH(g2_kernel, end_kernel, lw - 2)
    return;
  }
  const int rg0 = (row0 + wave * 256) >> 4;
  const u32* asrc = l2f + (size_t)rg0 * 20 * 256 + li * 16 + g * 4;
#define G1_AD 5
  u4v fa[G1_AD][2];
#pragma unroll
  for (int k = 0; k < G1_AD; ++k) { fa[k][0] = *(const u4v*)(asrc + k * 256); fa[k][1] = *(const u4v*)(asrc + (20 + k) * 256); __builtin_amdgcn_sched_barrier(0); }
  f4 acc[7][2];
#pragma unroll
  for (int s = 0; s < 7; ++s) { acc[s][0] = f4{0, 0, 0, 0}; acc[s][1] = f4{0, 0, 0, 0}; }
  u4v fw[2][7];
  const char* wb = smem + lane * 16;
  auto rd = [&](int k) {
#pragma unroll
    for (int s = 0; s < 7; ++s) fw[k & 1][s] = *(const u4v*)(wb + (k * 7 + s) * 1024);
  };
  auto mm = [&](int k) {
#pragma unroll
    for (int s = 0; s < 7; ++s) {
      acc[s][0] = mfma_b(fw[k & 1][s], fa[k % G1_AD][0], acc[s][0]);
      acc[s][1] = mfma_b(fw[k & 1][s], fa[k % G1_AD][1], acc[s][1]);
    }
    if (k + G1_AD < 20) { fa[k % G1_AD][0] = *(const u4v*)(asrc + (k + G1_AD) * 256); fa[k % G1_AD][1] = *(const u4v*)(asrc + (20 + G1_AD + k) * 256); }
  };
  BAR(); STAMP(4,3); rd(0);
#pragma unroll
  for (int k = 1; k < 20; ++k) {
    if (!(k & 1)) BAR(); else __builtin_amdgcn_sched_barrier(0);
    if (k == 8) { STAMP(4,6); STAMPC(7,0); }
    if (k == 14) { STAMP(4,7); STAMPC(7,1); }
    rd(k);
    mm(k - 1);
#pragma unroll
    for (int i = 0; i < 7; ++i) { SGB(0x100, 1); SGB(0x8, 2); }
  }
  __builtin_amdgcn_sched_barrier(0);
  mm(19);
  STAMP(4,1);
  const int n2 = type == 0 ? 4 : 3;
  f4 zv[4][2];
  u2v z3[2];
  float lsum[2], lm2[2];
#pragma unroll
  for (int sf = 0; sf < 2; ++sf) {
    f2 s = f2{0.f, 0.f};
#pragma unroll
    for (int j = 0; j < 4; ++j) {
      const u2v t = gelu4(acc[j][sf]);
      if (j == 3) z3[sf] = t;
      zv[j][sf] = f4{bflo(t[0]), bfhi(t[0]), bflo(t[1]), bfhi(t[1])};
      if (j < 3 || type == 0) s += f2{zv[j][sf][0], zv[j][sf][1]} + f2{zv[j][sf][2], zv[j][sf][3]};
    }
    const float st = xsum_g(s[0] + s[1]);
    const float lmean = st * (type == 0 ? (1.0f / 64.0f) : (1.0f / 48.0f));
    f2 q = f2{0.f, 0.f};
#pragma unroll
    for (int j = 0; j < 4; ++j) {
      const f4 d = zv[j][sf] - lmean;
      if (j < 3 || type == 0) { q += f2{d[0], d[1]} * f2{d[0], d[1]}; q += f2{d[2], d[3]} * f2{d[2], d[3]}; }
    }
    lsum[sf] = st; lm2[sf] = xsum_g(q[0] + q[1]);
  }
  gu64* st = (gu64*)stats + (size_t)mt * 16 * 128 + wave * 32;
  if (lane < 32) {
    u32 a = __float_as_uint(g ? lsum[1] : lsum[0]), b = __float_as_uint(g ? lm2[1] : lm2[0]);
    if (a == ~0u) a = 0x7fc00000u;
    __hip_atomic_store(st + nb * 128 + lane, ((u64)b << 32) | a, __ATOMIC_RELAXED, __HIP_MEMORY_SCOPE_AGENT);
  }
  __builtin_amdgcn_sched_barrier(0);
  STAMP(4,2);
  const __amdgpu_buffer_rsrc_t rs = ws_rsrc(ws);
  u4v z1o[3];
#pragma unroll
  for (int j = 0; j < 3; ++j) {
    const u2v a = gelu4(acc[4 + j][0]), b = gelu4(acc[4 + j][1]);
    z1o[j] = u4v{a[0], a[1], b[0], b[1]};
    PIN(z1o[j]);
  }
  __builtin_amdgcn_sched_barrier(0);
  u64 gv[2][4];
  for (int spin = 0; spin < (1 << 16); ++spin) {
    bool ok = true;
#pragma unroll
    for (int sf = 0; sf < 2; ++sf)
#pragma unroll
      for (int i = 0; i < 4; ++i) {
        gv[sf][i] = __hip_atomic_load(st + (4 * g + i) * 128 + sf * 16 + li, __ATOMIC_RELAXED, __HIP_MEMORY_SCOPE_AGENT);
        ok &= ((u32)gv[sf][i] != ~0u);
      }
    if (__all(ok)) break;
    __builtin_amdgcn_s_sleep(1);
  }
  STAMP(4,4);
  {
    const u32 zo = (u32)WS_Z1 + ((rg0 >> 1) * 54 + z1s) * 1024 + lane * 16;
#pragma unroll
    for (int j = 0; j < 3; ++j) st16(rs, zo + j * 1024, z1o[j]);
    if (type == 1) st16(rs, zo + 3 * 1024, u4v{z3[0][0], z3[0][1], z3[1][0], z3[1][1]});
  }
  char* tb = smem + 140 * 1024 + wave * 4608;
#pragma unroll
  for (int sf = 0; sf < 2; ++sf) {
    float s = 0.f;
#pragma unroll
    for (int i = 0; i < 4; ++i) s += __uint_as_float((u32)gv[sf][i]);
    const float mean = xsum_g(s) * (1.0f / 864.0f);
    float q = 0.f;
#pragma unroll
    for (int i = 0; i < 4; ++i) {
      const bool big = (4 * g + i) < 6;
      const float dm = __uint_as_float((u32)gv[sf][i]) * (big ? (1.0f / 64.0f) : (1.0f / 48.0f)) - mean;
      q += __uint_as_float((u32)(gv[sf][i] >> 32)) + (big ? 64.f : 48.f) * dm * dm;
    }
    const float var = xsum_g(q) * (1.0f / 864.0f);
    const float mb = rbf(mean);
    const float rinv = __builtin_amdgcn_rcpf(rbf(__builtin_amdgcn_sqrtf(rbf(rbf(var) + 1e-5f))));
#pragma unroll
    for (int j = 0; j < 4; ++j) {
      const f4 o = (zv[j][sf] - mb) * rinv;
      *(u2v*)(tb + (sf * 16 + li) * 144 + j * 32 + g * 8) = u2v{pk_bf(o[0], o[1]), pk_bf(o[2], o[3])};
    }
  }
  {
    const int q = li >> 2, p = li & 3;
#pragma unroll
    for (int j = 0; j < 4; ++j) {
      s4 lo = __builtin_amdgcn_ds_read_tr16_b64_v4i16((LDS_AS s4*)(tb + (8 * g + q) * 144 + j * 32 + 8 * p));
      s4 hi = __builtin_amdgcn_ds_read_tr16_b64_v4i16((LDS_AS s4*)(tb + (8 * g + 4 + q) * 144 + j * 32 + 8 * p));
      u2v a = __builtin_bit_cast(u2v, lo), b = __builtin_bit_cast(u2v, hi);
      if (j < n2) st16(rs, (u32)WS_Z2 + (((zs + j) * 16 + li) * S + row0 + wave * 256 + 8 * g) * 2, u4v{a[0], a[1], b[0], b[1]});
    }
  }
  STAMP(4,5);
}

typedef __attribute__((address_space(1))) unsigned int gu32;
#define PSLEEP 1
constexpr int G2_NST = 7, G2_STB = 22 * 1024;
extern "C" __global__ __launch_bounds__(512) void g2_kernel() {
  LOAD_ARGS();
  char* ws = ap_->ws; const u16* w_sp = ap_->w_sp; const u16* b_sp = ap_->b_sp; u16* out = ap_->out;
  PINS(w_sp); PINS(b_sp); PINS(ws); PINS(out);
  const u16* z2t = (const u16*)(ws + WS_Z2); const u32* z1f = (const u32*)(ws + WS_Z1);
  __shared__ __attribute__((aligned(16))) char smem[G2_NST * G2_STB + 256];
  const int tid = threadIdx.x, lane = tid & 63;
  const int wave = __builtin_amdgcn_readfirstlane(tid >> 6);
  const int li = lane & 15, g = lane >> 4;
  STAMP(5,0);
  const int xcd = blockIdx.x & 7, idx = blockIdx.x >> 3;
  const int tt = (xcd >> 1) * 8 + (idx >> 2), ft = (xcd & 1) * 4 + (idx & 3);
  if (wave >= 4) {
    const int lw = wave - 4;
    const u16* src[6];
    const int swz = ((lane & 7) ^ (lane >> 3)) * 8;
#pragma unroll
    for (int j = 0; j < 6; ++j) {
      int i = lw + 4 * j; i = i < 22 ? i : 8;
      if (j < 2) src[j] = w_sp + (size_t)(tt * 64 + 8 * i + (lane >> 3)) * S + swz;
      else { int r = ft * 112 + 8 * (i - 8) + (lane >> 3); r = r < FH ? r : FH - 1; src[j] = z2t + (size_t)r * S + swz; }
    }
    auto stage = [&](int kt, int buf) {
      char* sb = smem + buf * G2_STB + lw * 1024;
      const int kk = kt * 64;
#pragma unroll
      for (int j = 0; j < 5; ++j) GLDS16(src[j] + kk, sb + j * 4096);
      if (lw < 2) GLDS16(src[5] + kk, sb + 5 * 4096);
    };
    const bool big = lw < 2;
    stage(0, 0); stage(1, 1); stage(2, 2);
    if (big) waitv<12>(); else waitv<10>(); BAR();
    stage(3, 3); stage(4, 4);
    if (big) waitv<18>(); else waitv<15>(); BAR();
    stage(5, 5); stage(6, 6);
    if (big) waitv<24>(); else waitv<20>(); BAR();
    stage(7, 0); stage(8, 1);
    int nbuf = 2;
    for (int kt = 3; kt < 26; ++kt) {
      if (big) waitv<30>(); else waitv<25>();
      BAR();
      stage(kt + 6, nbuf);
      nbuf = nbuf + 1 == G2_NST ? 0 : nbuf + 1;
    }
    const u32* wsrc = (const u32*)(ws + WS_WT3) + (size_t)(ft * 5) * 256 + lane * 4;
    for (int ps = 0; ps < 6; ++ps) {
      if (big) waitv<30>(); else waitv<25>();
      BAR();
      char* sb = smem + nbuf * G2_STB + lw * 1024;
#pragma unroll
      for (int j = 0; j < 6; ++j) {
        const int i = ps * 22 + lw + 4 * j, k = i / 5;
        if (j < 5 || big) GLDS16(wsrc + (size_t)(k * 35 + i) * 256, sb + j * 4096);
      }
      nbuf = nbuf + 1 == G2_NST ? 0 : nbuf + 1;
    }
    BAR();
    if (lw < 3) GLDS16(wsrc + (size_t)(26 * 35 + 132 + lw) * 256, smem + nbuf * G2_STB + lw * 1024);
    waitv<0>();
    BAR();
    return;
  }
  const int wt = wave & 1, wf = wave >> 1;
  const int n0 = (ft & 1) ? 3 : 4;
  const int f0 = wf ? n0 : 0, nf = wf ? 7 - n0 : n0;
  u4v z1v[4];
  float bias[2];
#pragma unroll
  for (int j = 0; j < 4; ++j) {
    int ff = ft * 7 + f0 + j; ff = ff < 54 ? ff : 53;
    z1v[j] = *(const u4v*)(z1f + ((size_t)(tt * 2 + wt) * 54 + ff) * 256 + lane * 4);
  }
  bias[0] = bflo(b_sp[tt * 64 + wt * 32 + li]); bias[1] = bflo(b_sp[tt * 64 + wt * 32 + 16 + li]);
  f4 acc[4][2];
#pragma unroll
  for (int j = 0; j < 4; ++j) { acc[j][0] = f4{0, 0, 0, 0}; acc[j][1] = f4{0, 0, 0, 0}; }
  const int sw = (g ^ (li & 7)) * 16;
  const int toff = (wt * 32 + li) * 128 + sw;
  const int foff = 8192 + (f0 * 16 + li) * 128 + sw;
  auto body = [&](auto NFc) {
    constexpr int NF = decltype(NFc)::value;
    u4v fb[2][2][2], fa[2][2][NF];
    auto rd = [&](int set, int buf) {
      const char* sb = smem + buf * G2_STB;
#pragma unroll
      for (int kk = 0; kk < 2; ++kk) {
        fb[set][kk][0] = *(const u4v*)(sb + (toff ^ (kk * 64)));
        fb[set][kk][1] = *(const u4v*)(sb + (toff ^ (kk * 64)) + 2048);
#pragma unroll
        for (int j = 0; j < NF; ++j) fa[set][kk][j] = *(const u4v*)(sb + (foff ^ (kk * 64)) + j * 2048);
      }
    };
    auto mm = [&](int set) {
#pragma unroll
      for (int kk = 0; kk < 2; ++kk)
#pragma unroll
        for (int j = 0; j < NF; ++j) {
          acc[j][0] = mfma_b(fa[set][kk][j], fb[set][kk][0], acc[j][0]);
          acc[j][1] = mfma_b(fa[set][kk][j], fb[set][kk][1], acc[j][1]);
        }
    };
    auto sched = [&]() {
#pragma unroll
      for (int i = 0; i < 2 * NF + 4; ++i) { SGB(0x100, 1); SGB(0x8, 1); }
      SGB(0x8, 2 * NF - 4);
    };
    BAR(); STAMP(5,3); rd(0, 0); WAITL0();
    int buf = 1;
    for (int kt = 1; kt < 31; kt += 2) {
      if (kt == 7) STAMP(5,4);
      if (kt == 23) STAMP(5,6);
      BAR(); rd(1, buf); mm(0); sched(); WAITL0();
      buf = buf + 1 == G2_NST ? 0 : buf + 1;
      BAR(); rd(0, buf); mm(1); sched(); WAITL0();
      buf = buf + 1 == G2_NST ? 0 : buf + 1;
    }
    BAR(); rd(1, buf); mm(0); sched(); WAITL0();
#pragma unroll
    for (int j = 0; j < 4; ++j) PIN(z1v[j]);
    PIN(bias[0]); PIN(bias[1]);
    BAR();
    mm(1);
  };
  if (nf == 4) body(std::integral_constant<int, 4>{}); else body(std::integral_constant<int, 3>{});
  STAMP(5,1);
  const __amdgpu_buffer_rsrc_t rs = ws_rsrc(ws);
  const int ff0 = ft * 7 + f0;
#pragma unroll
  for (int tf = 0; tf < 2; ++tf) {
    const int tfr = tt * 4 + wt * 2 + tf;
    u2v o[4];
#pragma unroll
    for (int j = 0; j < 4; ++j) {
      const u32 za = z1v[j][2 * tf], zb = z1v[j][2 * tf + 1];
      const f4 v = (acc[j][tf] + bias[tf]) * f4{bflo(za), bfhi(za), bflo(zb), bfhi(zb)};
      o[j] = u2v{pk_bf(v[0], v[1]), pk_bf(v[2], v[3])};
    }
    const u32 base = (u32)WS_P + (tfr * 27 + (ff0 >> 1)) * 1024 + lane * 16;
    if (!(ff0 & 1)) {
      st16(rs, base, u4v{o[0][0], o[0][1], o[1][0], o[1][1]});
      if (nf == 4) { if (ff0 + 3 < 54) st16(rs, base + 1024, u4v{o[2][0], o[2][1], o[3][0], o[3][1]}); }
      else st8(rs, base + 1024, o[2]);
    } else {
      st8(rs, base + 8, o[0]);
      st16(rs, base + 1024, u4v{o[1][0], o[1][1], o[2][0], o[2][1]});
    }
  }
  gu32* cnt = (gu32*)(ws + WS_CNT);
  waitv<0>();
  if (lane == 0) __hip_atomic_store(cnt + (tt * 2 + wt) * 32 + ft * 2 + wf, 1u, __ATOMIC_RELAXED, __HIP_MEMORY_SCOPE_AGENT);
  STAMP(5,2);
  const int trow = tt * 64 + wave * 16 + li;
  const u16* h = (const u16*)(ws + WS_H);
  u2v hv[5];
#pragma unroll
  for (int j = 0; j < 5; ++j) hv[j] = *(const u2v*)(h + (size_t)trow * D + ft * 80 + j * 16 + g * 4);
  for (int spin = 0; spin < (1 << 16); ++spin) {
    const u32 c = __hip_atomic_load(cnt + (tt * 2 + (wave >> 1)) * 32 + li, __ATOMIC_RELAXED, __HIP_MEMORY_SCOPE_AGENT);
    if (__all(c != 0u)) break;
    __builtin_amdgcn_s_sleep(PSLEEP);
  }
  asm volatile("" ::: "memory");
  STAMP(6,0);
  u4v fb[27];
  const u32 poff = (u32)WS_P + (tt * 4 + wave) * 27 * 1024 + lane * 16;
#pragma unroll
  for (int k = 0; k < 3 * FPD; ++k) { fb[k] = __builtin_amdgcn_raw_buffer_load_b128(rs, poff + k * 1024, 0, AUX_SC1); SB(); }
  f4 acc3[5];
#pragma unroll
  for (int j = 0; j < 5; ++j) acc3[j] = f4{0, 0, 0, 0};
  u4v fa[2][15];
  u32 wo[3] = {(u32)lane * 16, (u32)lane * 16 + 3 * G2_STB - 8192, (u32)lane * 16 + 5 * G2_STB};
  PIN(wo[1]); PIN(wo[2]);
  auto rd3 = [&](int kt) {
#pragma unroll
    for (int ii = 0; ii < 15; ++ii) {
      const int i = kt * 15 + ii;
      const int off = ((32 + i / 22) % G2_NST) * G2_STB + (i % 22) * 1024;
      const int w = off < 3 * G2_STB - 8192 ? 0 : (off < 5 * G2_STB ? 1 : 2);
      fa[kt & 1][ii] = *(const u4v*)(smem + wo[w] + (off - (w == 0 ? 0 : (w == 1 ? 3 * G2_STB - 8192 : 5 * G2_STB))));
    }
  };
  auto mm3 = [&](int kt) {
#pragma unroll
    for (int kk = 0; kk < 3; ++kk)
#pragma unroll
      for (int j = 0; j < 5; ++j) acc3[j] = mfma_b(fa[kt & 1][kk * 5 + j], fb[kt * 3 + kk], acc3[j]);
  };
  BAR();
  STAMP(6,3);
  rd3(0);
#pragma unroll
  for (int kt = 1; kt < 9; ++kt) {
    SB();
    if (kt - 1 + FPD < 9) {
#pragma unroll
      for (int k = 0; k < 3; ++k) fb[(kt - 1 + FPD) * 3 + k] = __builtin_amdgcn_raw_buffer_load_b128(rs, poff + ((kt - 1 + FPD) * 3 + k) * 1024, 0, AUX_SC1);
      SB();
    }
    rd3(kt);
    mm3(kt - 1);
#pragma unroll
    for (int i = 0; i < 15; ++i) { SGB(0x100, 1); SGB(0x8, 1); }
  }
  SB();
  mm3(8);
  STAMP(6,1);
  {
    u2v o[5];
#pragma unroll
    for (int j = 0; j < 5; ++j) {
      u32 t0 = pk_bf(acc3[j][0], acc3[j][1]), t1 = pk_bf(acc3[j][2], acc3[j][3]);
      o[j] = u2v{pk_bf(bflo(t0) + bflo(hv[j][0]), bfhi(t0) + bfhi(hv[j][0])), pk_bf(bflo(t1) + bflo(hv[j][1]), bfhi(t1) + bfhi(hv[j][1]))};
    }
    const __amdgpu_buffer_rsrc_t ro = __builtin_amdgcn_make_buffer_rsrc((void*)out, 0, S * D * 2, 0x00020000);
    const u32 oo = (trow * D + ft * 80) * 2;
#define OUT_AUX 16
#pragma unroll
    for (int j = 0; j < 4; j += 2) {
      auto s0 = __builtin_amdgcn_permlane16_swap(o[j][0], o[j + 1][0], false, false);
      auto s1 = __builtin_amdgcn_permlane16_swap(o[j][1], o[j + 1][1], false, false);
      __builtin_amdgcn_raw_buffer_store_b128(u4v{s0[0], s1[0], s0[1], s1[1]}, ro, oo + (j + (g & 1)) * 32 + (g >> 1) * 16, 0, OUT_AUX);
    }
    __builtin_amdgcn_raw_buffer_store_b64(o[4], ro, oo + 128 + g * 8, 0, OUT_AUX);
  }
  STAMP(6,2);
}

extern "C" __global__ void end_kernel() {}

extern "C" { long long dbg_offsets[16] = {(long long)WS_H, -1, -1, (long long)WS_Z2, -1, -1, -1}; }

__global__ void diag_kernel() {
  unsigned long long t0 = __builtin_amdgcn_s_memrealtime();
  unsigned long long ta = g_t[SELA], tb = SELB < 0 ? t0 : g_t[SELB < 0 ? 0 : SELB];
  unsigned long long d = (tb - ta) * 10;
  if (d > 100000) d = 100000;
  while (__builtin_amdgcn_s_memrealtime() - t0 < d) __builtin_amdgcn_s_sleep(1);
}
extern "C" void kernel_launch(void* const* d_in, const int* in_sizes, int n_in,
                              void* d_out, int out_size, void* d_ws, size_t ws_size,
                              hipStream_t stream) {
  const u16* x=(const u16*)d_in[0]; const u16* wr=(const u16*)d_in[1]; const u16* wi=(const u16*)d_in[2];
  const u16* w_in=(const u16*)d_in[4]; const u16* w_sp=(const u16*)d_in[5]; const u16* b_sp=(const u16*)d_in[6]; const u16* w_out=(const u16*)d_in[7];
  char* ws=(char*)d_ws;
  k1_kernel<<<256,512,0,stream>>>(x,wr,wi,ws,w_sp,w_in,w_out,b_sp,(u16*)d_out);
  (void)hipFuncSetAttribute((const void*)k2_kernel, hipFuncAttributeMaxDynamicSharedMemorySize, 84 * 1024);
  k2_kernel<<<K2_FFT_BLOCKS+K2_PREP_BLOCKS,512,84 * 1024,stream>>>();
  k3_kernel<<<256,256,0,stream>>>();
  g1_kernel<<<256,512,0,stream>>>();
  g2_kernel<<<256,512,0,stream>>>();
  diag_kernel<<<1,64,0,stream>>>();
}
```

```cpp
#include <hip/hip_runtime.h>
#include <hip/hip_bf16.h>
#include <stdint.h>
typedef unsigned short u16;
typedef unsigned int u32;
typedef _Float16 h2 __attribute__((ext_vector_type(2)));
typedef _Float16 h8 __attribute__((ext_vector_type(8)));
typedef __bf16 bf2 __attribute__((ext_vector_type(2)));
typedef __bf16 bf8 __attribute__((ext_vector_type(8)));
typedef float f2 __attribute__((ext_vector_type(2)));
typedef float f4 __attribute__((ext_vector_type(4)));
typedef short s4 __attribute__((ext_vector_type(4)));
typedef u32 u2v __attribute__((ext_vector_type(2)));
typedef u32 u4v __attribute__((ext_vector_type(4)));
#define DI __device__ __forceinline__
#define LDS_AS __attribute__((address_space(3)))
#define PIN(x) asm volatile("" : "+v"(x))
#define GLDS16(src, dst) __builtin_amdgcn_global_load_lds((const u32*)(src), (LDS_AS u32*)(dst), 16, 0, 0)
#define WAITV(n) asm volatile("s_waitcnt vmcnt(%0)" ::"n"(n) : "memory")

constexpr int S = 2048, D = 640, DF = 321, FF = 1728, FH = 864;
constexpr int TROW_UNITS = 648;
constexpr int NUNITS = 641;
constexpr int NPREP = 168;

constexpr size_t WS_T = 0, WS_WP = 2688u << 10, WS_TP = 5376u << 10, WS_WT1 = 8064u << 10, WS_WT3 = 10240u << 10, WS_H = 11392u << 10,
                 WS_L2 = 13952u << 10, WS_Z1 = 16512u << 10, WS_Z2 = 19968u << 10, WS_STATS = 23424u << 10, WS_W2 = 23680u << 10,
                 WS_WMAX = 23692u << 10, WS_UNS = 23696u << 10, WS_CNT = 23700u << 10, WS_P = WS_T;
#define PINS(x) asm volatile("" ::"s"(x))
#define GLDS4(src, dst) __builtin_amdgcn_global_load_lds((const u32*)(src), (LDS_AS u32*)(dst), 4, 0, 0)
#define touch(p) GLDS4(p, scratch)

constexpr int AUX_SC1 = 16;
constexpr u32 DROP = 0x80000000u;
DI __amdgpu_buffer_rsrc_t ws_rsrc(char* ws) { return __builtin_amdgcn_make_buffer_rsrc((void*)ws, 0, 64 << 20, 0x00020000); }
DI void st16(__amdgpu_buffer_rsrc_t rs, u32 off, u4v v) { __builtin_amdgcn_raw_buffer_store_b128(v, rs, off, 0, AUX_SC1); }
DI void st8(__amdgpu_buffer_rsrc_t rs, u32 off, u2v v) { __builtin_amdgcn_raw_buffer_store_b64(v, rs, off, 0, AUX_SC1); }
#define CODE_ADDR(var, sym)                                                                                     \
  const char* var;                                                                                              \
  {                                                                                                             \
    u32 lo_, hi_;                                                                                               \
    asm volatile("s_getpc_b64 s[40:41]\n\ts_add_u32 %0, s40, " #sym "@rel32@lo+4\n\ts_addc_u32 %1, s41, " #sym "@rel32@hi+12" \
                 : "=s"(lo_), "=s"(hi_)::"s40", "s41", "scc");                                                  \
    var = (const char*)(((unsigned long long)hi_ << 32) | lo_);                                                 \
  }
#define CODE_TOUCH(a, b, c)                                                           \
  {                                                                                   \
    CODE_ADDR(pa_, a) CODE_ADDR(pb_, b)                                               \
    const char* p_ = pa_ + ((c) * 64 + lane) * 128;                                   \
    if (p_ < pb_) touch(p_);                                                          \
  }
struct Args { char* ws; const u16* x; const u16* w_sp; const u16* w_in; const u16* w_out; const u16* b_sp; u16* out; void* pad; };
static __device__ __attribute__((aligned(128))) Args g_args;
typedef const __attribute__((address_space(4))) Args* ArgsPtr;
#define LOAD_ARGS() ArgsPtr ap_ = (ArgsPtr)(unsigned long long)&g_args
DI float bflo(u32 v) { return __uint_as_float(v << 16); }
DI float bfhi(u32 v) { return __uint_as_float(v & 0xffff0000u); }
DI u32 pk_bf(float a, float b) { f2 v = {a, b}; return __builtin_bit_cast(u32, __builtin_convertvector(v, bf2)); }
DI u32 pk_h(float a, float b) { f2 v = {a, b}; return __builtin_bit_cast(u32, __builtin_convertvector(v, h2)); }
DI float rbf(float a) { return bflo(pk_bf(a, a)); }
DI float cosr(float rev) { return __builtin_amdgcn_cosf(rev); }
DI float sinr(float rev) { return __builtin_amdgcn_sinf(rev); }
DI f4 mfma_h(u4v a, u4v b, f4 c) {
  return __builtin_amdgcn_mfma_f32_16x16x32_f16(__builtin_bit_cast(h8, a), __builtin_bit_cast(h8, b), c, 0, 0, 0);
}
template <int CTRL> DI float dpp_add(float v) {
  int t = __builtin_amdgcn_update_dpp(0, __float_as_int(v), CTRL, 0xf, 0xf, true);
  return v + __int_as_float(t);
}
DI float xsum_g(float v) {
  u32 b = __float_as_uint(v);
  auto r = __builtin_amdgcn_permlane16_swap(b, b, false, false);
  v = __uint_as_float(r[0]) + __uint_as_float(r[1]);
  b = __float_as_uint(v);
  auto s = __builtin_amdgcn_permlane32_swap(b, b, false, false);
  return __uint_as_float(s[0]) + __uint_as_float(s[1]);
}
template <int CTRL> DI u32 dpp_maxu(u32 v) {
  u32 t = (u32)__builtin_amdgcn_update_dpp(0, (int)v, CTRL, 0xf, 0xf, true);
  return v > t ? v : t;
}
DI u32 max64u(u32 v) {
  v = dpp_maxu<0xB1>(v); v = dpp_maxu<0x4E>(v); v = dpp_maxu<0x141>(v); v = dpp_maxu<0x140>(v);
  auto r = __builtin_amdgcn_permlane16_swap(v, v, false, false);
  v = r[0] > r[1] ? r[0] : r[1];
  auto s = __builtin_amdgcn_permlane32_swap(v, v, false, false);
  return s[0] > s[1] ? s[0] : s[1];
}
DI float sum32(float v) {
  v = dpp_add<0xB1>(v); v = dpp_add<0x4E>(v); v = dpp_add<0x141>(v); v = dpp_add<0x140>(v);
  u32 b = __float_as_uint(v);
  auto r = __builtin_amdgcn_permlane16_swap(b, b, false, false);
  return __uint_as_float(r[0]) + __uint_as_float(r[1]);
}
DI void ln_row(f2 (&v)[10]) {
  f2 s = ((v[0] + v[1]) + (v[2] + v[3])) + ((v[4] + v[5]) + (v[6] + v[7])) + (v[8] + v[9]);
  const float mean = sum32(s[0] + s[1]) * (1.0f / 640.0f);
  f2 q0 = f2{0.f, 0.f}, q1 = f2{0.f, 0.f};
#pragma unroll
  for (int i = 0; i < 10; i += 2) { v[i] -= mean; v[i + 1] -= mean; q0 += v[i] * v[i]; q1 += v[i + 1] * v[i + 1]; }
  q0 += q1;
  const float var = sum32(q0[0] + q0[1]) * (1.0f / 640.0f);
  const float rinv = __builtin_amdgcn_rcpf(rbf(__builtin_amdgcn_sqrtf(rbf(rbf(var) + 1e-5f))));
  const float off = (mean - rbf(mean)) * rinv;
#pragma unroll
  for (int i = 0; i < 10; ++i) v[i] = v[i] * rinv + off;
}
#define SB() __builtin_amdgcn_sched_barrier(0)
template <bool SWAP> DI u4v cmul_pack(f4 re, f4 im, f4 wr, f4 wi) {
  const f4 ore = re * wr - im * wi, oim = re * wi + im * wr;
  u4v v;
#pragma unroll
  for (int r = 0; r < 4; ++r) v[r] = SWAP ? pk_h(oim[r], ore[r]) : pk_h(ore[r], oim[r]);
  return v;
}

struct __attribute__((packed, aligned(2))) U4U { u32 a, b, c, d; };

DI void lds_signal(char* p) {
  asm volatile("s_waitcnt lgkmcnt(0)" ::: "memory");
  if ((threadIdx.x & 63) == 0) __hip_atomic_fetch_add((LDS_AS u32*)p, 1u, __ATOMIC_RELAXED, __HIP_MEMORY_SCOPE_WORKGROUP);
}
DI void lds_wait(char* p, u32 n) {
  while (__builtin_amdgcn_readfirstlane(__hip_atomic_load((LDS_AS u32*)p, __ATOMIC_RELAXED, __HIP_MEMORY_SCOPE_WORKGROUP)) < n) {}
  asm volatile("" ::: "memory");
}
constexpr int K1_F40 = 10272, K1_TW = K1_F40 + 18432, K1_R16 = K1_TW + 6144, K1_LDS = K1_R16 + 4096 + 256 + 16;
extern "C" __global__ __launch_bounds__(512) void k1_kernel(const u16* __restrict__ x, const u16* __restrict__ wr,
                                                            const u16* __restrict__ wi, char* __restrict__ ws,
                                                            const u16* __restrict__ w_sp, const u16* w_in, const u16* w_out,
                                                            const u16* b_sp, u16* out) {
  __shared__ __attribute__((aligned(16))) char smem[K1_LDS];
  const int tid = threadIdx.x, lane = tid & 63;
  const int wave = __builtin_amdgcn_readfirstlane(tid >> 6);
  const int li = lane & 15, g = lane >> 4;
  char* scratch = smem + K1_R16 + 4096;
  char* flags = scratch + 256;
  if (wave == 7) CODE_TOUCH(k1_kernel, end_kernel, (blockIdx.x >> 3) & 7)
  PINS(x); PINS(wr); PINS(wi); PINS(ws); PINS(w_sp);
  auto constants = [&]() {
#pragma unroll
    for (int it = 0; it < 2; ++it) {
      const int item = wave + 8 * it;
      if (it == 0 || wave == 0) {
        const int to = item / 3, tk = item - 3 * to;
        const int kb = 16 * to + li;
        const float s8 = (tk < 2 || g < 2) ? 0.125f : 0.f;
        u4v fre, fim;
#pragma unroll
        for (int jj = 0; jj < 4; ++jj) {
          const float ang = (float)(kb * (16 * tk + 4 * g + jj)) * (1.0f / 40.0f);
          const float c = cosr(ang) * s8, s = sinr(ang) * s8;
          fre[jj] = pk_h(c, s);
          fim[jj] = pk_h(-s, c);
        }
        *(u4v*)(smem + K1_F40 + item * 2048 + lane * 16) = fre;
        *(u4v*)(smem + K1_F40 + item * 2048 + 1024 + lane * 16) = fim;
      } else if (wave < 4) {
        const int t = wave - 1;
        f4 twr, twi;
#pragma unroll
        for (int r = 0; r < 4; ++r) {
          const float ang = (float)((16 * t + 4 * g + r) * li) * (1.0f / 640.0f);
          twr[r] = cosr(ang) * 0.125f; twi[r] = -sinr(ang) * 0.125f;
        }
        *(f4*)(smem + K1_TW + t * 2048 + lane * 16) = twr;
        *(f4*)(smem + K1_TW + t * 2048 + 1024 + lane * 16) = twi;
      } else {
        const int ph = 2 * (wave - 4) + (lane >> 5);
        float cs[8];
#pragma unroll
        for (int jj = 0; jj < 8; ++jj) cs[jj] = cosr((float)(((8 * (g & 1) + jj) * li + 2 * ph) & 15) * 0.0625f);
        *(u4v*)(smem + K1_R16 + (wave - 4) * 1024 + lane * 16) = u4v{pk_h(cs[0], cs[1]), pk_h(cs[2], cs[3]), pk_h(cs[4], cs[5]), pk_h(cs[6], cs[7])};
      }
    }
    if (tid < 4) { *(u32*)(smem + 10240 + tid * 4) = 0u; *(u32*)(flags + tid * 4) = 0u; }
    asm volatile("s_waitcnt lgkmcnt(0)" ::: "memory");
    __builtin_amdgcn_s_barrier();
    asm volatile("" ::: "memory");
  };
  if (wave >= 4) {
    const int pb = blockIdx.x < NPREP ? blockIdx.x : 0;
    const int cp = pb & 7, cc = pb >> 3;
    const int c0 = cc < 20 ? cc * 16 : 305;
    const bool mir = cp > 4;
    const bool sp = (cp & 3) == 0;
    const int e = mir ? 255 - (tid - 256) : tid - 256;
    const size_t roff = (size_t)(cp + 8 * e) * DF + c0;
    const U4U* pr = (const U4U*)(wr + roff);
    const U4U* pi = (const U4U*)(wi + roff);
    U4U r0, r1, i0, i1;
    if (blockIdx.x < NPREP) { r0 = pr[0]; r1 = pr[1]; i0 = pi[0]; i1 = pi[1]; }
    SB();
    constants();
    if (blockIdx.x >= NPREP) {
      if (blockIdx.x == 255 && tid == 256) g_args = Args{ws, x, w_sp, w_in, w_out, b_sp, out, nullptr};
      WAITV(0);
      return;
    }
    const u32 flip = mir ? 0x80000000u : 0u;
    const int q1 = (mir ? 8 - cp : cp) - 1;
    const int kb = mir ? 39 - cc : cc;
    const int t1 = sp ? 1920 + 32 * cc + (cp >> 2) : (q1 * 160 + (kb >> 2) * 16 + (mir ? 16 : 0)) * 4 + (kb & 3);
    const int step = sp ? 2 : (mir ? -4 : 4);
    const int t0 = mir ? (q1 * 160 + ((kb + 1) >> 2) * 16) * 4 + ((kb + 1) & 3) : t1;
    const bool edge = (cc == 0 || cc == 20);
    const __amdgpu_buffer_rsrc_t rs = ws_rsrc(ws);
    const u32 slot = (u32)WS_WP + ((wave - 4) * 16 + li) * 16;
    u32 doff[4];
#pragma unroll
    for (int m = 0; m < 4; ++m) {
      const int jc = 4 * m + g;
      doff[m] = slot + (u32)(t1 + step * jc) * 1024u;
      if (cc == 20) doff[m] = DROP;
    }
    if (g == 0 && cc < 20) doff[0] = (mir && edge) ? DROP : slot + (u32)t0 * 1024u;
    if (g == 3 && cc == 20 && !mir) doff[3] = slot + (u32)t0 * 1024u;
    u32 rr[8] = {r0.a, r0.b, r0.c, r0.d, r1.a, r1.b, r1.c, r1.d};
    u32 ii[8] = {i0.a, i0.b, i0.c, i0.d, i1.a, i1.b, i1.c, i1.d};
    u32 vals[16];
#pragma unroll
    for (int j = 0; j < 16; ++j) {
      u32 a = rr[j >> 1], b = ii[j >> 1];
      vals[j] = ((j & 1) ? __builtin_amdgcn_perm(b, a, 0x07060302u) : __builtin_amdgcn_perm(b, a, 0x05040100u)) ^ flip;
    }
    if (edge && (mir || sp)) {
      const u32 v0 = cc < 20 ? vals[0] : vals[15];
      const int e2 = (cp == 0) ? ((256 - e) & 255) : 255 - e;
      const int dpos2 = ((e2 & 15) + 16 * (e2 >> 6)) * 4 + ((e2 >> 4) & 3);
      const int cls = mir ? 8 - cp : cp;
      ((u32*)(ws + WS_W2))[(cls * 2 + (cc == 20)) * 256 + dpos2] = v0 ^ flip ^ 0x80000000u;
    }
    u32 mx = 0;
#pragma unroll
    for (int j = 0; j < 16; ++j) {
      const u32 a = vals[j] & 0x7fff7fffu;
      mx = max(mx, max(a & 0xffffu, a >> 16));
    }
#pragma unroll
    for (int m = 0; m < 4; ++m) {
      auto p02 = __builtin_amdgcn_permlane32_swap(vals[4 * m], vals[4 * m + 2], false, false);
      auto p13 = __builtin_amdgcn_permlane32_swap(vals[4 * m + 1], vals[4 * m + 3], false, false);
      auto q01 = __builtin_amdgcn_permlane16_swap(p02[0], p13[0], false, false);
      auto q23 = __builtin_amdgcn_permlane16_swap(p02[1], p13[1], false, false);
      st16(rs, doff[m], u4v{q01[0], q01[1], q23[0], q23[1]});
    }
    mx = max64u(mx);
    if (lane == 0) {
      __hip_atomic_fetch_max((LDS_AS u32*)(flags + 4), mx, __ATOMIC_RELAXED, __HIP_MEMORY_SCOPE_WORKGROUP);
      asm volatile("s_waitcnt lgkmcnt(0)" ::: "memory");
      if (__hip_atomic_fetch_add((LDS_AS u32*)(flags + 8), 1u, __ATOMIC_RELAXED, __HIP_MEMORY_SCOPE_WORKGROUP) == 3u)
        ((u32*)(ws + WS_WMAX))[pb] = __hip_atomic_load((LDS_AS u32*)(flags + 4), __ATOMIC_RELAXED, __HIP_MEMORY_SCOPE_WORKGROUP);
    }
    return;
  }
  const int b = (blockIdx.x & 7) * 32 + (blockIdx.x >> 3);
  const int a = 2 * wave + (lane >> 5), j = lane & 31;
  const u16* xr = x + (size_t)(256 * a + b) * D;
  const u4v x0 = *(const u4v*)(xr + 8 * j);
  const u4v x1 = *(const u4v*)(xr + 256 + 8 * j);
  const u2v x2 = *(const u2v*)(xr + 512 + 4 * j);
  SB();
  constants();
  const int q = wave;
  u4v bre[4], bim[4], fre[9], fim[9];
  f4 twr[3], twi[3];
  u32 toff[3][4];
  {
    const int a0 = g >> 1;
#pragma unroll
    for (int ks = 0; ks < 4; ++ks) {
      const int ph = (q == 0) ? (a0 ? 1 : 7) : (2 * ks + a0) * q;
      bre[ks] = *(const u4v*)(smem + K1_R16 + (ph & 7) * 512 + (lane & 31) * 16);
      bim[ks] = *(const u4v*)(smem + K1_R16 + ((ph + 2) & 7) * 512 + (lane & 31) * 16);
    }
#pragma unroll
    for (int i = 0; i < 9; ++i) {
      fre[i] = *(const u4v*)(smem + K1_F40 + i * 2048 + lane * 16);
      fim[i] = *(const u4v*)(smem + K1_F40 + i * 2048 + 1024 + lane * 16);
    }
    const float qs = q == 0 ? 0.70710678118f : 1.f;
#pragma unroll
    for (int t = 0; t < 3; ++t) {
      twr[t] = *(const f4*)(smem + K1_TW + t * 2048 + lane * 16) * qs;
      twi[t] = *(const f4*)(smem + K1_TW + t * 2048 + 1024 + lane * 16) * qs;
      PIN(twr[t]); PIN(twi[t]);
#pragma unroll
      for (int r = 0; r < 4; ++r) {
        const int k2 = li + 16 * (16 * t + 4 * g + r);
        const int pos = k2 <= 320 ? 2 * k2 : 2 * (640 - k2) + 1;
        toff[t][r] = (u32)WS_T + (u32)b * (TROW_UNITS * 16) + (q > 0 ? ((q - 1) * 160 + 64 * t + lane) * 16 : 480 * 16 + pos * 4);
        if (t == 2 && lane >= 32) toff[t][r] = DROP;
        PIN(toff[t][r]);
      }
    }
#pragma unroll
    for (int ks = 0; ks < 4; ++ks) { PIN(bre[ks]); PIN(bim[ks]); }
#pragma unroll
    for (int i = 0; i < 9; ++i) { PIN(fre[i]); PIN(fim[i]); }
  }
  {
    f2 v[10];
#pragma unroll
    for (int i = 0; i < 4; ++i) { v[i] = f2{bflo(x0[i]), bfhi(x0[i])}; v[4 + i] = f2{bflo(x1[i]), bfhi(x1[i])}; }
    v[8] = f2{bflo(x2[0]), bfhi(x2[0])}; v[9] = f2{bflo(x2[1]), bfhi(x2[1])};
    ln_row(v);
    char* ub = smem + a * 1280;
    *(u4v*)(ub + 16 * j) = u4v{pk_h(v[0][0], v[0][1]), pk_h(v[1][0], v[1][1]), pk_h(v[2][0], v[2][1]), pk_h(v[3][0], v[3][1])};
    *(u4v*)(ub + 512 + 16 * j) = u4v{pk_h(v[4][0], v[4][1]), pk_h(v[5][0], v[5][1]), pk_h(v[6][0], v[6][1]), pk_h(v[7][0], v[7][1])};
    *(u2v*)(ub + 1024 + 8 * j) = u2v{pk_h(v[8][0], v[8][1]), pk_h(v[9][0], v[9][1])};
  }
  lds_signal(flags);
  lds_wait(flags, 4);
  u4v b2[3];
  {
    const int qq = li >> 2, p = li & 3;
    const char* abase = smem + (8 * g + qq) * 80 + 8 * p;
    u4v af[3][4];
#pragma unroll
    for (int t = 0; t < 3; ++t)
#pragma unroll
      for (int ks = 0; ks < 4; ++ks) {
        s4 lo = __builtin_amdgcn_ds_read_tr16_b64_v4i16((LDS_AS s4*)(abase + ks * 2560 + t * 32));
        s4 hi = __builtin_amdgcn_ds_read_tr16_b64_v4i16((LDS_AS s4*)(abase + ks * 2560 + 320 + t * 32));
        u2v l2 = __builtin_bit_cast(u2v, lo), h2v = __builtin_bit_cast(u2v, hi);
        af[t][ks] = u4v{l2[0], l2[1], h2v[0], h2v[1]};
      }
    SB();
    f4 are[3], aim[3];
#pragma unroll
    for (int t = 0; t < 3; ++t) {
      are[t] = f4{0, 0, 0, 0}; aim[t] = f4{0, 0, 0, 0};
#pragma unroll
      for (int ks = 0; ks < 4; ++ks) {
        are[t] = mfma_h(af[t][ks], bre[ks], are[t]);
        aim[t] = mfma_h(af[t][ks], bim[ks], aim[t]);
      }
    }
#pragma unroll
    for (int t = 0; t < 3; ++t) b2[t] = cmul_pack<false>(are[t], aim[t], twr[t], twi[t]);
  }
  const __amdgpu_buffer_rsrc_t rs = ws_rsrc(ws);
  f4 ore[3], oim[3];
#pragma unroll
  for (int tk = 0; tk < 3; ++tk)
#pragma unroll
    for (int to = 0; to < 3; ++to) {
      ore[to] = mfma_h(fre[to * 3 + tk], b2[tk], tk ? ore[to] : f4{0, 0, 0, 0});
      oim[to] = mfma_h(fim[to * 3 + tk], b2[tk], tk ? oim[to] : f4{0, 0, 0, 0});
    }
  u4v o[3];
#pragma unroll
  for (int to = 0; to < 3; ++to)
#pragma unroll
    for (int r = 0; r < 4; ++r) o[to][r] = pk_h(ore[to][r], oim[to][r]);
  if (q > 0) {
#pragma unroll
    for (int to = 0; to < 3; ++to) st16(rs, toff[to][0], o[to]);
  } else {
#pragma unroll
    for (int to = 0; to < 3; ++to)
#pragma unroll
      for (int r = 0; r < 4; ++r) __builtin_amdgcn_raw_buffer_store_b32(o[to][r], rs, toff[to][r], 0, 0);
    __builtin_amdgcn_raw_buffer_store_b32(lane ? o[1][0] : o[0][0], rs, lane == 0 ? toff[0][0] + 4 : (lane == 16 ? toff[1][0] + 4 : DROP), 0, 0);
  }
}

template <int NT, int LDW, int NCG, int NTASK, bool PERM>
DI void retile(const u16* __restrict__ W, __amdgpu_buffer_rsrc_t rs, u32 dstbase, int wid, int nw, char* lds, int lane) {
  const int li = lane & 15, g = lane >> 4, q = li >> 2, p = li & 3;
  u32 dst[NT];
#pragma unroll
  for (int i = 0; i < NT; ++i) {
    const int task = wid + i * nw;
    const int t = task < NTASK ? task : 0;
    const int ks = t / NCG, cg = t - ks * NCG;
    const u16* src = W + (size_t)(ks * 32 + (lane >> 3)) * LDW + cg * 64 + (lane & 7) * 8;
#pragma unroll
    for (int j = 0; j < 4; ++j) GLDS16(src + (size_t)j * 8 * LDW, lds + i * 4096 + j * 1024);
    dst[i] = task < NTASK ? dstbase + (u32)t * 4096u + lane * 16 : DROP;
  }
  const char* rb = lds + (PERM ? 4 * g + q : 8 * g + q) * 128 + 8 * p;
  WAITV(0);
#pragma unroll
  for (int i = 0; i < NT; ++i)
#pragma unroll
    for (int f = 0; f < 4; ++f) {
      s4 lo = __builtin_amdgcn_ds_read_tr16_b64_v4i16((LDS_AS s4*)(rb + i * 4096 + f * 32));
      s4 hi = __builtin_amdgcn_ds_read_tr16_b64_v4i16((LDS_AS s4*)(rb + i * 4096 + f * 32 + (PERM ? 16 : 4) * 128));
      u2v a = __builtin_bit_cast(u2v, lo), b = __builtin_bit_cast(u2v, hi);
      st16(rs, dst[i] + f * 1024, u4v{a[0], a[1], b[0], b[1]});
    }
}
constexpr int NRT1 = 20 * 27, NRT3 = 27 * 10;
constexpr int K2_FFT_BLOCKS = 168, K2_PREP_BLOCKS = 88;

extern "C" __global__ __launch_bounds__(512, 2) void k2_kernel() {
  LOAD_ARGS();
  char* ws = ap_->ws; const u16* w_in = ap_->w_in; const u16* w_out = ap_->w_out; const u16* w_sp = ap_->w_sp;
  PINS(ws); PINS(w_in); PINS(w_out); PINS(w_sp);
  const u32* T = (const u32*)(ws + WS_T); const u32* Wp = (const u32*)(ws + WS_WP); const u32* W2 = (const u32*)(ws + WS_W2);
  const u32* wmax = (const u32*)(ws + WS_WMAX); u32* Tp = (u32*)(ws + WS_TP); float* unscale = (float*)(ws + WS_UNS);
  u32* Wt1 = (u32*)(ws + WS_WT1); u32* Wt3 = (u32*)(ws + WS_WT3); u32* stats = (u32*)(ws + WS_STATS);
  extern __shared__ __attribute__((aligned(16))) char smem[];
  const int tid = threadIdx.x, lane = tid & 63;
  const int wave = __builtin_amdgcn_readfirstlane(tid >> 6);
  const int li = lane & 15, g = lane >> 4;
  if (blockIdx.x >= K2_FFT_BLOCKS) {
    const int wid = (blockIdx.x - K2_FFT_BLOCKS) * 8 + wave;
    const __amdgpu_buffer_rsrc_t rs = ws_rsrc(ws);
    if (wid < 256) st16(rs, (u32)WS_STATS + wid * 1024 + lane * 16, u4v{~0u, ~0u, ~0u, ~0u});
    else if (wid < 264) st16(rs, (u32)WS_CNT + (wid - 256) * 1024 + lane * 16, u4v{0u, 0u, 0u, 0u});
    if (wid < NRT1) retile<1, FF, 27, NRT1, false>(w_in, rs, (u32)WS_WT1, wid, 0, smem + wave * 8192, lane);
    else retile<2, D, 10, NRT3, true>(w_out, rs, (u32)WS_WT3, wid - NRT1, 164, smem + wave * 8192, lane);
    return;
  }
  const int P = (blockIdx.x & 7) * 21 + (blockIdx.x >> 3);
  if (P > 160) return;
  const int u = wave & 3, hh = wave >> 2;
  const int U = 4 * P + u;
#pragma unroll
  for (int i = 0; i < 2; ++i) {
    const int bb = (wave * 2 + i) * 16 + (lane >> 2);
    GLDS16(T + ((size_t)bb * TROW_UNITS + 4 * P + ((lane & 3) ^ ((lane >> 4) & 3))) * 4, smem + (wave * 2 + i) * 1024);
  }
  const u4v mxv = *(const u4v*)(wmax + (lane < NPREP / 4 ? lane * 4 : 0));
  u4v Wv[2];
#pragma unroll
  for (int t = 0; t < 2; ++t) Wv[t] = *(const u4v*)(Wp + ((size_t)((U < NUNITS ? U : 0) * 4 + 2 * hh + t) * 64 + lane) * 4);
  const bool special = P >= 120;
  const int cB = special ? 4 : (P / 40 + 1);
  const int ur = special ? U - 480 : U % 160;
  const bool e320 = special ? ur == 160 : ur == 80;
  const bool edge = hh == 0 && (ur == 0 || e320);
  u4v w2, w4;
  if (edge) {
    w2 = *(const u4v*)(W2 + ((special ? 0 : cB) * 2 + (e320 ? 1 : 0)) * 256 + lane * 4);
    w4 = *(const u4v*)(W2 + (4 * 2 + (e320 ? 1 : 0)) * 256 + lane * 4);
  }
  {
    char* cb = smem + 16384 + wave * 2048 + lane * 16;
    const int cc = wave < 4 ? cB : 0;
    const float inv = u < 2 ? (1.0f / 128.0f) : (1.0f / 2048.0f);
    const float amp = u == 0 ? 1.f : ((u == 1 && special) ? 0.125f : 0.25f);
    f4 c, s;
#pragma unroll
    for (int jj = 0; jj < 4; ++jj) {
      const int a = (u & 1) ? li * (cc + 8 * (4 * g + jj)) : (4 * g + jj) * (cc + 8 * li);
      const float ang = (float)a * inv;
      c[jj] = cosr(ang) * amp; s[jj] = sinr(ang) * amp;
    }
    if (u < 2) {
      *(u4v*)(cb) = u4v{pk_h(c[0], s[0]), pk_h(c[1], s[1]), pk_h(c[2], s[2]), pk_h(c[3], s[3])};
      *(u4v*)(cb + 1024) = u4v{pk_h(-s[0], c[0]), pk_h(-s[1], c[1]), pk_h(-s[2], c[2]), pk_h(-s[3], c[3])};
    } else {
      *(f4*)(cb) = c;
      *(f4*)(cb + 1024) = s;
    }
  }
  asm volatile("s_waitcnt lgkmcnt(0)" ::: "memory");
  __builtin_amdgcn_s_barrier();
  asm volatile("" ::: "memory");
  u4v Fre, Fim, P1re[2], P1im[2], P4re[2], P4im[2];
  f4 tfr[2], tfs[2], tir[2], tis[2];
  {
    const char* cb = smem + 16384 + lane * 16;
    Fre = *(const u4v*)(cb + 4 * 2048); Fim = *(const u4v*)(cb + 4 * 2048 + 1024);
#pragma unroll
    for (int c = 0; c < 2; ++c) {
      const char* cs = cb + ((c == 0 && special) ? 8192 : 0);
      P1re[c] = *(const u4v*)(cs); P1im[c] = *(const u4v*)(cs + 1024);
      P4re[c] = *(const u4v*)(cs + 2048); P4im[c] = *(const u4v*)(cs + 3072);
      tfr[c] = *(const f4*)(cs + 4096); tfs[c] = *(const f4*)(cs + 5120);
      tir[c] = *(const f4*)(cs + 6144); tis[c] = *(const f4*)(cs + 7168);
    }
    PIN(Fre); PIN(Fim);
#pragma unroll
    for (int c = 0; c < 2; ++c) { PIN(P1re[c]); PIN(P1im[c]); PIN(P4re[c]); PIN(P4im[c]); PIN(tfr[c]); PIN(tfs[c]); PIN(tir[c]); PIN(tis[c]); }
  }
  asm volatile("s_waitcnt vmcnt(0)" ::: "memory");
  __builtin_amdgcn_s_barrier();
  asm volatile("" ::: "memory");
  const int loff = (64 * g + li) * 64 + ((u ^ (li >> 2)) & 3) * 16 + hh * 8;
  u4v X[2];
#pragma unroll
  for (int jj = 0; jj < 4; ++jj) {
    const u2v L = *(const u2v*)(smem + loff + jj * 1024);
    X[0][jj] = L[0]; X[1][jj] = L[1];
  }
  if (special) {
#pragma unroll
    for (int jj = 0; jj < 4; ++jj) {
      const u32 au = X[0][jj], bu = X[1][jj] ^ 0x80000000u;
      const h2 A = __builtin_bit_cast(h2, au);
      const h2 Bc = __builtin_bit_cast(h2, bu);
      X[0][jj] = __builtin_bit_cast(u32, A + Bc);
      X[1][jj] = __builtin_bit_cast(u32, A - Bc);
    }
  }
  const f4 z4 = f4{0, 0, 0, 0};
  f4 re[2], im[2];
#pragma unroll
  for (int t = 0; t < 2; ++t) { re[t] = mfma_h(X[t], P1re[t], z4); im[t] = mfma_h(X[t], P1im[t], z4); }
  const u32 mx = max64u(max(max(mxv[0], mxv[1]), max(mxv[2], mxv[3])));
  int E = (int)(mx >> 7);
  E = E < 1 ? 1 : (E > 253 ? 253 : E);
  const float sig = __uint_as_float((u32)(254 - E) << 23) * 0.25f;
#pragma unroll
  for (int t = 0; t < 2; ++t) {
    const u4v v = cmul_pack<false>(re[t], im[t], tfr[t], -tfs[t]);
    re[t] = mfma_h(Fre, v, z4); im[t] = mfma_h(Fim, v, z4);
  }
  f4 wr4[2], wi4[2];
#pragma unroll
  for (int t = 0; t < 2; ++t)
#pragma unroll
    for (int r = 0; r < 4; ++r) { wr4[t][r] = bflo(Wv[t][r]); wi4[t][r] = bfhi(Wv[t][r]); }
  if (edge) {
#pragma unroll
    for (int r = 0; r < 4; ++r) {
      wr4[0][r] = (wr4[0][r] + bflo(w2[r])) * 0.5f; wi4[0][r] = (wi4[0][r] + bfhi(w2[r])) * 0.5f;
      if (special) { wr4[1][r] = (wr4[1][r] + bflo(w4[r])) * 0.5f; wi4[1][r] = (wi4[1][r] + bfhi(w4[r])) * 0.5f; }
    }
    if (U == 0 && lane == 0) unscale[0] = __uint_as_float((u32)E << 23);
  }
#pragma unroll
  for (int t = 0; t < 2; ++t) {
    const u4v v = cmul_pack<true>(re[t], im[t], wr4[t] * sig, wi4[t] * sig);
    im[t] = mfma_h(v, Fre, z4); re[t] = mfma_h(v, Fim, z4);
  }
#pragma unroll
  for (int t = 0; t < 2; ++t) {
    const u4v v = cmul_pack<true>(re[t], im[t], tir[t], tis[t]);
    im[t] = mfma_h(P4re[t], v, z4); re[t] = mfma_h(P4im[t], v, z4);
  }
  if (special) {
    const f4 yr = re[0], yi = im[0], zr = re[1], zi = im[1];
    re[0] = yr + zr; im[0] = yi + zi;
    re[1] = yr - zr; im[1] = zi - yi;
  }
#pragma unroll
  for (int r = 0; r < 4; ++r) *(u2v*)(smem + loff + r * 1024) = u2v{pk_h(re[0][r], im[0][r]), pk_h(re[1][r], im[1][r])};
  asm volatile("s_waitcnt lgkmcnt(0)" ::: "memory");
  __builtin_amdgcn_s_barrier();
  asm volatile("" ::: "memory");
  const __amdgpu_buffer_rsrc_t rs = ws_rsrc(ws);
#pragma unroll
  for (int i = 0; i < 2; ++i) {
    const u4v o = *(const u4v*)(smem + (wave * 2 + i) * 1024 + lane * 16);
    st16(rs, (u32)WS_TP + P * 16384 + (wave * 2 + i) * 1024 + (lane & ~3) * 16 + ((lane & 3) ^ ((lane >> 4) & 3)) * 16, o);
  }
  { char* scratch = smem + 49152; if (wave == 0) CODE_TOUCH(k3_kernel, g1_kernel, 0) }
  WAITV(0);
}

extern "C" __global__ __launch_bounds__(256, 2) void k3_kernel() {
  LOAD_ARGS();
  char* ws = ap_->ws; const u16* x = ap_->x; const u16* w_sp = ap_->w_sp;
  PINS(x); PINS(ws); PINS(w_sp);
  const u32* Tp = (const u32*)(ws + WS_TP);
  constexpr int K3_Y = 10752, K3_CB = K3_Y + 10240;
  __shared__ __attribute__((aligned(16))) char smem[K3_CB + 8192 + 512];
  const int tid = threadIdx.x, lane = tid & 63;
  const int wave = __builtin_amdgcn_readfirstlane(tid >> 6);
  const int li = lane & 15, g = lane >> 4;
  const int b = (blockIdx.x & 7) * 32 + (blockIdx.x >> 3);
#pragma unroll
  for (int i = 0; i < 2; ++i) {
    int U = (wave * 2 + i) * 64 + lane;
    U = U < 480 ? U : 479;
    GLDS16(Tp + (size_t)(U >> 2) * 4096 + b * 16 + (U & 3) * 4, smem + (wave * 2 + i) * 1024);
  }
#pragma unroll
  for (int it = 0; it < 4; ++it) {
    const int i = it < 2 ? wave * 2 + it : 6 + it;
    if (it < 2 || wave == 3) {
      const int k2 = (lane >> 2) + 16 * (4 * i + (lane & 3));
      const int pos = k2 <= 320 ? 2 * k2 : 2 * (640 - k2) + 1;
      GLDS4(Tp + (size_t)(120 + (pos >> 4)) * 4096 + b * 16 + (pos & 15), smem + 8192 + i * 256);
    }
  }
  char* scratch = smem + K3_CB + 8192;
  if (wave == 0) GLDS4(ws + WS_UNS, scratch + 256);
  const int a = 2 * wave + (lane >> 5), j = lane & 31;
  const int row = 256 * a + b;
  const u16* xr = x + (size_t)row * D;
  u4v x0 = *(const u4v*)(xr + 8 * j);
  u4v x1 = *(const u4v*)(xr + 256 + 8 * j);
  u2v x2 = *(const u2v*)(xr + 512 + 4 * j);
  char* cb = smem + K3_CB;
  const int t = wave;
  u4v Bre[3], Bim[3];
  if (wave < 3) {
#pragma unroll
    for (int tk = 0; tk < 3; ++tk) {
      const float sc = (tk < 2 || g < 2) ? 1.f : 0.f;
#pragma unroll
      for (int jj = 0; jj < 4; ++jj) {
        const float ang = (float)((16 * tk + 4 * g + jj) * (16 * t + li)) * (1.0f / 40.0f);
        const float c = cosr(ang) * sc, s = sinr(ang) * sc;
        Bre[tk][jj] = pk_h(c, -s);
        Bim[tk][jj] = pk_h(s, c);
      }
      PIN(Bre[tk]); PIN(Bim[tk]);
    }
  } else {
#pragma unroll
    for (int tt = 0; tt < 3; ++tt) {
      f4 twr, twi;
#pragma unroll
      for (int jj = 0; jj < 4; ++jj) {
        const float ang = (float)((16 * tt + li) * (4 * g + jj)) * (1.0f / 640.0f);
        twr[jj] = cosr(ang) * 0.125f; twi[jj] = sinr(ang) * 0.125f;
      }
      *(f4*)(cb + tt * 2048 + lane * 16) = twr;
      *(f4*)(cb + tt * 2048 + 1024 + lane * 16) = twi;
    }
    u4v gre, gim;
#pragma unroll
    for (int jj = 0; jj < 4; ++jj) {
      const float ang = (float)(li * (4 * g + jj)) * 0.0625f;
      const float c = cosr(ang), s = sinr(ang);
      gre[jj] = pk_h(c, -s); gim[jj] = pk_h(s, c);
    }
    *(u4v*)(cb + 6144 + lane * 16) = gre;
    *(u4v*)(cb + 7168 + lane * 16) = gim;
  }
  asm volatile("s_waitcnt vmcnt(0)" ::: "memory");
  PIN(x0); PIN(x1); PIN(x2);
  __syncthreads();
  char* yb = smem + K3_Y;
  if (wave < 3) {
    const f4 z4 = f4{0, 0, 0, 0};
    u4v af[4][3];
#pragma unroll
    for (int q = 0; q < 4; ++q) {
      const char* base = smem + (q == 0 ? 8192 : (q - 1) * 2560);
#pragma unroll
      for (int tk = 0; tk < 3; ++tk) af[q][tk] = *(const u4v*)(base + (tk < 2 ? (64 * tk + lane) : (128 + (lane & 31))) * 16);
    }
    const f4 twr = *(const f4*)(cb + t * 2048 + lane * 16), twi = *(const f4*)(cb + t * 2048 + 1024 + lane * 16);
    const u4v Gre = *(const u4v*)(cb + 6144 + lane * 16), Gim = *(const u4v*)(cb + 7168 + lane * 16);
    const float usc = *(const float*)(scratch + 256);
    SB();
    f4 vre[4], vim[4];
#pragma unroll
    for (int q = 0; q < 4; ++q) {
      f4 re = z4, im = z4;
#pragma unroll
      for (int tk = 0; tk < 3; ++tk) {
        re = mfma_h(af[q][tk], Bre[tk], re);
        im = mfma_h(af[q][tk], Bim[tk], im);
      }
      vre[q] = re; vim[q] = im;
    }
#pragma unroll
    for (int q = 0; q < 4; ++q) {
      const u4v v = cmul_pack<false>(vre[q], vim[q], twr, twi);
      vre[q] = mfma_h(v, Gre, z4);
      vim[q] = mfma_h(v, Gim, z4);
    }
    const float fs = usc * (2.0f * 1024.0f * 128.0f / (2048.0f * 640.0f));
    const float fh = fs * 0.70710678118f;
    const f4 E = (vre[0] + vim[0]) * fs, O = (vre[0] - vim[0]) * fs;
    const f4 s13 = vre[1] + vre[3], dx = vre[1] - vre[3], sy = vim[1] + vim[3], dy = vim[3] - vim[1];
    const f4 Ep = E + vre[2] * fs, Em = E - vre[2] * fs, Om = O - vim[2] * fs, Op = O + vim[2] * fs;
    const f4 A = dx - sy, B = dx + sy;
    f4 y[8];
    y[0] = Ep + s13 * fs; y[4] = Ep - s13 * fs; y[2] = Em + dy * fs; y[6] = Em - dy * fs;
    y[1] = Om + A * fh; y[5] = Om - A * fh; y[3] = Op - B * fh; y[7] = Op + B * fh;
    if (t < 2 || lane < 32) {
      char* yp = yb + (40 * li + 16 * t + 4 * g) * 2;
#pragma unroll
      for (int aa = 0; aa < 8; ++aa) *(u2v*)(yp + aa * 1280) = u2v{pk_bf(y[aa][0], y[aa][1]), pk_bf(y[aa][2], y[aa][3])};
    }
  }
  else {
    CODE_TOUCH(g1_kernel, g2_kernel, (blockIdx.x >> 3) & 1)
#pragma unroll
    for (int i = 0; i < 2; ++i) touch((const char*)w_sp + ((size_t)(512 + i * 256 + blockIdx.x) * 64 + lane) * 128);
  }
  asm volatile("s_waitcnt lgkmcnt(0)" ::: "memory");
  __builtin_amdgcn_s_barrier();
  asm volatile("" ::: "memory");
  {
    const __amdgpu_buffer_rsrc_t rs = ws_rsrc(ws);
    const u4v y0 = *(const u4v*)(yb + a * 1280 + 16 * j);
    const u4v y1 = *(const u4v*)(yb + a * 1280 + 512 + 16 * j);
    const u2v y2 = *(const u2v*)(yb + a * 1280 + 1024 + 8 * j);
    u32 hx[10];
#pragma unroll
    for (int i = 0; i < 4; ++i) {
      hx[i] = pk_bf(bflo(x0[i]) + bflo(y0[i]), bfhi(x0[i]) + bfhi(y0[i]));
      hx[4 + i] = pk_bf(bflo(x1[i]) + bflo(y1[i]), bfhi(x1[i]) + bfhi(y1[i]));
    }
    hx[8] = pk_bf(bflo(x2[0]) + bflo(y2[0]), bfhi(x2[0]) + bfhi(y2[0]));
    hx[9] = pk_bf(bflo(x2[1]) + bflo(y2[1]), bfhi(x2[1]) + bfhi(y2[1]));
    auto pairup = [&](u32 v0, u32 v1) {
      return u4v{v0, v1, (u32)__builtin_amdgcn_update_dpp(0, (int)v0, 0xB1, 0xf, 0xf, true), (u32)__builtin_amdgcn_update_dpp(0, (int)v1, 0xB1, 0xf, 0xf, true)};
    };
    const u32 drop = (lane & 1) ? DROP : 0u;
    const u32 ho = (u32)WS_H + row * (D * 2);
    st16(rs, ho + 16 * j, u4v{hx[0], hx[1], hx[2], hx[3]});
    st16(rs, ho + 512 + 16 * j, u4v{hx[4], hx[5], hx[6], hx[7]});
    st16(rs, (ho + 1024 + 8 * j) | drop, pairup(hx[8], hx[9]));
    f2 v[10];
#pragma unroll
    for (int i = 0; i < 10; ++i) v[i] = f2{bflo(hx[i]), bfhi(hx[i])};
    ln_row(v);
#pragma unroll
    for (int i = 0; i < 10; ++i) hx[i] = pk_bf(v[i][0], v[i][1]);
    const u32 lo = (u32)WS_L2 + (row >> 4) * 20480 + (row & 15) * 64;
    st16(rs, lo + (j >> 2) * 1024 + (j & 3) * 16, u4v{hx[0], hx[1], hx[2], hx[3]});
    st16(rs, lo + (8 + (j >> 2)) * 1024 + (j & 3) * 16, u4v{hx[4], hx[5], hx[6], hx[7]});
    st16(rs, (lo + (16 + (j >> 3)) * 1024 + (j & 7) * 8) | drop, pairup(hx[8], hx[9]));
  }
  WAITV(0);
}

constexpr int FPD = 2;
#include <type_traits>
typedef unsigned long long u64;
typedef __attribute__((address_space(1))) unsigned long long gu64;
#define WAITL0() do { __builtin_amdgcn_sched_barrier(0); asm volatile("s_waitcnt lgkmcnt(0)" ::: "memory"); __builtin_amdgcn_sched_barrier(0); } while (0)
#define BAR() do { asm volatile("" ::: "memory"); __builtin_amdgcn_sched_barrier(0); __builtin_amdgcn_s_barrier(); __builtin_amdgcn_sched_barrier(0); asm volatile("" ::: "memory"); } while (0)
#define SGB(mask, n) __builtin_amdgcn_sched_group_barrier(mask, n, 0)
DI f4 mfma_b(u4v a, u4v b, f4 c) {
  return __builtin_amdgcn_mfma_f32_16x16x32_bf16(__builtin_bit_cast(bf8, a), __builtin_bit_cast(bf8, b), c, 0, 0, 0);
}
DI f2 gelu2(f2 x) {
  const float k1 = -2.0f * 0.7978845608028654f * 1.4426950408889634f;
  const float k2 = k1 * 0.044715f;
  f2 p = x * x * k2 + k1;
  f2 a = x * p;
  f2 e = f2{__builtin_amdgcn_exp2f(a[0]), __builtin_amdgcn_exp2f(a[1])} + 1.0f;
  f2 r = f2{__builtin_amdgcn_rcpf(e[0]), __builtin_amdgcn_rcpf(e[1])};
  return x * r;
}
DI u2v gelu4(f4 a) {
  f2 g0 = gelu2(f2{a[0], a[1]}), g1 = gelu2(f2{a[2], a[3]});
  return u2v{pk_bf(g0[0], g0[1]), pk_bf(g1[0], g1[1])};
}
template <int N> DI void waitv() { asm volatile("s_waitcnt vmcnt(%0)" ::"n"(N) : "memory"); }

constexpr int G1_DEPTH = 3, G1_AD = 5;
extern "C" __global__ __launch_bounds__(512) void g1_kernel() {
  LOAD_ARGS();
  char* ws = ap_->ws; const u16* w_sp = ap_->w_sp;
  PINS(ws); PINS(w_sp);
  const u32* l2f = (const u32*)(ws + WS_L2); const u32* Wt1 = (const u32*)(ws + WS_WT1);
  u32* z1f = (u32*)(ws + WS_Z1); u16* z2t = (u16*)(ws + WS_Z2); u64* stats = (u64*)(ws + WS_STATS);
  __shared__ __attribute__((aligned(16))) char smem[140 * 1024 + 4 * 4608 + 256];
  const int tid = threadIdx.x, lane = tid & 63;
  const int wave = __builtin_amdgcn_readfirstlane(tid >> 6);
  const int li = lane & 15, g = lane >> 4;
  const int xcd = blockIdx.x & 7, idx = blockIdx.x >> 3;
  const int mt = (xcd >> 1) * 4 + (idx >> 3), nb = (xcd & 1) * 8 + (idx & 7);
  const int row0 = 1024 * (mt & 1) + 32 * (mt >> 1);
  const int zs = nb < 6 ? 4 * nb : 24 + 3 * (nb - 6);
  const int z1s = nb < 6 ? 3 * nb : (nb < 12 ? 18 + 4 * (nb - 6) : 42 + 3 * (nb - 12));
  const int type = nb < 6 ? 0 : (nb < 12 ? 1 : 2);
  if (wave >= 4) {
    const int lw = wave - 4;
    auto slot_frag = [&](int s) { return s < 3 ? 54 + zs + s : (s == 3 ? (type == 0 ? 54 + zs + 3 : (type == 1 ? z1s + 3 : z1s)) : z1s + s - 4); };
    const u32* bsrc[4];
#pragma unroll
    for (int j = 0; j < 4; ++j) {
      int i = lw + 4 * j; i = i < 14 ? i : 0;
      const int kk = i / 7, s = i - 7 * kk;
      bsrc[j] = Wt1 + (size_t)(kk * 108 + slot_frag(s)) * 256 + lane * 4;
    }
    auto stage = [&](int kt) {
      char* sb = smem + kt * 14336 + lw * 1024;
#pragma unroll
      for (int j = 0; j < 3; ++j) GLDS16(bsrc[j] + (size_t)kt * 2 * 108 * 256, sb + j * 4096);
      if (lw < 2) GLDS16(bsrc[3] + (size_t)kt * 2 * 108 * 256, sb + 3 * 4096);
    };
#pragma unroll
    for (int s = 0; s < G1_DEPTH; ++s) stage(s);
    for (int kt = 0; kt < 10 - G1_DEPTH; ++kt) {
      if (lw < 2) waitv<4 * (G1_DEPTH - 1)>(); else waitv<3 * (G1_DEPTH - 1)>();
      BAR();
      stage(kt + G1_DEPTH);
    }
    if (lw < 2) waitv<8>(); else waitv<6>(); BAR();
    if (lw < 2) waitv<4>(); else waitv<3>(); BAR();
    waitv<0>();
    char* scratch = smem + 140 * 1024 + 4 * 4608;
    if (lw < 2) touch((const char*)w_sp + ((size_t)(blockIdx.x * 2 + lw) * 64 + lane) * 128);
    BAR();
    if (lw >= 2) CODE_TOUCH(g2_kernel, end_kernel, lw - 2)
    waitv<0>();
    return;
  }
  const int rg0 = (row0 + wave * 256) >> 4;
  const u32* asrc = l2f + (size_t)rg0 * 20 * 256 + li * 16 + g * 4;
  u4v fa[G1_AD][2];
#pragma unroll
  for (int k = 0; k < G1_AD; ++k) { fa[k][0] = *(const u4v*)(asrc + k * 256); fa[k][1] = *(const u4v*)(asrc + (20 + k) * 256); __builtin_amdgcn_sched_barrier(0); }
  f4 acc[7][2];
#pragma unroll
  for (int s = 0; s < 7; ++s) { acc[s][0] = f4{0, 0, 0, 0}; acc[s][1] = f4{0, 0, 0, 0}; }
  u4v fw[2][7];
  const char* wb = smem + lane * 16;
  auto rd = [&](int k) {
#pragma unroll
    for (int s = 0; s < 7; ++s) fw[k & 1][s] = *(const u4v*)(wb + (k * 7 + s) * 1024);
  };
  auto mm = [&](int k) {
#pragma unroll
    for (int s = 0; s < 7; ++s) {
      acc[s][0] = mfma_b(fw[k & 1][s], fa[k % G1_AD][0], acc[s][0]);
      acc[s][1] = mfma_b(fw[k & 1][s], fa[k % G1_AD][1], acc[s][1]);
    }
    if (k + G1_AD < 20) { fa[k % G1_AD][0] = *(const u4v*)(asrc + (k + G1_AD) * 256); fa[k % G1_AD][1] = *(const u4v*)(asrc + (20 + G1_AD + k) * 256); }
  };
  BAR(); rd(0);
#pragma unroll
  for (int k = 1; k < 20; ++k) {
    if (!(k & 1)) BAR(); else __builtin_amdgcn_sched_barrier(0);
    rd(k);
    mm(k - 1);
#pragma unroll
    for (int i = 0; i < 7; ++i) { SGB(0x100, 1); SGB(0x8, 2); }
  }
  __builtin_amdgcn_sched_barrier(0);
  mm(19);
  const int n2 = type == 0 ? 4 : 3;
  f4 zv[4][2];
  u2v z3[2];
  float lsum[2], lm2[2];
#pragma unroll
  for (int sf = 0; sf < 2; ++sf) {
    f2 s = f2{0.f, 0.f};
#pragma unroll
    for (int j = 0; j < 4; ++j) {
      const u2v t = gelu4(acc[j][sf]);
      if (j == 3) z3[sf] = t;
      zv[j][sf] = f4{bflo(t[0]), bfhi(t[0]), bflo(t[1]), bfhi(t[1])};
      if (j < 3 || type == 0) s += f2{zv[j][sf][0], zv[j][sf][1]} + f2{zv[j][sf][2], zv[j][sf][3]};
    }
    const float st = xsum_g(s[0] + s[1]);
    const float lmean = st * (type == 0 ? (1.0f / 64.0f) : (1.0f / 48.0f));
    f2 q = f2{0.f, 0.f};
#pragma unroll
    for (int j = 0; j < 4; ++j) {
      const f4 d = zv[j][sf] - lmean;
      if (j < 3 || type == 0) { q += f2{d[0], d[1]} * f2{d[0], d[1]}; q += f2{d[2], d[3]} * f2{d[2], d[3]}; }
    }
    lsum[sf] = st; lm2[sf] = xsum_g(q[0] + q[1]);
  }
  gu64* st = (gu64*)stats + (size_t)mt * 16 * 128 + wave * 32;
  if (lane < 32) {
    u32 a = __float_as_uint(g ? lsum[1] : lsum[0]), b = __float_as_uint(g ? lm2[1] : lm2[0]);
    if (a == ~0u) a = 0x7fc00000u;
    __hip_atomic_store(st + nb * 128 + lane, ((u64)b << 32) | a, __ATOMIC_RELAXED, __HIP_MEMORY_SCOPE_AGENT);
  }
  __builtin_amdgcn_sched_barrier(0);
  const __amdgpu_buffer_rsrc_t rs = ws_rsrc(ws);
  u4v z1o[3];
#pragma unroll
  for (int j = 0; j < 3; ++j) {
    const u2v a = gelu4(acc[4 + j][0]), b = gelu4(acc[4 + j][1]);
    z1o[j] = u4v{a[0], a[1], b[0], b[1]};
    PIN(z1o[j]);
  }
  __builtin_amdgcn_sched_barrier(0);
  u64 gv[2][4];
  for (int spin = 0; spin < (1 << 16); ++spin) {
    bool ok = true;
#pragma unroll
    for (int sf = 0; sf < 2; ++sf)
#pragma unroll
      for (int i = 0; i < 4; ++i) {
        gv[sf][i] = __hip_atomic_load(st + (4 * g + i) * 128 + sf * 16 + li, __ATOMIC_RELAXED, __HIP_MEMORY_SCOPE_AGENT);
        ok &= ((u32)gv[sf][i] != ~0u);
      }
    if (__all(ok)) break;
    __builtin_amdgcn_s_sleep(1);
  }
  {
    const u32 zo = (u32)WS_Z1 + ((rg0 >> 1) * 54 + z1s) * 1024 + lane * 16;
#pragma unroll
    for (int j = 0; j < 3; ++j) st16(rs, zo + j * 1024, z1o[j]);
    if (type == 1) st16(rs, zo + 3 * 1024, u4v{z3[0][0], z3[0][1], z3[1][0], z3[1][1]});
  }
  char* tb = smem + 140 * 1024 + wave * 4608;
#pragma unroll
  for (int sf = 0; sf < 2; ++sf) {
    float s = 0.f;
#pragma unroll
    for (int i = 0; i < 4; ++i) s += __uint_as_float((u32)gv[sf][i]);
    const float mean = xsum_g(s) * (1.0f / 864.0f);
    float q = 0.f;
#pragma unroll
    for (int i = 0; i < 4; ++i) {
      const bool big = (4 * g + i) < 6;
      const float dm = __uint_as_float((u32)gv[sf][i]) * (big ? (1.0f / 64.0f) : (1.0f / 48.0f)) - mean;
      q += __uint_as_float((u32)(gv[sf][i] >> 32)) + (big ? 64.f : 48.f) * dm * dm;
    }
    const float var = xsum_g(q) * (1.0f / 864.0f);
    const float mb = rbf(mean);
    const float rinv = __builtin_amdgcn_rcpf(rbf(__builtin_amdgcn_sqrtf(rbf(rbf(var) + 1e-5f))));
#pragma unroll
    for (int j = 0; j < 4; ++j) {
      const f4 o = (zv[j][sf] - mb) * rinv;
      *(u2v*)(tb + (sf * 16 + li) * 144 + j * 32 + g * 8) = u2v{pk_bf(o[0], o[1]), pk_bf(o[2], o[3])};
    }
  }
  {
    const int q = li >> 2, p = li & 3;
#pragma unroll
    for (int j = 0; j < 4; ++j) {
      s4 lo = __builtin_amdgcn_ds_read_tr16_b64_v4i16((LDS_AS s4*)(tb + (8 * g + q) * 144 + j * 32 + 8 * p));
      s4 hi = __builtin_amdgcn_ds_read_tr16_b64_v4i16((LDS_AS s4*)(tb + (8 * g + 4 + q) * 144 + j * 32 + 8 * p));
      u2v a = __builtin_bit_cast(u2v, lo), b = __builtin_bit_cast(u2v, hi);
      if (j < n2) st16(rs, (u32)WS_Z2 + (((zs + j) * 16 + li) * S + row0 + wave * 256 + 8 * g) * 2, u4v{a[0], a[1], b[0], b[1]});
    }
  }
}

typedef __attribute__((address_space(1))) unsigned int gu32;
constexpr int G2_NST = 7, G2_STB = 22 * 1024;
extern "C" __global__ __launch_bounds__(512) void g2_kernel() {
  LOAD_ARGS();
  char* ws = ap_->ws; const u16* w_sp = ap_->w_sp; const u16* b_sp = ap_->b_sp; u16* out = ap_->out;
  PINS(w_sp); PINS(b_sp); PINS(ws); PINS(out);
  const u16* z2t = (const u16*)(ws + WS_Z2); const u32* z1f = (const u32*)(ws + WS_Z1);
  __shared__ __attribute__((aligned(16))) char smem[G2_NST * G2_STB + 256];
  const int tid = threadIdx.x, lane = tid & 63;
  const int wave = __builtin_amdgcn_readfirstlane(tid >> 6);
  const int li = lane & 15, g = lane >> 4;
  const int xcd = blockIdx.x & 7, idx = blockIdx.x >> 3;
  const int tt = (xcd >> 1) * 8 + (idx >> 2), ft = (xcd & 1) * 4 + (idx & 3);
  if (wave >= 4) {
    const int lw = wave - 4;
    const u16* src[6];
    const int swz = ((lane & 7) ^ (lane >> 3)) * 8;
#pragma unroll
    for (int j = 0; j < 6; ++j) {
      int i = lw + 4 * j; i = i < 22 ? i : 8;
      if (j < 2) src[j] = w_sp + (size_t)(tt * 64 + 8 * i + (lane >> 3)) * S + swz;
      else { int r = ft * 112 + 8 * (i - 8) + (lane >> 3); r = r < FH ? r : FH - 1; src[j] = z2t + (size_t)r * S + swz; }
    }
    auto stage = [&](int kt, int buf) {
      char* sb = smem + buf * G2_STB + lw * 1024;
      const int kk = kt * 64;
#pragma unroll
      for (int j = 0; j < 5; ++j) GLDS16(src[j] + kk, sb + j * 4096);
      if (lw < 2) GLDS16(src[5] + kk, sb + 5 * 4096);
    };
    const bool big = lw < 2;
    stage(0, 0); stage(1, 1); stage(2, 2);
    if (big) waitv<12>(); else waitv<10>(); BAR();
    stage(3, 3); stage(4, 4);
    if (big) waitv<18>(); else waitv<15>(); BAR();
    stage(5, 5); stage(6, 6);
    if (big) waitv<24>(); else waitv<20>(); BAR();
    stage(7, 0); stage(8, 1);
    int nbuf = 2;
    for (int kt = 3; kt < 26; ++kt) {
      if (big) waitv<30>(); else waitv<25>();
      BAR();
      stage(kt + 6, nbuf);
      nbuf = nbuf + 1 == G2_NST ? 0 : nbuf + 1;
    }
    const u32* wsrc = (const u32*)(ws + WS_WT3) + (size_t)(ft * 5) * 256 + lane * 4;
    for (int ps = 0; ps < 6; ++ps) {
      if (big) waitv<30>(); else waitv<25>();
      BAR();
      char* sb = smem + nbuf * G2_STB + lw * 1024;
#pragma unroll
      for (int j = 0; j < 6; ++j) {
        const int i = ps * 22 + lw + 4 * j, k = i / 5;
        if (j < 5 || big) GLDS16(wsrc + (size_t)(k * 35 + i) * 256, sb + j * 4096);
      }
      nbuf = nbuf + 1 == G2_NST ? 0 : nbuf + 1;
    }
    BAR();
    if (lw < 3) GLDS16(wsrc + (size_t)(26 * 35 + 132 + lw) * 256, smem + nbuf * G2_STB + lw * 1024);
    waitv<0>();
    BAR();
    return;
  }
  const int wt = wave & 1, wf = wave >> 1;
  const int n0 = (ft & 1) ? 3 : 4;
  const int f0 = wf ? n0 : 0, nf = wf ? 7 - n0 : n0;
  u4v z1v[4];
  float bias[2];
#pragma unroll
  for (int j = 0; j < 4; ++j) {
    int ff = ft * 7 + f0 + j; ff = ff < 54 ? ff : 53;
    z1v[j] = *(const u4v*)(z1f + ((size_t)(tt * 2 + wt) * 54 + ff) * 256 + lane * 4);
  }
  bias[0] = bflo(b_sp[tt * 64 + wt * 32 + li]); bias[1] = bflo(b_sp[tt * 64 + wt * 32 + 16 + li]);
  f4 acc[4][2];
#pragma unroll
  for (int j = 0; j < 4; ++j) { acc[j][0] = f4{0, 0, 0, 0}; acc[j][1] = f4{0, 0, 0, 0}; }
  const int sw = (g ^ (li & 7)) * 16;
  const int toff = (wt * 32 + li) * 128 + sw;
  const int foff = 8192 + (f0 * 16 + li) * 128 + sw;
  auto body = [&](auto NFc) {
    constexpr int NF = decltype(NFc)::value;
    u4v fb[2][2][2], fa[2][2][NF];
    auto rd = [&](int set, int buf) {
      const char* sb = smem + buf * G2_STB;
#pragma unroll
      for (int kk = 0; kk < 2; ++kk) {
        fb[set][kk][0] = *(const u4v*)(sb + (toff ^ (kk * 64)));
        fb[set][kk][1] = *(const u4v*)(sb + (toff ^ (kk * 64)) + 2048);
#pragma unroll
        for (int j = 0; j < NF; ++j) fa[set][kk][j] = *(const u4v*)(sb + (foff ^ (kk * 64)) + j * 2048);
      }
    };
    auto mm = [&](int set) {
#pragma unroll
      for (int kk = 0; kk < 2; ++kk)
#pragma unroll
        for (int j = 0; j < NF; ++j) {
          acc[j][0] = mfma_b(fa[set][kk][j], fb[set][kk][0], acc[j][0]);
          acc[j][1] = mfma_b(fa[set][kk][j], fb[set][kk][1], acc[j][1]);
        }
    };
    auto sched = [&]() {
#pragma unroll
      for (int i = 0; i < 2 * NF + 4; ++i) { SGB(0x100, 1); SGB(0x8, 1); }
      SGB(0x8, 2 * NF - 4);
    };
    BAR(); rd(0, 0); WAITL0();
    int buf = 1;
    for (int kt = 1; kt < 31; kt += 2) {
      BAR(); rd(1, buf); mm(0); sched(); WAITL0();
      buf = buf + 1 == G2_NST ? 0 : buf + 1;
      BAR(); rd(0, buf); mm(1); sched(); WAITL0();
      buf = buf + 1 == G2_NST ? 0 : buf + 1;
    }
    BAR(); rd(1, buf); mm(0); sched(); WAITL0();
#pragma unroll
    for (int j = 0; j < 4; ++j) PIN(z1v[j]);
    PIN(bias[0]); PIN(bias[1]);
    BAR();
    mm(1);
  };
  if (nf == 4) body(std::integral_constant<int, 4>{}); else body(std::integral_constant<int, 3>{});
  const __amdgpu_buffer_rsrc_t rs = ws_rsrc(ws);
  const int ff0 = ft * 7 + f0;
#pragma unroll
  for (int tf = 0; tf < 2; ++tf) {
    const int tfr = tt * 4 + wt * 2 + tf;
    u2v o[4];
#pragma unroll
    for (int j = 0; j < 4; ++j) {
      const u32 za = z1v[j][2 * tf], zb = z1v[j][2 * tf + 1];
      const f4 v = (acc[j][tf] + bias[tf]) * f4{bflo(za), bfhi(za), bflo(zb), bfhi(zb)};
      o[j] = u2v{pk_bf(v[0], v[1]), pk_bf(v[2], v[3])};
    }
    const u32 base = (u32)WS_P + (tfr * 27 + (ff0 >> 1)) * 1024 + lane * 16;
    if (!(ff0 & 1)) {
      st16(rs, base, u4v{o[0][0], o[0][1], o[1][0], o[1][1]});
      if (nf == 4) { if (ff0 + 3 < 54) st16(rs, base + 1024, u4v{o[2][0], o[2][1], o[3][0], o[3][1]}); }
      else st8(rs, base + 1024, o[2]);
    } else {
      st8(rs, base + 8, o[0]);
      st16(rs, base + 1024, u4v{o[1][0], o[1][1], o[2][0], o[2][1]});
    }
  }
  gu32* cnt = (gu32*)(ws + WS_CNT);
  waitv<0>();
  if (lane == 0) __hip_atomic_store(cnt + (tt * 2 + wt) * 32 + ft * 2 + wf, 1u, __ATOMIC_RELAXED, __HIP_MEMORY_SCOPE_AGENT);
  const int trow = tt * 64 + wave * 16 + li;
  const u16* h = (const u16*)(ws + WS_H);
  u2v hv[5];
#pragma unroll
  for (int j = 0; j < 5; ++j) hv[j] = *(const u2v*)(h + (size_t)trow * D + ft * 80 + j * 16 + g * 4);
  for (int spin = 0; spin < (1 << 16); ++spin) {
    const u32 c = __hip_atomic_load(cnt + (tt * 2 + (wave >> 1)) * 32 + li, __ATOMIC_RELAXED, __HIP_MEMORY_SCOPE_AGENT);
    if (__all(c != 0u)) break;
    __builtin_amdgcn_s_sleep(1);
  }
  asm volatile("" ::: "memory");
  u4v fb[27];
  const u32 poff = (u32)WS_P + (tt * 4 + wave) * 27 * 1024 + lane * 16;
#pragma unroll
  for (int k = 0; k < 3 * FPD; ++k) { fb[k] = __builtin_amdgcn_raw_buffer_load_b128(rs, poff + k * 1024, 0, AUX_SC1); SB(); }
  f4 acc3[5];
#pragma unroll
  for (int j = 0; j < 5; ++j) acc3[j] = f4{0, 0, 0, 0};
  u4v fa[2][15];
  u32 wo[3] = {(u32)lane * 16, (u32)lane * 16 + 3 * G2_STB - 8192, (u32)lane * 16 + 5 * G2_STB};
  PIN(wo[1]); PIN(wo[2]);
  auto rd3 = [&](int kt) {
#pragma unroll
    for (int ii = 0; ii < 15; ++ii) {
      const int i = kt * 15 + ii;
      const int off = ((32 + i / 22) % G2_NST) * G2_STB + (i % 22) * 1024;
      const int w = off < 3 * G2_STB - 8192 ? 0 : (off < 5 * G2_STB ? 1 : 2);
      fa[kt & 1][ii] = *(const u4v*)(smem + wo[w] + (off - (w == 0 ? 0 : (w == 1 ? 3 * G2_STB - 8192 : 5 * G2_STB))));
    }
  };
  auto mm3 = [&](int kt) {
#pragma unroll
    for (int kk = 0; kk < 3; ++kk)
#pragma unroll
      for (int j = 0; j < 5; ++j) acc3[j] = mfma_b(fa[kt & 1][kk * 5 + j], fb[kt * 3 + kk], acc3[j]);
  };
  BAR();
  rd3(0);
#pragma unroll
  for (int kt = 1; kt < 9; ++kt) {
    SB();
    if (kt - 1 + FPD < 9) {
#pragma unroll
      for (int k = 0; k < 3; ++k) fb[(kt - 1 + FPD) * 3 + k] = __builtin_amdgcn_raw_buffer_load_b128(rs, poff + ((kt - 1 + FPD) * 3 + k) * 1024, 0, AUX_SC1);
      SB();
    }
    rd3(kt);
    mm3(kt - 1);
#pragma unroll
    for (int i = 0; i < 15; ++i) { SGB(0x100, 1); SGB(0x8, 1); }
  }
  SB();
  mm3(8);
  {
    u2v o[5];
#pragma unroll
    for (int j = 0; j < 5; ++j) {
      u32 t0 = pk_bf(acc3[j][0], acc3[j][1]), t1 = pk_bf(acc3[j][2], acc3[j][3]);
      o[j] = u2v{pk_bf(bflo(t0) + bflo(hv[j][0]), bfhi(t0) + bfhi(hv[j][0])), pk_bf(bflo(t1) + bflo(hv[j][1]), bfhi(t1) + bfhi(hv[j][1]))};
    }
    const __amdgpu_buffer_rsrc_t ro = __builtin_amdgcn_make_buffer_rsrc((void*)out, 0, S * D * 2, 0x00020000);
    const u32 oo = (trow * D + ft * 80) * 2;
#pragma unroll
    for (int j = 0; j < 4; j += 2) {
      auto s0 = __builtin_amdgcn_permlane16_swap(o[j][0], o[j + 1][0], false, false);
      auto s1 = __builtin_amdgcn_permlane16_swap(o[j][1], o[j + 1][1], false, false);
      __builtin_amdgcn_raw_buffer_store_b128(u4v{s0[0], s1[0], s0[1], s1[1]}, ro, oo + (j + (g & 1)) * 32 + (g >> 1) * 16, 0, AUX_SC1);
    }
    __builtin_amdgcn_raw_buffer_store_b64(o[4], ro, oo + 128 + g * 8, 0, AUX_SC1);
  }
}

extern "C" __global__ void end_kernel() {}


extern "C" void kernel_launch(void* const* d_in, const int* in_sizes, int n_in,
                              void* d_out, int out_size, void* d_ws, size_t ws_size,
                              hipStream_t stream) {
  const u16* x=(const u16*)d_in[0]; const u16* wr=(const u16*)d_in[1]; const u16* wi=(const u16*)d_in[2];
  const u16* w_in=(const u16*)d_in[4]; const u16* w_sp=(const u16*)d_in[5]; const u16* b_sp=(const u16*)d_in[6]; const u16* w_out=(const u16*)d_in[7];
  char* ws=(char*)d_ws;
  k1_kernel<<<256,512,0,stream>>>(x,wr,wi,ws,w_sp,w_in,w_out,b_sp,(u16*)d_out);
  (void)hipFuncSetAttribute((const void*)k2_kernel, hipFuncAttributeMaxDynamicSharedMemorySize, 84 * 1024);
  k2_kernel<<<K2_FFT_BLOCKS+K2_PREP_BLOCKS,512,84 * 1024,stream>>>();
  k3_kernel<<<256,256,0,stream>>>();
  g1_kernel<<<256,512,0,stream>>>();
  g2_kernel<<<256,512,0,stream>>>();
}
```

```cpp
#include <hip/hip_runtime.h>
#include <hip/hip_bf16.h>
#include <stdint.h>
typedef unsigned short u16;
typedef unsigned int u32;
typedef _Float16 h2 __attribute__((ext_vector_type(2)));
typedef _Float16 h8 __attribute__((ext_vector_type(8)));
typedef __bf16 bf2 __attribute__((ext_vector_type(2)));
typedef __bf16 bf8 __attribute__((ext_vector_type(8)));
typedef float f2 __attribute__((ext_vector_type(2)));
typedef float f4 __attribute__((ext_vector_type(4)));
typedef short s4 __attribute__((ext_vector_type(4)));
typedef u32 u2v __attribute__((ext_vector_type(2)));
typedef u32 u4v __attribute__((ext_vector_type(4)));
#define DI __device__ __forceinline__
#define LDS_AS __attribute__((address_space(3)))
#define PIN(x) asm volatile("" : "+v"(x))
#define GLDS16(src, dst) __builtin_amdgcn_global_load_lds((const u32*)(src), (LDS_AS u32*)(dst), 16, 0, 0)
#define WAITV(n) asm volatile("s_waitcnt vmcnt(%0)" ::"n"(n) : "memory")

constexpr int S = 2048, D = 640, DF = 321, FF = 1728, FH = 864;
constexpr int TROW_UNITS = 648;
constexpr int NUNITS = 641;

constexpr size_t WS_T = 0, WS_WP = 2688u << 10, WS_TP = 5376u << 10, WS_WT1 = 8064u << 10, WS_WT3 = 10240u << 10, WS_H = 11392u << 10,
                 WS_L2 = 13952u << 10, WS_Z1 = 16512u << 10, WS_Z2 = 19968u << 10, WS_STATS = 23424u << 10, WS_W2 = 23680u << 10,
                 WS_WMAX = 23692u << 10, WS_UNS = 23696u << 10, WS_CNT = 23700u << 10, WS_P = WS_T;
#define PINS(x) asm volatile("" ::"s"(x))
#define GLDS4(src, dst) __builtin_amdgcn_global_load_lds((const u32*)(src), (LDS_AS u32*)(dst), 4, 0, 0)
#define touch(p) GLDS4(p, scratch)

constexpr int AUX_SC1 = 16;
constexpr u32 DROP = 0x80000000u;
DI __amdgpu_buffer_rsrc_t ws_rsrc(char* ws) { return __builtin_amdgcn_make_buffer_rsrc((void*)ws, 0, 64 << 20, 0x00020000); }
DI void st16(__amdgpu_buffer_rsrc_t rs, u32 off, u4v v) { __builtin_amdgcn_raw_buffer_store_b128(v, rs, off, 0, AUX_SC1); }
DI void st8(__amdgpu_buffer_rsrc_t rs, u32 off, u2v v) { __builtin_amdgcn_raw_buffer_store_b64(v, rs, off, 0, AUX_SC1); }
#define CODE_ADDR(var, sym)                                                                                     \
  const char* var;                                                                                              \
  {                                                                                                             \
    u32 lo_, hi_;                                                                                               \
    asm volatile("s_getpc_b64 s[40:41]\n\ts_add_u32 %0, s40, " #sym "@rel32@lo+4\n\ts_addc_u32 %1, s41, " #sym "@rel32@hi+12" \
                 : "=s"(lo_), "=s"(hi_)::"s40", "s41", "scc");                                                  \
    var = (const char*)(((unsigned long long)hi_ << 32) | lo_);                                                 \
  }
#define CODE_TOUCH(a, b, c)                                                           \
  {                                                                                   \
    CODE_ADDR(pa_, a) CODE_ADDR(pb_, b)                                               \
    const char* p_ = pa_ + ((c) * 64 + lane) * 128;                                   \
    if (p_ < pb_) touch(p_);                                                          \
  }
struct Args { char* ws; const u16* x; const u16* w_sp; const u16* w_in; const u16* w_out; const u16* b_sp; u16* out; void* pad; };
static __device__ __attribute__((aligned(128))) Args g_args;
typedef const __attribute__((address_space(4))) Args* ArgsPtr;
#define LOAD_ARGS() ArgsPtr ap_ = (ArgsPtr)(unsigned long long)&g_args
DI float bflo(u32 v) { return __uint_as_float(v << 16); }
DI float bfhi(u32 v) { return __uint_as_float(v & 0xffff0000u); }
DI u32 pk_bf(float a, float b) { f2 v = {a, b}; return __builtin_bit_cast(u32, __builtin_convertvector(v, bf2)); }
DI u32 pk_h(float a, float b) { f2 v = {a, b}; return __builtin_bit_cast(u32, __builtin_convertvector(v, h2)); }
DI float rbf(float a) { return bflo(pk_bf(a, a)); }
DI float cosr(float rev) { return __builtin_amdgcn_cosf(rev); }
DI float sinr(float rev) { return __builtin_amdgcn_sinf(rev); }
DI f4 mfma_h(u4v a, u4v b, f4 c) {
  return __builtin_amdgcn_mfma_f32_16x16x32_f16(__builtin_bit_cast(h8, a), __builtin_bit_cast(h8, b), c, 0, 0, 0);
}
template <int CTRL> DI float dpp_add(float v) {
  int t = __builtin_amdgcn_update_dpp(0, __float_as_int(v), CTRL, 0xf, 0xf, true);
  return v + __int_as_float(t);
}
DI float xsum_g(float v) {
  u32 b = __float_as_uint(v);
  auto r = __builtin_amdgcn_permlane16_swap(b, b, false, false);
  v = __uint_as_float(r[0]) + __uint_as_float(r[1]);
  b = __float_as_uint(v);
  auto s = __builtin_amdgcn_permlane32_swap(b, b, false, false);
  return __uint_as_float(s[0]) + __uint_as_float(s[1]);
}
template <int CTRL> DI u32 dpp_maxu(u32 v) {
  u32 t = (u32)__builtin_amdgcn_update_dpp(0, (int)v, CTRL, 0xf, 0xf, true);
  return v > t ? v : t;
}
DI u32 max64u(u32 v) {
  v = dpp_maxu<0xB1>(v); v = dpp_maxu<0x4E>(v); v = dpp_maxu<0x141>(v); v = dpp_maxu<0x140>(v);
  auto r = __builtin_amdgcn_permlane16_swap(v, v, false, false);
  v = r[0] > r[1] ? r[0] : r[1];
  auto s = __builtin_amdgcn_permlane32_swap(v, v, false, false);
  return s[0] > s[1] ? s[0] : s[1];
}
DI float sum32(float v) {
  v = dpp_add<0xB1>(v); v = dpp_add<0x4E>(v); v = dpp_add<0x141>(v); v = dpp_add<0x140>(v);
  u32 b = __float_as_uint(v);
  auto r = __builtin_amdgcn_permlane16_swap(b, b, false, false);
  return __uint_as_float(r[0]) + __uint_as_float(r[1]);
}
DI void ln_row(f2 (&v)[10]) {
  f2 s = ((v[0] + v[1]) + (v[2] + v[3])) + ((v[4] + v[5]) + (v[6] + v[7])) + (v[8] + v[9]);
  const float mean = sum32(s[0] + s[1]) * (1.0f / 640.0f);
  f2 q0 = f2{0.f, 0.f}, q1 = f2{0.f, 0.f};
#pragma unroll
  for (int i = 0; i < 10; i += 2) { v[i] -= mean; v[i + 1] -= mean; q0 += v[i] * v[i]; q1 += v[i + 1] * v[i + 1]; }
  q0 += q1;
  const float var = sum32(q0[0] + q0[1]) * (1.0f / 640.0f);
  const float rinv = __builtin_amdgcn_rcpf(rbf(__builtin_amdgcn_sqrtf(rbf(rbf(var) + 1e-5f))));
  const float off = (mean - rbf(mean)) * rinv;
#pragma unroll
  for (int i = 0; i < 10; ++i) v[i] = v[i] * rinv + off;
}
#define SB() __builtin_amdgcn_sched_barrier(0)
template <bool SWAP> DI u4v cmul_pack(f4 re, f4 im, f4 wr, f4 wi) {
  const f4 ore = re * wr - im * wi, oim = re * wi + im * wr;
  u4v v;
#pragma unroll
  for (int r = 0; r < 4; ++r) v[r] = SWAP ? pk_h(oim[r], ore[r]) : pk_h(ore[r], oim[r]);
  return v;
}

struct __attribute__((packed, aligned(2))) U4U { u32 a, b, c, d; };

DI void lds_signal(char* p) {
  asm volatile("s_waitcnt lgkmcnt(0)" ::: "memory");
  if ((threadIdx.x & 63) == 0) __hip_atomic_fetch_add((LDS_AS u32*)p, 1u, __ATOMIC_RELAXED, __HIP_MEMORY_SCOPE_WORKGROUP);
}
DI void lds_wait(char* p, u32 n) {
  while (__builtin_amdgcn_readfirstlane(__hip_atomic_load((LDS_AS u32*)p, __ATOMIC_RELAXED, __HIP_MEMORY_SCOPE_WORKGROUP)) < n) {}
  asm volatile("" ::: "memory");
}
constexpr int K1_F40 = 10272, K1_TW = K1_F40 + 18432, K1_R16 = K1_TW + 6144, K1_LDS = K1_R16 + 4096 + 256 + 16;
extern "C" __global__ __launch_bounds__(512) void k1_kernel(const u16* __restrict__ x, const u16* __restrict__ wr,
                                                            const u16* __restrict__ wi, char* __restrict__ ws,
                                                            const u16* __restrict__ w_sp, const u16* w_in, const u16* w_out,
                                                            const u16* b_sp, u16* out) {
  __shared__ __attribute__((aligned(16))) char smem[K1_LDS];
  const int tid = threadIdx.x, lane = tid & 63;
  const int wave = __builtin_amdgcn_readfirstlane(tid >> 6);
  const int li = lane & 15, g = lane >> 4;
  char* scratch = smem + K1_R16 + 4096;
  char* flags = scratch + 256;
  if (wave == 7) CODE_TOUCH(k1_kernel, end_kernel, (blockIdx.x >> 3) & 7)
  PINS(x); PINS(wr); PINS(wi); PINS(ws); PINS(w_sp);
  auto constants = [&]() {
#pragma unroll
    for (int it = 0; it < 2; ++it) {
      const int item = wave + 8 * it;
      if (it == 0 || wave == 0) {
        const int to = item / 3, tk = item - 3 * to;
        const int kb = 16 * to + li;
        const float s8 = (tk < 2 || g < 2) ? 0.125f : 0.f;
        u4v fre, fim;
#pragma unroll
        for (int jj = 0; jj < 4; ++jj) {
          const float ang = (float)(kb * (16 * tk + 4 * g + jj)) * (1.0f / 40.0f);
          const float c = cosr(ang) * s8, s = sinr(ang) * s8;
          fre[jj] = pk_h(c, s);
          fim[jj] = pk_h(-s, c);
        }
        *(u4v*)(smem + K1_F40 + item * 2048 + lane * 16) = fre;
        *(u4v*)(smem + K1_F40 + item * 2048 + 1024 + lane * 16) = fim;
      } else if (wave < 4) {
        const int t = wave - 1;
        f4 twr, twi;
#pragma unroll
        for (int r = 0; r < 4; ++r) {
          const float ang = (float)((16 * t + 4 * g + r) * li) * (1.0f / 640.0f);
          twr[r] = cosr(ang) * 0.125f; twi[r] = -sinr(ang) * 0.125f;
        }
        *(f4*)(smem + K1_TW + t * 2048 + lane * 16) = twr;
        *(f4*)(smem + K1_TW + t * 2048 + 1024 + lane * 16) = twi;
      } else {
        const int ph = 2 * (wave - 4) + (lane >> 5);
        float cs[8];
#pragma unroll
        for (int jj = 0; jj < 8; ++jj) cs[jj] = cosr((float)(((8 * (g & 1) + jj) * li + 2 * ph) & 15) * 0.0625f);
        *(u4v*)(smem + K1_R16 + (wave - 4) * 1024 + lane * 16) = u4v{pk_h(cs[0], cs[1]), pk_h(cs[2], cs[3]), pk_h(cs[4], cs[5]), pk_h(cs[6], cs[7])};
      }
    }
    if (tid < 4) { *(u32*)(smem + 10240 + tid * 4) = 0u; *(u32*)(flags + tid * 4) = 0u; }
    asm volatile("s_waitcnt lgkmcnt(0)" ::: "memory");
    __builtin_amdgcn_s_barrier();
    asm volatile("" ::: "memory");
  };
  if (wave >= 4) {
    const int slotid = (blockIdx.x >> 3) * 3 + (wave - 4);
    const bool act = wave < 7 && slotid < 84;
    const int cp = blockIdx.x & 7, cc = act ? slotid >> 2 : 0, qt = slotid & 3;
    const int c0 = cc < 20 ? cc * 16 : 305;
    const bool mir = cp > 4;
    const bool sp = (cp & 3) == 0;
    const int e = mir ? 255 - (qt * 64 + lane) : qt * 64 + lane;
    const size_t roff = (size_t)(cp + 8 * e) * DF + c0;
    const U4U* pr = (const U4U*)(wr + roff);
    const U4U* pi = (const U4U*)(wi + roff);
    U4U r0, r1, i0, i1;
    __builtin_amdgcn_s_sleep(3);
    if (act) { r0 = pr[0]; r1 = pr[1]; i0 = pi[0]; i1 = pi[1]; }
    SB();
    constants();
    if (!act) {
      if (blockIdx.x == 255 && tid == 256) g_args = Args{ws, x, w_sp, w_in, w_out, b_sp, out, nullptr};
      if (blockIdx.x >= 224 && tid == 448) ((u32*)(ws + WS_WMAX))[blockIdx.x] = 0u;
      WAITV(0);
      return;
    }
    const u32 flip = mir ? 0x80000000u : 0u;
    const int q1 = (mir ? 8 - cp : cp) - 1;
    const int kb = mir ? 39 - cc : cc;
    const int t1 = sp ? 1920 + 32 * cc + (cp >> 2) : (q1 * 160 + (kb >> 2) * 16 + (mir ? 16 : 0)) * 4 + (kb & 3);
    const int step = sp ? 2 : (mir ? -4 : 4);
    const int t0 = mir ? (q1 * 160 + ((kb + 1) >> 2) * 16) * 4 + ((kb + 1) & 3) : t1;
    const bool edge = (cc == 0 || cc == 20);
    const __amdgpu_buffer_rsrc_t rs = ws_rsrc(ws);
    const u32 slot = (u32)WS_WP + (qt * 16 + li) * 16;
    u32 doff[4];
#pragma unroll
    for (int m = 0; m < 4; ++m) {
      const int jc = 4 * m + g;
      doff[m] = slot + (u32)(t1 + step * jc) * 1024u;
      if (cc == 20) doff[m] = DROP;
    }
    if (g == 0 && cc < 20) doff[0] = (mir && edge) ? DROP : slot + (u32)t0 * 1024u;
    if (g == 3 && cc == 20 && !mir) doff[3] = slot + (u32)t0 * 1024u;
    u32 rr[8] = {r0.a, r0.b, r0.c, r0.d, r1.a, r1.b, r1.c, r1.d};
    u32 ii[8] = {i0.a, i0.b, i0.c, i0.d, i1.a, i1.b, i1.c, i1.d};
    u32 vals[16];
#pragma unroll
    for (int j = 0; j < 16; ++j) {
      u32 a = rr[j >> 1], b = ii[j >> 1];
      vals[j] = ((j & 1) ? __builtin_amdgcn_perm(b, a, 0x07060302u) : __builtin_amdgcn_perm(b, a, 0x05040100u)) ^ flip;
    }
    if (edge && (mir || sp)) {
      const u32 v0 = cc < 20 ? vals[0] : vals[15];
      const int e2 = (cp == 0) ? ((256 - e) & 255) : 255 - e;
      const int dpos2 = ((e2 & 15) + 16 * (e2 >> 6)) * 4 + ((e2 >> 4) & 3);
      const int cls = mir ? 8 - cp : cp;
      ((u32*)(ws + WS_W2))[(cls * 2 + (cc == 20)) * 256 + dpos2] = v0 ^ flip ^ 0x80000000u;
    }
    u32 mx = 0;
#pragma unroll
    for (int j = 0; j < 16; ++j) {
      const u32 a = vals[j] & 0x7fff7fffu;
      mx = max(mx, max(a & 0xffffu, a >> 16));
    }
#pragma unroll
    for (int m = 0; m < 4; ++m) {
      auto p02 = __builtin_amdgcn_permlane32_swap(vals[4 * m], vals[4 * m + 2], false, false);
      auto p13 = __builtin_amdgcn_permlane32_swap(vals[4 * m + 1], vals[4 * m + 3], false, false);
      auto q01 = __builtin_amdgcn_permlane16_swap(p02[0], p13[0], false, false);
      auto q23 = __builtin_amdgcn_permlane16_swap(p02[1], p13[1], false, false);
      st16(rs, doff[m], u4v{q01[0], q01[1], q23[0], q23[1]});
    }
    mx = max64u(mx);
    if (lane == 0) {
      __hip_atomic_fetch_max((LDS_AS u32*)(flags + 4), mx, __ATOMIC_RELAXED, __HIP_MEMORY_SCOPE_WORKGROUP);
      asm volatile("s_waitcnt lgkmcnt(0)" ::: "memory");
      if (__hip_atomic_fetch_add((LDS_AS u32*)(flags + 8), 1u, __ATOMIC_RELAXED, __HIP_MEMORY_SCOPE_WORKGROUP) == 2u)
        ((u32*)(ws + WS_WMAX))[blockIdx.x] = __hip_atomic_load((LDS_AS u32*)(flags + 4), __ATOMIC_RELAXED, __HIP_MEMORY_SCOPE_WORKGROUP);
    }
    return;
  }
  const int b = (blockIdx.x & 7) * 32 + (blockIdx.x >> 3);
  const int a = 2 * wave + (lane >> 5), j = lane & 31;
  const u16* xr = x + (size_t)(256 * a + b) * D;
  const u4v x0 = *(const u4v*)(xr + 8 * j);
  const u4v x1 = *(const u4v*)(xr + 256 + 8 * j);
  const u2v x2 = *(const u2v*)(xr + 512 + 4 * j);
  SB();
  constants();
  const int q = wave;
  u4v bre[4], bim[4], fre[9], fim[9];
  f4 twr[3], twi[3];
  u32 toff[3][4];
  {
    const int a0 = g >> 1;
#pragma unroll
    for (int ks = 0; ks < 4; ++ks) {
      const int ph = (q == 0) ? (a0 ? 1 : 7) : (2 * ks + a0) * q;
      bre[ks] = *(const u4v*)(smem + K1_R16 + (ph & 7) * 512 + (lane & 31) * 16);
      bim[ks] = *(const u4v*)(smem + K1_R16 + ((ph + 2) & 7) * 512 + (lane & 31) * 16);
    }
#pragma unroll
    for (int i = 0; i < 9; ++i) {
      fre[i] = *(const u4v*)(smem + K1_F40 + i * 2048 + lane * 16);
      fim[i] = *(const u4v*)(smem + K1_F40 + i * 2048 + 1024 + lane * 16);
    }
    const float qs = q == 0 ? 0.70710678118f : 1.f;
#pragma unroll
    for (int t = 0; t < 3; ++t) {
      twr[t] = *(const f4*)(smem + K1_TW + t * 2048 + lane * 16) * qs;
      twi[t] = *(const f4*)(smem + K1_TW + t * 2048 + 1024 + lane * 16) * qs;
      PIN(twr[t]); PIN(twi[t]);
#pragma unroll
      for (int r = 0; r < 4; ++r) {
        const int k2 = li + 16 * (16 * t + 4 * g + r);
        const int pos = k2 <= 320 ? 2 * k2 : 2 * (640 - k2) + 1;
        toff[t][r] = (u32)WS_T + (u32)b * (TROW_UNITS * 16) + (q > 0 ? ((q - 1) * 160 + 64 * t + lane) * 16 : 480 * 16 + pos * 4);
        if (t == 2 && lane >= 32) toff[t][r] = DROP;
        PIN(toff[t][r]);
      }
    }
#pragma unroll
    for (int ks = 0; ks < 4; ++ks) { PIN(bre[ks]); PIN(bim[ks]); }
#pragma unroll
    for (int i = 0; i < 9; ++i) { PIN(fre[i]); PIN(fim[i]); }
  }
  {
    f2 v[10];
#pragma unroll
    for (int i = 0; i < 4; ++i) { v[i] = f2{bflo(x0[i]), bfhi(x0[i])}; v[4 + i] = f2{bflo(x1[i]), bfhi(x1[i])}; }
    v[8] = f2{bflo(x2[0]), bfhi(x2[0])}; v[9] = f2{bflo(x2[1]), bfhi(x2[1])};
    ln_row(v);
    char* ub = smem + a * 1280;
    *(u4v*)(ub + 16 * j) = u4v{pk_h(v[0][0], v[0][1]), pk_h(v[1][0], v[1][1]), pk_h(v[2][0], v[2][1]), pk_h(v[3][0], v[3][1])};
    *(u4v*)(ub + 512 + 16 * j) = u4v{pk_h(v[4][0], v[4][1]), pk_h(v[5][0], v[5][1]), pk_h(v[6][0], v[6][1]), pk_h(v[7][0], v[7][1])};
    *(u2v*)(ub + 1024 + 8 * j) = u2v{pk_h(v[8][0], v[8][1]), pk_h(v[9][0], v[9][1])};
  }
  lds_signal(flags);
  lds_wait(flags, 4);
  u4v b2[3];
  {
    const int qq = li >> 2, p = li & 3;
    const char* abase = smem + (8 * g + qq) * 80 + 8 * p;
    u4v af[3][4];
#pragma unroll
    for (int t = 0; t < 3; ++t)
#pragma unroll
      for (int ks = 0; ks < 4; ++ks) {
        s4 lo = __builtin_amdgcn_ds_read_tr16_b64_v4i16((LDS_AS s4*)(abase + ks * 2560 + t * 32));
        s4 hi = __builtin_amdgcn_ds_read_tr16_b64_v4i16((LDS_AS s4*)(abase + ks * 2560 + 320 + t * 32));
        u2v l2 = __builtin_bit_cast(u2v, lo), h2v = __builtin_bit_cast(u2v, hi);
        af[t][ks] = u4v{l2[0], l2[1], h2v[0], h2v[1]};
      }
    SB();
    f4 are[3], aim[3];
#pragma unroll
    for (int t = 0; t < 3; ++t) {
      are[t] = f4{0, 0, 0, 0}; aim[t] = f4{0, 0, 0, 0};
#pragma unroll
      for (int ks = 0; ks < 4; ++ks) {
        are[t] = mfma_h(af[t][ks], bre[ks], are[t]);
        aim[t] = mfma_h(af[t][ks], bim[ks], aim[t]);
      }
    }
#pragma unroll
    for (int t = 0; t < 3; ++t) b2[t] = cmul_pack<false>(are[t], aim[t], twr[t], twi[t]);
  }
  const __amdgpu_buffer_rsrc_t rs = ws_rsrc(ws);
  f4 ore[3], oim[3];
#pragma unroll
  for (int tk = 0; tk < 3; ++tk)
#pragma unroll
    for (int to = 0; to < 3; ++to) {
      ore[to] = mfma_h(fre[to * 3 + tk], b2[tk], tk ? ore[to] : f4{0, 0, 0, 0});
      oim[to] = mfma_h(fim[to * 3 + tk], b2[tk], tk ? oim[to] : f4{0, 0, 0, 0});
    }
  u4v o[3];
#pragma unroll
  for (int to = 0; to < 3; ++to)
#pragma unroll
    for (int r = 0; r < 4; ++r) o[to][r] = pk_h(ore[to][r], oim[to][r]);
  if (q > 0) {
#pragma unroll
    for (int to = 0; to < 3; ++to) st16(rs, toff[to][0], o[to]);
  } else {
#pragma unroll
    for (int to = 0; to < 3; ++to)
#pragma unroll
      for (int r = 0; r < 4; ++r) __builtin_amdgcn_raw_buffer_store_b32(o[to][r], rs, toff[to][r], 0, 0);
    __builtin_amdgcn_raw_buffer_store_b32(lane ? o[1][0] : o[0][0], rs, lane == 0 ? toff[0][0] + 4 : (lane == 16 ? toff[1][0] + 4 : DROP), 0, 0);
  }
}

template <int NT, int LDW, int NCG, int NTASK, bool PERM>
DI void retile(const u16* __restrict__ W, __amdgpu_buffer_rsrc_t rs, u32 dstbase, int wid, int nw, char* lds, int lane) {
  const int li = lane & 15, g = lane >> 4, q = li >> 2, p = li & 3;
  u32 dst[NT];
#pragma unroll
  for (int i = 0; i < NT; ++i) {
    const int task = wid + i * nw;
    const int t = task < NTASK ? task : 0;
    const int ks = t / NCG, cg = t - ks * NCG;
    const u16* src = W + (size_t)(ks * 32 + (lane >> 3)) * LDW + cg * 64 + (lane & 7) * 8;
#pragma unroll
    for (int j = 0; j < 4; ++j) GLDS16(src + (size_t)j * 8 * LDW, lds + i * 4096 + j * 1024);
    dst[i] = task < NTASK ? dstbase + (u32)t * 4096u + lane * 16 : DROP;
  }
  const char* rb = lds + (PERM ? 4 * g + q : 8 * g + q) * 128 + 8 * p;
  WAITV(0);
#pragma unroll
  for (int i = 0; i < NT; ++i)
#pragma unroll
    for (int f = 0; f < 4; ++f) {
      s4 lo = __builtin_amdgcn_ds_read_tr16_b64_v4i16((LDS_AS s4*)(rb + i * 4096 + f * 32));
      s4 hi = __builtin_amdgcn_ds_read_tr16_b64_v4i16((LDS_AS s4*)(rb + i * 4096 + f * 32 + (PERM ? 16 : 4) * 128));
      u2v a = __builtin_bit_cast(u2v, lo), b = __builtin_bit_cast(u2v, hi);
      st16(rs, dst[i] + f * 1024, u4v{a[0], a[1], b[0], b[1]});
    }
}
constexpr int NRT1 = 20 * 27, NRT3 = 27 * 10;
constexpr int K2_FFT_BLOCKS = 168, K2_PREP_BLOCKS = 88;

extern "C" __global__ __launch_bounds__(512, 2) void k2_kernel() {
  LOAD_ARGS();
  char* ws = ap_->ws; const u16* w_in = ap_->w_in; const u16* w_out = ap_->w_out; const u16* w_sp = ap_->w_sp;
  PINS(ws); PINS(w_in); PINS(w_out); PINS(w_sp);
  const u32* T = (const u32*)(ws + WS_T); const u32* Wp = (const u32*)(ws + WS_WP); const u32* W2 = (const u32*)(ws + WS_W2);
  const u32* wmax = (const u32*)(ws + WS_WMAX); u32* Tp = (u32*)(ws + WS_TP); float* unscale = (float*)(ws + WS_UNS);
  u32* Wt1 = (u32*)(ws + WS_WT1); u32* Wt3 = (u32*)(ws + WS_WT3); u32* stats = (u32*)(ws + WS_STATS);
  extern __shared__ __attribute__((aligned(16))) char smem[];
  const int tid = threadIdx.x, lane = tid & 63;
  const int wave = __builtin_amdgcn_readfirstlane(tid >> 6);
  const int li = lane & 15, g = lane >> 4;
  if (blockIdx.x >= K2_FFT_BLOCKS) {
    const int wid = (blockIdx.x - K2_FFT_BLOCKS) * 8 + wave;
    const __amdgpu_buffer_rsrc_t rs = ws_rsrc(ws);
    if (wid < 256) st16(rs, (u32)WS_STATS + wid * 1024 + lane * 16, u4v{~0u, ~0u, ~0u, ~0u});
    else if (wid < 264) st16(rs, (u32)WS_CNT + (wid - 256) * 1024 + lane * 16, u4v{0u, 0u, 0u, 0u});
    if (wid < NRT1) retile<1, FF, 27, NRT1, false>(w_in, rs, (u32)WS_WT1, wid, 0, smem + wave * 8192, lane);
    else retile<2, D, 10, NRT3, true>(w_out, rs, (u32)WS_WT3, wid - NRT1, 164, smem + wave * 8192, lane);
    return;
  }
  const int P = (blockIdx.x & 7) * 21 + (blockIdx.x >> 3);
  if (P > 160) return;
  const int u = wave & 3, hh = wave >> 2;
  const int U = 4 * P + u;
#pragma unroll
  for (int i = 0; i < 2; ++i) {
    const int bb = (wave * 2 + i) * 16 + (lane >> 2);
    GLDS16(T + ((size_t)bb * TROW_UNITS + 4 * P + ((lane & 3) ^ ((lane >> 4) & 3))) * 4, smem + (wave * 2 + i) * 1024);
  }
  const u4v mxv = *(const u4v*)(wmax + lane * 4);
  u4v Wv[2];
#pragma unroll
  for (int t = 0; t < 2; ++t) Wv[t] = *(const u4v*)(Wp + ((size_t)((U < NUNITS ? U : 0) * 4 + 2 * hh + t) * 64 + lane) * 4);
  const bool special = P >= 120;
  const int cB = special ? 4 : (P / 40 + 1);
  const int ur = special ? U - 480 : U % 160;
  const bool e320 = special ? ur == 160 : ur == 80;
  const bool edge = hh == 0 && (ur == 0 || e320);
  u4v w2, w4;
  if (edge) {
    w2 = *(const u4v*)(W2 + ((special ? 0 : cB) * 2 + (e320 ? 1 : 0)) * 256 + lane * 4);
    w4 = *(const u4v*)(W2 + (4 * 2 + (e320 ? 1 : 0)) * 256 + lane * 4);
  }
  {
    char* cb = smem + 16384 + wave * 2048 + lane * 16;
    const int cc = wave < 4 ? cB : 0;
    const float inv = u < 2 ? (1.0f / 128.0f) : (1.0f / 2048.0f);
    const float amp = u == 0 ? 1.f : ((u == 1 && special) ? 0.125f : 0.25f);
    f4 c, s;
#pragma unroll
    for (int jj = 0; jj < 4; ++jj) {
      const int a = (u & 1) ? li * (cc + 8 * (4 * g + jj)) : (4 * g + jj) * (cc + 8 * li);
      const float ang = (float)a * inv;
      c[jj] = cosr(ang) * amp; s[jj] = sinr(ang) * amp;
    }
    if (u < 2) {
      *(u4v*)(cb) = u4v{pk_h(c[0], s[0]), pk_h(c[1], s[1]), pk_h(c[2], s[2]), pk_h(c[3], s[3])};
      *(u4v*)(cb + 1024) = u4v{pk_h(-s[0], c[0]), pk_h(-s[1], c[1]), pk_h(-s[2], c[2]), pk_h(-s[3], c[3])};
    } else {
      *(f4*)(cb) = c;
      *(f4*)(cb + 1024) = s;
    }
  }
  asm volatile("s_waitcnt lgkmcnt(0)" ::: "memory");
  __builtin_amdgcn_s_barrier();
  asm volatile("" ::: "memory");
  u4v Fre, Fim, P1re[2], P1im[2], P4re[2], P4im[2];
  f4 tfr[2], tfs[2], tir[2], tis[2];
  {
    const char* cb = smem + 16384 + lane * 16;
    Fre = *(const u4v*)(cb + 4 * 2048); Fim = *(const u4v*)(cb + 4 * 2048 + 1024);
#pragma unroll
    for (int c = 0; c < 2; ++c) {
      const char* cs = cb + ((c == 0 && special) ? 8192 : 0);
      P1re[c] = *(const u4v*)(cs); P1im[c] = *(const u4v*)(cs + 1024);
      P4re[c] = *(const u4v*)(cs + 2048); P4im[c] = *(const u4v*)(cs + 3072);
      tfr[c] = *(const f4*)(cs + 4096); tfs[c] = *(const f4*)(cs + 5120);
      tir[c] = *(const f4*)(cs + 6144); tis[c] = *(const f4*)(cs + 7168);
    }
    PIN(Fre); PIN(Fim);
#pragma unroll
    for (int c = 0; c < 2; ++c) { PIN(P1re[c]); PIN(P1im[c]); PIN(P4re[c]); PIN(P4im[c]); PIN(tfr[c]); PIN(tfs[c]); PIN(tir[c]); PIN(tis[c]); }
  }
  asm volatile("s_waitcnt vmcnt(0)" ::: "memory");
  __builtin_amdgcn_s_barrier();
  asm volatile("" ::: "memory");
  const int loff = (64 * g + li) * 64 + ((u ^ (li >> 2)) & 3) * 16 + hh * 8;
  u4v X[2];
#pragma unroll
  for (int jj = 0; jj < 4; ++jj) {
    const u2v L = *(const u2v*)(smem + loff + jj * 1024);
    X[0][jj] = L[0]; X[1][jj] = L[1];
  }
  if (special) {
#pragma unroll
    for (int jj = 0; jj < 4; ++jj) {
      const u32 au = X[0][jj], bu = X[1][jj] ^ 0x80000000u;
      const h2 A = __builtin_bit_cast(h2, au);
      const h2 Bc = __builtin_bit_cast(h2, bu);
      X[0][jj] = __builtin_bit_cast(u32, A + Bc);
      X[1][jj] = __builtin_bit_cast(u32, A - Bc);
    }
  }
  const f4 z4 = f4{0, 0, 0, 0};
  f4 re[2], im[2];
#pragma unroll
  for (int t = 0; t < 2; ++t) { re[t] = mfma_h(X[t], P1re[t], z4); im[t] = mfma_h(X[t], P1im[t], z4); }
  const u32 mx = max64u(max(max(mxv[0], mxv[1]), max(mxv[2], mxv[3])));
  int E = (int)(mx >> 7);
  E = E < 1 ? 1 : (E > 253 ? 253 : E);
  const float sig = __uint_as_float((u32)(254 - E) << 23) * 0.25f;
#pragma unroll
  for (int t = 0; t < 2; ++t) {
    const u4v v = cmul_pack<false>(re[t], im[t], tfr[t], -tfs[t]);
    re[t] = mfma_h(Fre, v, z4); im[t] = mfma_h(Fim, v, z4);
  }
  f4 wr4[2], wi4[2];
#pragma unroll
  for (int t = 0; t < 2; ++t)
#pragma unroll
    for (int r = 0; r < 4; ++r) { wr4[t][r] = bflo(Wv[t][r]); wi4[t][r] = bfhi(Wv[t][r]); }
  if (edge) {
#pragma unroll
    for (int r = 0; r < 4; ++r) {
      wr4[0][r] = (wr4[0][r] + bflo(w2[r])) * 0.5f; wi4[0][r] = (wi4[0][r] + bfhi(w2[r])) * 0.5f;
      if (special) { wr4[1][r] = (wr4[1][r] + bflo(w4[r])) * 0.5f; wi4[1][r] = (wi4[1][r] + bfhi(w4[r])) * 0.5f; }
    }
    if (U == 0 && lane == 0) unscale[0] = __uint_as_float((u32)E << 23);
  }
#pragma unroll
  for (int t = 0; t < 2; ++t) {
    const u4v v = cmul_pack<true>(re[t], im[t], wr4[t] * sig, wi4[t] * sig);
    im[t] = mfma_h(v, Fre, z4); re[t] = mfma_h(v, Fim, z4);
  }
#pragma unroll
  for (int t = 0; t < 2; ++t) {
    const u4v v = cmul_pack<true>(re[t], im[t], tir[t], tis[t]);
    im[t] = mfma_h(P4re[t], v, z4); re[t] = mfma_h(P4im[t], v, z4);
  }
  if (special) {
    const f4 yr = re[0], yi = im[0], zr = re[1], zi = im[1];
    re[0] = yr + zr; im[0] = yi + zi;
    re[1] = yr - zr; im[1] = zi - yi;
  }
#pragma unroll
  for (int r = 0; r < 4; ++r) *(u2v*)(smem + loff + r * 1024) = u2v{pk_h(re[0][r], im[0][r]), pk_h(re[1][r], im[1][r])};
  asm volatile("s_waitcnt lgkmcnt(0)" ::: "memory");
  __builtin_amdgcn_s_barrier();
  asm volatile("" ::: "memory");
  const __amdgpu_buffer_rsrc_t rs = ws_rsrc(ws);
#pragma unroll
  for (int i = 0; i < 2; ++i) {
    const u4v o = *(const u4v*)(smem + (wave * 2 + i) * 1024 + lane * 16);
    st16(rs, (u32)WS_TP + P * 16384 + (wave * 2 + i) * 1024 + (lane & ~3) * 16 + ((lane & 3) ^ ((lane >> 4) & 3)) * 16, o);
  }
  { char* scratch = smem + 49152; if (wave == 0) CODE_TOUCH(k3_kernel, g1_kernel, 0) }
  WAITV(0);
}

extern "C" __global__ __launch_bounds__(256, 2) void k3_kernel() {
  LOAD_ARGS();
  char* ws = ap_->ws; const u16* x = ap_->x; const u16* w_sp = ap_->w_sp;
  PINS(x); PINS(ws); PINS(w_sp);
  const u32* Tp = (const u32*)(ws + WS_TP);
  constexpr int K3_Y = 10752, K3_CB = K3_Y + 10240;
  __shared__ __attribute__((aligned(16))) char smem[K3_CB + 8192 + 512];
  const int tid = threadIdx.x, lane = tid & 63;
  const int wave = __builtin_amdgcn_readfirstlane(tid >> 6);
  const int li = lane & 15, g = lane >> 4;
  const int b = (blockIdx.x & 7) * 32 + (blockIdx.x >> 3);
#pragma unroll
  for (int i = 0; i < 2; ++i) {
    int U = (wave * 2 + i) * 64 + lane;
    U = U < 480 ? U : 479;
    GLDS16(Tp + (size_t)(U >> 2) * 4096 + b * 16 + (U & 3) * 4, smem + (wave * 2 + i) * 1024);
  }
#pragma unroll
  for (int it = 0; it < 4; ++it) {
    const int i = it < 2 ? wave * 2 + it : 6 + it;
    if (it < 2 || wave == 3) {
      const int k2 = (lane >> 2) + 16 * (4 * i + (lane & 3));
      const int pos = k2 <= 320 ? 2 * k2 : 2 * (640 - k2) + 1;
      GLDS4(Tp + (size_t)(120 + (pos >> 4)) * 4096 + b * 16 + (pos & 15), smem + 8192 + i * 256);
    }
  }
  char* scratch = smem + K3_CB + 8192;
  if (wave == 0) GLDS4(ws + WS_UNS, scratch + 256);
  const int a = 2 * wave + (lane >> 5), j = lane & 31;
  const int row = 256 * a + b;
  const u16* xr = x + (size_t)row * D;
  u4v x0 = *(const u4v*)(xr + 8 * j);
  u4v x1 = *(const u4v*)(xr + 256 + 8 * j);
  u2v x2 = *(const u2v*)(xr + 512 + 4 * j);
  char* cb = smem + K3_CB;
  const int t = wave;
  u4v Bre[3], Bim[3];
  if (wave < 3) {
#pragma unroll
    for (int tk = 0; tk < 3; ++tk) {
      const float sc = (tk < 2 || g < 2) ? 1.f : 0.f;
#pragma unroll
      for (int jj = 0; jj < 4; ++jj) {
        const float ang = (float)((16 * tk + 4 * g + jj) * (16 * t + li)) * (1.0f / 40.0f);
        const float c = cosr(ang) * sc, s = sinr(ang) * sc;
        Bre[tk][jj] = pk_h(c, -s);
        Bim[tk][jj] = pk_h(s, c);
      }
      PIN(Bre[tk]); PIN(Bim[tk]);
    }
  } else {
#pragma unroll
    for (int tt = 0; tt < 3; ++tt) {
      f4 twr, twi;
#pragma unroll
      for (int jj = 0; jj < 4; ++jj) {
        const float ang = (float)((16 * tt + li) * (4 * g + jj)) * (1.0f / 640.0f);
        twr[jj] = cosr(ang) * 0.125f; twi[jj] = sinr(ang) * 0.125f;
      }
      *(f4*)(cb + tt * 2048 + lane * 16) = twr;
      *(f4*)(cb + tt * 2048 + 1024 + lane * 16) = twi;
    }
    u4v gre, gim;
#pragma unroll
    for (int jj = 0; jj < 4; ++jj) {
      const float ang = (float)(li * (4 * g + jj)) * 0.0625f;
      const float c = cosr(ang), s = sinr(ang);
      gre[jj] = pk_h(c, -s); gim[jj] = pk_h(s, c);
    }
    *(u4v*)(cb + 6144 + lane * 16) = gre;
    *(u4v*)(cb + 7168 + lane * 16) = gim;
  }
  asm volatile("s_waitcnt vmcnt(0)" ::: "memory");
  PIN(x0); PIN(x1); PIN(x2);
  __syncthreads();
  char* yb = smem + K3_Y;
  if (wave < 3) {
    const f4 z4 = f4{0, 0, 0, 0};
    u4v af[4][3];
#pragma unroll
    for (int q = 0; q < 4; ++q) {
      const char* base = smem + (q == 0 ? 8192 : (q - 1) * 2560);
#pragma unroll
      for (int tk = 0; tk < 3; ++tk) af[q][tk] = *(const u4v*)(base + (tk < 2 ? (64 * tk + lane) : (128 + (lane & 31))) * 16);
    }
    const f4 twr = *(const f4*)(cb + t * 2048 + lane * 16), twi = *(const f4*)(cb + t * 2048 + 1024 + lane * 16);
    const u4v Gre = *(const u4v*)(cb + 6144 + lane * 16), Gim = *(const u4v*)(cb + 7168 + lane * 16);
    const float usc = *(const float*)(scratch + 256);
    SB();
    f4 vre[4], vim[4];
#pragma unroll
    for (int q = 0; q < 4; ++q) {
      f4 re = z4, im = z4;
#pragma unroll
      for (int tk = 0; tk < 3; ++tk) {
        re = mfma_h(af[q][tk], Bre[tk], re);
        im = mfma_h(af[q][tk], Bim[tk], im);
      }
      vre[q] = re; vim[q] = im;
    }
#pragma unroll
    for (int q = 0; q < 4; ++q) {
      const u4v v = cmul_pack<false>(vre[q], vim[q], twr, twi);
      vre[q] = mfma_h(v, Gre, z4);
      vim[q] = mfma_h(v, Gim, z4);
    }
    const float fs = usc * (2.0f * 1024.0f * 128.0f / (2048.0f * 640.0f));
    const float fh = fs * 0.70710678118f;
    const f4 E = (vre[0] + vim[0]) * fs, O = (vre[0] - vim[0]) * fs;
    const f4 s13 = vre[1] + vre[3], dx = vre[1] - vre[3], sy = vim[1] + vim[3], dy = vim[3] - vim[1];
    const f4 Ep = E + vre[2] * fs, Em = E - vre[2] * fs, Om = O - vim[2] * fs, Op = O + vim[2] * fs;
    const f4 A = dx - sy, B = dx + sy;
    f4 y[8];
    y[0] = Ep + s13 * fs; y[4] = Ep - s13 * fs; y[2] = Em + dy * fs; y[6] = Em - dy * fs;
    y[1] = Om + A * fh; y[5] = Om - A * fh; y[3] = Op - B * fh; y[7] = Op + B * fh;
    if (t < 2 || lane < 32) {
      char* yp = yb + (40 * li + 16 * t + 4 * g) * 2;
#pragma unroll
      for (int aa = 0; aa < 8; ++aa) *(u2v*)(yp + aa * 1280) = u2v{pk_bf(y[aa][0], y[aa][1]), pk_bf(y[aa][2], y[aa][3])};
    }
  }
  else {
    CODE_TOUCH(g1_kernel, g2_kernel, (blockIdx.x >> 3) & 1)
#pragma unroll
    for (int i = 0; i < 2; ++i) touch((const char*)w_sp + ((size_t)(512 + i * 256 + blockIdx.x) * 64 + lane) * 128);
  }
  asm volatile("s_waitcnt lgkmcnt(0)" ::: "memory");
  __builtin_amdgcn_s_barrier();
  asm volatile("" ::: "memory");
  {
    const __amdgpu_buffer_rsrc_t rs = ws_rsrc(ws);
    const u4v y0 = *(const u4v*)(yb + a * 1280 + 16 * j);
    const u4v y1 = *(const u4v*)(yb + a * 1280 + 512 + 16 * j);
    const u2v y2 = *(const u2v*)(yb + a * 1280 + 1024 + 8 * j);
    u32 hx[10];
#pragma unroll
    for (int i = 0; i < 4; ++i) {
      hx[i] = pk_bf(bflo(x0[i]) + bflo(y0[i]), bfhi(x0[i]) + bfhi(y0[i]));
      hx[4 + i] = pk_bf(bflo(x1[i]) + bflo(y1[i]), bfhi(x1[i]) + bfhi(y1[i]));
    }
    hx[8] = pk_bf(bflo(x2[0]) + bflo(y2[0]), bfhi(x2[0]) + bfhi(y2[0]));
    hx[9] = pk_bf(bflo(x2[1]) + bflo(y2[1]), bfhi(x2[1]) + bfhi(y2[1]));
    auto pairup = [&](u32 v0, u32 v1) {
      return u4v{v0, v1, (u32)__builtin_amdgcn_update_dpp(0, (int)v0, 0xB1, 0xf, 0xf, true), (u32)__builtin_amdgcn_update_dpp(0, (int)v1, 0xB1, 0xf, 0xf, true)};
    };
    const u32 drop = (lane & 1) ? DROP : 0u;
    const u32 ho = (u32)WS_H + row * (D * 2);
    st16(rs, ho + 16 * j, u4v{hx[0], hx[1], hx[2], hx[3]});
    st16(rs, ho + 512 + 16 * j, u4v{hx[4], hx[5], hx[6], hx[7]});
    st16(rs, (ho + 1024 + 8 * j) | drop, pairup(hx[8], hx[9]));
    f2 v[10];
#pragma unroll
    for (int i = 0; i < 10; ++i) v[i] = f2{bflo(hx[i]), bfhi(hx[i])};
    ln_row(v);
#pragma unroll
    for (int i = 0; i < 10; ++i) hx[i] = pk_bf(v[i][0], v[i][1]);
    const u32 lo = (u32)WS_L2 + (row >> 4) * 20480 + (row & 15) * 64;
    st16(rs, lo + (j >> 2) * 1024 + (j & 3) * 16, u4v{hx[0], hx[1], hx[2], hx[3]});
    st16(rs, lo + (8 + (j >> 2)) * 1024 + (j & 3) * 16, u4v{hx[4], hx[5], hx[6], hx[7]});
    st16(rs, (lo + (16 + (j >> 3)) * 1024 + (j & 7) * 8) | drop, pairup(hx[8], hx[9]));
  }
  WAITV(0);
}

constexpr int FPD = 2;
#include <type_traits>
typedef unsigned long long u64;
typedef __attribute__((address_space(1))) unsigned long long gu64;
#define WAITL0() do { __builtin_amdgcn_sched_barrier(0); asm volatile("s_waitcnt lgkmcnt(0)" ::: "memory"); __builtin_amdgcn_sched_barrier(0); } while (0)
#define BAR() do { asm volatile("" ::: "memory"); __builtin_amdgcn_sched_barrier(0); __builtin_amdgcn_s_barrier(); __builtin_amdgcn_sched_barrier(0); asm volatile("" ::: "memory"); } while (0)
#define SGB(mask, n) __builtin_amdgcn_sched_group_barrier(mask, n, 0)
DI f4 mfma_b(u4v a, u4v b, f4 c) {
  return __builtin_amdgcn_mfma_f32_16x16x32_bf16(__builtin_bit_cast(bf8, a), __builtin_bit_cast(bf8, b), c, 0, 0, 0);
}
DI f2 gelu2(f2 x) {
  const float k1 = -2.0f * 0.7978845608028654f * 1.4426950408889634f;
  const float k2 = k1 * 0.044715f;
  f2 p = x * x * k2 + k1;
  f2 a = x * p;
  f2 e = f2{__builtin_amdgcn_exp2f(a[0]), __builtin_amdgcn_exp2f(a[1])} + 1.0f;
  f2 r = f2{__builtin_amdgcn_rcpf(e[0]), __builtin_amdgcn_rcpf(e[1])};
  return x * r;
}
DI u2v gelu4(f4 a) {
  f2 g0 = gelu2(f2{a[0], a[1]}), g1 = gelu2(f2{a[2], a[3]});
  return u2v{pk_bf(g0[0], g0[1]), pk_bf(g1[0], g1[1])};
}
template <int N> DI void waitv() { asm volatile("s_waitcnt vmcnt(%0)" ::"n"(N) : "memory"); }

constexpr int G1_DEPTH = 3, G1_AD = 5;
extern "C" __global__ __launch_bounds__(512) void g1_kernel() {
  LOAD_ARGS();
  char* ws = ap_->ws; const u16* w_sp = ap_->w_sp;
  PINS(ws); PINS(w_sp);
  const u32* l2f = (const u32*)(ws + WS_L2); const u32* Wt1 = (const u32*)(ws + WS_WT1);
  u32* z1f = (u32*)(ws + WS_Z1); u16* z2t = (u16*)(ws + WS_Z2); u64* stats = (u64*)(ws + WS_STATS);
  __shared__ __attribute__((aligned(16))) char smem[140 * 1024 + 4 * 4608 + 256];
  const int tid = threadIdx.x, lane = tid & 63;
  const int wave = __builtin_amdgcn_readfirstlane(tid >> 6);
  const int li = lane & 15, g = lane >> 4;
  const int xcd = blockIdx.x & 7, idx = blockIdx.x >> 3;
  const int mt = (xcd >> 1) * 4 + (idx >> 3), nb = (xcd & 1) * 8 + (idx & 7);
  const int row0 = 1024 * (mt & 1) + 32 * (mt >> 1);
  const int zs = nb < 6 ? 4 * nb : 24 + 3 * (nb - 6);
  const int z1s = nb < 6 ? 3 * nb : (nb < 12 ? 18 + 4 * (nb - 6) : 42 + 3 * (nb - 12));
  const int type = nb < 6 ? 0 : (nb < 12 ? 1 : 2);
  if (wave >= 4) {
    const int lw = wave - 4;
    auto slot_frag = [&](int s) { return s < 3 ? 54 + zs + s : (s == 3 ? (type == 0 ? 54 + zs + 3 : (type == 1 ? z1s + 3 : z1s)) : z1s + s - 4); };
    const u32* bsrc[4];
#pragma unroll
    for (int j = 0; j < 4; ++j) {
      int i = lw + 4 * j; i = i < 14 ? i : 0;
      const int kk = i / 7, s = i - 7 * kk;
      bsrc[j] = Wt1 + (size_t)(kk * 108 + slot_frag(s)) * 256 + lane * 4;
    }
    auto stage = [&](int kt) {
      char* sb = smem + kt * 14336 + lw * 1024;
#pragma unroll
      for (int j = 0; j < 3; ++j) GLDS16(bsrc[j] + (size_t)kt * 2 * 108 * 256, sb + j * 4096);
      if (lw < 2) GLDS16(bsrc[3] + (size_t)kt * 2 * 108 * 256, sb + 3 * 4096);
    };
#pragma unroll
    for (int s = 0; s < G1_DEPTH; ++s) stage(s);
    for (int kt = 0; kt < 10 - G1_DEPTH; ++kt) {
      if (lw < 2) waitv<4 * (G1_DEPTH - 1)>(); else waitv<3 * (G1_DEPTH - 1)>();
      BAR();
      stage(kt + G1_DEPTH);
    }
    if (lw < 2) waitv<8>(); else waitv<6>(); BAR();
    if (lw < 2) waitv<4>(); else waitv<3>(); BAR();
    waitv<0>();
    char* scratch = smem + 140 * 1024 + 4 * 4608;
    if (lw < 2) touch((const char*)w_sp + ((size_t)(blockIdx.x * 2 + lw) * 64 + lane) * 128);
    BAR();
    if (lw >= 2) CODE_TOUCH(g2_kernel, end_kernel, lw - 2)
    waitv<0>();
    return;
  }
  const int rg0 = (row0 + wave * 256) >> 4;
  const u32* asrc = l2f + (size_t)rg0 * 20 * 256 + li * 16 + g * 4;
  u4v fa[G1_AD][2];
#pragma unroll
  for (int k = 0; k < G1_AD; ++k) { fa[k][0] = *(const u4v*)(asrc + k * 256); fa[k][1] = *(const u4v*)(asrc + (20 + k) * 256); __builtin_amdgcn_sched_barrier(0); }
  f4 acc[7][2];
#pragma unroll
  for (int s = 0; s < 7; ++s) { acc[s][0] = f4{0, 0, 0, 0}; acc[s][1] = f4{0, 0, 0, 0}; }
  u4v fw[2][7];
  const char* wb = smem + lane * 16;
  auto rd = [&](int k) {
#pragma unroll
    for (int s = 0; s < 7; ++s) fw[k & 1][s] = *(const u4v*)(wb + (k * 7 + s) * 1024);
  };
  auto mm = [&](int k) {
#pragma unroll
    for (int s = 0; s < 7; ++s) {
      acc[s][0] = mfma_b(fw[k & 1][s], fa[k % G1_AD][0], acc[s][0]);
      acc[s][1] = mfma_b(fw[k & 1][s], fa[k % G1_AD][1], acc[s][1]);
    }
    if (k + G1_AD < 20) { fa[k % G1_AD][0] = *(const u4v*)(asrc + (k + G1_AD) * 256); fa[k % G1_AD][1] = *(const u4v*)(asrc + (20 + G1_AD + k) * 256); }
  };
  BAR(); rd(0);
#pragma unroll
  for (int k = 1; k < 20; ++k) {
    if (!(k & 1)) BAR(); else __builtin_amdgcn_sched_barrier(0);
    rd(k);
    mm(k - 1);
#pragma unroll
    for (int i = 0; i < 7; ++i) { SGB(0x100, 1); SGB(0x8, 2); }
  }
  __builtin_amdgcn_sched_barrier(0);
  mm(19);
  const int n2 = type == 0 ? 4 : 3;
  f4 zv[4][2];
  u2v z3[2];
  float lsum[2], lm2[2];
#pragma unroll
  for (int sf = 0; sf < 2; ++sf) {
    f2 s = f2{0.f, 0.f};
#pragma unroll
    for (int j = 0; j < 4; ++j) {
      const u2v t = gelu4(acc[j][sf]);
      if (j == 3) z3[sf] = t;
      zv[j][sf] = f4{bflo(t[0]), bfhi(t[0]), bflo(t[1]), bfhi(t[1])};
      if (j < 3 || type == 0) s += f2{zv[j][sf][0], zv[j][sf][1]} + f2{zv[j][sf][2], zv[j][sf][3]};
    }
    const float st = xsum_g(s[0] + s[1]);
    const float lmean = st * (type == 0 ? (1.0f / 64.0f) : (1.0f / 48.0f));
    f2 q = f2{0.f, 0.f};
#pragma unroll
    for (int j = 0; j < 4; ++j) {
      const f4 d = zv[j][sf] - lmean;
      if (j < 3 || type == 0) { q += f2{d[0], d[1]} * f2{d[0], d[1]}; q += f2{d[2], d[3]} * f2{d[2], d[3]}; }
    }
    lsum[sf] = st; lm2[sf] = xsum_g(q[0] + q[1]);
  }
  gu64* st = (gu64*)stats + (size_t)mt * 16 * 128 + wave * 32;
  if (lane < 32) {
    u32 a = __float_as_uint(g ? lsum[1] : lsum[0]), b = __float_as_uint(g ? lm2[1] : lm2[0]);
    if (a == ~0u) a = 0x7fc00000u;
    __hip_atomic_store(st + nb * 128 + lane, ((u64)b << 32) | a, __ATOMIC_RELAXED, __HIP_MEMORY_SCOPE_AGENT);
  }
  __builtin_amdgcn_sched_barrier(0);
  const __amdgpu_buffer_rsrc_t rs = ws_rsrc(ws);
  u4v z1o[3];
#pragma unroll
  for (int j = 0; j < 3; ++j) {
    const u2v a = gelu4(acc[4 + j][0]), b = gelu4(acc[4 + j][1]);
    z1o[j] = u4v{a[0], a[1], b[0], b[1]};
    PIN(z1o[j]);
  }
  __builtin_amdgcn_sched_barrier(0);
  u64 gv[2][4];
  for (int spin = 0; spin < (1 << 16); ++spin) {
    bool ok = true;
#pragma unroll
    for (int sf = 0; sf < 2; ++sf)
#pragma unroll
      for (int i = 0; i < 4; ++i) {
        gv[sf][i] = __hip_atomic_load(st + (4 * g + i) * 128 + sf * 16 + li, __ATOMIC_RELAXED, __HIP_MEMORY_SCOPE_AGENT);
        ok &= ((u32)gv[sf][i] != ~0u);
      }
    if (__all(ok)) break;
    __builtin_amdgcn_s_sleep(1);
  }
  {
    const u32 zo = (u32)WS_Z1 + ((rg0 >> 1) * 54 + z1s) * 1024 + lane * 16;
#pragma unroll
    for (int j = 0; j < 3; ++j) st16(rs, zo + j * 1024, z1o[j]);
    if (type == 1) st16(rs, zo + 3 * 1024, u4v{z3[0][0], z3[0][1], z3[1][0], z3[1][1]});
  }
  char* tb = smem + 140 * 1024 + wave * 4608;
#pragma unroll
  for (int sf = 0; sf < 2; ++sf) {
    float s = 0.f;
#pragma unroll
    for (int i = 0; i < 4; ++i) s += __uint_as_float((u32)gv[sf][i]);
    const float mean = xsum_g(s) * (1.0f / 864.0f);
    float q = 0.f;
#pragma unroll
    for (int i = 0; i < 4; ++i) {
      const bool big = (4 * g + i) < 6;
      const float dm = __uint_as_float((u32)gv[sf][i]) * (big ? (1.0f / 64.0f) : (1.0f / 48.0f)) - mean;
      q += __uint_as_float((u32)(gv[sf][i] >> 32)) + (big ? 64.f : 48.f) * dm * dm;
    }
    const float var = xsum_g(q) * (1.0f / 864.0f);
    const float mb = rbf(mean);
    const float rinv = __builtin_amdgcn_rcpf(rbf(__builtin_amdgcn_sqrtf(rbf(rbf(var) + 1e-5f))));
#pragma unroll
    for (int j = 0; j < 4; ++j) {
      const f4 o = (zv[j][sf] - mb) * rinv;
      *(u2v*)(tb + (sf * 16 + li) * 144 + j * 32 + g * 8) = u2v{pk_bf(o[0], o[1]), pk_bf(o[2], o[3])};
    }
  }
  {
    const int q = li >> 2, p = li & 3;
#pragma unroll
    for (int j = 0; j < 4; ++j) {
      s4 lo = __builtin_amdgcn_ds_read_tr16_b64_v4i16((LDS_AS s4*)(tb + (8 * g + q) * 144 + j * 32 + 8 * p));
      s4 hi = __builtin_amdgcn_ds_read_tr16_b64_v4i16((LDS_AS s4*)(tb + (8 * g + 4 + q) * 144 + j * 32 + 8 * p));
      u2v a = __builtin_bit_cast(u2v, lo), b = __builtin_bit_cast(u2v, hi);
      if (j < n2) st16(rs, (u32)WS_Z2 + (((zs + j) * 16 + li) * S + row0 + wave * 256 + 8 * g) * 2, u4v{a[0], a[1], b[0], b[1]});
    }
  }
}

typedef __attribute__((address_space(1))) unsigned int gu32;
constexpr int G2_NST = 7, G2_STB = 22 * 1024;
extern "C" __global__ __launch_bounds__(512) void g2_kernel() {
  LOAD_ARGS();
  char* ws = ap_->ws; const u16* w_sp = ap_->w_sp; const u16* b_sp = ap_->b_sp; u16* out = ap_->out;
  PINS(w_sp); PINS(b_sp); PINS(ws); PINS(out);
  const u16* z2t = (const u16*)(ws + WS_Z2); const u32* z1f = (const u32*)(ws + WS_Z1);
  __shared__ __attribute__((aligned(16))) char smem[G2_NST * G2_STB + 256];
  const int tid = threadIdx.x, lane = tid & 63;
  const int wave = __builtin_amdgcn_readfirstlane(tid >> 6);
  const int li = lane & 15, g = lane >> 4;
  const int xcd = blockIdx.x & 7, idx = blockIdx.x >> 3;
  const int tt = (xcd >> 1) * 8 + (idx >> 2), ft = (xcd & 1) * 4 + (idx & 3);
  if (wave >= 4) {
    const int lw = wave - 4;
    const u16* src[6];
    const int swz = ((lane & 7) ^ (lane >> 3)) * 8;
#pragma unroll
    for (int j = 0; j < 6; ++j) {
      int i = lw + 4 * j; i = i < 22 ? i : 8;
      if (j < 2) src[j] = w_sp + (size_t)(tt * 64 + 8 * i + (lane >> 3)) * S + swz;
      else { int r = ft * 112 + 8 * (i - 8) + (lane >> 3); r = r < FH ? r : FH - 1; src[j] = z2t + (size_t)r * S + swz; }
    }
    auto stage = [&](int kt, int buf) {
      char* sb = smem + buf * G2_STB + lw * 1024;
      const int kk = kt * 64;
#pragma unroll
      for (int j = 0; j < 5; ++j) GLDS16(src[j] + kk, sb + j * 4096);
      if (lw < 2) GLDS16(src[5] + kk, sb + 5 * 4096);
    };
    const bool big = lw < 2;
    stage(0, 0); stage(1, 1); stage(2, 2);
    if (big) waitv<12>(); else waitv<10>(); BAR();
    stage(3, 3); stage(4, 4);
    if (big) waitv<18>(); else waitv<15>(); BAR();
    stage(5, 5); stage(6, 6);
    if (big) waitv<24>(); else waitv<20>(); BAR();
    stage(7, 0); stage(8, 1);
    int nbuf = 2;
    for (int kt = 3; kt < 26; ++kt) {
      if (big) waitv<30>(); else waitv<25>();
      BAR();
      stage(kt + 6, nbuf);
      nbuf = nbuf + 1 == G2_NST ? 0 : nbuf + 1;
    }
    const u32* wsrc = (const u32*)(ws + WS_WT3) + (size_t)(ft * 5) * 256 + lane * 4;
    for (int ps = 0; ps < 6; ++ps) {
      if (big) waitv<30>(); else waitv<25>();
      BAR();
      char* sb = smem + nbuf * G2_STB + lw * 1024;
#pragma unroll
      for (int j = 0; j < 6; ++j) {
        const int i = ps * 22 + lw + 4 * j, k = i / 5;
        if (j < 5 || big) GLDS16(wsrc + (size_t)(k * 35 + i) * 256, sb + j * 4096);
      }
      nbuf = nbuf + 1 == G2_NST ? 0 : nbuf + 1;
    }
    BAR();
    if (lw < 3) GLDS16(wsrc + (size_t)(26 * 35 + 132 + lw) * 256, smem + nbuf * G2_STB + lw * 1024);
    waitv<0>();
    BAR();
    return;
  }
  const int wt = wave & 1, wf = wave >> 1;
  const int n0 = (ft & 1) ? 3 : 4;
  const int f0 = wf ? n0 : 0, nf = wf ? 7 - n0 : n0;
  u4v z1v[4];
  float bias[2];
#pragma unroll
  for (int j = 0; j < 4; ++j) {
    int ff = ft * 7 + f0 + j; ff = ff < 54 ? ff : 53;
    z1v[j] = *(const u4v*)(z1f + ((size_t)(tt * 2 + wt) * 54 + ff) * 256 + lane * 4);
  }
  bias[0] = bflo(b_sp[tt * 64 + wt * 32 + li]); bias[1] = bflo(b_sp[tt * 64 + wt * 32 + 16 + li]);
  f4 acc[4][2];
#pragma unroll
  for (int j = 0; j < 4; ++j) { acc[j][0] = f4{0, 0, 0, 0}; acc[j][1] = f4{0, 0, 0, 0}; }
  const int sw = (g ^ (li & 7)) * 16;
  const int toff = (wt * 32 + li) * 128 + sw;
  const int foff = 8192 + (f0 * 16 + li) * 128 + sw;
  auto body = [&](auto NFc) {
    constexpr int NF = decltype(NFc)::value;
    u4v fb[2][2][2], fa[2][2][NF];
    auto rd = [&](int set, int buf) {
      const char* sb = smem + buf * G2_STB;
#pragma unroll
      for (int kk = 0; kk < 2; ++kk) {
        fb[set][kk][0] = *(const u4v*)(sb + (toff ^ (kk * 64)));
        fb[set][kk][1] = *(const u4v*)(sb + (toff ^ (kk * 64)) + 2048);
#pragma unroll
        for (int j = 0; j < NF; ++j) fa[set][kk][j] = *(const u4v*)(sb + (foff ^ (kk * 64)) + j * 2048);
      }
    };
    auto mm = [&](int set) {
#pragma unroll
      for (int kk = 0; kk < 2; ++kk)
#pragma unroll
        for (int j = 0; j < NF; ++j) {
          acc[j][0] = mfma_b(fa[set][kk][j], fb[set][kk][0], acc[j][0]);
          acc[j][1] = mfma_b(fa[set][kk][j], fb[set][kk][1], acc[j][1]);
        }
    };
    auto sched = [&]() {
#pragma unroll
      for (int i = 0; i < 2 * NF + 4; ++i) { SGB(0x100, 1); SGB(0x8, 1); }
      SGB(0x8, 2 * NF - 4);
    };
    BAR(); rd(0, 0); WAITL0();
    int buf = 1;
    for (int kt = 1; kt < 31; kt += 2) {
      BAR(); rd(1, buf); mm(0); sched(); WAITL0();
      buf = buf + 1 == G2_NST ? 0 : buf + 1;
      BAR(); rd(0, buf); mm(1); sched(); WAITL0();
      buf = buf + 1 == G2_NST ? 0 : buf + 1;
    }
    BAR(); rd(1, buf); mm(0); sched(); WAITL0();
#pragma unroll
    for (int j = 0; j < 4; ++j) PIN(z1v[j]);
    PIN(bias[0]); PIN(bias[1]);
    BAR();
    mm(1);
  };
  if (nf == 4) body(std::integral_constant<int, 4>{}); else body(std::integral_constant<int, 3>{});
  const __amdgpu_buffer_rsrc_t rs = ws_rsrc(ws);
  const int ff0 = ft * 7 + f0;
#pragma unroll
  for (int tf = 0; tf < 2; ++tf) {
    const int tfr = tt * 4 + wt * 2 + tf;
    u2v o[4];
#pragma unroll
    for (int j = 0; j < 4; ++j) {
      const u32 za = z1v[j][2 * tf], zb = z1v[j][2 * tf + 1];
      const f4 v = (acc[j][tf] + bias[tf]) * f4{bflo(za), bfhi(za), bflo(zb), bfhi(zb)};
      o[j] = u2v{pk_bf(v[0], v[1]), pk_bf(v[2], v[3])};
    }
    const u32 base = (u32)WS_P + (tfr * 27 + (ff0 >> 1)) * 1024 + lane * 16;
    if (!(ff0 & 1)) {
      st16(rs, base, u4v{o[0][0], o[0][1], o[1][0], o[1][1]});
      if (nf == 4) { if (ff0 + 3 < 54) st16(rs, base + 1024, u4v{o[2][0], o[2][1], o[3][0], o[3][1]}); }
      else st8(rs, base + 1024, o[2]);
    } else {
      st8(rs, base + 8, o[0]);
      st16(rs, base + 1024, u4v{o[1][0], o[1][1], o[2][0], o[2][1]});
    }
  }
  gu32* cnt = (gu32*)(ws + WS_CNT);
  waitv<0>();
  if (lane == 0) __hip_atomic_store(cnt + (tt * 2 + wt) * 32 + ft * 2 + wf, 1u, __ATOMIC_RELAXED, __HIP_MEMORY_SCOPE_AGENT);
  const int trow = tt * 64 + wave * 16 + li;
  const u16* h = (const u16*)(ws + WS_H);
  u2v hv[5];
#pragma unroll
  for (int j = 0; j < 5; ++j) hv[j] = *(const u2v*)(h + (size_t)trow * D + ft * 80 + j * 16 + g * 4);
  for (int spin = 0; spin < (1 << 16); ++spin) {
    const u32 c = __hip_atomic_load(cnt + (tt * 2 + (wave >> 1)) * 32 + li, __ATOMIC_RELAXED, __HIP_MEMORY_SCOPE_AGENT);
    if (__all(c != 0u)) break;
    __builtin_amdgcn_s_sleep(1);
  }
  asm volatile("" ::: "memory");
  u4v fb[27];
  const u32 poff = (u32)WS_P + (tt * 4 + wave) * 27 * 1024 + lane * 16;
#pragma unroll
  for (int k = 0; k < 3 * FPD; ++k) { fb[k] = __builtin_amdgcn_raw_buffer_load_b128(rs, poff + k * 1024, 0, AUX_SC1); SB(); }
  f4 acc3[5];
#pragma unroll
  for (int j = 0; j < 5; ++j) acc3[j] = f4{0, 0, 0, 0};
  u4v fa[2][15];
  u32 wo[3] = {(u32)lane * 16, (u32)lane * 16 + 3 * G2_STB - 8192, (u32)lane * 16 + 5 * G2_STB};
  PIN(wo[1]); PIN(wo[2]);
  auto rd3 = [&](int kt) {
#pragma unroll
    for (int ii = 0; ii < 15; ++ii) {
      const int i = kt * 15 + ii;
      const int off = ((32 + i / 22) % G2_NST) * G2_STB + (i % 22) * 1024;
      const int w = off < 3 * G2_STB - 8192 ? 0 : (off < 5 * G2_STB ? 1 : 2);
      fa[kt & 1][ii] = *(const u4v*)(smem + wo[w] + (off - (w == 0 ? 0 : (w == 1 ? 3 * G2_STB - 8192 : 5 * G2_STB))));
    }
  };
  auto mm3 = [&](int kt) {
#pragma unroll
    for (int kk = 0; kk < 3; ++kk)
#pragma unroll
      for (int j = 0; j < 5; ++j) acc3[j] = mfma_b(fa[kt & 1][kk * 5 + j], fb[kt * 3 + kk], acc3[j]);
  };
  BAR();
  rd3(0);
#pragma unroll
  for (int kt = 1; kt < 9; ++kt) {
    SB();
    if (kt - 1 + FPD < 9) {
#pragma unroll
      for (int k = 0; k < 3; ++k) fb[(kt - 1 + FPD) * 3 + k] = __builtin_amdgcn_raw_buffer_load_b128(rs, poff + ((kt - 1 + FPD) * 3 + k) * 1024, 0, AUX_SC1);
      SB();
    }
    rd3(kt);
    mm3(kt - 1);
#pragma unroll
    for (int i = 0; i < 15; ++i) { SGB(0x100, 1); SGB(0x8, 1); }
  }
  SB();
  mm3(8);
  {
    u2v o[5];
#pragma unroll
    for (int j = 0; j < 5; ++j) {
      u32 t0 = pk_bf(acc3[j][0], acc3[j][1]), t1 = pk_bf(acc3[j][2], acc3[j][3]);
      o[j] = u2v{pk_bf(bflo(t0) + bflo(hv[j][0]), bfhi(t0) + bfhi(hv[j][0])), pk_bf(bflo(t1) + bflo(hv[j][1]), bfhi(t1) + bfhi(hv[j][1]))};
    }
    const __amdgpu_buffer_rsrc_t ro = __builtin_amdgcn_make_buffer_rsrc((void*)out, 0, S * D * 2, 0x00020000);
    const u32 oo = (trow * D + ft * 80) * 2;
#pragma unroll
    for (int j = 0; j < 4; j += 2) {
      auto s0 = __builtin_amdgcn_permlane16_swap(o[j][0], o[j + 1][0], false, false);
      auto s1 = __builtin_amdgcn_permlane16_swap(o[j][1], o[j + 1][1], false, false);
      __builtin_amdgcn_raw_buffer_store_b128(u4v{s0[0], s1[0], s0[1], s1[1]}, ro, oo + (j + (g & 1)) * 32 + (g >> 1) * 16, 0, AUX_SC1);
    }
    __builtin_amdgcn_raw_buffer_store_b64(o[4], ro, oo + 128 + g * 8, 0, AUX_SC1);
  }
}

extern "C" __global__ void end_kernel() {}


extern "C" void kernel_launch(void* const* d_in, const int* in_sizes, int n_in,
                              void* d_out, int out_size, void* d_ws, size_t ws_size,
                              hipStream_t stream) {
  const u16* x=(const u16*)d_in[0]; const u16* wr=(const u16*)d_in[1]; const u16* wi=(const u16*)d_in[2];
  const u16* w_in=(const u16*)d_in[4]; const u16* w_sp=(const u16*)d_in[5]; const u16* b_sp=(const u16*)d_in[6]; const u16* w_out=(const u16*)d_in[7];
  char* ws=(char*)d_ws;
  k1_kernel<<<256,512,0,stream>>>(x,wr,wi,ws,w_sp,w_in,w_out,b_sp,(u16*)d_out);
  (void)hipFuncSetAttribute((const void*)k2_kernel, hipFuncAttributeMaxDynamicSharedMemorySize, 84 * 1024);
  k2_kernel<<<K2_FFT_BLOCKS+K2_PREP_BLOCKS,512,84 * 1024,stream>>>();
  k3_kernel<<<256,256,0,stream>>>();
  g1_kernel<<<256,512,0,stream>>>();
  g2_kernel<<<256,512,0,stream>>>();
}
```

```cpp
#include <hip/hip_runtime.h>
#include <hip/hip_bf16.h>
#include <stdint.h>
__device__ __attribute__((visibility("protected"))) unsigned long long g_t[16];
typedef unsigned short u16;
typedef unsigned int u32;
typedef _Float16 h2 __attribute__((ext_vector_type(2)));
typedef _Float16 h8 __attribute__((ext_vector_type(8)));
typedef __bf16 bf2 __attribute__((ext_vector_type(2)));
typedef __bf16 bf8 __attribute__((ext_vector_type(8)));
typedef float f2 __attribute__((ext_vector_type(2)));
typedef float f4 __attribute__((ext_vector_type(4)));
typedef short s4 __attribute__((ext_vector_type(4)));
typedef u32 u2v __attribute__((ext_vector_type(2)));
typedef u32 u4v __attribute__((ext_vector_type(4)));
#define DI __device__ __forceinline__
#define LDS_AS __attribute__((address_space(3)))
#define PIN(x) asm volatile("" : "+v"(x))
#define GLDS16(src, dst) __builtin_amdgcn_global_load_lds((const u32*)(src), (LDS_AS u32*)(dst), 16, 0, 0)
#define WAITV(n) asm volatile("s_waitcnt vmcnt(%0)" ::"n"(n) : "memory")

constexpr int S = 2048, D = 640, DF = 321, FF = 1728, FH = 864;
constexpr int TROW_UNITS = 648;
constexpr int NUNITS = 641;

constexpr size_t WS_T = 0, WS_WP = 2688u << 10, WS_TP = 5376u << 10, WS_WT1 = 8064u << 10, WS_WT3 = 10240u << 10, WS_H = 11392u << 10,
                 WS_L2 = 13952u << 10, WS_Z1 = 16512u << 10, WS_Z2 = 19968u << 10, WS_STATS = 23424u << 10, WS_W2 = 23680u << 10,
                 WS_WMAX = 23692u << 10, WS_UNS = 23696u << 10, WS_CNT = 23700u << 10, WS_P = WS_T;
#define PINS(x) asm volatile("" ::"s"(x))
#define GLDS4(src, dst) __builtin_amdgcn_global_load_lds((const u32*)(src), (LDS_AS u32*)(dst), 4, 0, 0)
#define touch(p) GLDS4(p, scratch)

constexpr int AUX_SC1 = 16;
constexpr u32 DROP = 0x80000000u;
DI __amdgpu_buffer_rsrc_t ws_rsrc(char* ws) { return __builtin_amdgcn_make_buffer_rsrc((void*)ws, 0, 64 << 20, 0x00020000); }
DI void st16(__amdgpu_buffer_rsrc_t rs, u32 off, u4v v) { __builtin_amdgcn_raw_buffer_store_b128(v, rs, off, 0, AUX_SC1); }
DI void st8(__amdgpu_buffer_rsrc_t rs, u32 off, u2v v) { __builtin_amdgcn_raw_buffer_store_b64(v, rs, off, 0, AUX_SC1); }
#define CODE_ADDR(var, sym)                                                                                     \
  const char* var;                                                                                              \
  {                                                                                                             \
    u32 lo_, hi_;                                                                                               \
    asm volatile("s_getpc_b64 s[40:41]\n\ts_add_u32 %0, s40, " #sym "@rel32@lo+4\n\ts_addc_u32 %1, s41, " #sym "@rel32@hi+12" \
                 : "=s"(lo_), "=s"(hi_)::"s40", "s41", "scc");                                                  \
    var = (const char*)(((unsigned long long)hi_ << 32) | lo_);                                                 \
  }
#define CODE_TOUCH(a, b, c)                                                           \
  {                                                                                   \
    CODE_ADDR(pa_, a) CODE_ADDR(pb_, b)                                               \
    const char* p_ = pa_ + ((c) * 64 + lane) * 128;                                   \
    if (p_ < pb_) touch(p_);                                                          \
  }
struct Args { char* ws; const u16* x; const u16* w_sp; const u16* w_in; const u16* w_out; const u16* b_sp; u16* out; void* pad; };
static __device__ __attribute__((aligned(128))) Args g_args;
typedef const __attribute__((address_space(4))) Args* ArgsPtr;
#define LOAD_ARGS() ArgsPtr ap_ = (ArgsPtr)(unsigned long long)&g_args
DI float bflo(u32 v) { return __uint_as_float(v << 16); }
DI float bfhi(u32 v) { return __uint_as_float(v & 0xffff0000u); }
DI u32 pk_bf(float a, float b) { f2 v = {a, b}; return __builtin_bit_cast(u32, __builtin_convertvector(v, bf2)); }
DI u32 pk_h(float a, float b) { f2 v = {a, b}; return __builtin_bit_cast(u32, __builtin_convertvector(v, h2)); }
DI float rbf(float a) { return bflo(pk_bf(a, a)); }
DI float cosr(float rev) { return __builtin_amdgcn_cosf(rev); }
DI float sinr(float rev) { return __builtin_amdgcn_sinf(rev); }
DI f4 mfma_h(u4v a, u4v b, f4 c) {
  return __builtin_amdgcn_mfma_f32_16x16x32_f16(__builtin_bit_cast(h8, a), __builtin_bit_cast(h8, b), c, 0, 0, 0);
}
template <int CTRL> DI float dpp_add(float v) {
  int t = __builtin_amdgcn_update_dpp(0, __float_as_int(v), CTRL, 0xf, 0xf, true);
  return v + __int_as_float(t);
}
DI float xsum_g(float v) {
  u32 b = __float_as_uint(v);
  auto r = __builtin_amdgcn_permlane16_swap(b, b, false, false);
  v = __uint_as_float(r[0]) + __uint_as_float(r[1]);
  b = __float_as_uint(v);
  auto s = __builtin_amdgcn_permlane32_swap(b, b, false, false);
  return __uint_as_float(s[0]) + __uint_as_float(s[1]);
}
template <int CTRL> DI u32 dpp_maxu(u32 v) {
  u32 t = (u32)__builtin_amdgcn_update_dpp(0, (int)v, CTRL, 0xf, 0xf, true);
  return v > t ? v : t;
}
DI u32 max64u(u32 v) {
  v = dpp_maxu<0xB1>(v); v = dpp_maxu<0x4E>(v); v = dpp_maxu<0x141>(v); v = dpp_maxu<0x140>(v);
  auto r = __builtin_amdgcn_permlane16_swap(v, v, false, false);
  v = r[0] > r[1] ? r[0] : r[1];
  auto s = __builtin_amdgcn_permlane32_swap(v, v, false, false);
  return s[0] > s[1] ? s[0] : s[1];
}
DI float sum32(float v) {
  v = dpp_add<0xB1>(v); v = dpp_add<0x4E>(v); v = dpp_add<0x141>(v); v = dpp_add<0x140>(v);
  u32 b = __float_as_uint(v);
  auto r = __builtin_amdgcn_permlane16_swap(b, b, false, false);
  return __uint_as_float(r[0]) + __uint_as_float(r[1]);
}
DI void ln_row(f2 (&v)[10]) {
  f2 s = ((v[0] + v[1]) + (v[2] + v[3])) + ((v[4] + v[5]) + (v[6] + v[7])) + (v[8] + v[9]);
  const float mean = sum32(s[0] + s[1]) * (1.0f / 640.0f);
  f2 q0 = f2{0.f, 0.f}, q1 = f2{0.f, 0.f};
#pragma unroll
  for (int i = 0; i < 10; i += 2) { v[i] -= mean; v[i + 1] -= mean; q0 += v[i] * v[i]; q1 += v[i + 1] * v[i + 1]; }
  q0 += q1;
  const float var = sum32(q0[0] + q0[1]) * (1.0f / 640.0f);
  const float rinv = __builtin_amdgcn_rcpf(rbf(__builtin_amdgcn_sqrtf(rbf(rbf(var) + 1e-5f))));
  const float off = (mean - rbf(mean)) * rinv;
#pragma unroll
  for (int i = 0; i < 10; ++i) v[i] = v[i] * rinv + off;
}
#define SB() __builtin_amdgcn_sched_barrier(0)
template <bool SWAP> DI u4v cmul_pack(f4 re, f4 im, f4 wr, f4 wi) {
  const f4 ore = re * wr - im * wi, oim = re * wi + im * wr;
  u4v v;
#pragma unroll
  for (int r = 0; r < 4; ++r) v[r] = SWAP ? pk_h(oim[r], ore[r]) : pk_h(ore[r], oim[r]);
  return v;
}

struct __attribute__((packed, aligned(2))) U4U { u32 a, b, c, d; };

DI void lds_signal(char* p) {
  asm volatile("s_waitcnt lgkmcnt(0)" ::: "memory");
  if ((threadIdx.x & 63) == 0) __hip_atomic_fetch_add((LDS_AS u32*)p, 1u, __ATOMIC_RELAXED, __HIP_MEMORY_SCOPE_WORKGROUP);
}
DI void lds_wait(char* p, u32 n) {
  while (__builtin_amdgcn_readfirstlane(__hip_atomic_load((LDS_AS u32*)p, __ATOMIC_RELAXED, __HIP_MEMORY_SCOPE_WORKGROUP)) < n) {}
  asm volatile("" ::: "memory");
}
constexpr int K1_F40 = 10272, K1_TW = K1_F40 + 18432, K1_R16 = K1_TW + 6144, K1_LDS = K1_R16 + 4096 + 256 + 16;
extern "C" __global__ __launch_bounds__(512) void k1_kernel(const u16* __restrict__ x, const u16* __restrict__ wr,
                                                            const u16* __restrict__ wi, char* __restrict__ ws,
                                                            const u16* __restrict__ w_sp, const u16* w_in, const u16* w_out,
                                                            const u16* b_sp, u16* out) {
  __shared__ __attribute__((aligned(16))) char smem[K1_LDS];
  const int tid = threadIdx.x, lane = tid & 63;
  const int wave = __builtin_amdgcn_readfirstlane(tid >> 6);
  const int li = lane & 15, g = lane >> 4;
  if (blockIdx.x < 8 && threadIdx.x == 0) g_t[blockIdx.x] = __builtin_amdgcn_s_memrealtime();
  char* scratch = smem + K1_R16 + 4096;
  char* flags = scratch + 256;
  if (wave == 7) CODE_TOUCH(k1_kernel, end_kernel, (blockIdx.x >> 3) & 7)
  PINS(x); PINS(wr); PINS(wi); PINS(ws); PINS(w_sp);
  auto constants = [&]() {
#pragma unroll
    for (int it = 0; it < 2; ++it) {
      const int item = wave + 8 * it;
      if (it == 0 || wave == 0) {
        const int to = item / 3, tk = item - 3 * to;
        const int kb = 16 * to + li;
        const float s8 = (tk < 2 || g < 2) ? 0.125f : 0.f;
        u4v fre, fim;
#pragma unroll
        for (int jj = 0; jj < 4; ++jj) {
          const float ang = (float)(kb * (16 * tk + 4 * g + jj)) * (1.0f / 40.0f);
          const float c = cosr(ang) * s8, s = sinr(ang) * s8;
          fre[jj] = pk_h(c, s);
          fim[jj] = pk_h(-s, c);
        }
        *(u4v*)(smem + K1_F40 + item * 2048 + lane * 16) = fre;
        *(u4v*)(smem + K1_F40 + item * 2048 + 1024 + lane * 16) = fim;
      } else if (wave < 4) {
        const int t = wave - 1;
        f4 twr, twi;
#pragma unroll
        for (int r = 0; r < 4; ++r) {
          const float ang = (float)((16 * t + 4 * g + r) * li) * (1.0f / 640.0f);
          twr[r] = cosr(ang) * 0.125f; twi[r] = -sinr(ang) * 0.125f;
        }
        *(f4*)(smem + K1_TW + t * 2048 + lane * 16) = twr;
        *(f4*)(smem + K1_TW + t * 2048 + 1024 + lane * 16) = twi;
      } else {
        const int ph = 2 * (wave - 4) + (lane >> 5);
        float cs[8];
#pragma unroll
        for (int jj = 0; jj < 8; ++jj) cs[jj] = cosr((float)(((8 * (g & 1) + jj) * li + 2 * ph) & 15) * 0.0625f);
        *(u4v*)(smem + K1_R16 + (wave - 4) * 1024 + lane * 16) = u4v{pk_h(cs[0], cs[1]), pk_h(cs[2], cs[3]), pk_h(cs[4], cs[5]), pk_h(cs[6], cs[7])};
      }
    }
    if (tid < 4) { *(u32*)(smem + 10240 + tid * 4) = 0u; *(u32*)(flags + tid * 4) = 0u; }
    asm volatile("s_waitcnt lgkmcnt(0)" ::: "memory");
    __builtin_amdgcn_s_barrier();
    asm volatile("" ::: "memory");
  };
  if (wave >= 4) {
    const int slotid = (blockIdx.x >> 3) * 3 + (wave - 4);
    const bool act = wave < 7 && slotid < 84;
    const int cp = blockIdx.x & 7, cc = act ? slotid >> 2 : 0, qt = slotid & 3;
    const int c0 = cc < 20 ? cc * 16 : 305;
    const bool mir = cp > 4;
    const bool sp = (cp & 3) == 0;
    const int e = mir ? 255 - (qt * 64 + lane) : qt * 64 + lane;
    const size_t roff = (size_t)(cp + 8 * e) * DF + c0;
    const U4U* pr = (const U4U*)(wr + roff);
    const U4U* pi = (const U4U*)(wi + roff);
    U4U r0, r1, i0, i1;
    __builtin_amdgcn_s_sleep(3);
    if (act) { r0 = pr[0]; r1 = pr[1]; i0 = pi[0]; i1 = pi[1]; }
    SB();
    constants();
    if (!act) {
      if (blockIdx.x == 255 && tid == 256) g_args = Args{ws, x, w_sp, w_in, w_out, b_sp, out, nullptr};
      if (blockIdx.x >= 224 && tid == 448) ((u32*)(ws + WS_WMAX))[blockIdx.x] = 0u;
      WAITV(0);
      return;
    }
    const u32 flip = mir ? 0x80000000u : 0u;
    const int q1 = (mir ? 8 - cp : cp) - 1;
    const int kb = mir ? 39 - cc : cc;
    const int t1 = sp ? 1920 + 32 * cc + (cp >> 2) : (q1 * 160 + (kb >> 2) * 16 + (mir ? 16 : 0)) * 4 + (kb & 3);
    const int step = sp ? 2 : (mir ? -4 : 4);
    const int t0 = mir ? (q1 * 160 + ((kb + 1) >> 2) * 16) * 4 + ((kb + 1) & 3) : t1;
    const bool edge = (cc == 0 || cc == 20);
    const __amdgpu_buffer_rsrc_t rs = ws_rsrc(ws);
    const u32 slot = (u32)WS_WP + (qt * 16 + li) * 16;
    u32 doff[4];
#pragma unroll
    for (int m = 0; m < 4; ++m) {
      const int jc = 4 * m + g;
      doff[m] = slot + (u32)(t1 + step * jc) * 1024u;
      if (cc == 20) doff[m] = DROP;
    }
    if (g == 0 && cc < 20) doff[0] = (mir && edge) ? DROP : slot + (u32)t0 * 1024u;
    if (g == 3 && cc == 20 && !mir) doff[3] = slot + (u32)t0 * 1024u;
    u32 rr[8] = {r0.a, r0.b, r0.c, r0.d, r1.a, r1.b, r1.c, r1.d};
    u32 ii[8] = {i0.a, i0.b, i0.c, i0.d, i1.a, i1.b, i1.c, i1.d};
    u32 vals[16];
#pragma unroll
    for (int j = 0; j < 16; ++j) {
      u32 a = rr[j >> 1], b = ii[j >> 1];
      vals[j] = ((j & 1) ? __builtin_amdgcn_perm(b, a, 0x07060302u) : __builtin_amdgcn_perm(b, a, 0x05040100u)) ^ flip;
    }
    if (edge && (mir || sp)) {
      const u32 v0 = cc < 20 ? vals[0] : vals[15];
      const int e2 = (cp == 0) ? ((256 - e) & 255) : 255 - e;
      const int dpos2 = ((e2 & 15) + 16 * (e2 >> 6)) * 4 + ((e2 >> 4) & 3);
      const int cls = mir ? 8 - cp : cp;
      ((u32*)(ws + WS_W2))[(cls * 2 + (cc == 20)) * 256 + dpos2] = v0 ^ flip ^ 0x80000000u;
    }
    u32 mx = 0;
#pragma unroll
    for (int j = 0; j < 16; ++j) {
      const u32 a = vals[j] & 0x7fff7fffu;
      mx = max(mx, max(a & 0xffffu, a >> 16));
    }
#pragma unroll
    for (int m = 0; m < 4; ++m) {
      auto p02 = __builtin_amdgcn_permlane32_swap(vals[4 * m], vals[4 * m + 2], false, false);
      auto p13 = __builtin_amdgcn_permlane32_swap(vals[4 * m + 1], vals[4 * m + 3], false, false);
      auto q01 = __builtin_amdgcn_permlane16_swap(p02[0], p13[0], false, false);
      auto q23 = __builtin_amdgcn_permlane16_swap(p02[1], p13[1], false, false);
      st16(rs, doff[m], u4v{q01[0], q01[1], q23[0], q23[1]});
    }
    mx = max64u(mx);
    if (lane == 0) {
      __hip_atomic_fetch_max((LDS_AS u32*)(flags + 4), mx, __ATOMIC_RELAXED, __HIP_MEMORY_SCOPE_WORKGROUP);
      asm volatile("s_waitcnt lgkmcnt(0)" ::: "memory");
      if (__hip_atomic_fetch_add((LDS_AS u32*)(flags + 8), 1u, __ATOMIC_RELAXED, __HIP_MEMORY_SCOPE_WORKGROUP) == 2u)
        ((u32*)(ws + WS_WMAX))[blockIdx.x] = __hip_atomic_load((LDS_AS u32*)(flags + 4), __ATOMIC_RELAXED, __HIP_MEMORY_SCOPE_WORKGROUP);
    }
    return;
  }
  const int b = (blockIdx.x & 7) * 32 + (blockIdx.x >> 3);
  const int a = 2 * wave + (lane >> 5), j = lane & 31;
  const u16* xr = x + (size_t)(256 * a + b) * D;
  const u4v x0 = *(const u4v*)(xr + 8 * j);
  const u4v x1 = *(const u4v*)(xr + 256 + 8 * j);
  const u2v x2 = *(const u2v*)(xr + 512 + 4 * j);
  SB();
  constants();
  const int q = wave;
  u4v bre[4], bim[4], fre[9], fim[9];
  f4 twr[3], twi[3];
  u32 toff[3][4];
  {
    const int a0 = g >> 1;
#pragma unroll
    for (int ks = 0; ks < 4; ++ks) {
      const int ph = (q == 0) ? (a0 ? 1 : 7) : (2 * ks + a0) * q;
      bre[ks] = *(const u4v*)(smem + K1_R16 + (ph & 7) * 512 + (lane & 31) * 16);
      bim[ks] = *(const u4v*)(smem + K1_R16 + ((ph + 2) & 7) * 512 + (lane & 31) * 16);
    }
#pragma unroll
    for (int i = 0; i < 9; ++i) {
      fre[i] = *(const u4v*)(smem + K1_F40 + i * 2048 + lane * 16);
      fim[i] = *(const u4v*)(smem + K1_F40 + i * 2048 + 1024 + lane * 16);
    }
    const float qs = q == 0 ? 0.70710678118f : 1.f;
#pragma unroll
    for (int t = 0; t < 3; ++t) {
      twr[t] = *(const f4*)(smem + K1_TW + t * 2048 + lane * 16) * qs;
      twi[t] = *(const f4*)(smem + K1_TW + t * 2048 + 1024 + lane * 16) * qs;
      PIN(twr[t]); PIN(twi[t]);
#pragma unroll
      for (int r = 0; r < 4; ++r) {
        const int k2 = li + 16 * (16 * t + 4 * g + r);
        const int pos = k2 <= 320 ? 2 * k2 : 2 * (640 - k2) + 1;
        toff[t][r] = (u32)WS_T + (u32)b * (TROW_UNITS * 16) + (q > 0 ? ((q - 1) * 160 + 64 * t + lane) * 16 : 480 * 16 + pos * 4);
        if (t == 2 && lane >= 32) toff[t][r] = DROP;
        PIN(toff[t][r]);
      }
    }
#pragma unroll
    for (int ks = 0; ks < 4; ++ks) { PIN(bre[ks]); PIN(bim[ks]); }
#pragma unroll
    for (int i = 0; i < 9; ++i) { PIN(fre[i]); PIN(fim[i]); }
  }
  {
    f2 v[10];
#pragma unroll
    for (int i = 0; i < 4; ++i) { v[i] = f2{bflo(x0[i]), bfhi(x0[i])}; v[4 + i] = f2{bflo(x1[i]), bfhi(x1[i])}; }
    v[8] = f2{bflo(x2[0]), bfhi(x2[0])}; v[9] = f2{bflo(x2[1]), bfhi(x2[1])};
    ln_row(v);
    char* ub = smem + a * 1280;
    *(u4v*)(ub + 16 * j) = u4v{pk_h(v[0][0], v[0][1]), pk_h(v[1][0], v[1][1]), pk_h(v[2][0], v[2][1]), pk_h(v[3][0], v[3][1])};
    *(u4v*)(ub + 512 + 16 * j) = u4v{pk_h(v[4][0], v[4][1]), pk_h(v[5][0], v[5][1]), pk_h(v[6][0], v[6][1]), pk_h(v[7][0], v[7][1])};
    *(u2v*)(ub + 1024 + 8 * j) = u2v{pk_h(v[8][0], v[8][1]), pk_h(v[9][0], v[9][1])};
  }
  lds_signal(flags);
  lds_wait(flags, 4);
  u4v b2[3];
  {
    const int qq = li >> 2, p = li & 3;
    const char* abase = smem + (8 * g + qq) * 80 + 8 * p;
    u4v af[3][4];
#pragma unroll
    for (int t = 0; t < 3; ++t)
#pragma unroll
      for (int ks = 0; ks < 4; ++ks) {
        s4 lo = __builtin_amdgcn_ds_read_tr16_b64_v4i16((LDS_AS s4*)(abase + ks * 2560 + t * 32));
        s4 hi = __builtin_amdgcn_ds_read_tr16_b64_v4i16((LDS_AS s4*)(abase + ks * 2560 + 320 + t * 32));
        u2v l2 = __builtin_bit_cast(u2v, lo), h2v = __builtin_bit_cast(u2v, hi);
        af[t][ks] = u4v{l2[0], l2[1], h2v[0], h2v[1]};
      }
    SB();
    f4 are[3], aim[3];
#pragma unroll
    for (int t = 0; t < 3; ++t) {
      are[t] = f4{0, 0, 0, 0}; aim[t] = f4{0, 0, 0, 0};
#pragma unroll
      for (int ks = 0; ks < 4; ++ks) {
        are[t] = mfma_h(af[t][ks], bre[ks], are[t]);
        aim[t] = mfma_h(af[t][ks], bim[ks], aim[t]);
      }
    }
#pragma unroll
    for (int t = 0; t < 3; ++t) b2[t] = cmul_pack<false>(are[t], aim[t], twr[t], twi[t]);
  }
  const __amdgpu_buffer_rsrc_t rs = ws_rsrc(ws);
  f4 ore[3], oim[3];
#pragma unroll
  for (int tk = 0; tk < 3; ++tk)
#pragma unroll
    for (int to = 0; to < 3; ++to) {
      ore[to] = mfma_h(fre[to * 3 + tk], b2[tk], tk ? ore[to] : f4{0, 0, 0, 0});
      oim[to] = mfma_h(fim[to * 3 + tk], b2[tk], tk ? oim[to] : f4{0, 0, 0, 0});
    }
  u4v o[3];
#pragma unroll
  for (int to = 0; to < 3; ++to)
#pragma unroll
    for (int r = 0; r < 4; ++r) o[to][r] = pk_h(ore[to][r], oim[to][r]);
  if (q > 0) {
#pragma unroll
    for (int to = 0; to < 3; ++to) st16(rs, toff[to][0], o[to]);
  } else {
#pragma unroll
    for (int to = 0; to < 3; ++to)
#pragma unroll
      for (int r = 0; r < 4; ++r) __builtin_amdgcn_raw_buffer_store_b32(o[to][r], rs, toff[to][r], 0, 0);
    __builtin_amdgcn_raw_buffer_store_b32(lane ? o[1][0] : o[0][0], rs, lane == 0 ? toff[0][0] + 4 : (lane == 16 ? toff[1][0] + 4 : DROP), 0, 0);
  }
}

template <int NT, int LDW, int NCG, int NTASK, bool PERM>
DI void retile(const u16* __restrict__ W, __amdgpu_buffer_rsrc_t rs, u32 dstbase, int wid, int nw, char* lds, int lane) {
  const int li = lane & 15, g = lane >> 4, q = li >> 2, p = li & 3;
  u32 dst[NT];
#pragma unroll
  for (int i = 0; i < NT; ++i) {
    const int task = wid + i * nw;
    const int t = task < NTASK ? task : 0;
    const int ks = t / NCG, cg = t - ks * NCG;
    const u16* src = W + (size_t)(ks * 32 + (lane >> 3)) * LDW + cg * 64 + (lane & 7) * 8;
#pragma unroll
    for (int j = 0; j < 4; ++j) GLDS16(src + (size_t)j * 8 * LDW, lds + i * 4096 + j * 1024);
    dst[i] = task < NTASK ? dstbase + (u32)t * 4096u + lane * 16 : DROP;
  }
  const char* rb = lds + (PERM ? 4 * g + q : 8 * g + q) * 128 + 8 * p;
  WAITV(0);
#pragma unroll
  for (int i = 0; i < NT; ++i)
#pragma unroll
    for (int f = 0; f < 4; ++f) {
      s4 lo = __builtin_amdgcn_ds_read_tr16_b64_v4i16((LDS_AS s4*)(rb + i * 4096 + f * 32));
      s4 hi = __builtin_amdgcn_ds_read_tr16_b64_v4i16((LDS_AS s4*)(rb + i * 4096 + f * 32 + (PERM ? 16 : 4) * 128));
      u2v a = __builtin_bit_cast(u2v, lo), b = __builtin_bit_cast(u2v, hi);
      st16(rs, dst[i] + f * 1024, u4v{a[0], a[1], b[0], b[1]});
    }
}
constexpr int NRT1 = 20 * 27, NRT3 = 27 * 10;
constexpr int K2_FFT_BLOCKS = 168, K2_PREP_BLOCKS = 88;

extern "C" __global__ __launch_bounds__(512, 2) void k2_kernel() {
  LOAD_ARGS();
  char* ws = ap_->ws; const u16* w_in = ap_->w_in; const u16* w_out = ap_->w_out; const u16* w_sp = ap_->w_sp;
  PINS(ws); PINS(w_in); PINS(w_out); PINS(w_sp);
  const u32* T = (const u32*)(ws + WS_T); const u32* Wp = (const u32*)(ws + WS_WP); const u32* W2 = (const u32*)(ws + WS_W2);
  const u32* wmax = (const u32*)(ws + WS_WMAX); u32* Tp = (u32*)(ws + WS_TP); float* unscale = (float*)(ws + WS_UNS);
  u32* Wt1 = (u32*)(ws + WS_WT1); u32* Wt3 = (u32*)(ws + WS_WT3); u32* stats = (u32*)(ws + WS_STATS);
  extern __shared__ __attribute__((aligned(16))) char smem[];
  const int tid = threadIdx.x, lane = tid & 63;
  const int wave = __builtin_amdgcn_readfirstlane(tid >> 6);
  const int li = lane & 15, g = lane >> 4;
  if (blockIdx.x >= K2_FFT_BLOCKS) {
    const int wid = (blockIdx.x - K2_FFT_BLOCKS) * 8 + wave;
    const __amdgpu_buffer_rsrc_t rs = ws_rsrc(ws);
    if (wid < 256) st16(rs, (u32)WS_STATS + wid * 1024 + lane * 16, u4v{~0u, ~0u, ~0u, ~0u});
    else if (wid < 264) st16(rs, (u32)WS_CNT + (wid - 256) * 1024 + lane * 16, u4v{0u, 0u, 0u, 0u});
    if (wid < NRT1) retile<1, FF, 27, NRT1, false>(w_in, rs, (u32)WS_WT1, wid, 0, smem + wave * 8192, lane);
    else retile<2, D, 10, NRT3, true>(w_out, rs, (u32)WS_WT3, wid - NRT1, 164, smem + wave * 8192, lane);
    return;
  }
  const int P = (blockIdx.x & 7) * 21 + (blockIdx.x >> 3);
  if (P > 160) return;
  const int u = wave & 3, hh = wave >> 2;
  const int U = 4 * P + u;
#pragma unroll
  for (int i = 0; i < 2; ++i) {
    const int bb = (wave * 2 + i) * 16 + (lane >> 2);
    GLDS16(T + ((size_t)bb * TROW_UNITS + 4 * P + ((lane & 3) ^ ((lane >> 4) & 3))) * 4, smem + (wave * 2 + i) * 1024);
  }
  const u4v mxv = *(const u4v*)(wmax + lane * 4);
  u4v Wv[2];
#pragma unroll
  for (int t = 0; t < 2; ++t) Wv[t] = *(const u4v*)(Wp + ((size_t)((U < NUNITS ? U : 0) * 4 + 2 * hh + t) * 64 + lane) * 4);
  const bool special = P >= 120;
  const int cB = special ? 4 : (P / 40 + 1);
  const int ur = special ? U - 480 : U % 160;
  const bool e320 = special ? ur == 160 : ur == 80;
  const bool edge = hh == 0 && (ur == 0 || e320);
  u4v w2, w4;
  if (edge) {
    w2 = *(const u4v*)(W2 + ((special ? 0 : cB) * 2 + (e320 ? 1 : 0)) * 256 + lane * 4);
    w4 = *(const u4v*)(W2 + (4 * 2 + (e320 ? 1 : 0)) * 256 + lane * 4);
  }
  {
    char* cb = smem + 16384 + wave * 2048 + lane * 16;
    const int cc = wave < 4 ? cB : 0;
    const float inv = u < 2 ? (1.0f / 128.0f) : (1.0f / 2048.0f);
    const float amp = u == 0 ? 1.f : ((u == 1 && special) ? 0.125f : 0.25f);
    f4 c, s;
#pragma unroll
    for (int jj = 0; jj < 4; ++jj) {
      const int a = (u & 1) ? li * (cc + 8 * (4 * g + jj)) : (4 * g + jj) * (cc + 8 * li);
      const float ang = (float)a * inv;
      c[jj] = cosr(ang) * amp; s[jj] = sinr(ang) * amp;
    }
    if (u < 2) {
      *(u4v*)(cb) = u4v{pk_h(c[0], s[0]), pk_h(c[1], s[1]), pk_h(c[2], s[2]), pk_h(c[3], s[3])};
      *(u4v*)(cb + 1024) = u4v{pk_h(-s[0], c[0]), pk_h(-s[1], c[1]), pk_h(-s[2], c[2]), pk_h(-s[3], c[3])};
    } else {
      *(f4*)(cb) = c;
      *(f4*)(cb + 1024) = s;
    }
  }
  asm volatile("s_waitcnt lgkmcnt(0)" ::: "memory");
  __builtin_amdgcn_s_barrier();
  asm volatile("" ::: "memory");
  u4v Fre, Fim, P1re[2], P1im[2], P4re[2], P4im[2];
  f4 tfr[2], tfs[2], tir[2], tis[2];
  {
    const char* cb = smem + 16384 + lane * 16;
    Fre = *(const u4v*)(cb + 4 * 2048); Fim = *(const u4v*)(cb + 4 * 2048 + 1024);
#pragma unroll
    for (int c = 0; c < 2; ++c) {
      const char* cs = cb + ((c == 0 && special) ? 8192 : 0);
      P1re[c] = *(const u4v*)(cs); P1im[c] = *(const u4v*)(cs + 1024);
      P4re[c] = *(const u4v*)(cs + 2048); P4im[c] = *(const u4v*)(cs + 3072);
      tfr[c] = *(const f4*)(cs + 4096); tfs[c] = *(const f4*)(cs + 5120);
      tir[c] = *(const f4*)(cs + 6144); tis[c] = *(const f4*)(cs + 7168);
    }
    PIN(Fre); PIN(Fim);
#pragma unroll
    for (int c = 0; c < 2; ++c) { PIN(P1re[c]); PIN(P1im[c]); PIN(P4re[c]); PIN(P4im[c]); PIN(tfr[c]); PIN(tfs[c]); PIN(tir[c]); PIN(tis[c]); }
  }
  asm volatile("s_waitcnt vmcnt(0)" ::: "memory");
  __builtin_amdgcn_s_barrier();
  asm volatile("" ::: "memory");
  const int loff = (64 * g + li) * 64 + ((u ^ (li >> 2)) & 3) * 16 + hh * 8;
  u4v X[2];
#pragma unroll
  for (int jj = 0; jj < 4; ++jj) {
    const u2v L = *(const u2v*)(smem + loff + jj * 1024);
    X[0][jj] = L[0]; X[1][jj] = L[1];
  }
  if (special) {
#pragma unroll
    for (int jj = 0; jj < 4; ++jj) {
      const u32 au = X[0][jj], bu = X[1][jj] ^ 0x80000000u;
      const h2 A = __builtin_bit_cast(h2, au);
      const h2 Bc = __builtin_bit_cast(h2, bu);
      X[0][jj] = __builtin_bit_cast(u32, A + Bc);
      X[1][jj] = __builtin_bit_cast(u32, A - Bc);
    }
  }
  const f4 z4 = f4{0, 0, 0, 0};
  f4 re[2], im[2];
#pragma unroll
  for (int t = 0; t < 2; ++t) { re[t] = mfma_h(X[t], P1re[t], z4); im[t] = mfma_h(X[t], P1im[t], z4); }
  const u32 mx = max64u(max(max(mxv[0], mxv[1]), max(mxv[2], mxv[3])));
  int E = (int)(mx >> 7);
  E = E < 1 ? 1 : (E > 253 ? 253 : E);
  const float sig = __uint_as_float((u32)(254 - E) << 23) * 0.25f;
#pragma unroll
  for (int t = 0; t < 2; ++t) {
    const u4v v = cmul_pack<false>(re[t], im[t], tfr[t], -tfs[t]);
    re[t] = mfma_h(Fre, v, z4); im[t] = mfma_h(Fim, v, z4);
  }
  f4 wr4[2], wi4[2];
#pragma unroll
  for (int t = 0; t < 2; ++t)
#pragma unroll
    for (int r = 0; r < 4; ++r) { wr4[t][r] = bflo(Wv[t][r]); wi4[t][r] = bfhi(Wv[t][r]); }
  if (edge) {
#pragma unroll
    for (int r = 0; r < 4; ++r) {
      wr4[0][r] = (wr4[0][r] + bflo(w2[r])) * 0.5f; wi4[0][r] = (wi4[0][r] + bfhi(w2[r])) * 0.5f;
      if (special) { wr4[1][r] = (wr4[1][r] + bflo(w4[r])) * 0.5f; wi4[1][r] = (wi4[1][r] + bfhi(w4[r])) * 0.5f; }
    }
    if (U == 0 && lane == 0) unscale[0] = __uint_as_float((u32)E << 23);
  }
#pragma unroll
  for (int t = 0; t < 2; ++t) {
    const u4v v = cmul_pack<true>(re[t], im[t], wr4[t] * sig, wi4[t] * sig);
    im[t] = mfma_h(v, Fre, z4); re[t] = mfma_h(v, Fim, z4);
  }
#pragma unroll
  for (int t = 0; t < 2; ++t) {
    const u4v v = cmul_pack<true>(re[t], im[t], tir[t], tis[t]);
    im[t] = mfma_h(P4re[t], v, z4); re[t] = mfma_h(P4im[t], v, z4);
  }
  if (special) {
    const f4 yr = re[0], yi = im[0], zr = re[1], zi = im[1];
    re[0] = yr + zr; im[0] = yi + zi;
    re[1] = yr - zr; im[1] = zi - yi;
  }
#pragma unroll
  for (int r = 0; r < 4; ++r) *(u2v*)(smem + loff + r * 1024) = u2v{pk_h(re[0][r], im[0][r]), pk_h(re[1][r], im[1][r])};
  asm volatile("s_waitcnt lgkmcnt(0)" ::: "memory");
  __builtin_amdgcn_s_barrier();
  asm volatile("" ::: "memory");
  const __amdgpu_buffer_rsrc_t rs = ws_rsrc(ws);
#pragma unroll
  for (int i = 0; i < 2; ++i) {
    const u4v o = *(const u4v*)(smem + (wave * 2 + i) * 1024 + lane * 16);
    st16(rs, (u32)WS_TP + P * 16384 + (wave * 2 + i) * 1024 + (lane & ~3) * 16 + ((lane & 3) ^ ((lane >> 4) & 3)) * 16, o);
  }
  { char* scratch = smem + 49152; if (wave == 0) CODE_TOUCH(k3_kernel, g1_kernel, 0) }
  WAITV(0);
}

extern "C" __global__ __launch_bounds__(256, 2) void k3_kernel() {
  LOAD_ARGS();
  char* ws = ap_->ws; const u16* x = ap_->x; const u16* w_sp = ap_->w_sp;
  PINS(x); PINS(ws); PINS(w_sp);
  const u32* Tp = (const u32*)(ws + WS_TP);
  constexpr int K3_Y = 10752, K3_CB = K3_Y + 10240;
  __shared__ __attribute__((aligned(16))) char smem[K3_CB + 8192 + 512];
  const int tid = threadIdx.x, lane = tid & 63;
  const int wave = __builtin_amdgcn_readfirstlane(tid >> 6);
  const int li = lane & 15, g = lane >> 4;
  const int b = (blockIdx.x & 7) * 32 + (blockIdx.x >> 3);
#pragma unroll
  for (int i = 0; i < 2; ++i) {
    int U = (wave * 2 + i) * 64 + lane;
    U = U < 480 ? U : 479;
    GLDS16(Tp + (size_t)(U >> 2) * 4096 + b * 16 + (U & 3) * 4, smem + (wave * 2 + i) * 1024);
  }
#pragma unroll
  for (int it = 0; it < 4; ++it) {
    const int i = it < 2 ? wave * 2 + it : 6 + it;
    if (it < 2 || wave == 3) {
      const int k2 = (lane >> 2) + 16 * (4 * i + (lane & 3));
      const int pos = k2 <= 320 ? 2 * k2 : 2 * (640 - k2) + 1;
      GLDS4(Tp + (size_t)(120 + (pos >> 4)) * 4096 + b * 16 + (pos & 15), smem + 8192 + i * 256);
    }
  }
  char* scratch = smem + K3_CB + 8192;
  if (wave == 0) GLDS4(ws + WS_UNS, scratch + 256);
  const int a = 2 * wave + (lane >> 5), j = lane & 31;
  const int row = 256 * a + b;
  const u16* xr = x + (size_t)row * D;
  u4v x0 = *(const u4v*)(xr + 8 * j);
  u4v x1 = *(const u4v*)(xr + 256 + 8 * j);
  u2v x2 = *(const u2v*)(xr + 512 + 4 * j);
  char* cb = smem + K3_CB;
  const int t = wave;
  u4v Bre[3], Bim[3];
  if (wave < 3) {
#pragma unroll
    for (int tk = 0; tk < 3; ++tk) {
      const float sc = (tk < 2 || g < 2) ? 1.f : 0.f;
#pragma unroll
      for (int jj = 0; jj < 4; ++jj) {
        const float ang = (float)((16 * tk + 4 * g + jj) * (16 * t + li)) * (1.0f / 40.0f);
        const float c = cosr(ang) * sc, s = sinr(ang) * sc;
        Bre[tk][jj] = pk_h(c, -s);
        Bim[tk][jj] = pk_h(s, c);
      }
      PIN(Bre[tk]); PIN(Bim[tk]);
    }
  } else {
#pragma unroll
    for (int tt = 0; tt < 3; ++tt) {
      f4 twr, twi;
#pragma unroll
      for (int jj = 0; jj < 4; ++jj) {
        const float ang = (float)((16 * tt + li) * (4 * g + jj)) * (1.0f / 640.0f);
        twr[jj] = cosr(ang) * 0.125f; twi[jj] = sinr(ang) * 0.125f;
      }
      *(f4*)(cb + tt * 2048 + lane * 16) = twr;
      *(f4*)(cb + tt * 2048 + 1024 + lane * 16) = twi;
    }
    u4v gre, gim;
#pragma unroll
    for (int jj = 0; jj < 4; ++jj) {
      const float ang = (float)(li * (4 * g + jj)) * 0.0625f;
      const float c = cosr(ang), s = sinr(ang);
      gre[jj] = pk_h(c, -s); gim[jj] = pk_h(s, c);
    }
    *(u4v*)(cb + 6144 + lane * 16) = gre;
    *(u4v*)(cb + 7168 + lane * 16) = gim;
  }
  asm volatile("s_waitcnt vmcnt(0)" ::: "memory");
  PIN(x0); PIN(x1); PIN(x2);
  __syncthreads();
  char* yb = smem + K3_Y;
  if (wave < 3) {
    const f4 z4 = f4{0, 0, 0, 0};
    u4v af[4][3];
#pragma unroll
    for (int q = 0; q < 4; ++q) {
      const char* base = smem + (q == 0 ? 8192 : (q - 1) * 2560);
#pragma unroll
      for (int tk = 0; tk < 3; ++tk) af[q][tk] = *(const u4v*)(base + (tk < 2 ? (64 * tk + lane) : (128 + (lane & 31))) * 16);
    }
    const f4 twr = *(const f4*)(cb + t * 2048 + lane * 16), twi = *(const f4*)(cb + t * 2048 + 1024 + lane * 16);
    const u4v Gre = *(const u4v*)(cb + 6144 + lane * 16), Gim = *(const u4v*)(cb + 7168 + lane * 16);
    const float usc = *(const float*)(scratch + 256);
    SB();
    f4 vre[4], vim[4];
#pragma unroll
    for (int q = 0; q < 4; ++q) {
      f4 re = z4, im = z4;
#pragma unroll
      for (int tk = 0; tk < 3; ++tk) {
        re = mfma_h(af[q][tk], Bre[tk], re);
        im = mfma_h(af[q][tk], Bim[tk], im);
      }
      vre[q] = re; vim[q] = im;
    }
#pragma unroll
    for (int q = 0; q < 4; ++q) {
      const u4v v = cmul_pack<false>(vre[q], vim[q], twr, twi);
      vre[q] = mfma_h(v, Gre, z4);
      vim[q] = mfma_h(v, Gim, z4);
    }
    const float fs = usc * (2.0f * 1024.0f * 128.0f / (2048.0f * 640.0f));
    const float fh = fs * 0.70710678118f;
    const f4 E = (vre[0] + vim[0]) * fs, O = (vre[0] - vim[0]) * fs;
    const f4 s13 = vre[1] + vre[3], dx = vre[1] - vre[3], sy = vim[1] + vim[3], dy = vim[3] - vim[1];
    const f4 Ep = E + vre[2] * fs, Em = E - vre[2] * fs, Om = O - vim[2] * fs, Op = O + vim[2] * fs;
    const f4 A = dx - sy, B = dx + sy;
    f4 y[8];
    y[0] = Ep + s13 * fs; y[4] = Ep - s13 * fs; y[2] = Em + dy * fs; y[6] = Em - dy * fs;
    y[1] = Om + A * fh; y[5] = Om - A * fh; y[3] = Op - B * fh; y[7] = Op + B * fh;
    if (t < 2 || lane < 32) {
      char* yp = yb + (40 * li + 16 * t + 4 * g) * 2;
#pragma unroll
      for (int aa = 0; aa < 8; ++aa) *(u2v*)(yp + aa * 1280) = u2v{pk_bf(y[aa][0], y[aa][1]), pk_bf(y[aa][2], y[aa][3])};
    }
  }
  else {
    CODE_TOUCH(g1_kernel, g2_kernel, (blockIdx.x >> 3) & 1)
#pragma unroll
    for (int i = 0; i < 2; ++i) touch((const char*)w_sp + ((size_t)(512 + i * 256 + blockIdx.x) * 64 + lane) * 128);
  }
  asm volatile("s_waitcnt lgkmcnt(0)" ::: "memory");
  __builtin_amdgcn_s_barrier();
  asm volatile("" ::: "memory");
  {
    const __amdgpu_buffer_rsrc_t rs = ws_rsrc(ws);
    const u4v y0 = *(const u4v*)(yb + a * 1280 + 16 * j);
    const u4v y1 = *(const u4v*)(yb + a * 1280 + 512 + 16 * j);
    const u2v y2 = *(const u2v*)(yb + a * 1280 + 1024 + 8 * j);
    u32 hx[10];
#pragma unroll
    for (int i = 0; i < 4; ++i) {
      hx[i] = pk_bf(bflo(x0[i]) + bflo(y0[i]), bfhi(x0[i]) + bfhi(y0[i]));
      hx[4 + i] = pk_bf(bflo(x1[i]) + bflo(y1[i]), bfhi(x1[i]) + bfhi(y1[i]));
    }
    hx[8] = pk_bf(bflo(x2[0]) + bflo(y2[0]), bfhi(x2[0]) + bfhi(y2[0]));
    hx[9] = pk_bf(bflo(x2[1]) + bflo(y2[1]), bfhi(x2[1]) + bfhi(y2[1]));
    auto pairup = [&](u32 v0, u32 v1) {
      return u4v{v0, v1, (u32)__builtin_amdgcn_update_dpp(0, (int)v0, 0xB1, 0xf, 0xf, true), (u32)__builtin_amdgcn_update_dpp(0, (int)v1, 0xB1, 0xf, 0xf, true)};
    };
    const u32 drop = (lane & 1) ? DROP : 0u;
    const u32 ho = (u32)WS_H + row * (D * 2);
    st16(rs, ho + 16 * j, u4v{hx[0], hx[1], hx[2], hx[3]});
    st16(rs, ho + 512 + 16 * j, u4v{hx[4], hx[5], hx[6], hx[7]});
    st16(rs, (ho + 1024 + 8 * j) | drop, pairup(hx[8], hx[9]));
    f2 v[10];
#pragma unroll
    for (int i = 0; i < 10; ++i) v[i] = f2{bflo(hx[i]), bfhi(hx[i])};
    ln_row(v);
#pragma unroll
    for (int i = 0; i < 10; ++i) hx[i] = pk_bf(v[i][0], v[i][1]);
    const u32 lo = (u32)WS_L2 + (row >> 4) * 20480 + (row & 15) * 64;
    st16(rs, lo + (j >> 2) * 1024 + (j & 3) * 16, u4v{hx[0], hx[1], hx[2], hx[3]});
    st16(rs, lo + (8 + (j >> 2)) * 1024 + (j & 3) * 16, u4v{hx[4], hx[5], hx[6], hx[7]});
    st16(rs, (lo + (16 + (j >> 3)) * 1024 + (j & 7) * 8) | drop, pairup(hx[8], hx[9]));
  }
  WAITV(0);
}

constexpr int FPD = 2;
#include <type_traits>
typedef unsigned long long u64;
typedef __attribute__((address_space(1))) unsigned long long gu64;
#define WAITL0() do { __builtin_amdgcn_sched_barrier(0); asm volatile("s_waitcnt lgkmcnt(0)" ::: "memory"); __builtin_amdgcn_sched_barrier(0); } while (0)
#define BAR() do { asm volatile("" ::: "memory"); __builtin_amdgcn_sched_barrier(0); __builtin_amdgcn_s_barrier(); __builtin_amdgcn_sched_barrier(0); asm volatile("" ::: "memory"); } while (0)
#define SGB(mask, n) __builtin_amdgcn_sched_group_barrier(mask, n, 0)
DI f4 mfma_b(u4v a, u4v b, f4 c) {
  return __builtin_amdgcn_mfma_f32_16x16x32_bf16(__builtin_bit_cast(bf8, a), __builtin_bit_cast(bf8, b), c, 0, 0, 0);
}
DI f2 gelu2(f2 x) {
  const float k1 = -2.0f * 0.7978845608028654f * 1.4426950408889634f;
  const float k2 = k1 * 0.044715f;
  f2 p = x * x * k2 + k1;
  f2 a = x * p;
  f2 e = f2{__builtin_amdgcn_exp2f(a[0]), __builtin_amdgcn_exp2f(a[1])} + 1.0f;
  f2 r = f2{__builtin_amdgcn_rcpf(e[0]), __builtin_amdgcn_rcpf(e[1])};
  return x * r;
}
DI u2v gelu4(f4 a) {
  f2 g0 = gelu2(f2{a[0], a[1]}), g1 = gelu2(f2{a[2], a[3]});
  return u2v{pk_bf(g0[0], g0[1]), pk_bf(g1[0], g1[1])};
}
template <int N> DI void waitv() { asm volatile("s_waitcnt vmcnt(%0)" ::"n"(N) : "memory"); }

constexpr int G1_DEPTH = 3, G1_AD = 5;
extern "C" __global__ __launch_bounds__(512) void g1_kernel() {
  LOAD_ARGS();
  char* ws = ap_->ws; const u16* w_sp = ap_->w_sp;
  PINS(ws); PINS(w_sp);
  const u32* l2f = (const u32*)(ws + WS_L2); const u32* Wt1 = (const u32*)(ws + WS_WT1);
  u32* z1f = (u32*)(ws + WS_Z1); u16* z2t = (u16*)(ws + WS_Z2); u64* stats = (u64*)(ws + WS_STATS);
  __shared__ __attribute__((aligned(16))) char smem[140 * 1024 + 4 * 4608 + 256];
  const int tid = threadIdx.x, lane = tid & 63;
  const int wave = __builtin_amdgcn_readfirstlane(tid >> 6);
  const int li = lane & 15, g = lane >> 4;
  const int xcd = blockIdx.x & 7, idx = blockIdx.x >> 3;
  const int mt = (xcd >> 1) * 4 + (idx >> 3), nb = (xcd & 1) * 8 + (idx & 7);
  const int row0 = 1024 * (mt & 1) + 32 * (mt >> 1);
  const int zs = nb < 6 ? 4 * nb : 24 + 3 * (nb - 6);
  const int z1s = nb < 6 ? 3 * nb : (nb < 12 ? 18 + 4 * (nb - 6) : 42 + 3 * (nb - 12));
  const int type = nb < 6 ? 0 : (nb < 12 ? 1 : 2);
  if (wave >= 4) {
    const int lw = wave - 4;
    auto slot_frag = [&](int s) { return s < 3 ? 54 + zs + s : (s == 3 ? (type == 0 ? 54 + zs + 3 : (type == 1 ? z1s + 3 : z1s)) : z1s + s - 4); };
    const u32* bsrc[4];
#pragma unroll
    for (int j = 0; j < 4; ++j) {
      int i = lw + 4 * j; i = i < 14 ? i : 0;
      const int kk = i / 7, s = i - 7 * kk;
      bsrc[j] = Wt1 + (size_t)(kk * 108 + slot_frag(s)) * 256 + lane * 4;
    }
    auto stage = [&](int kt) {
      char* sb = smem + kt * 14336 + lw * 1024;
#pragma unroll
      for (int j = 0; j < 3; ++j) GLDS16(bsrc[j] + (size_t)kt * 2 * 108 * 256, sb + j * 4096);
      if (lw < 2) GLDS16(bsrc[3] + (size_t)kt * 2 * 108 * 256, sb + 3 * 4096);
    };
#pragma unroll
    for (int s = 0; s < G1_DEPTH; ++s) stage(s);
    for (int kt = 0; kt < 10 - G1_DEPTH; ++kt) {
      if (lw < 2) waitv<4 * (G1_DEPTH - 1)>(); else waitv<3 * (G1_DEPTH - 1)>();
      BAR();
      stage(kt + G1_DEPTH);
    }
    if (lw < 2) waitv<8>(); else waitv<6>(); BAR();
    if (lw < 2) waitv<4>(); else waitv<3>(); BAR();
    waitv<0>();
    char* scratch = smem + 140 * 1024 + 4 * 4608;
    if (lw < 2) touch((const char*)w_sp + ((size_t)(blockIdx.x * 2 + lw) * 64 + lane) * 128);
    BAR();
    if (lw >= 2) CODE_TOUCH(g2_kernel, end_kernel, lw - 2)
    waitv<0>();
    return;
  }
  const int rg0 = (row0 + wave * 256) >> 4;
  const u32* asrc = l2f + (size_t)rg0 * 20 * 256 + li * 16 + g * 4;
  u4v fa[G1_AD][2];
#pragma unroll
  for (int k = 0; k < G1_AD; ++k) { fa[k][0] = *(const u4v*)(asrc + k * 256); fa[k][1] = *(const u4v*)(asrc + (20 + k) * 256); __builtin_amdgcn_sched_barrier(0); }
  f4 acc[7][2];
#pragma unroll
  for (int s = 0; s < 7; ++s) { acc[s][0] = f4{0, 0, 0, 0}; acc[s][1] = f4{0, 0, 0, 0}; }
  u4v fw[2][7];
  const char* wb = smem + lane * 16;
  auto rd = [&](int k) {
#pragma unroll
    for (int s = 0; s < 7; ++s) fw[k & 1][s] = *(const u4v*)(wb + (k * 7 + s) * 1024);
  };
  auto mm = [&](int k) {
#pragma unroll
    for (int s = 0; s < 7; ++s) {
      acc[s][0] = mfma_b(fw[k & 1][s], fa[k % G1_AD][0], acc[s][0]);
      acc[s][1] = mfma_b(fw[k & 1][s], fa[k % G1_AD][1], acc[s][1]);
    }
    if (k + G1_AD < 20) { fa[k % G1_AD][0] = *(const u4v*)(asrc + (k + G1_AD) * 256); fa[k % G1_AD][1] = *(const u4v*)(asrc + (20 + G1_AD + k) * 256); }
  };
  BAR(); rd(0);
#pragma unroll
  for (int k = 1; k < 20; ++k) {
    if (!(k & 1)) BAR(); else __builtin_amdgcn_sched_barrier(0);
    rd(k);
    mm(k - 1);
#pragma unroll
    for (int i = 0; i < 7; ++i) { SGB(0x100, 1); SGB(0x8, 2); }
  }
  __builtin_amdgcn_sched_barrier(0);
  mm(19);
  const int n2 = type == 0 ? 4 : 3;
  f4 zv[4][2];
  u2v z3[2];
  float lsum[2], lm2[2];
#pragma unroll
  for (int sf = 0; sf < 2; ++sf) {
    f2 s = f2{0.f, 0.f};
#pragma unroll
    for (int j = 0; j < 4; ++j) {
      const u2v t = gelu4(acc[j][sf]);
      if (j == 3) z3[sf] = t;
      zv[j][sf] = f4{bflo(t[0]), bfhi(t[0]), bflo(t[1]), bfhi(t[1])};
      if (j < 3 || type == 0) s += f2{zv[j][sf][0], zv[j][sf][1]} + f2{zv[j][sf][2], zv[j][sf][3]};
    }
    const float st = xsum_g(s[0] + s[1]);
    const float lmean = st * (type == 0 ? (1.0f / 64.0f) : (1.0f / 48.0f));
    f2 q = f2{0.f, 0.f};
#pragma unroll
    for (int j = 0; j < 4; ++j) {
      const f4 d = zv[j][sf] - lmean;
      if (j < 3 || type == 0) { q += f2{d[0], d[1]} * f2{d[0], d[1]}; q += f2{d[2], d[3]} * f2{d[2], d[3]}; }
    }
    lsum[sf] = st; lm2[sf] = xsum_g(q[0] + q[1]);
  }
  gu64* st = (gu64*)stats + (size_t)mt * 16 * 128 + wave * 32;
  if (lane < 32) {
    u32 a = __float_as_uint(g ? lsum[1] : lsum[0]), b = __float_as_uint(g ? lm2[1] : lm2[0]);
    if (a == ~0u) a = 0x7fc00000u;
    __hip_atomic_store(st + nb * 128 + lane, ((u64)b << 32) | a, __ATOMIC_RELAXED, __HIP_MEMORY_SCOPE_AGENT);
  }
  __builtin_amdgcn_sched_barrier(0);
  const __amdgpu_buffer_rsrc_t rs = ws_rsrc(ws);
  u4v z1o[3];
#pragma unroll
  for (int j = 0; j < 3; ++j) {
    const u2v a = gelu4(acc[4 + j][0]), b = gelu4(acc[4 + j][1]);
    z1o[j] = u4v{a[0], a[1], b[0], b[1]};
    PIN(z1o[j]);
  }
  __builtin_amdgcn_sched_barrier(0);
  u64 gv[2][4];
  for (int spin = 0; spin < (1 << 16); ++spin) {
    bool ok = true;
#pragma unroll
    for (int sf = 0; sf < 2; ++sf)
#pragma unroll
      for (int i = 0; i < 4; ++i) {
        gv[sf][i] = __hip_atomic_load(st + (4 * g + i) * 128 + sf * 16 + li, __ATOMIC_RELAXED, __HIP_MEMORY_SCOPE_AGENT);
        ok &= ((u32)gv[sf][i] != ~0u);
      }
    if (__all(ok)) break;
    __builtin_amdgcn_s_sleep(1);
  }
  {
    const u32 zo = (u32)WS_Z1 + ((rg0 >> 1) * 54 + z1s) * 1024 + lane * 16;
#pragma unroll
    for (int j = 0; j < 3; ++j) st16(rs, zo + j * 1024, z1o[j]);
    if (type == 1) st16(rs, zo + 3 * 1024, u4v{z3[0][0], z3[0][1], z3[1][0], z3[1][1]});
  }
  char* tb = smem + 140 * 1024 + wave * 4608;
#pragma unroll
  for (int sf = 0; sf < 2; ++sf) {
    float s = 0.f;
#pragma unroll
    for (int i = 0; i < 4; ++i) s += __uint_as_float((u32)gv[sf][i]);
    const float mean = xsum_g(s) * (1.0f / 864.0f);
    float q = 0.f;
#pragma unroll
    for (int i = 0; i < 4; ++i) {
      const bool big = (4 * g + i) < 6;
      const float dm = __uint_as_float((u32)gv[sf][i]) * (big ? (1.0f / 64.0f) : (1.0f / 48.0f)) - mean;
      q += __uint_as_float((u32)(gv[sf][i] >> 32)) + (big ? 64.f : 48.f) * dm * dm;
    }
    const float var = xsum_g(q) * (1.0f / 864.0f);
    const float mb = rbf(mean);
    const float rinv = __builtin_amdgcn_rcpf(rbf(__builtin_amdgcn_sqrtf(rbf(rbf(var) + 1e-5f))));
#pragma unroll
    for (int j = 0; j < 4; ++j) {
      const f4 o = (zv[j][sf] - mb) * rinv;
      *(u2v*)(tb + (sf * 16 + li) * 144 + j * 32 + g * 8) = u2v{pk_bf(o[0], o[1]), pk_bf(o[2], o[3])};
    }
  }
  {
    const int q = li >> 2, p = li & 3;
#pragma unroll
    for (int j = 0; j < 4; ++j) {
      s4 lo = __builtin_amdgcn_ds_read_tr16_b64_v4i16((LDS_AS s4*)(tb + (8 * g + q) * 144 + j * 32 + 8 * p));
      s4 hi = __builtin_amdgcn_ds_read_tr16_b64_v4i16((LDS_AS s4*)(tb + (8 * g + 4 + q) * 144 + j * 32 + 8 * p));
      u2v a = __builtin_bit_cast(u2v, lo), b = __builtin_bit_cast(u2v, hi);
      if (j < n2) st16(rs, (u32)WS_Z2 + (((zs + j) * 16 + li) * S + row0 + wave * 256 + 8 * g) * 2, u4v{a[0], a[1], b[0], b[1]});
    }
  }
}

typedef __attribute__((address_space(1))) unsigned int gu32;
constexpr int G2_NST = 7, G2_STB = 22 * 1024;
extern "C" __global__ __launch_bounds__(512) void g2_kernel() {
  LOAD_ARGS();
  char* ws = ap_->ws; const u16* w_sp = ap_->w_sp; const u16* b_sp = ap_->b_sp; u16* out = ap_->out;
  PINS(w_sp); PINS(b_sp); PINS(ws); PINS(out);
  const u16* z2t = (const u16*)(ws + WS_Z2); const u32* z1f = (const u32*)(ws + WS_Z1);
  __shared__ __attribute__((aligned(16))) char smem[G2_NST * G2_STB + 256];
  const int tid = threadIdx.x, lane = tid & 63;
  const int wave = __builtin_amdgcn_readfirstlane(tid >> 6);
  const int li = lane & 15, g = lane >> 4;
  const int xcd = blockIdx.x & 7, idx = blockIdx.x >> 3;
  const int tt = (xcd >> 1) * 8 + (idx >> 2), ft = (xcd & 1) * 4 + (idx & 3);
  if (wave >= 4) {
    const int lw = wave - 4;
    const u16* src[6];
    const int swz = ((lane & 7) ^ (lane >> 3)) * 8;
#pragma unroll
    for (int j = 0; j < 6; ++j) {
      int i = lw + 4 * j; i = i < 22 ? i : 8;
      if (j < 2) src[j] = w_sp + (size_t)(tt * 64 + 8 * i + (lane >> 3)) * S + swz;
      else { int r = ft * 112 + 8 * (i - 8) + (lane >> 3); r = r < FH ? r : FH - 1; src[j] = z2t + (size_t)r * S + swz; }
    }
    auto stage = [&](int kt, int buf) {
      char* sb = smem + buf * G2_STB + lw * 1024;
      const int kk = kt * 64;
#pragma unroll
      for (int j = 0; j < 5; ++j) GLDS16(src[j] + kk, sb + j * 4096);
      if (lw < 2) GLDS16(src[5] + kk, sb + 5 * 4096);
    };
    const bool big = lw < 2;
    stage(0, 0); stage(1, 1); stage(2, 2);
    if (big) waitv<12>(); else waitv<10>(); BAR();
    stage(3, 3); stage(4, 4);
    if (big) waitv<18>(); else waitv<15>(); BAR();
    stage(5, 5); stage(6, 6);
    if (big) waitv<24>(); else waitv<20>(); BAR();
    stage(7, 0); stage(8, 1);
    int nbuf = 2;
    for (int kt = 3; kt < 26; ++kt) {
      if (big) waitv<30>(); else waitv<25>();
      BAR();
      stage(kt + 6, nbuf);
      nbuf = nbuf + 1 == G2_NST ? 0 : nbuf + 1;
    }
    const u32* wsrc = (const u32*)(ws + WS_WT3) + (size_t)(ft * 5) * 256 + lane * 4;
    for (int ps = 0; ps < 6; ++ps) {
      if (big) waitv<30>(); else waitv<25>();
      BAR();
      char* sb = smem + nbuf * G2_STB + lw * 1024;
#pragma unroll
      for (int j = 0; j < 6; ++j) {
        const int i = ps * 22 + lw + 4 * j, k = i / 5;
        if (j < 5 || big) GLDS16(wsrc + (size_t)(k * 35 + i) * 256, sb + j * 4096);
      }
      nbuf = nbuf + 1 == G2_NST ? 0 : nbuf + 1;
    }
    BAR();
    if (lw < 3) GLDS16(wsrc + (size_t)(26 * 35 + 132 + lw) * 256, smem + nbuf * G2_STB + lw * 1024);
    waitv<0>();
    BAR();
    return;
  }
  const int wt = wave & 1, wf = wave >> 1;
  const int n0 = (ft & 1) ? 3 : 4;
  const int f0 = wf ? n0 : 0, nf = wf ? 7 - n0 : n0;
  u4v z1v[4];
  float bias[2];
#pragma unroll
  for (int j = 0; j < 4; ++j) {
    int ff = ft * 7 + f0 + j; ff = ff < 54 ? ff : 53;
    z1v[j] = *(const u4v*)(z1f + ((size_t)(tt * 2 + wt) * 54 + ff) * 256 + lane * 4);
  }
  bias[0] = bflo(b_sp[tt * 64 + wt * 32 + li]); bias[1] = bflo(b_sp[tt * 64 + wt * 32 + 16 + li]);
  f4 acc[4][2];
#pragma unroll
  for (int j = 0; j < 4; ++j) { acc[j][0] = f4{0, 0, 0, 0}; acc[j][1] = f4{0, 0, 0, 0}; }
  const int sw = (g ^ (li & 7)) * 16;
  const int toff = (wt * 32 + li) * 128 + sw;
  const int foff = 8192 + (f0 * 16 + li) * 128 + sw;
  auto body = [&](auto NFc) {
    constexpr int NF = decltype(NFc)::value;
    u4v fb[2][2][2], fa[2][2][NF];
    auto rd = [&](int set, int buf) {
      const char* sb = smem + buf * G2_STB;
#pragma unroll
      for (int kk = 0; kk < 2; ++kk) {
        fb[set][kk][0] = *(const u4v*)(sb + (toff ^ (kk * 64)));
        fb[set][kk][1] = *(const u4v*)(sb + (toff ^ (kk * 64)) + 2048);
#pragma unroll
        for (int j = 0; j < NF; ++j) fa[set][kk][j] = *(const u4v*)(sb + (foff ^ (kk * 64)) + j * 2048);
      }
    };
    auto mm = [&](int set) {
#pragma unroll
      for (int kk = 0; kk < 2; ++kk)
#pragma unroll
        for (int j = 0; j < NF; ++j) {
          acc[j][0] = mfma_b(fa[set][kk][j], fb[set][kk][0], acc[j][0]);
          acc[j][1] = mfma_b(fa[set][kk][j], fb[set][kk][1], acc[j][1]);
        }
    };
    auto sched = [&]() {
#pragma unroll
      for (int i = 0; i < 2 * NF + 4; ++i) { SGB(0x100, 1); SGB(0x8, 1); }
      SGB(0x8, 2 * NF - 4);
    };
    BAR(); rd(0, 0); WAITL0();
    int buf = 1;
    for (int kt = 1; kt < 31; kt += 2) {
      BAR(); rd(1, buf); mm(0); sched(); WAITL0();
      buf = buf + 1 == G2_NST ? 0 : buf + 1;
      BAR(); rd(0, buf); mm(1); sched(); WAITL0();
      buf = buf + 1 == G2_NST ? 0 : buf + 1;
    }
    BAR(); rd(1, buf); mm(0); sched(); WAITL0();
#pragma unroll
    for (int j = 0; j < 4; ++j) PIN(z1v[j]);
    PIN(bias[0]); PIN(bias[1]);
    BAR();
    mm(1);
  };
  if (nf == 4) body(std::integral_constant<int, 4>{}); else body(std::integral_constant<int, 3>{});
  const __amdgpu_buffer_rsrc_t rs = ws_rsrc(ws);
  const int ff0 = ft * 7 + f0;
#pragma unroll
  for (int tf = 0; tf < 2; ++tf) {
    const int tfr = tt * 4 + wt * 2 + tf;
    u2v o[4];
#pragma unroll
    for (int j = 0; j < 4; ++j) {
      const u32 za = z1v[j][2 * tf], zb = z1v[j][2 * tf + 1];
      const f4 v = (acc[j][tf] + bias[tf]) * f4{bflo(za), bfhi(za), bflo(zb), bfhi(zb)};
      o[j] = u2v{pk_bf(v[0], v[1]), pk_bf(v[2], v[3])};
    }
    const u32 base = (u32)WS_P + (tfr * 27 + (ff0 >> 1)) * 1024 + lane * 16;
    if (!(ff0 & 1)) {
      st16(rs, base, u4v{o[0][0], o[0][1], o[1][0], o[1][1]});
      if (nf == 4) { if (ff0 + 3 < 54) st16(rs, base + 1024, u4v{o[2][0], o[2][1], o[3][0], o[3][1]}); }
      else st8(rs, base + 1024, o[2]);
    } else {
      st8(rs, base + 8, o[0]);
      st16(rs, base + 1024, u4v{o[1][0], o[1][1], o[2][0], o[2][1]});
    }
  }
  gu32* cnt = (gu32*)(ws + WS_CNT);
  waitv<0>();
  if (lane == 0) __hip_atomic_store(cnt + (tt * 2 + wt) * 32 + ft * 2 + wf, 1u, __ATOMIC_RELAXED, __HIP_MEMORY_SCOPE_AGENT);
  const int trow = tt * 64 + wave * 16 + li;
  const u16* h = (const u16*)(ws + WS_H);
  u2v hv[5];
#pragma unroll
  for (int j = 0; j < 5; ++j) hv[j] = *(const u2v*)(h + (size_t)trow * D + ft * 80 + j * 16 + g * 4);
  for (int spin = 0; spin < (1 << 16); ++spin) {
    const u32 c = __hip_atomic_load(cnt + (tt * 2 + (wave >> 1)) * 32 + li, __ATOMIC_RELAXED, __HIP_MEMORY_SCOPE_AGENT);
    if (__all(c != 0u)) break;
    __builtin_amdgcn_s_sleep(1);
  }
  asm volatile("" ::: "memory");
  u4v fb[27];
  const u32 poff = (u32)WS_P + (tt * 4 + wave) * 27 * 1024 + lane * 16;
#pragma unroll
  for (int k = 0; k < 3 * FPD; ++k) { fb[k] = __builtin_amdgcn_raw_buffer_load_b128(rs, poff + k * 1024, 0, AUX_SC1); SB(); }
  f4 acc3[5];
#pragma unroll
  for (int j = 0; j < 5; ++j) acc3[j] = f4{0, 0, 0, 0};
  u4v fa[2][15];
  u32 wo[3] = {(u32)lane * 16, (u32)lane * 16 + 3 * G2_STB - 8192, (u32)lane * 16 + 5 * G2_STB};
  PIN(wo[1]); PIN(wo[2]);
  auto rd3 = [&](int kt) {
#pragma unroll
    for (int ii = 0; ii < 15; ++ii) {
      const int i = kt * 15 + ii;
      const int off = ((32 + i / 22) % G2_NST) * G2_STB + (i % 22) * 1024;
      const int w = off < 3 * G2_STB - 8192 ? 0 : (off < 5 * G2_STB ? 1 : 2);
      fa[kt & 1][ii] = *(const u4v*)(smem + wo[w] + (off - (w == 0 ? 0 : (w == 1 ? 3 * G2_STB - 8192 : 5 * G2_STB))));
    }
  };
  auto mm3 = [&](int kt) {
#pragma unroll
    for (int kk = 0; kk < 3; ++kk)
#pragma unroll
      for (int j = 0; j < 5; ++j) acc3[j] = mfma_b(fa[kt & 1][kk * 5 + j], fb[kt * 3 + kk], acc3[j]);
  };
  BAR();
  rd3(0);
#pragma unroll
  for (int kt = 1; kt < 9; ++kt) {
    SB();
    if (kt - 1 + FPD < 9) {
#pragma unroll
      for (int k = 0; k < 3; ++k) fb[(kt - 1 + FPD) * 3 + k] = __builtin_amdgcn_raw_buffer_load_b128(rs, poff + ((kt - 1 + FPD) * 3 + k) * 1024, 0, AUX_SC1);
      SB();
    }
    rd3(kt);
    mm3(kt - 1);
#pragma unroll
    for (int i = 0; i < 15; ++i) { SGB(0x100, 1); SGB(0x8, 1); }
  }
  SB();
  mm3(8);
  {
    u2v o[5];
#pragma unroll
    for (int j = 0; j < 5; ++j) {
      u32 t0 = pk_bf(acc3[j][0], acc3[j][1]), t1 = pk_bf(acc3[j][2], acc3[j][3]);
      o[j] = u2v{pk_bf(bflo(t0) + bflo(hv[j][0]), bfhi(t0) + bfhi(hv[j][0])), pk_bf(bflo(t1) + bflo(hv[j][1]), bfhi(t1) + bfhi(hv[j][1]))};
    }
    const __amdgpu_buffer_rsrc_t ro = __builtin_amdgcn_make_buffer_rsrc((void*)out, 0, S * D * 2, 0x00020000);
    const u32 oo = (trow * D + ft * 80) * 2;
#pragma unroll
    for (int j = 0; j < 4; j += 2) {
      auto s0 = __builtin_amdgcn_permlane16_swap(o[j][0], o[j + 1][0], false, false);
      auto s1 = __builtin_amdgcn_permlane16_swap(o[j][1], o[j + 1][1], false, false);
      __builtin_amdgcn_raw_buffer_store_b128(u4v{s0[0], s1[0], s0[1], s1[1]}, ro, oo + (j + (g & 1)) * 32 + (g >> 1) * 16, 0, 0);
    }
    __builtin_amdgcn_raw_buffer_store_b64(o[4], ro, oo + 128 + g * 8, 0, 0);
  }
}

extern "C" __global__ void end_kernel() {}


__global__ void diag_kernel() {
  unsigned long long t0 = __builtin_amdgcn_s_memrealtime();
  unsigned long long ta = g_t[0];
  for (int i = 1; i < 8; ++i) ta = g_t[i] < ta ? g_t[i] : ta;
  unsigned long long d = (t0 - ta) * 8;
  if (d > 100000) d = 100000;
  while (__builtin_amdgcn_s_memrealtime() - t0 < d) __builtin_amdgcn_s_sleep(1);
}
extern "C" void kernel_launch(void* const* d_in, const int* in_sizes, int n_in,
                              void* d_out, int out_size, void* d_ws, size_t ws_size,
                              hipStream_t stream) {
  const u16* x=(const u16*)d_in[0]; const u16* wr=(const u16*)d_in[1]; const u16* wi=(const u16*)d_in[2];
  const u16* w_in=(const u16*)d_in[4]; const u16* w_sp=(const u16*)d_in[5]; const u16* b_sp=(const u16*)d_in[6]; const u16* w_out=(const u16*)d_in[7];
  char* ws=(char*)d_ws;
  k1_kernel<<<256,512,0,stream>>>(x,wr,wi,ws,w_sp,w_in,w_out,b_sp,(u16*)d_out);
  (void)hipFuncSetAttribute((const void*)k2_kernel, hipFuncAttributeMaxDynamicSharedMemorySize, 84 * 1024);
  k2_kernel<<<K2_FFT_BLOCKS+K2_PREP_BLOCKS,512,84 * 1024,stream>>>();
  k3_kernel<<<256,256,0,stream>>>();
  g1_kernel<<<256,512,0,stream>>>();
  g2_kernel<<<256,512,0,stream>>>();
  diag_kernel<<<1,64,0,stream>>>();
}
```

```cpp
#include <hip/hip_runtime.h>
#include <hip/hip_bf16.h>
#include <stdint.h>
__device__ __attribute__((visibility("protected"))) unsigned long long g_t[16];
typedef unsigned short u16;
typedef unsigned int u32;
typedef _Float16 h2 __attribute__((ext_vector_type(2)));
typedef _Float16 h8 __attribute__((ext_vector_type(8)));
typedef __bf16 bf2 __attribute__((ext_vector_type(2)));
typedef __bf16 bf8 __attribute__((ext_vector_type(8)));
typedef float f2 __attribute__((ext_vector_type(2)));
typedef float f4 __attribute__((ext_vector_type(4)));
typedef short s4 __attribute__((ext_vector_type(4)));
typedef u32 u2v __attribute__((ext_vector_type(2)));
typedef u32 u4v __attribute__((ext_vector_type(4)));
#define DI __device__ __forceinline__
#define LDS_AS __attribute__((address_space(3)))
#define PIN(x) asm volatile("" : "+v"(x))
#define GLDS16(src, dst) __builtin_amdgcn_global_load_lds((const u32*)(src), (LDS_AS u32*)(dst), 16, 0, 0)
#define WAITV(n) asm volatile("s_waitcnt vmcnt(%0)" ::"n"(n) : "memory")

constexpr int S = 2048, D = 640, DF = 321, FF = 1728, FH = 864;
constexpr int TROW_UNITS = 648;
constexpr int NUNITS = 641;

constexpr size_t WS_T = 0, WS_WP = 2688u << 10, WS_TP = 5376u << 10, WS_WT1 = 8064u << 10, WS_WT3 = 10240u << 10, WS_H = 11392u << 10,
                 WS_L2 = 13952u << 10, WS_Z1 = 16512u << 10, WS_Z2 = 19968u << 10, WS_STATS = 23424u << 10, WS_W2 = 23680u << 10,
                 WS_WMAX = 23692u << 10, WS_UNS = 23696u << 10, WS_CNT = 23700u << 10, WS_P = WS_T;
#define PINS(x) asm volatile("" ::"s"(x))
#define GLDS4(src, dst) __builtin_amdgcn_global_load_lds((const u32*)(src), (LDS_AS u32*)(dst), 4, 0, 0)
#define touch(p) GLDS4(p, scratch)

constexpr int AUX_SC1 = 16;
constexpr u32 DROP = 0x80000000u;
DI __amdgpu_buffer_rsrc_t ws_rsrc(char* ws) { return __builtin_amdgcn_make_buffer_rsrc((void*)ws, 0, 64 << 20, 0x00020000); }
DI void st16(__amdgpu_buffer_rsrc_t rs, u32 off, u4v v) { __builtin_amdgcn_raw_buffer_store_b128(v, rs, off, 0, AUX_SC1); }
DI void st16p(__amdgpu_buffer_rsrc_t rs, u32 off, u4v v) { __builtin_amdgcn_raw_buffer_store_b128(v, rs, off, 0, 0); }
DI void st8(__amdgpu_buffer_rsrc_t rs, u32 off, u2v v) { __builtin_amdgcn_raw_buffer_store_b64(v, rs, off, 0, AUX_SC1); }
#define CODE_ADDR(var, sym)                                                                                     \
  const char* var;                                                                                              \
  {                                                                                                             \
    u32 lo_, hi_;                                                                                               \
    asm volatile("s_getpc_b64 s[40:41]\n\ts_add_u32 %0, s40, " #sym "@rel32@lo+4\n\ts_addc_u32 %1, s41, " #sym "@rel32@hi+12" \
                 : "=s"(lo_), "=s"(hi_)::"s40", "s41", "scc");                                                  \
    var = (const char*)(((unsigned long long)hi_ << 32) | lo_);                                                 \
  }
#define CODE_TOUCH(a, b, c)                                                           \
  {                                                                                   \
    CODE_ADDR(pa_, a) CODE_ADDR(pb_, b)                                               \
    const char* p_ = pa_ + ((c) * 64 + lane) * 128;                                   \
    if (p_ < pb_) touch(p_);                                                          \
  }
struct Args { char* ws; const u16* x; const u16* w_sp; const u16* w_in; const u16* w_out; const u16* b_sp; u16* out; void* pad; };
static __device__ __attribute__((aligned(128))) Args g_args;
typedef const __attribute__((address_space(4))) Args* ArgsPtr;
#define LOAD_ARGS() ArgsPtr ap_ = (ArgsPtr)(unsigned long long)&g_args
DI float bflo(u32 v) { return __uint_as_float(v << 16); }
DI float bfhi(u32 v) { return __uint_as_float(v & 0xffff0000u); }
DI u32 pk_bf(float a, float b) { f2 v = {a, b}; return __builtin_bit_cast(u32, __builtin_convertvector(v, bf2)); }
DI u32 pk_h(float a, float b) { f2 v = {a, b}; return __builtin_bit_cast(u32, __builtin_convertvector(v, h2)); }
DI float rbf(float a) { return bflo(pk_bf(a, a)); }
DI float cosr(float rev) { return __builtin_amdgcn_cosf(rev); }
DI float sinr(float rev) { return __builtin_amdgcn_sinf(rev); }
DI f4 mfma_h(u4v a, u4v b, f4 c) {
  return __builtin_amdgcn_mfma_f32_16x16x32_f16(__builtin_bit_cast(h8, a), __builtin_bit_cast(h8, b), c, 0, 0, 0);
}
template <int CTRL> DI float dpp_add(float v) {
  int t = __builtin_amdgcn_update_dpp(0, __float_as_int(v), CTRL, 0xf, 0xf, true);
  return v + __int_as_float(t);
}
DI float xsum_g(float v) {
  u32 b = __float_as_uint(v);
  auto r = __builtin_amdgcn_permlane16_swap(b, b, false, false);
  v = __uint_as_float(r[0]) + __uint_as_float(r[1]);
  b = __float_as_uint(v);
  auto s = __builtin_amdgcn_permlane32_swap(b, b, false, false);
  return __uint_as_float(s[0]) + __uint_as_float(s[1]);
}
template <int CTRL> DI u32 dpp_maxu(u32 v) {
  u32 t = (u32)__builtin_amdgcn_update_dpp(0, (int)v, CTRL, 0xf, 0xf, true);
  return v > t ? v : t;
}
DI u32 max64u(u32 v) {
  v = dpp_maxu<0xB1>(v); v = dpp_maxu<0x4E>(v); v = dpp_maxu<0x141>(v); v = dpp_maxu<0x140>(v);
  auto r = __builtin_amdgcn_permlane16_swap(v, v, false, false);
  v = r[0] > r[1] ? r[0] : r[1];
  auto s = __builtin_amdgcn_permlane32_swap(v, v, false, false);
  return s[0] > s[1] ? s[0] : s[1];
}
DI float sum32(float v) {
  v = dpp_add<0xB1>(v); v = dpp_add<0x4E>(v); v = dpp_add<0x141>(v); v = dpp_add<0x140>(v);
  u32 b = __float_as_uint(v);
  auto r = __builtin_amdgcn_permlane16_swap(b, b, false, false);
  return __uint_as_float(r[0]) + __uint_as_float(r[1]);
}
DI void ln_row(f2 (&v)[10]) {
  f2 s = ((v[0] + v[1]) + (v[2] + v[3])) + ((v[4] + v[5]) + (v[6] + v[7])) + (v[8] + v[9]);
  const float mean = sum32(s[0] + s[1]) * (1.0f / 640.0f);
  f2 q0 = f2{0.f, 0.f}, q1 = f2{0.f, 0.f};
#pragma unroll
  for (int i = 0; i < 10; i += 2) { v[i] -= mean; v[i + 1] -= mean; q0 += v[i] * v[i]; q1 += v[i + 1] * v[i + 1]; }
  q0 += q1;
  const float var = sum32(q0[0] + q0[1]) * (1.0f / 640.0f);
  const float rinv = __builtin_amdgcn_rcpf(rbf(__builtin_amdgcn_sqrtf(rbf(rbf(var) + 1e-5f))));
  const float off = (mean - rbf(mean)) * rinv;
#pragma unroll
  for (int i = 0; i < 10; ++i) v[i] = v[i] * rinv + off;
}
#define SB() __builtin_amdgcn_sched_barrier(0)
template <bool SWAP> DI u4v cmul_pack(f4 re, f4 im, f4 wr, f4 wi) {
  const f4 ore = re * wr - im * wi, oim = re * wi + im * wr;
  u4v v;
#pragma unroll
  for (int r = 0; r < 4; ++r) v[r] = SWAP ? pk_h(oim[r], ore[r]) : pk_h(ore[r], oim[r]);
  return v;
}

struct __attribute__((packed, aligned(2))) U4U { u32 a, b, c, d; };

DI void lds_signal(char* p) {
  asm volatile("s_waitcnt lgkmcnt(0)" ::: "memory");
  if ((threadIdx.x & 63) == 0) __hip_atomic_fetch_add((LDS_AS u32*)p, 1u, __ATOMIC_RELAXED, __HIP_MEMORY_SCOPE_WORKGROUP);
}
DI void lds_wait(char* p, u32 n) {
  while (__builtin_amdgcn_readfirstlane(__hip_atomic_load((LDS_AS u32*)p, __ATOMIC_RELAXED, __HIP_MEMORY_SCOPE_WORKGROUP)) < n) {}
  asm volatile("" ::: "memory");
}
constexpr int K1_F40 = 10272, K1_TW = K1_F40 + 18432, K1_R16 = K1_TW + 6144, K1_LDS = K1_R16 + 4096 + 256 + 16;
extern "C" __global__ __launch_bounds__(512) void k1_kernel(const u16* __restrict__ x, const u16* __restrict__ wr,
                                                            const u16* __restrict__ wi, char* __restrict__ ws,
                                                            const u16* __restrict__ w_sp, const u16* w_in, const u16* w_out,
                                                            const u16* b_sp, u16* out) {
  __shared__ __attribute__((aligned(16))) char smem[K1_LDS];
  const int tid = threadIdx.x, lane = tid & 63;
  const int wave = __builtin_amdgcn_readfirstlane(tid >> 6);
  const int li = lane & 15, g = lane >> 4;
  if (blockIdx.x < 8 && threadIdx.x == 0) g_t[blockIdx.x] = __builtin_amdgcn_s_memrealtime();
  char* scratch = smem + K1_R16 + 4096;
  char* flags = scratch + 256;
  if (wave == 7) CODE_TOUCH(k1_kernel, end_kernel, (blockIdx.x >> 3) & 7)
  PINS(x); PINS(wr); PINS(wi); PINS(ws); PINS(w_sp);
  auto constants = [&]() {
#pragma unroll
    for (int it = 0; it < 2; ++it) {
      const int item = wave + 8 * it;
      if (it == 0 || wave == 0) {
        const int to = item / 3, tk = item - 3 * to;
        const int kb = 16 * to + li;
        const float s8 = (tk < 2 || g < 2) ? 0.125f : 0.f;
        u4v fre, fim;
#pragma unroll
        for (int jj = 0; jj < 4; ++jj) {
          const float ang = (float)(kb * (16 * tk + 4 * g + jj)) * (1.0f / 40.0f);
          const float c = cosr(ang) * s8, s = sinr(ang) * s8;
          fre[jj] = pk_h(c, s);
          fim[jj] = pk_h(-s, c);
        }
        *(u4v*)(smem + K1_F40 + item * 2048 + lane * 16) = fre;
        *(u4v*)(smem + K1_F40 + item * 2048 + 1024 + lane * 16) = fim;
      } else if (wave < 4) {
        const int t = wave - 1;
        f4 twr, twi;
#pragma unroll
        for (int r = 0; r < 4; ++r) {
          const float ang = (float)((16 * t + 4 * g + r) * li) * (1.0f / 640.0f);
          twr[r] = cosr(ang) * 0.125f; twi[r] = -sinr(ang) * 0.125f;
        }
        *(f4*)(smem + K1_TW + t * 2048 + lane * 16) = twr;
        *(f4*)(smem + K1_TW + t * 2048 + 1024 + lane * 16) = twi;
      } else {
        const int ph = 2 * (wave - 4) + (lane >> 5);
        float cs[8];
#pragma unroll
        for (int jj = 0; jj < 8; ++jj) cs[jj] = cosr((float)(((8 * (g & 1) + jj) * li + 2 * ph) & 15) * 0.0625f);
        *(u4v*)(smem + K1_R16 + (wave - 4) * 1024 + lane * 16) = u4v{pk_h(cs[0], cs[1]), pk_h(cs[2], cs[3]), pk_h(cs[4], cs[5]), pk_h(cs[6], cs[7])};
      }
    }
    if (tid < 4) { *(u32*)(smem + 10240 + tid * 4) = 0u; *(u32*)(flags + tid * 4) = 0u; }
    asm volatile("s_waitcnt lgkmcnt(0)" ::: "memory");
    __builtin_amdgcn_s_barrier();
    asm volatile("" ::: "memory");
  };
  if (wave >= 4) {
    const int slotid = (blockIdx.x >> 3) * 3 + (wave - 4);
    const bool act = wave < 7 && slotid < 84;
    const int cp = blockIdx.x & 7, cc = act ? slotid >> 2 : 0, qt = slotid & 3;
    const int c0 = cc < 20 ? cc * 16 : 305;
    const bool mir = cp > 4;
    const bool sp = (cp & 3) == 0;
    const int e = mir ? 255 - (qt * 64 + lane) : qt * 64 + lane;
    const size_t roff = (size_t)(cp + 8 * e) * DF + c0;
    const U4U* pr = (const U4U*)(wr + roff);
    const U4U* pi = (const U4U*)(wi + roff);
    U4U r0, r1, i0, i1;
    __builtin_amdgcn_s_sleep(3);
    if (act) { r0 = pr[0]; r1 = pr[1]; i0 = pi[0]; i1 = pi[1]; }
    SB();
    constants();
    if (!act) {
      if (blockIdx.x == 255 && tid == 256) g_args = Args{ws, x, w_sp, w_in, w_out, b_sp, out, nullptr};
      if (blockIdx.x >= 224 && tid == 448) ((u32*)(ws + WS_WMAX))[blockIdx.x] = 0u;
      WAITV(0);
      return;
    }
    const u32 flip = mir ? 0x80000000u : 0u;
    const int q1 = (mir ? 8 - cp : cp) - 1;
    const int kb = mir ? 39 - cc : cc;
    const int t1 = sp ? 1920 + 32 * cc + (cp >> 2) : (q1 * 160 + (kb >> 2) * 16 + (mir ? 16 : 0)) * 4 + (kb & 3);
    const int step = sp ? 2 : (mir ? -4 : 4);
    const int t0 = mir ? (q1 * 160 + ((kb + 1) >> 2) * 16) * 4 + ((kb + 1) & 3) : t1;
    const bool edge = (cc == 0 || cc == 20);
    const __amdgpu_buffer_rsrc_t rs = ws_rsrc(ws);
    const u32 slot = (u32)WS_WP + (qt * 16 + li) * 16;
    u32 doff[4];
#pragma unroll
    for (int m = 0; m < 4; ++m) {
      const int jc = 4 * m + g;
      doff[m] = slot + (u32)(t1 + step * jc) * 1024u;
      if (cc == 20) doff[m] = DROP;
    }
    if (g == 0 && cc < 20) doff[0] = (mir && edge) ? DROP : slot + (u32)t0 * 1024u;
    if (g == 3 && cc == 20 && !mir) doff[3] = slot + (u32)t0 * 1024u;
    u32 rr[8] = {r0.a, r0.b, r0.c, r0.d, r1.a, r1.b, r1.c, r1.d};
    u32 ii[8] = {i0.a, i0.b, i0.c, i0.d, i1.a, i1.b, i1.c, i1.d};
    u32 vals[16];
#pragma unroll
    for (int j = 0; j < 16; ++j) {
      u32 a = rr[j >> 1], b = ii[j >> 1];
      vals[j] = ((j & 1) ? __builtin_amdgcn_perm(b, a, 0x07060302u) : __builtin_amdgcn_perm(b, a, 0x05040100u)) ^ flip;
    }
    if (edge && (mir || sp)) {
      const u32 v0 = cc < 20 ? vals[0] : vals[15];
      const int e2 = (cp == 0) ? ((256 - e) & 255) : 255 - e;
      const int dpos2 = ((e2 & 15) + 16 * (e2 >> 6)) * 4 + ((e2 >> 4) & 3);
      const int cls = mir ? 8 - cp : cp;
      ((u32*)(ws + WS_W2))[(cls * 2 + (cc == 20)) * 256 + dpos2] = v0 ^ flip ^ 0x80000000u;
    }
    u32 mx = 0;
#pragma unroll
    for (int j = 0; j < 16; ++j) {
      const u32 a = vals[j] & 0x7fff7fffu;
      mx = max(mx, max(a & 0xffffu, a >> 16));
    }
#pragma unroll
    for (int m = 0; m < 4; ++m) {
      auto p02 = __builtin_amdgcn_permlane32_swap(vals[4 * m], vals[4 * m + 2], false, false);
      auto p13 = __builtin_amdgcn_permlane32_swap(vals[4 * m + 1], vals[4 * m + 3], false, false);
      auto q01 = __builtin_amdgcn_permlane16_swap(p02[0], p13[0], false, false);
      auto q23 = __builtin_amdgcn_permlane16_swap(p02[1], p13[1], false, false);
      st16(rs, doff[m], u4v{q01[0], q01[1], q23[0], q23[1]});
    }
    mx = max64u(mx);
    if (lane == 0) {
      __hip_atomic_fetch_max((LDS_AS u32*)(flags + 4), mx, __ATOMIC_RELAXED, __HIP_MEMORY_SCOPE_WORKGROUP);
      asm volatile("s_waitcnt lgkmcnt(0)" ::: "memory");
      if (__hip_atomic_fetch_add((LDS_AS u32*)(flags + 8), 1u, __ATOMIC_RELAXED, __HIP_MEMORY_SCOPE_WORKGROUP) == 2u)
        ((u32*)(ws + WS_WMAX))[blockIdx.x] = __hip_atomic_load((LDS_AS u32*)(flags + 4), __ATOMIC_RELAXED, __HIP_MEMORY_SCOPE_WORKGROUP);
    }
    return;
  }
  const int b = (blockIdx.x & 7) * 32 + (blockIdx.x >> 3);
  const int a = 2 * wave + (lane >> 5), j = lane & 31;
  const u16* xr = x + (size_t)(256 * a + b) * D;
  const u4v x0 = *(const u4v*)(xr + 8 * j);
  const u4v x1 = *(const u4v*)(xr + 256 + 8 * j);
  const u2v x2 = *(const u2v*)(xr + 512 + 4 * j);
  SB();
  constants();
  const int q = wave;
  u4v bre[4], bim[4], fre[9], fim[9];
  f4 twr[3], twi[3];
  u32 toff[3][4];
  {
    const int a0 = g >> 1;
#pragma unroll
    for (int ks = 0; ks < 4; ++ks) {
      const int ph = (q == 0) ? (a0 ? 1 : 7) : (2 * ks + a0) * q;
      bre[ks] = *(const u4v*)(smem + K1_R16 + (ph & 7) * 512 + (lane & 31) * 16);
      bim[ks] = *(const u4v*)(smem + K1_R16 + ((ph + 2) & 7) * 512 + (lane & 31) * 16);
    }
#pragma unroll
    for (int i = 0; i < 9; ++i) {
      fre[i] = *(const u4v*)(smem + K1_F40 + i * 2048 + lane * 16);
      fim[i] = *(const u4v*)(smem + K1_F40 + i * 2048 + 1024 + lane * 16);
    }
    const float qs = q == 0 ? 0.70710678118f : 1.f;
#pragma unroll
    for (int t = 0; t < 3; ++t) {
      twr[t] = *(const f4*)(smem + K1_TW + t * 2048 + lane * 16) * qs;
      twi[t] = *(const f4*)(smem + K1_TW + t * 2048 + 1024 + lane * 16) * qs;
      PIN(twr[t]); PIN(twi[t]);
#pragma unroll
      for (int r = 0; r < 4; ++r) {
        const int k2 = li + 16 * (16 * t + 4 * g + r);
        const int pos = k2 <= 320 ? 2 * k2 : 2 * (640 - k2) + 1;
        toff[t][r] = (u32)WS_T + (u32)b * (TROW_UNITS * 16) + (q > 0 ? ((q - 1) * 160 + 64 * t + lane) * 16 : 480 * 16 + pos * 4);
        if (t == 2 && lane >= 32) toff[t][r] = DROP;
        PIN(toff[t][r]);
      }
    }
#pragma unroll
    for (int ks = 0; ks < 4; ++ks) { PIN(bre[ks]); PIN(bim[ks]); }
#pragma unroll
    for (int i = 0; i < 9; ++i) { PIN(fre[i]); PIN(fim[i]); }
  }
  {
    f2 v[10];
#pragma unroll
    for (int i = 0; i < 4; ++i) { v[i] = f2{bflo(x0[i]), bfhi(x0[i])}; v[4 + i] = f2{bflo(x1[i]), bfhi(x1[i])}; }
    v[8] = f2{bflo(x2[0]), bfhi(x2[0])}; v[9] = f2{bflo(x2[1]), bfhi(x2[1])};
    ln_row(v);
    char* ub = smem + a * 1280;
    *(u4v*)(ub + 16 * j) = u4v{pk_h(v[0][0], v[0][1]), pk_h(v[1][0], v[1][1]), pk_h(v[2][0], v[2][1]), pk_h(v[3][0], v[3][1])};
    *(u4v*)(ub + 512 + 16 * j) = u4v{pk_h(v[4][0], v[4][1]), pk_h(v[5][0], v[5][1]), pk_h(v[6][0], v[6][1]), pk_h(v[7][0], v[7][1])};
    *(u2v*)(ub + 1024 + 8 * j) = u2v{pk_h(v[8][0], v[8][1]), pk_h(v[9][0], v[9][1])};
  }
  lds_signal(flags);
  lds_wait(flags, 4);
  u4v b2[3];
  {
    const int qq = li >> 2, p = li & 3;
    const char* abase = smem + (8 * g + qq) * 80 + 8 * p;
    u4v af[3][4];
#pragma unroll
    for (int t = 0; t < 3; ++t)
#pragma unroll
      for (int ks = 0; ks < 4; ++ks) {
        s4 lo = __builtin_amdgcn_ds_read_tr16_b64_v4i16((LDS_AS s4*)(abase + ks * 2560 + t * 32));
        s4 hi = __builtin_amdgcn_ds_read_tr16_b64_v4i16((LDS_AS s4*)(abase + ks * 2560 + 320 + t * 32));
        u2v l2 = __builtin_bit_cast(u2v, lo), h2v = __builtin_bit_cast(u2v, hi);
        af[t][ks] = u4v{l2[0], l2[1], h2v[0], h2v[1]};
      }
    SB();
    f4 are[3], aim[3];
#pragma unroll
    for (int t = 0; t < 3; ++t) {
      are[t] = f4{0, 0, 0, 0}; aim[t] = f4{0, 0, 0, 0};
#pragma unroll
      for (int ks = 0; ks < 4; ++ks) {
        are[t] = mfma_h(af[t][ks], bre[ks], are[t]);
        aim[t] = mfma_h(af[t][ks], bim[ks], aim[t]);
      }
    }
#pragma unroll
    for (int t = 0; t < 3; ++t) b2[t] = cmul_pack<false>(are[t], aim[t], twr[t], twi[t]);
  }
  const __amdgpu_buffer_rsrc_t rs = ws_rsrc(ws);
  f4 ore[3], oim[3];
#pragma unroll
  for (int tk = 0; tk < 3; ++tk)
#pragma unroll
    for (int to = 0; to < 3; ++to) {
      ore[to] = mfma_h(fre[to * 3 + tk], b2[tk], tk ? ore[to] : f4{0, 0, 0, 0});
      oim[to] = mfma_h(fim[to * 3 + tk], b2[tk], tk ? oim[to] : f4{0, 0, 0, 0});
    }
  u4v o[3];
#pragma unroll
  for (int to = 0; to < 3; ++to)
#pragma unroll
    for (int r = 0; r < 4; ++r) o[to][r] = pk_h(ore[to][r], oim[to][r]);
  if (q > 0) {
#pragma unroll
    for (int to = 0; to < 3; ++to) st16(rs, toff[to][0], o[to]);
  } else {
#pragma unroll
    for (int to = 0; to < 3; ++to)
#pragma unroll
      for (int r = 0; r < 4; ++r) __builtin_amdgcn_raw_buffer_store_b32(o[to][r], rs, toff[to][r], 0, 0);
    __builtin_amdgcn_raw_buffer_store_b32(lane ? o[1][0] : o[0][0], rs, lane == 0 ? toff[0][0] + 4 : (lane == 16 ? toff[1][0] + 4 : DROP), 0, 0);
  }
}

template <int NT, int LDW, int NCG, int NTASK, bool PERM>
DI void retile(const u16* __restrict__ W, __amdgpu_buffer_rsrc_t rs, u32 dstbase, int wid, int nw, char* lds, int lane) {
  const int li = lane & 15, g = lane >> 4, q = li >> 2, p = li & 3;
  u32 dst[NT];
#pragma unroll
  for (int i = 0; i < NT; ++i) {
    const int task = wid + i * nw;
    const int t = task < NTASK ? task : 0;
    const int ks = t / NCG, cg = t - ks * NCG;
    const u16* src = W + (size_t)(ks * 32 + (lane >> 3)) * LDW + cg * 64 + (lane & 7) * 8;
#pragma unroll
    for (int j = 0; j < 4; ++j) GLDS16(src + (size_t)j * 8 * LDW, lds + i * 4096 + j * 1024);
    dst[i] = task < NTASK ? dstbase + (u32)t * 4096u + lane * 16 : DROP;
  }
  const char* rb = lds + (PERM ? 4 * g + q : 8 * g + q) * 128 + 8 * p;
  WAITV(0);
#pragma unroll
  for (int i = 0; i < NT; ++i)
#pragma unroll
    for (int f = 0; f < 4; ++f) {
      s4 lo = __builtin_amdgcn_ds_read_tr16_b64_v4i16((LDS_AS s4*)(rb + i * 4096 + f * 32));
      s4 hi = __builtin_amdgcn_ds_read_tr16_b64_v4i16((LDS_AS s4*)(rb + i * 4096 + f * 32 + (PERM ? 16 : 4) * 128));
      u2v a = __builtin_bit_cast(u2v, lo), b = __builtin_bit_cast(u2v, hi);
      st16(rs, dst[i] + f * 1024, u4v{a[0], a[1], b[0], b[1]});
    }
}
constexpr int NRT1 = 20 * 27, NRT3 = 27 * 10;
constexpr int K2_FFT_BLOCKS = 168, K2_PREP_BLOCKS = 88;

extern "C" __global__ __launch_bounds__(512, 2) void k2_kernel() {
  LOAD_ARGS();
  char* ws = ap_->ws; const u16* w_in = ap_->w_in; const u16* w_out = ap_->w_out; const u16* w_sp = ap_->w_sp;
  PINS(ws); PINS(w_in); PINS(w_out); PINS(w_sp);
  const u32* T = (const u32*)(ws + WS_T); const u32* Wp = (const u32*)(ws + WS_WP); const u32* W2 = (const u32*)(ws + WS_W2);
  const u32* wmax = (const u32*)(ws + WS_WMAX); u32* Tp = (u32*)(ws + WS_TP); float* unscale = (float*)(ws + WS_UNS);
  u32* Wt1 = (u32*)(ws + WS_WT1); u32* Wt3 = (u32*)(ws + WS_WT3); u32* stats = (u32*)(ws + WS_STATS);
  extern __shared__ __attribute__((aligned(16))) char smem[];
  const int tid = threadIdx.x, lane = tid & 63;
  const int wave = __builtin_amdgcn_readfirstlane(tid >> 6);
  const int li = lane & 15, g = lane >> 4;
  if (blockIdx.x >= K2_FFT_BLOCKS) {
    const int wid = (blockIdx.x - K2_FFT_BLOCKS) * 8 + wave;
    const __amdgpu_buffer_rsrc_t rs = ws_rsrc(ws);
    if (wid < 256) st16(rs, (u32)WS_STATS + wid * 1024 + lane * 16, u4v{~0u, ~0u, ~0u, ~0u});
    else if (wid < 264) st16(rs, (u32)WS_CNT + (wid - 256) * 1024 + lane * 16, u4v{0u, 0u, 0u, 0u});
    const int pb = blockIdx.x - K2_FFT_BLOCKS;
    const int task = wave < 6 ? pb * 6 + wave : (wave == 6 && pb < 12 ? 528 + pb : NRT1);
    if (task < NRT1) retile<1, FF, 27, NRT1, false>(w_in, rs, (u32)WS_WT1, task, 0, smem + wave * 8192, lane);
    return;
  }
  const int P = (blockIdx.x & 7) * 21 + (blockIdx.x >> 3);
  if (P > 160) return;
  const int u = wave & 3, hh = wave >> 2;
  const int U = 4 * P + u;
#pragma unroll
  for (int i = 0; i < 2; ++i) {
    const int bb = (wave * 2 + i) * 16 + (lane >> 2);
    GLDS16(T + ((size_t)bb * TROW_UNITS + 4 * P + ((lane & 3) ^ ((lane >> 4) & 3))) * 4, smem + (wave * 2 + i) * 1024);
  }
  const u4v mxv = *(const u4v*)(wmax + lane * 4);
  u4v Wv[2];
#pragma unroll
  for (int t = 0; t < 2; ++t) Wv[t] = *(const u4v*)(Wp + ((size_t)((U < NUNITS ? U : 0) * 4 + 2 * hh + t) * 64 + lane) * 4);
  const bool special = P >= 120;
  const int cB = special ? 4 : (P / 40 + 1);
  const int ur = special ? U - 480 : U % 160;
  const bool e320 = special ? ur == 160 : ur == 80;
  const bool edge = hh == 0 && (ur == 0 || e320);
  u4v w2, w4;
  if (edge) {
    w2 = *(const u4v*)(W2 + ((special ? 0 : cB) * 2 + (e320 ? 1 : 0)) * 256 + lane * 4);
    w4 = *(const u4v*)(W2 + (4 * 2 + (e320 ? 1 : 0)) * 256 + lane * 4);
  }
  {
    char* cb = smem + 16384 + wave * 2048 + lane * 16;
    const int cc = wave < 4 ? cB : 0;
    const float inv = u < 2 ? (1.0f / 128.0f) : (1.0f / 2048.0f);
    const float amp = u == 0 ? 1.f : ((u == 1 && special) ? 0.125f : 0.25f);
    f4 c, s;
#pragma unroll
    for (int jj = 0; jj < 4; ++jj) {
      const int a = (u & 1) ? li * (cc + 8 * (4 * g + jj)) : (4 * g + jj) * (cc + 8 * li);
      const float ang = (float)a * inv;
      c[jj] = cosr(ang) * amp; s[jj] = sinr(ang) * amp;
    }
    if (u < 2) {
      *(u4v*)(cb) = u4v{pk_h(c[0], s[0]), pk_h(c[1], s[1]), pk_h(c[2], s[2]), pk_h(c[3], s[3])};
      *(u4v*)(cb + 1024) = u4v{pk_h(-s[0], c[0]), pk_h(-s[1], c[1]), pk_h(-s[2], c[2]), pk_h(-s[3], c[3])};
    } else {
      *(f4*)(cb) = c;
      *(f4*)(cb + 1024) = s;
    }
  }
  asm volatile("s_waitcnt lgkmcnt(0)" ::: "memory");
  __builtin_amdgcn_s_barrier();
  asm volatile("" ::: "memory");
  u4v Fre, Fim, P1re[2], P1im[2], P4re[2], P4im[2];
  f4 tfr[2], tfs[2], tir[2], tis[2];
  {
    const char* cb = smem + 16384 + lane * 16;
    Fre = *(const u4v*)(cb + 4 * 2048); Fim = *(const u4v*)(cb + 4 * 2048 + 1024);
#pragma unroll
    for (int c = 0; c < 2; ++c) {
      const char* cs = cb + ((c == 0 && special) ? 8192 : 0);
      P1re[c] = *(const u4v*)(cs); P1im[c] = *(const u4v*)(cs + 1024);
      P4re[c] = *(const u4v*)(cs + 2048); P4im[c] = *(const u4v*)(cs + 3072);
      tfr[c] = *(const f4*)(cs + 4096); tfs[c] = *(const f4*)(cs + 5120);
      tir[c] = *(const f4*)(cs + 6144); tis[c] = *(const f4*)(cs + 7168);
    }
    PIN(Fre); PIN(Fim);
#pragma unroll
    for (int c = 0; c < 2; ++c) { PIN(P1re[c]); PIN(P1im[c]); PIN(P4re[c]); PIN(P4im[c]); PIN(tfr[c]); PIN(tfs[c]); PIN(tir[c]); PIN(tis[c]); }
  }
  asm volatile("s_waitcnt vmcnt(0)" ::: "memory");
  __builtin_amdgcn_s_barrier();
  asm volatile("" ::: "memory");
  const int loff = (64 * g + li) * 64 + ((u ^ (li >> 2)) & 3) * 16 + hh * 8;
  u4v X[2];
#pragma unroll
  for (int jj = 0; jj < 4; ++jj) {
    const u2v L = *(const u2v*)(smem + loff + jj * 1024);
    X[0][jj] = L[0]; X[1][jj] = L[1];
  }
  if (special) {
#pragma unroll
    for (int jj = 0; jj < 4; ++jj) {
      const u32 au = X[0][jj], bu = X[1][jj] ^ 0x80000000u;
      const h2 A = __builtin_bit_cast(h2, au);
      const h2 Bc = __builtin_bit_cast(h2, bu);
      X[0][jj] = __builtin_bit_cast(u32, A + Bc);
      X[1][jj] = __builtin_bit_cast(u32, A - Bc);
    }
  }
  const f4 z4 = f4{0, 0, 0, 0};
  f4 re[2], im[2];
#pragma unroll
  for (int t = 0; t < 2; ++t) { re[t] = mfma_h(X[t], P1re[t], z4); im[t] = mfma_h(X[t], P1im[t], z4); }
  const u32 mx = max64u(max(max(mxv[0], mxv[1]), max(mxv[2], mxv[3])));
  int E = (int)(mx >> 7);
  E = E < 1 ? 1 : (E > 253 ? 253 : E);
  const float sig = __uint_as_float((u32)(254 - E) << 23) * 0.25f;
#pragma unroll
  for (int t = 0; t < 2; ++t) {
    const u4v v = cmul_pack<false>(re[t], im[t], tfr[t], -tfs[t]);
    re[t] = mfma_h(Fre, v, z4); im[t] = mfma_h(Fim, v, z4);
  }
  f4 wr4[2], wi4[2];
#pragma unroll
  for (int t = 0; t < 2; ++t)
#pragma unroll
    for (int r = 0; r < 4; ++r) { wr4[t][r] = bflo(Wv[t][r]); wi4[t][r] = bfhi(Wv[t][r]); }
  if (edge) {
#pragma unroll
    for (int r = 0; r < 4; ++r) {
      wr4[0][r] = (wr4[0][r] + bflo(w2[r])) * 0.5f; wi4[0][r] = (wi4[0][r] + bfhi(w2[r])) * 0.5f;
      if (special) { wr4[1][r] = (wr4[1][r] + bflo(w4[r])) * 0.5f; wi4[1][r] = (wi4[1][r] + bfhi(w4[r])) * 0.5f; }
    }
    if (U == 0 && lane == 0) unscale[0] = __uint_as_float((u32)E << 23);
  }
#pragma unroll
  for (int t = 0; t < 2; ++t) {
    const u4v v = cmul_pack<true>(re[t], im[t], wr4[t] * sig, wi4[t] * sig);
    im[t] = mfma_h(v, Fre, z4); re[t] = mfma_h(v, Fim, z4);
  }
#pragma unroll
  for (int t = 0; t < 2; ++t) {
    const u4v v = cmul_pack<true>(re[t], im[t], tir[t], tis[t]);
    im[t] = mfma_h(P4re[t], v, z4); re[t] = mfma_h(P4im[t], v, z4);
  }
  if (special) {
    const f4 yr = re[0], yi = im[0], zr = re[1], zi = im[1];
    re[0] = yr + zr; im[0] = yi + zi;
    re[1] = yr - zr; im[1] = zi - yi;
  }
#pragma unroll
  for (int r = 0; r < 4; ++r) *(u2v*)(smem + loff + r * 1024) = u2v{pk_h(re[0][r], im[0][r]), pk_h(re[1][r], im[1][r])};
  asm volatile("s_waitcnt lgkmcnt(0)" ::: "memory");
  __builtin_amdgcn_s_barrier();
  asm volatile("" ::: "memory");
  const __amdgpu_buffer_rsrc_t rs = ws_rsrc(ws);
#pragma unroll
  for (int i = 0; i < 2; ++i) {
    const u4v o = *(const u4v*)(smem + (wave * 2 + i) * 1024 + lane * 16);
    st16p(rs, (u32)WS_TP + P * 16384 + (wave * 2 + i) * 1024 + (lane & ~3) * 16 + ((lane & 3) ^ ((lane >> 4) & 3)) * 16, o);
  }
  { char* scratch = smem + 49152; if (wave == 0) CODE_TOUCH(k3_kernel, g1_kernel, 0) }
  WAITV(0);
}

extern "C" __global__ __launch_bounds__(256, 2) void k3_kernel() {
  LOAD_ARGS();
  char* ws = ap_->ws; const u16* x = ap_->x; const u16* w_sp = ap_->w_sp;
  PINS(x); PINS(ws); PINS(w_sp);
  const u32* Tp = (const u32*)(ws + WS_TP);
  constexpr int K3_Y = 10752, K3_CB = K3_Y + 10240;
  __shared__ __attribute__((aligned(16))) char smem[K3_CB + 8192 + 512];
  const int tid = threadIdx.x, lane = tid & 63;
  const int wave = __builtin_amdgcn_readfirstlane(tid >> 6);
  const int li = lane & 15, g = lane >> 4;
  const int b = (blockIdx.x & 7) * 32 + (blockIdx.x >> 3);
#pragma unroll
  for (int i = 0; i < 2; ++i) {
    int U = (wave * 2 + i) * 64 + lane;
    U = U < 480 ? U : 479;
    GLDS16(Tp + (size_t)(U >> 2) * 4096 + b * 16 + (U & 3) * 4, smem + (wave * 2 + i) * 1024);
  }
#pragma unroll
  for (int it = 0; it < 4; ++it) {
    const int i = it < 2 ? wave * 2 + it : 6 + it;
    if (it < 2 || wave == 3) {
      const int k2 = (lane >> 2) + 16 * (4 * i + (lane & 3));
      const int pos = k2 <= 320 ? 2 * k2 : 2 * (640 - k2) + 1;
      GLDS4(Tp + (size_t)(120 + (pos >> 4)) * 4096 + b * 16 + (pos & 15), smem + 8192 + i * 256);
    }
  }
  char* scratch = smem + K3_CB + 8192;
  if (wave == 0) GLDS4(ws + WS_UNS, scratch + 256);
  const int a = 2 * wave + (lane >> 5), j = lane & 31;
  const int row = 256 * a + b;
  const u16* xr = x + (size_t)row * D;
  u4v x0 = *(const u4v*)(xr + 8 * j);
  u4v x1 = *(const u4v*)(xr + 256 + 8 * j);
  u2v x2 = *(const u2v*)(xr + 512 + 4 * j);
  char* cb = smem + K3_CB;
  const int t = wave;
  u4v Bre[3], Bim[3];
  if (wave < 3) {
#pragma unroll
    for (int tk = 0; tk < 3; ++tk) {
      const float sc = (tk < 2 || g < 2) ? 1.f : 0.f;
#pragma unroll
      for (int jj = 0; jj < 4; ++jj) {
        const float ang = (float)((16 * tk + 4 * g + jj) * (16 * t + li)) * (1.0f / 40.0f);
        const float c = cosr(ang) * sc, s = sinr(ang) * sc;
        Bre[tk][jj] = pk_h(c, -s);
        Bim[tk][jj] = pk_h(s, c);
      }
      PIN(Bre[tk]); PIN(Bim[tk]);
    }
  } else {
#pragma unroll
    for (int tt = 0; tt < 3; ++tt) {
      f4 twr, twi;
#pragma unroll
      for (int jj = 0; jj < 4; ++jj) {
        const float ang = (float)((16 * tt + li) * (4 * g + jj)) * (1.0f / 640.0f);
        twr[jj] = cosr(ang) * 0.125f; twi[jj] = sinr(ang) * 0.125f;
      }
      *(f4*)(cb + tt * 2048 + lane * 16) = twr;
      *(f4*)(cb + tt * 2048 + 1024 + lane * 16) = twi;
    }
    u4v gre, gim;
#pragma unroll
    for (int jj = 0; jj < 4; ++jj) {
      const float ang = (float)(li * (4 * g + jj)) * 0.0625f;
      const float c = cosr(ang), s = sinr(ang);
      gre[jj] = pk_h(c, -s); gim[jj] = pk_h(s, c);
    }
    *(u4v*)(cb + 6144 + lane * 16) = gre;
    *(u4v*)(cb + 7168 + lane * 16) = gim;
  }
  asm volatile("s_waitcnt vmcnt(0)" ::: "memory");
  PIN(x0); PIN(x1); PIN(x2);
  __syncthreads();
  char* yb = smem + K3_Y;
  if (wave < 3) {
    const f4 z4 = f4{0, 0, 0, 0};
    u4v af[4][3];
#pragma unroll
    for (int q = 0; q < 4; ++q) {
      const char* base = smem + (q == 0 ? 8192 : (q - 1) * 2560);
#pragma unroll
      for (int tk = 0; tk < 3; ++tk) af[q][tk] = *(const u4v*)(base + (tk < 2 ? (64 * tk + lane) : (128 + (lane & 31))) * 16);
    }
    const f4 twr = *(const f4*)(cb + t * 2048 + lane * 16), twi = *(const f4*)(cb + t * 2048 + 1024 + lane * 16);
    const u4v Gre = *(const u4v*)(cb + 6144 + lane * 16), Gim = *(const u4v*)(cb + 7168 + lane * 16);
    const float usc = *(const float*)(scratch + 256);
    SB();
    f4 vre[4], vim[4];
#pragma unroll
    for (int q = 0; q < 4; ++q) {
      f4 re = z4, im = z4;
#pragma unroll
      for (int tk = 0; tk < 3; ++tk) {
        re = mfma_h(af[q][tk], Bre[tk], re);
        im = mfma_h(af[q][tk], Bim[tk], im);
      }
      vre[q] = re; vim[q] = im;
    }
#pragma unroll
    for (int q = 0; q < 4; ++q) {
      const u4v v = cmul_pack<false>(vre[q], vim[q], twr, twi);
      vre[q] = mfma_h(v, Gre, z4);
      vim[q] = mfma_h(v, Gim, z4);
    }
    const float fs = usc * (2.0f * 1024.0f * 128.0f / (2048.0f * 640.0f));
    const float fh = fs * 0.70710678118f;
    const f4 E = (vre[0] + vim[0]) * fs, O = (vre[0] - vim[0]) * fs;
    const f4 s13 = vre[1] + vre[3], dx = vre[1] - vre[3], sy = vim[1] + vim[3], dy = vim[3] - vim[1];
    const f4 Ep = E + vre[2] * fs, Em = E - vre[2] * fs, Om = O - vim[2] * fs, Op = O + vim[2] * fs;
    const f4 A = dx - sy, B = dx + sy;
    f4 y[8];
    y[0] = Ep + s13 * fs; y[4] = Ep - s13 * fs; y[2] = Em + dy * fs; y[6] = Em - dy * fs;
    y[1] = Om + A * fh; y[5] = Om - A * fh; y[3] = Op - B * fh; y[7] = Op + B * fh;
    if (t < 2 || lane < 32) {
      char* yp = yb + (40 * li + 16 * t + 4 * g) * 2;
#pragma unroll
      for (int aa = 0; aa < 8; ++aa) *(u2v*)(yp + aa * 1280) = u2v{pk_bf(y[aa][0], y[aa][1]), pk_bf(y[aa][2], y[aa][3])};
    }
  }
  else {
    CODE_TOUCH(g1_kernel, g2_kernel, (blockIdx.x >> 3) & 1)
#pragma unroll
    for (int i = 0; i < 2; ++i) touch((const char*)w_sp + ((size_t)(512 + i * 256 + blockIdx.x) * 64 + lane) * 128);
  }
  asm volatile("s_waitcnt lgkmcnt(0)" ::: "memory");
  __builtin_amdgcn_s_barrier();
  asm volatile("" ::: "memory");
  {
    const __amdgpu_buffer_rsrc_t rs = ws_rsrc(ws);
    const u4v y0 = *(const u4v*)(yb + a * 1280 + 16 * j);
    const u4v y1 = *(const u4v*)(yb + a * 1280 + 512 + 16 * j);
    const u2v y2 = *(const u2v*)(yb + a * 1280 + 1024 + 8 * j);
    u32 hx[10];
#pragma unroll
    for (int i = 0; i < 4; ++i) {
      hx[i] = pk_bf(bflo(x0[i]) + bflo(y0[i]), bfhi(x0[i]) + bfhi(y0[i]));
      hx[4 + i] = pk_bf(bflo(x1[i]) + bflo(y1[i]), bfhi(x1[i]) + bfhi(y1[i]));
    }
    hx[8] = pk_bf(bflo(x2[0]) + bflo(y2[0]), bfhi(x2[0]) + bfhi(y2[0]));
    hx[9] = pk_bf(bflo(x2[1]) + bflo(y2[1]), bfhi(x2[1]) + bfhi(y2[1]));
    auto pairup = [&](u32 v0, u32 v1) {
      return u4v{v0, v1, (u32)__builtin_amdgcn_update_dpp(0, (int)v0, 0xB1, 0xf, 0xf, true), (u32)__builtin_amdgcn_update_dpp(0, (int)v1, 0xB1, 0xf, 0xf, true)};
    };
    const u32 drop = (lane & 1) ? DROP : 0u;
    const u32 ho = (u32)WS_H + row * (D * 2);
    st16(rs, ho + 16 * j, u4v{hx[0], hx[1], hx[2], hx[3]});
    st16(rs, ho + 512 + 16 * j, u4v{hx[4], hx[5], hx[6], hx[7]});
    st16(rs, (ho + 1024 + 8 * j) | drop, pairup(hx[8], hx[9]));
    f2 v[10];
#pragma unroll
    for (int i = 0; i < 10; ++i) v[i] = f2{bflo(hx[i]), bfhi(hx[i])};
    ln_row(v);
#pragma unroll
    for (int i = 0; i < 10; ++i) hx[i] = pk_bf(v[i][0], v[i][1]);
    const u32 lo = (u32)WS_L2 + (row >> 4) * 20480 + (row & 15) * 64;
    st16(rs, lo + (j >> 2) * 1024 + (j & 3) * 16, u4v{hx[0], hx[1], hx[2], hx[3]});
    st16(rs, lo + (8 + (j >> 2)) * 1024 + (j & 3) * 16, u4v{hx[4], hx[5], hx[6], hx[7]});
    st16(rs, (lo + (16 + (j >> 3)) * 1024 + (j & 7) * 8) | drop, pairup(hx[8], hx[9]));
  }
  WAITV(0);
}

constexpr int FPD = 2;
#include <type_traits>
typedef unsigned long long u64;
typedef __attribute__((address_space(1))) unsigned long long gu64;
#define WAITL0() do { __builtin_amdgcn_sched_barrier(0); asm volatile("s_waitcnt lgkmcnt(0)" ::: "memory"); __builtin_amdgcn_sched_barrier(0); } while (0)
#define BAR() do { asm volatile("" ::: "memory"); __builtin_amdgcn_sched_barrier(0); __builtin_amdgcn_s_barrier(); __builtin_amdgcn_sched_barrier(0); asm volatile("" ::: "memory"); } while (0)
#define SGB(mask, n) __builtin_amdgcn_sched_group_barrier(mask, n, 0)
DI f4 mfma_b(u4v a, u4v b, f4 c) {
  return __builtin_amdgcn_mfma_f32_16x16x32_bf16(__builtin_bit_cast(bf8, a), __builtin_bit_cast(bf8, b), c, 0, 0, 0);
}
DI f2 gelu2(f2 x) {
  const float k1 = -2.0f * 0.7978845608028654f * 1.4426950408889634f;
  const float k2 = k1 * 0.044715f;
  f2 p = x * x * k2 + k1;
  f2 a = x * p;
  f2 e = f2{__builtin_amdgcn_exp2f(a[0]), __builtin_amdgcn_exp2f(a[1])} + 1.0f;
  f2 r = f2{__builtin_amdgcn_rcpf(e[0]), __builtin_amdgcn_rcpf(e[1])};
  return x * r;
}
DI u2v gelu4(f4 a) {
  f2 g0 = gelu2(f2{a[0], a[1]}), g1 = gelu2(f2{a[2], a[3]});
  return u2v{pk_bf(g0[0], g0[1]), pk_bf(g1[0], g1[1])};
}
template <int N> DI void waitv() { asm volatile("s_waitcnt vmcnt(%0)" ::"n"(N) : "memory"); }

constexpr int G1_DEPTH = 3, G1_AD = 5;
extern "C" __global__ __launch_bounds__(512) void g1_kernel() {
  LOAD_ARGS();
  char* ws = ap_->ws; const u16* w_sp = ap_->w_sp; const u16* w_out = ap_->w_out;
  PINS(ws); PINS(w_sp); PINS(w_out);
  const u32* l2f = (const u32*)(ws + WS_L2); const u32* Wt1 = (const u32*)(ws + WS_WT1);
  u32* z1f = (u32*)(ws + WS_Z1); u16* z2t = (u16*)(ws + WS_Z2); u64* stats = (u64*)(ws + WS_STATS);
  __shared__ __attribute__((aligned(16))) char smem[140 * 1024 + 4 * 4608 + 256];
  const int tid = threadIdx.x, lane = tid & 63;
  const int wave = __builtin_amdgcn_readfirstlane(tid >> 6);
  const int li = lane & 15, g = lane >> 4;
  const int xcd = blockIdx.x & 7, idx = blockIdx.x >> 3;
  const int mt = (xcd >> 1) * 4 + (idx >> 3), nb = (xcd & 1) * 8 + (idx & 7);
  const int row0 = 1024 * (mt & 1) + 32 * (mt >> 1);
  const int zs = nb < 6 ? 4 * nb : 24 + 3 * (nb - 6);
  const int z1s = nb < 6 ? 3 * nb : (nb < 12 ? 18 + 4 * (nb - 6) : 42 + 3 * (nb - 12));
  const int type = nb < 6 ? 0 : (nb < 12 ? 1 : 2);
  if (wave >= 4) {
    const int lw = wave - 4;
    auto slot_frag = [&](int s) { return s < 3 ? 54 + zs + s : (s == 3 ? (type == 0 ? 54 + zs + 3 : (type == 1 ? z1s + 3 : z1s)) : z1s + s - 4); };
    const u32* bsrc[4];
#pragma unroll
    for (int j = 0; j < 4; ++j) {
      int i = lw + 4 * j; i = i < 14 ? i : 0;
      const int kk = i / 7, s = i - 7 * kk;
      bsrc[j] = Wt1 + (size_t)(kk * 108 + slot_frag(s)) * 256 + lane * 4;
    }
    auto stage = [&](int kt) {
      char* sb = smem + kt * 14336 + lw * 1024;
#pragma unroll
      for (int j = 0; j < 3; ++j) GLDS16(bsrc[j] + (size_t)kt * 2 * 108 * 256, sb + j * 4096);
      if (lw < 2) GLDS16(bsrc[3] + (size_t)kt * 2 * 108 * 256, sb + 3 * 4096);
    };
#pragma unroll
    for (int s = 0; s < G1_DEPTH; ++s) stage(s);
    for (int kt = 0; kt < 10 - G1_DEPTH; ++kt) {
      if (lw < 2) waitv<4 * (G1_DEPTH - 1)>(); else waitv<3 * (G1_DEPTH - 1)>();
      BAR();
      stage(kt + G1_DEPTH);
    }
    if (lw < 2) waitv<8>(); else waitv<6>(); BAR();
    if (lw < 2) waitv<4>(); else waitv<3>(); BAR();
    waitv<0>();
    char* scratch = smem + 140 * 1024 + 4 * 4608;
    if (lw < 2) touch((const char*)w_sp + ((size_t)(blockIdx.x * 2 + lw) * 64 + lane) * 128);
    if (lw >= 2) {
      CODE_TOUCH(g2_kernel, end_kernel, lw - 2)
      const int task = (lw - 2) * 256 + blockIdx.x;
      BAR();
      if (task < NRT3) retile<1, D, 10, NRT3, true>(w_out, ws_rsrc(ws), (u32)WS_WT3, task, 0, smem + lw * 4096, lane);
    } else {
      BAR();
    }
    waitv<0>();
    return;
  }
  const int rg0 = (row0 + wave * 256) >> 4;
  const u32* asrc = l2f + (size_t)rg0 * 20 * 256 + li * 16 + g * 4;
  u4v fa[G1_AD][2];
#pragma unroll
  for (int k = 0; k < G1_AD; ++k) { fa[k][0] = *(const u4v*)(asrc + k * 256); fa[k][1] = *(const u4v*)(asrc + (20 + k) * 256); __builtin_amdgcn_sched_barrier(0); }
  f4 acc[7][2];
#pragma unroll
  for (int s = 0; s < 7; ++s) { acc[s][0] = f4{0, 0, 0, 0}; acc[s][1] = f4{0, 0, 0, 0}; }
  u4v fw[2][7];
  const char* wb = smem + lane * 16;
  auto rd = [&](int k) {
#pragma unroll
    for (int s = 0; s < 7; ++s) fw[k & 1][s] = *(const u4v*)(wb + (k * 7 + s) * 1024);
  };
  auto mm = [&](int k) {
#pragma unroll
    for (int s = 0; s < 7; ++s) {
      acc[s][0] = mfma_b(fw[k & 1][s], fa[k % G1_AD][0], acc[s][0]);
      acc[s][1] = mfma_b(fw[k & 1][s], fa[k % G1_AD][1], acc[s][1]);
    }
    if (k + G1_AD < 20) { fa[k % G1_AD][0] = *(const u4v*)(asrc + (k + G1_AD) * 256); fa[k % G1_AD][1] = *(const u4v*)(asrc + (20 + G1_AD + k) * 256); }
  };
  BAR(); rd(0);
#pragma unroll
  for (int k = 1; k < 20; ++k) {
    if (!(k & 1)) BAR(); else __builtin_amdgcn_sched_barrier(0);
    rd(k);
    mm(k - 1);
#pragma unroll
    for (int i = 0; i < 7; ++i) { SGB(0x100, 1); SGB(0x8, 2); }
  }
  __builtin_amdgcn_sched_barrier(0);
  mm(19);
  const int n2 = type == 0 ? 4 : 3;
  f4 zv[4][2];
  u2v z3[2];
  float lsum[2], lm2[2];
#pragma unroll
  for (int sf = 0; sf < 2; ++sf) {
    f2 s = f2{0.f, 0.f};
#pragma unroll
    for (int j = 0; j < 4; ++j) {
      const u2v t = gelu4(acc[j][sf]);
      if (j == 3) z3[sf] = t;
      zv[j][sf] = f4{bflo(t[0]), bfhi(t[0]), bflo(t[1]), bfhi(t[1])};
      if (j < 3 || type == 0) s += f2{zv[j][sf][0], zv[j][sf][1]} + f2{zv[j][sf][2], zv[j][sf][3]};
    }
    const float st = xsum_g(s[0] + s[1]);
    const float lmean = st * (type == 0 ? (1.0f / 64.0f) : (1.0f / 48.0f));
    f2 q = f2{0.f, 0.f};
#pragma unroll
    for (int j = 0; j < 4; ++j) {
      const f4 d = zv[j][sf] - lmean;
      if (j < 3 || type == 0) { q += f2{d[0], d[1]} * f2{d[0], d[1]}; q += f2{d[2], d[3]} * f2{d[2], d[3]}; }
    }
    lsum[sf] = st; lm2[sf] = xsum_g(q[0] + q[1]);
  }
  gu64* st = (gu64*)stats + (size_t)mt * 16 * 128 + wave * 32;
  if (lane < 32) {
    u32 a = __float_as_uint(g ? lsum[1] : lsum[0]), b = __float_as_uint(g ? lm2[1] : lm2[0]);
    if (a == ~0u) a = 0x7fc00000u;
    __hip_atomic_store(st + nb * 128 + lane, ((u64)b << 32) | a, __ATOMIC_RELAXED, __HIP_MEMORY_SCOPE_AGENT);
  }
  __builtin_amdgcn_sched_barrier(0);
  const __amdgpu_buffer_rsrc_t rs = ws_rsrc(ws);
  u4v z1o[3];
#pragma unroll
  for (int j = 0; j < 3; ++j) {
    const u2v a = gelu4(acc[4 + j][0]), b = gelu4(acc[4 + j][1]);
    z1o[j] = u4v{a[0], a[1], b[0], b[1]};
    PIN(z1o[j]);
  }
  __builtin_amdgcn_sched_barrier(0);
  u64 gv[2][4];
  for (int spin = 0; spin < (1 << 16); ++spin) {
    bool ok = true;
#pragma unroll
    for (int sf = 0; sf < 2; ++sf)
#pragma unroll
      for (int i = 0; i < 4; ++i) {
        gv[sf][i] = __hip_atomic_load(st + (4 * g + i) * 128 + sf * 16 + li, __ATOMIC_RELAXED, __HIP_MEMORY_SCOPE_AGENT);
        ok &= ((u32)gv[sf][i] != ~0u);
      }
    if (__all(ok)) break;
    __builtin_amdgcn_s_sleep(1);
  }
  {
    const u32 zo = (u32)WS_Z1 + ((rg0 >> 1) * 54 + z1s) * 1024 + lane * 16;
#pragma unroll
    for (int j = 0; j < 3; ++j) st16(rs, zo + j * 1024, z1o[j]);
    if (type == 1) st16(rs, zo + 3 * 1024, u4v{z3[0][0], z3[0][1], z3[1][0], z3[1][1]});
  }
  char* tb = smem + 140 * 1024 + wave * 4608;
#pragma unroll
  for (int sf = 0; sf < 2; ++sf) {
    float s = 0.f;
#pragma unroll
    for (int i = 0; i < 4; ++i) s += __uint_as_float((u32)gv[sf][i]);
    const float mean = xsum_g(s) * (1.0f / 864.0f);
    float q = 0.f;
#pragma unroll
    for (int i = 0; i < 4; ++i) {
      const bool big = (4 * g + i) < 6;
      const float dm = __uint_as_float((u32)gv[sf][i]) * (big ? (1.0f / 64.0f) : (1.0f / 48.0f)) - mean;
      q += __uint_as_float((u32)(gv[sf][i] >> 32)) + (big ? 64.f : 48.f) * dm * dm;
    }
    const float var = xsum_g(q) * (1.0f / 864.0f);
    const float mb = rbf(mean);
    const float rinv = __builtin_amdgcn_rcpf(rbf(__builtin_amdgcn_sqrtf(rbf(rbf(var) + 1e-5f))));
#pragma unroll
    for (int j = 0; j < 4; ++j) {
      const f4 o = (zv[j][sf] - mb) * rinv;
      *(u2v*)(tb + (sf * 16 + li) * 144 + j * 32 + g * 8) = u2v{pk_bf(o[0], o[1]), pk_bf(o[2], o[3])};
    }
  }
  {
    const int q = li >> 2, p = li & 3;
#pragma unroll
    for (int j = 0; j < 4; ++j) {
      s4 lo = __builtin_amdgcn_ds_read_tr16_b64_v4i16((LDS_AS s4*)(tb + (8 * g + q) * 144 + j * 32 + 8 * p));
      s4 hi = __builtin_amdgcn_ds_read_tr16_b64_v4i16((LDS_AS s4*)(tb + (8 * g + 4 + q) * 144 + j * 32 + 8 * p));
      u2v a = __builtin_bit_cast(u2v, lo), b = __builtin_bit_cast(u2v, hi);
      if (j < n2) st16(rs, (u32)WS_Z2 + (((zs + j) * 16 + li) * S + row0 + wave * 256 + 8 * g) * 2, u4v{a[0], a[1], b[0], b[1]});
    }
  }
}

typedef __attribute__((address_space(1))) unsigned int gu32;
constexpr int G2_NST = 7, G2_STB = 22 * 1024;
extern "C" __global__ __launch_bounds__(512) void g2_kernel() {
  LOAD_ARGS();
  char* ws = ap_->ws; const u16* w_sp = ap_->w_sp; const u16* b_sp = ap_->b_sp; u16* out = ap_->out;
  PINS(w_sp); PINS(b_sp); PINS(ws); PINS(out);
  const u16* z2t = (const u16*)(ws + WS_Z2); const u32* z1f = (const u32*)(ws + WS_Z1);
  __shared__ __attribute__((aligned(16))) char smem[G2_NST * G2_STB + 256];
  const int tid = threadIdx.x, lane = tid & 63;
  const int wave = __builtin_amdgcn_readfirstlane(tid >> 6);
  const int li = lane & 15, g = lane >> 4;
  const int xcd = blockIdx.x & 7, idx = blockIdx.x >> 3;
  const int tt = (xcd >> 1) * 8 + (idx >> 2), ft = (xcd & 1) * 4 + (idx & 3);
  if (wave >= 4) {
    const int lw = wave - 4;
    const u16* src[6];
    const int swz = ((lane & 7) ^ (lane >> 3)) * 8;
#pragma unroll
    for (int j = 0; j < 6; ++j) {
      int i = lw + 4 * j; i = i < 22 ? i : 8;
      if (j < 2) src[j] = w_sp + (size_t)(tt * 64 + 8 * i + (lane >> 3)) * S + swz;
      else { int r = ft * 112 + 8 * (i - 8) + (lane >> 3); r = r < FH ? r : FH - 1; src[j] = z2t + (size_t)r * S + swz; }
    }
    auto stage = [&](int kt, int buf) {
      char* sb = smem + buf * G2_STB + lw * 1024;
      const int kk = kt * 64;
#pragma unroll
      for (int j = 0; j < 5; ++j) GLDS16(src[j] + kk, sb + j * 4096);
      if (lw < 2) GLDS16(src[5] + kk, sb + 5 * 4096);
    };
    const bool big = lw < 2;
    stage(0, 0); stage(1, 1); stage(2, 2);
    if (big) waitv<12>(); else waitv<10>(); BAR();
    stage(3, 3); stage(4, 4);
    if (big) waitv<18>(); else waitv<15>(); BAR();
    stage(5, 5); stage(6, 6);
    if (big) waitv<24>(); else waitv<20>(); BAR();
    stage(7, 0); stage(8, 1);
    int nbuf = 2;
    for (int kt = 3; kt < 26; ++kt) {
      if (big) waitv<30>(); else waitv<25>();
      BAR();
      stage(kt + 6, nbuf);
      nbuf = nbuf + 1 == G2_NST ? 0 : nbuf + 1;
    }
    const u32* wsrc = (const u32*)(ws + WS_WT3) + (size_t)(ft * 5) * 256 + lane * 4;
    for (int ps = 0; ps < 6; ++ps) {
      if (big) waitv<30>(); else waitv<25>();
      BAR();
      char* sb = smem + nbuf * G2_STB + lw * 1024;
#pragma unroll
      for (int j = 0; j < 6; ++j) {
        const int i = ps * 22 + lw + 4 * j, k = i / 5;
        if (j < 5 || big) GLDS16(wsrc + (size_t)(k * 35 + i) * 256, sb + j * 4096);
      }
      nbuf = nbuf + 1 == G2_NST ? 0 : nbuf + 1;
    }
    BAR();
    if (lw < 3) GLDS16(wsrc + (size_t)(26 * 35 + 132 + lw) * 256, smem + nbuf * G2_STB + lw * 1024);
    waitv<0>();
    BAR();
    return;
  }
  const int wt = wave & 1, wf = wave >> 1;
  const int n0 = (ft & 1) ? 3 : 4;
  const int f0 = wf ? n0 : 0, nf = wf ? 7 - n0 : n0;
  u4v z1v[4];
  float bias[2];
#pragma unroll
  for (int j = 0; j < 4; ++j) {
    int ff = ft * 7 + f0 + j; ff = ff < 54 ? ff : 53;
    z1v[j] = *(const u4v*)(z1f + ((size_t)(tt * 2 + wt) * 54 + ff) * 256 + lane * 4);
  }
  bias[0] = bflo(b_sp[tt * 64 + wt * 32 + li]); bias[1] = bflo(b_sp[tt * 64 + wt * 32 + 16 + li]);
  f4 acc[4][2];
#pragma unroll
  for (int j = 0; j < 4; ++j) { acc[j][0] = f4{0, 0, 0, 0}; acc[j][1] = f4{0, 0, 0, 0}; }
  const int sw = (g ^ (li & 7)) * 16;
  const int toff = (wt * 32 + li) * 128 + sw;
  const int foff = 8192 + (f0 * 16 + li) * 128 + sw;
  auto body = [&](auto NFc) {
    constexpr int NF = decltype(NFc)::value;
    u4v fb[2][2][2], fa[2][2][NF];
    auto rd = [&](int set, int buf) {
      const char* sb = smem + buf * G2_STB;
#pragma unroll
      for (int kk = 0; kk < 2; ++kk) {
        fb[set][kk][0] = *(const u4v*)(sb + (toff ^ (kk * 64)));
        fb[set][kk][1] = *(const u4v*)(sb + (toff ^ (kk * 64)) + 2048);
#pragma unroll
        for (int j = 0; j < NF; ++j) fa[set][kk][j] = *(const u4v*)(sb + (foff ^ (kk * 64)) + j * 2048);
      }
    };
    auto mm = [&](int set) {
#pragma unroll
      for (int kk = 0; kk < 2; ++kk)
#pragma unroll
        for (int j = 0; j < NF; ++j) {
          acc[j][0] = mfma_b(fa[set][kk][j], fb[set][kk][0], acc[j][0]);
          acc[j][1] = mfma_b(fa[set][kk][j], fb[set][kk][1], acc[j][1]);
        }
    };
    auto sched = [&]() {
#pragma unroll
      for (int i = 0; i < 2 * NF + 4; ++i) { SGB(0x100, 1); SGB(0x8, 1); }
      SGB(0x8, 2 * NF - 4);
    };
    BAR(); rd(0, 0); WAITL0();
    int buf = 1;
    for (int kt = 1; kt < 31; kt += 2) {
      BAR(); rd(1, buf); mm(0); sched(); WAITL0();
      buf = buf + 1 == G2_NST ? 0 : buf + 1;
      BAR(); rd(0, buf); mm(1); sched(); WAITL0();
      buf = buf + 1 == G2_NST ? 0 : buf + 1;
    }
    BAR(); rd(1, buf); mm(0); sched(); WAITL0();
#pragma unroll
    for (int j = 0; j < 4; ++j) PIN(z1v[j]);
    PIN(bias[0]); PIN(bias[1]);
    BAR();
    mm(1);
  };
  if (nf == 4) body(std::integral_constant<int, 4>{}); else body(std::integral_constant<int, 3>{});
  const __amdgpu_buffer_rsrc_t rs = ws_rsrc(ws);
  const int ff0 = ft * 7 + f0;
#pragma unroll
  for (int tf = 0; tf < 2; ++tf) {
    const int tfr = tt * 4 + wt * 2 + tf;
    u2v o[4];
#pragma unroll
    for (int j = 0; j < 4; ++j) {
      const u32 za = z1v[j][2 * tf], zb = z1v[j][2 * tf + 1];
      const f4 v = (acc[j][tf] + bias[tf]) * f4{bflo(za), bfhi(za), bflo(zb), bfhi(zb)};
      o[j] = u2v{pk_bf(v[0], v[1]), pk_bf(v[2], v[3])};
    }
    const u32 base = (u32)WS_P + (tfr * 27 + (ff0 >> 1)) * 1024 + lane * 16;
    if (!(ff0 & 1)) {
      st16(rs, base, u4v{o[0][0], o[0][1], o[1][0], o[1][1]});
      if (nf == 4) { if (ff0 + 3 < 54) st16(rs, base + 1024, u4v{o[2][0], o[2][1], o[3][0], o[3][1]}); }
      else st8(rs, base + 1024, o[2]);
    } else {
      st8(rs, base + 8, o[0]);
      st16(rs, base + 1024, u4v{o[1][0], o[1][1], o[2][0], o[2][1]});
    }
  }
  gu32* cnt = (gu32*)(ws + WS_CNT);
  waitv<0>();
  if (lane == 0) __hip_atomic_store(cnt + (tt * 2 + wt) * 32 + ft * 2 + wf, 1u, __ATOMIC_RELAXED, __HIP_MEMORY_SCOPE_AGENT);
  const int trow = tt * 64 + wave * 16 + li;
  const u16* h = (const u16*)(ws + WS_H);
  u2v hv[5];
#pragma unroll
  for (int j = 0; j < 5; ++j) hv[j] = *(const u2v*)(h + (size_t)trow * D + ft * 80 + j * 16 + g * 4);
  for (int spin = 0; spin < (1 << 16); ++spin) {
    const u32 c = __hip_atomic_load(cnt + (tt * 2 + (wave >> 1)) * 32 + li, __ATOMIC_RELAXED, __HIP_MEMORY_SCOPE_AGENT);
    if (__all(c != 0u)) break;
    __builtin_amdgcn_s_sleep(1);
  }
  asm volatile("" ::: "memory");
  u4v fb[27];
  const u32 poff = (u32)WS_P + (tt * 4 + wave) * 27 * 1024 + lane * 16;
#pragma unroll
  for (int k = 0; k < 3 * FPD; ++k) { fb[k] = __builtin_amdgcn_raw_buffer_load_b128(rs, poff + k * 1024, 0, AUX_SC1); SB(); }
  f4 acc3[5];
#pragma unroll
  for (int j = 0; j < 5; ++j) acc3[j] = f4{0, 0, 0, 0};
  u4v fa[2][15];
  u32 wo[3] = {(u32)lane * 16, (u32)lane * 16 + 3 * G2_STB - 8192, (u32)lane * 16 + 5 * G2_STB};
  PIN(wo[1]); PIN(wo[2]);
  auto rd3 = [&](int kt) {
#pragma unroll
    for (int ii = 0; ii < 15; ++ii) {
      const int i = kt * 15 + ii;
      const int off = ((32 + i / 22) % G2_NST) * G2_STB + (i % 22) * 1024;
      const int w = off < 3 * G2_STB - 8192 ? 0 : (off < 5 * G2_STB ? 1 : 2);
      fa[kt & 1][ii] = *(const u4v*)(smem + wo[w] + (off - (w == 0 ? 0 : (w == 1 ? 3 * G2_STB - 8192 : 5 * G2_STB))));
    }
  };
  auto mm3 = [&](int kt) {
#pragma unroll
    for (int kk = 0; kk < 3; ++kk)
#pragma unroll
      for (int j = 0; j < 5; ++j) acc3[j] = mfma_b(fa[kt & 1][kk * 5 + j], fb[kt * 3 + kk], acc3[j]);
  };
  BAR();
  rd3(0);
#pragma unroll
  for (int kt = 1; kt < 9; ++kt) {
    SB();
    if (kt - 1 + FPD < 9) {
#pragma unroll
      for (int k = 0; k < 3; ++k) fb[(kt - 1 + FPD) * 3 + k] = __builtin_amdgcn_raw_buffer_load_b128(rs, poff + ((kt - 1 + FPD) * 3 + k) * 1024, 0, AUX_SC1);
      SB();
    }
    rd3(kt);
    mm3(kt - 1);
#pragma unroll
    for (int i = 0; i < 15; ++i) { SGB(0x100, 1); SGB(0x8, 1); }
  }
  SB();
  mm3(8);
  {
    u2v o[5];
#pragma unroll
    for (int j = 0; j < 5; ++j) {
      u32 t0 = pk_bf(acc3[j][0], acc3[j][1]), t1 = pk_bf(acc3[j][2], acc3[j][3]);
      o[j] = u2v{pk_bf(bflo(t0) + bflo(hv[j][0]), bfhi(t0) + bfhi(hv[j][0])), pk_bf(bflo(t1) + bflo(hv[j][1]), bfhi(t1) + bfhi(hv[j][1]))};
    }
    const __amdgpu_buffer_rsrc_t ro = __builtin_amdgcn_make_buffer_rsrc((void*)out, 0, S * D * 2, 0x00020000);
    const u32 oo = (trow * D + ft * 80) * 2;
#pragma unroll
    for (int j = 0; j < 4; j += 2) {
      auto s0 = __builtin_amdgcn_permlane16_swap(o[j][0], o[j + 1][0], false, false);
      auto s1 = __builtin_amdgcn_permlane16_swap(o[j][1], o[j + 1][1], false, false);
      __builtin_amdgcn_raw_buffer_store_b128(u4v{s0[0], s1[0], s0[1], s1[1]}, ro, oo + (j + (g & 1)) * 32 + (g >> 1) * 16, 0, 0);
    }
    __builtin_amdgcn_raw_buffer_store_b64(o[4], ro, oo + 128 + g * 8, 0, 0);
  }
}

extern "C" __global__ void end_kernel() {}


__global__ void diag_kernel() {
  unsigned long long t0 = __builtin_amdgcn_s_memrealtime();
  unsigned long long ta = g_t[0];
  for (int i = 1; i < 8; ++i) ta = g_t[i] < ta ? g_t[i] : ta;
  unsigned long long d = (t0 - ta) * 8;
  if (d > 100000) d = 100000;
  while (__builtin_amdgcn_s_memrealtime() - t0 < d) __builtin_amdgcn_s_sleep(1);
}
extern "C" void kernel_launch(void* const* d_in, const int* in_sizes, int n_in,
                              void* d_out, int out_size, void* d_ws, size_t ws_size,
                              hipStream_t stream) {
  const u16* x=(const u16*)d_in[0]; const u16* wr=(const u16*)d_in[1]; const u16* wi=(const u16*)d_in[2];
  const u16* w_in=(const u16*)d_in[4]; const u16* w_sp=(const u16*)d_in[5]; const u16* b_sp=(const u16*)d_in[6]; const u16* w_out=(const u16*)d_in[7];
  char* ws=(char*)d_ws;
  k1_kernel<<<256,512,0,stream>>>(x,wr,wi,ws,w_sp,w_in,w_out,b_sp,(u16*)d_out);
  (void)hipFuncSetAttribute((const void*)k2_kernel, hipFuncAttributeMaxDynamicSharedMemorySize, 84 * 1024);
  k2_kernel<<<K2_FFT_BLOCKS+K2_PREP_BLOCKS,512,84 * 1024,stream>>>();
  k3_kernel<<<256,256,0,stream>>>();
  g1_kernel<<<256,512,0,stream>>>();
  g2_kernel<<<256,512,0,stream>>>();
  diag_kernel<<<1,64,0,stream>>>();
}
```

```cpp
#include <hip/hip_runtime.h>
#include <hip/hip_bf16.h>
#include <stdint.h>
typedef unsigned short u16;
typedef unsigned int u32;
typedef _Float16 h2 __attribute__((ext_vector_type(2)));
typedef _Float16 h8 __attribute__((ext_vector_type(8)));
typedef __bf16 bf2 __attribute__((ext_vector_type(2)));
typedef __bf16 bf8 __attribute__((ext_vector_type(8)));
typedef float f2 __attribute__((ext_vector_type(2)));
typedef float f4 __attribute__((ext_vector_type(4)));
typedef short s4 __attribute__((ext_vector_type(4)));
typedef u32 u2v __attribute__((ext_vector_type(2)));
typedef u32 u4v __attribute__((ext_vector_type(4)));
#define DI __device__ __forceinline__
#define LDS_AS __attribute__((address_space(3)))
#define PIN(x) asm volatile("" : "+v"(x))
#define GLDS16(src, dst) __builtin_amdgcn_global_load_lds((const u32*)(src), (LDS_AS u32*)(dst), 16, 0, 0)
#define WAITV(n) asm volatile("s_waitcnt vmcnt(%0)" ::"n"(n) : "memory")

constexpr int S = 2048, D = 640, DF = 321, FF = 1728, FH = 864;
constexpr int TROW_UNITS = 648;
constexpr int NUNITS = 641;

constexpr size_t WS_T = 0, WS_WP = 2688u << 10, WS_TP = 5376u << 10, WS_WT1 = 8064u << 10, WS_WT3 = 10240u << 10, WS_H = 11392u << 10,
                 WS_L2 = 13952u << 10, WS_Z1 = 16512u << 10, WS_Z2 = 19968u << 10, WS_STATS = 23424u << 10, WS_W2 = 23680u << 10,
                 WS_WMAX = 23692u << 10, WS_UNS = 23696u << 10, WS_CNT = 23700u << 10, WS_P = WS_T;
#define PINS(x) asm volatile("" ::"s"(x))
#define GLDS4(src, dst) __builtin_amdgcn_global_load_lds((const u32*)(src), (LDS_AS u32*)(dst), 4, 0, 0)
#define touch(p) GLDS4(p, scratch)

constexpr int AUX_SC1 = 16;
constexpr u32 DROP = 0x80000000u;
DI __amdgpu_buffer_rsrc_t ws_rsrc(char* ws) { return __builtin_amdgcn_make_buffer_rsrc((void*)ws, 0, 64 << 20, 0x00020000); }
DI void st16(__amdgpu_buffer_rsrc_t rs, u32 off, u4v v) { __builtin_amdgcn_raw_buffer_store_b128(v, rs, off, 0, AUX_SC1); }
DI void st8(__amdgpu_buffer_rsrc_t rs, u32 off, u2v v) { __builtin_amdgcn_raw_buffer_store_b64(v, rs, off, 0, AUX_SC1); }
#define CODE_ADDR(var, sym)                                                                                     \
  const char* var;                                                                                              \
  {                                                                                                             \
    u32 lo_, hi_;                                                                                               \
    asm volatile("s_getpc_b64 s[40:41]\n\ts_add_u32 %0, s40, " #sym "@rel32@lo+4\n\ts_addc_u32 %1, s41, " #sym "@rel32@hi+12" \
                 : "=s"(lo_), "=s"(hi_)::"s40", "s41", "scc");                                                  \
    var = (const char*)(((unsigned long long)hi_ << 32) | lo_);                                                 \
  }
#define CODE_TOUCH(a, b, c)                                                           \
  {                                                                                   \
    CODE_ADDR(pa_, a) CODE_ADDR(pb_, b)                                               \
    const char* p_ = pa_ + ((c) * 64 + lane) * 128;                                   \
    if (p_ < pb_) touch(p_);                                                          \
  }
struct Args { char* ws; const u16* x; const u16* w_sp; const u16* w_in; const u16* w_out; const u16* b_sp; u16* out; void* pad; };
static __device__ __attribute__((aligned(128))) Args g_args;
typedef const __attribute__((address_space(4))) Args* ArgsPtr;
#define LOAD_ARGS() ArgsPtr ap_ = (ArgsPtr)(unsigned long long)&g_args
DI float bflo(u32 v) { return __uint_as_float(v << 16); }
DI float bfhi(u32 v) { return __uint_as_float(v & 0xffff0000u); }
DI u32 pk_bf(float a, float b) { f2 v = {a, b}; return __builtin_bit_cast(u32, __builtin_convertvector(v, bf2)); }
DI u32 pk_h(float a, float b) { f2 v = {a, b}; return __builtin_bit_cast(u32, __builtin_convertvector(v, h2)); }
DI float rbf(float a) { return bflo(pk_bf(a, a)); }
DI float cosr(float rev) { return __builtin_amdgcn_cosf(rev); }
DI float sinr(float rev) { return __builtin_amdgcn_sinf(rev); }
DI f4 mfma_h(u4v a, u4v b, f4 c) {
  return __builtin_amdgcn_mfma_f32_16x16x32_f16(__builtin_bit_cast(h8, a), __builtin_bit_cast(h8, b), c, 0, 0, 0);
}
template <int CTRL> DI float dpp_add(float v) {
  int t = __builtin_amdgcn_update_dpp(0, __float_as_int(v), CTRL, 0xf, 0xf, true);
  return v + __int_as_float(t);
}
DI float xsum_g(float v) {
  u32 b = __float_as_uint(v);
  auto r = __builtin_amdgcn_permlane16_swap(b, b, false, false);
  v = __uint_as_float(r[0]) + __uint_as_float(r[1]);
  b = __float_as_uint(v);
  auto s = __builtin_amdgcn_permlane32_swap(b, b, false, false);
  return __uint_as_float(s[0]) + __uint_as_float(s[1]);
}
template <int CTRL> DI u32 dpp_maxu(u32 v) {
  u32 t = (u32)__builtin_amdgcn_update_dpp(0, (int)v, CTRL, 0xf, 0xf, true);
  return v > t ? v : t;
}
DI u32 max64u(u32 v) {
  v = dpp_maxu<0xB1>(v); v = dpp_maxu<0x4E>(v); v = dpp_maxu<0x141>(v); v = dpp_maxu<0x140>(v);
  auto r = __builtin_amdgcn_permlane16_swap(v, v, false, false);
  v = r[0] > r[1] ? r[0] : r[1];
  auto s = __builtin_amdgcn_permlane32_swap(v, v, false, false);
  return s[0] > s[1] ? s[0] : s[1];
}
DI float sum32(float v) {
  v = dpp_add<0xB1>(v); v = dpp_add<0x4E>(v); v = dpp_add<0x141>(v); v = dpp_add<0x140>(v);
  u32 b = __float_as_uint(v);
  auto r = __builtin_amdgcn_permlane16_swap(b, b, false, false);
  return __uint_as_float(r[0]) + __uint_as_float(r[1]);
}
DI void ln_row(f2 (&v)[10]) {
  f2 s = ((v[0] + v[1]) + (v[2] + v[3])) + ((v[4] + v[5]) + (v[6] + v[7])) + (v[8] + v[9]);
  const float mean = sum32(s[0] + s[1]) * (1.0f / 640.0f);
  f2 q0 = f2{0.f, 0.f}, q1 = f2{0.f, 0.f};
#pragma unroll
  for (int i = 0; i < 10; i += 2) { v[i] -= mean; v[i + 1] -= mean; q0 += v[i] * v[i]; q1 += v[i + 1] * v[i + 1]; }
  q0 += q1;
  const float var = sum32(q0[0] + q0[1]) * (1.0f / 640.0f);
  const float rinv = __builtin_amdgcn_rcpf(rbf(__builtin_amdgcn_sqrtf(rbf(rbf(var) + 1e-5f))));
  const float off = (mean - rbf(mean)) * rinv;
#pragma unroll
  for (int i = 0; i < 10; ++i) v[i] = v[i] * rinv + off;
}
#define SB() __builtin_amdgcn_sched_barrier(0)
template <bool SWAP> DI u4v cmul_pack(f4 re, f4 im, f4 wr, f4 wi) {
  const f4 ore = re * wr - im * wi, oim = re * wi + im * wr;
  u4v v;
#pragma unroll
  for (int r = 0; r < 4; ++r) v[r] = SWAP ? pk_h(oim[r], ore[r]) : pk_h(ore[r], oim[r]);
  return v;
}

struct __attribute__((packed, aligned(2))) U4U { u32 a, b, c, d; };

DI void lds_signal(char* p) {
  asm volatile("s_waitcnt lgkmcnt(0)" ::: "memory");
  if ((threadIdx.x & 63) == 0) __hip_atomic_fetch_add((LDS_AS u32*)p, 1u, __ATOMIC_RELAXED, __HIP_MEMORY_SCOPE_WORKGROUP);
}
DI void lds_wait(char* p, u32 n) {
  while (__builtin_amdgcn_readfirstlane(__hip_atomic_load((LDS_AS u32*)p, __ATOMIC_RELAXED, __HIP_MEMORY_SCOPE_WORKGROUP)) < n) {}
  asm volatile("" ::: "memory");
}
constexpr int K1_F40 = 10272, K1_TW = K1_F40 + 18432, K1_R16 = K1_TW + 6144, K1_LDS = K1_R16 + 4096 + 256 + 16;
extern "C" __global__ __launch_bounds__(512) void k1_kernel(const u16* __restrict__ x, const u16* __restrict__ wr,
                                                            const u16* __restrict__ wi, char* __restrict__ ws,
                                                            const u16* __restrict__ w_sp, const u16* w_in, const u16* w_out,
                                                            const u16* b_sp, u16* out) {
  __shared__ __attribute__((aligned(16))) char smem[K1_LDS];
  const int tid = threadIdx.x, lane = tid & 63;
  const int wave = __builtin_amdgcn_readfirstlane(tid >> 6);
  const int li = lane & 15, g = lane >> 4;
  char* scratch = smem + K1_R16 + 4096;
  char* flags = scratch + 256;
  if (wave == 7) CODE_TOUCH(k1_kernel, end_kernel, (blockIdx.x >> 3) & 7)
  PINS(x); PINS(wr); PINS(wi); PINS(ws); PINS(w_sp);
  auto constants = [&]() {
#pragma unroll
    for (int it = 0; it < 2; ++it) {
      const int item = wave + 8 * it;
      if (it == 0 || wave == 0) {
        const int to = item / 3, tk = item - 3 * to;
        const int kb = 16 * to + li;
        const float s8 = (tk < 2 || g < 2) ? 0.125f : 0.f;
        u4v fre, fim;
#pragma unroll
        for (int jj = 0; jj < 4; ++jj) {
          const float ang = (float)(kb * (16 * tk + 4 * g + jj)) * (1.0f / 40.0f);
          const float c = cosr(ang) * s8, s = sinr(ang) * s8;
          fre[jj] = pk_h(c, s);
          fim[jj] = pk_h(-s, c);
        }
        *(u4v*)(smem + K1_F40 + item * 2048 + lane * 16) = fre;
        *(u4v*)(smem + K1_F40 + item * 2048 + 1024 + lane * 16) = fim;
      } else if (wave < 4) {
        const int t = wave - 1;
        f4 twr, twi;
#pragma unroll
        for (int r = 0; r < 4; ++r) {
          const float ang = (float)((16 * t + 4 * g + r) * li) * (1.0f / 640.0f);
          twr[r] = cosr(ang) * 0.125f; twi[r] = -sinr(ang) * 0.125f;
        }
        *(f4*)(smem + K1_TW + t * 2048 + lane * 16) = twr;
        *(f4*)(smem + K1_TW + t * 2048 + 1024 + lane * 16) = twi;
      } else {
        const int ph = 2 * (wave - 4) + (lane >> 5);
        float cs[8];
#pragma unroll
        for (int jj = 0; jj < 8; ++jj) cs[jj] = cosr((float)(((8 * (g & 1) + jj) * li + 2 * ph) & 15) * 0.0625f);
        *(u4v*)(smem + K1_R16 + (wave - 4) * 1024 + lane * 16) = u4v{pk_h(cs[0], cs[1]), pk_h(cs[2], cs[3]), pk_h(cs[4], cs[5]), pk_h(cs[6], cs[7])};
      }
    }
    if (tid < 4) { *(u32*)(smem + 10240 + tid * 4) = 0u; *(u32*)(flags + tid * 4) = 0u; }
    asm volatile("s_waitcnt lgkmcnt(0)" ::: "memory");
    __builtin_amdgcn_s_barrier();
    asm volatile("" ::: "memory");
  };
  if (wave >= 4) {
    const int slotid = (blockIdx.x >> 3) * 3 + (wave - 4);
    const bool act = wave < 7 && slotid < 84;
    const int cp = blockIdx.x & 7, cc = act ? slotid >> 2 : 0, qt = slotid & 3;
    const int c0 = cc < 20 ? cc * 16 : 305;
    const bool mir = cp > 4;
    const bool sp = (cp & 3) == 0;
    const int e = mir ? 255 - (qt * 64 + lane) : qt * 64 + lane;
    const size_t roff = (size_t)(cp + 8 * e) * DF + c0;
    const U4U* pr = (const U4U*)(wr + roff);
    const U4U* pi = (const U4U*)(wi + roff);
    U4U r0, r1, i0, i1;
    __builtin_amdgcn_s_sleep(3);
    if (act) { r0 = pr[0]; r1 = pr[1]; i0 = pi[0]; i1 = pi[1]; }
    SB();
    constants();
    if (!act) {
      if (blockIdx.x == 255 && tid == 256) g_args = Args{ws, x, w_sp, w_in, w_out, b_sp, out, nullptr};
      if (blockIdx.x >= 224 && tid == 448) ((u32*)(ws + WS_WMAX))[blockIdx.x] = 0u;
      WAITV(0);
      return;
    }
    const u32 flip = mir ? 0x80000000u : 0u;
    const int q1 = (mir ? 8 - cp : cp) - 1;
    const int kb = mir ? 39 - cc : cc;
    const int t1 = sp ? 1920 + 32 * cc + (cp >> 2) : (q1 * 160 + (kb >> 2) * 16 + (mir ? 16 : 0)) * 4 + (kb & 3);
    const int step = sp ? 2 : (mir ? -4 : 4);
    const int t0 = mir ? (q1 * 160 + ((kb + 1) >> 2) * 16) * 4 + ((kb + 1) & 3) : t1;
    const bool edge = (cc == 0 || cc == 20);
    const __amdgpu_buffer_rsrc_t rs = ws_rsrc(ws);
    const u32 slot = (u32)WS_WP + (qt * 16 + li) * 16;
    u32 doff[4];
#pragma unroll
    for (int m = 0; m < 4; ++m) {
      const int jc = 4 * m + g;
      doff[m] = slot + (u32)(t1 + step * jc) * 1024u;
      if (cc == 20) doff[m] = DROP;
    }
    if (g == 0 && cc < 20) doff[0] = (mir && edge) ? DROP : slot + (u32)t0 * 1024u;
    if (g == 3 && cc == 20 && !mir) doff[3] = slot + (u32)t0 * 1024u;
    u32 rr[8] = {r0.a, r0.b, r0.c, r0.d, r1.a, r1.b, r1.c, r1.d};
    u32 ii[8] = {i0.a, i0.b, i0.c, i0.d, i1.a, i1.b, i1.c, i1.d};
    u32 vals[16];
#pragma unroll
    for (int j = 0; j < 16; ++j) {
      u32 a = rr[j >> 1], b = ii[j >> 1];
      vals[j] = ((j & 1) ? __builtin_amdgcn_perm(b, a, 0x07060302u) : __builtin_amdgcn_perm(b, a, 0x05040100u)) ^ flip;
    }
    if (edge && (mir || sp)) {
      const u32 v0 = cc < 20 ? vals[0] : vals[15];
      const int e2 = (cp == 0) ? ((256 - e) & 255) : 255 - e;
      const int dpos2 = ((e2 & 15) + 16 * (e2 >> 6)) * 4 + ((e2 >> 4) & 3);
      const int cls = mir ? 8 - cp : cp;
      ((u32*)(ws + WS_W2))[(cls * 2 + (cc == 20)) * 256 + dpos2] = v0 ^ flip ^ 0x80000000u;
    }
    u32 mx = 0;
#pragma unroll
    for (int j = 0; j < 16; ++j) {
      const u32 a = vals[j] & 0x7fff7fffu;
      mx = max(mx, max(a & 0xffffu, a >> 16));
    }
#pragma unroll
    for (int m = 0; m < 4; ++m) {
      auto p02 = __builtin_amdgcn_permlane32_swap(vals[4 * m], vals[4 * m + 2], false, false);
      auto p13 = __builtin_amdgcn_permlane32_swap(vals[4 * m + 1], vals[4 * m + 3], false, false);
      auto q01 = __builtin_amdgcn_permlane16_swap(p02[0], p13[0], false, false);
      auto q23 = __builtin_amdgcn_permlane16_swap(p02[1], p13[1], false, false);
      st16(rs, doff[m], u4v{q01[0], q01[1], q23[0], q23[1]});
    }
    mx = max64u(mx);
    if (lane == 0) {
      __hip_atomic_fetch_max((LDS_AS u32*)(flags + 4), mx, __ATOMIC_RELAXED, __HIP_MEMORY_SCOPE_WORKGROUP);
      asm volatile("s_waitcnt lgkmcnt(0)" ::: "memory");
      if (__hip_atomic_fetch_add((LDS_AS u32*)(flags + 8), 1u, __ATOMIC_RELAXED, __HIP_MEMORY_SCOPE_WORKGROUP) == 2u)
        ((u32*)(ws + WS_WMAX))[blockIdx.x] = __hip_atomic_load((LDS_AS u32*)(flags + 4), __ATOMIC_RELAXED, __HIP_MEMORY_SCOPE_WORKGROUP);
    }
    return;
  }
  const int b = (blockIdx.x & 7) * 32 + (blockIdx.x >> 3);
  const int a = 2 * wave + (lane >> 5), j = lane & 31;
  const u16* xr = x + (size_t)(256 * a + b) * D;
  const u4v x0 = *(const u4v*)(xr + 8 * j);
  const u4v x1 = *(const u4v*)(xr + 256 + 8 * j);
  const u2v x2 = *(const u2v*)(xr + 512 + 4 * j);
  SB();
  constants();
  const int q = wave;
  u4v bre[4], bim[4], fre[9], fim[9];
  f4 twr[3], twi[3];
  u32 toff[3][4];
  {
    const int a0 = g >> 1;
#pragma unroll
    for (int ks = 0; ks < 4; ++ks) {
      const int ph = (q == 0) ? (a0 ? 1 : 7) : (2 * ks + a0) * q;
      bre[ks] = *(const u4v*)(smem + K1_R16 + (ph & 7) * 512 + (lane & 31) * 16);
      bim[ks] = *(const u4v*)(smem + K1_R16 + ((ph + 2) & 7) * 512 + (lane & 31) * 16);
    }
#pragma unroll
    for (int i = 0; i < 9; ++i) {
      fre[i] = *(const u4v*)(smem + K1_F40 + i * 2048 + lane * 16);
      fim[i] = *(const u4v*)(smem + K1_F40 + i * 2048 + 1024 + lane * 16);
    }
    const float qs = q == 0 ? 0.70710678118f : 1.f;
#pragma unroll
    for (int t = 0; t < 3; ++t) {
      twr[t] = *(const f4*)(smem + K1_TW + t * 2048 + lane * 16) * qs;
      twi[t] = *(const f4*)(smem + K1_TW + t * 2048 + 1024 + lane * 16) * qs;
      PIN(twr[t]); PIN(twi[t]);
#pragma unroll
      for (int r = 0; r < 4; ++r) {
        const int k2 = li + 16 * (16 * t + 4 * g + r);
        const int pos = k2 <= 320 ? 2 * k2 : 2 * (640 - k2) + 1;
        toff[t][r] = (u32)WS_T + (u32)b * (TROW_UNITS * 16) + (q > 0 ? ((q - 1) * 160 + 64 * t + lane) * 16 : 480 * 16 + pos * 4);
        if (t == 2 && lane >= 32) toff[t][r] = DROP;
        PIN(toff[t][r]);
      }
    }
#pragma unroll
    for (int ks = 0; ks < 4; ++ks) { PIN(bre[ks]); PIN(bim[ks]); }
#pragma unroll
    for (int i = 0; i < 9; ++i) { PIN(fre[i]); PIN(fim[i]); }
  }
  {
    f2 v[10];
#pragma unroll
    for (int i = 0; i < 4; ++i) { v[i] = f2{bflo(x0[i]), bfhi(x0[i])}; v[4 + i] = f2{bflo(x1[i]), bfhi(x1[i])}; }
    v[8] = f2{bflo(x2[0]), bfhi(x2[0])}; v[9] = f2{bflo(x2[1]), bfhi(x2[1])};
    ln_row(v);
    char* ub = smem + a * 1280;
    *(u4v*)(ub + 16 * j) = u4v{pk_h(v[0][0], v[0][1]), pk_h(v[1][0], v[1][1]), pk_h(v[2][0], v[2][1]), pk_h(v[3][0], v[3][1])};
    *(u4v*)(ub + 512 + 16 * j) = u4v{pk_h(v[4][0], v[4][1]), pk_h(v[5][0], v[5][1]), pk_h(v[6][0], v[6][1]), pk_h(v[7][0], v[7][1])};
    *(u2v*)(ub + 1024 + 8 * j) = u2v{pk_h(v[8][0], v[8][1]), pk_h(v[9][0], v[9][1])};
  }
  lds_signal(flags);
  lds_wait(flags, 4);
  u4v b2[3];
  {
    const int qq = li >> 2, p = li & 3;
    const char* abase = smem + (8 * g + qq) * 80 + 8 * p;
    u4v af[3][4];
#pragma unroll
    for (int t = 0; t < 3; ++t)
#pragma unroll
      for (int ks = 0; ks < 4; ++ks) {
        s4 lo = __builtin_amdgcn_ds_read_tr16_b64_v4i16((LDS_AS s4*)(abase + ks * 2560 + t * 32));
        s4 hi = __builtin_amdgcn_ds_read_tr16_b64_v4i16((LDS_AS s4*)(abase + ks * 2560 + 320 + t * 32));
        u2v l2 = __builtin_bit_cast(u2v, lo), h2v = __builtin_bit_cast(u2v, hi);
        af[t][ks] = u4v{l2[0], l2[1], h2v[0], h2v[1]};
      }
    SB();
    f4 are[3], aim[3];
#pragma unroll
    for (int t = 0; t < 3; ++t) {
      are[t] = f4{0, 0, 0, 0}; aim[t] = f4{0, 0, 0, 0};
#pragma unroll
      for (int ks = 0; ks < 4; ++ks) {
        are[t] = mfma_h(af[t][ks], bre[ks], are[t]);
        aim[t] = mfma_h(af[t][ks], bim[ks], aim[t]);
      }
    }
#pragma unroll
    for (int t = 0; t < 3; ++t) b2[t] = cmul_pack<false>(are[t], aim[t], twr[t], twi[t]);
  }
  const __amdgpu_buffer_rsrc_t rs = ws_rsrc(ws);
  f4 ore[3], oim[3];
#pragma unroll
  for (int tk = 0; tk < 3; ++tk)
#pragma unroll
    for (int to = 0; to < 3; ++to) {
      ore[to] = mfma_h(fre[to * 3 + tk], b2[tk], tk ? ore[to] : f4{0, 0, 0, 0});
      oim[to] = mfma_h(fim[to * 3 + tk], b2[tk], tk ? oim[to] : f4{0, 0, 0, 0});
    }
  u4v o[3];
#pragma unroll
  for (int to = 0; to < 3; ++to)
#pragma unroll
    for (int r = 0; r < 4; ++r) o[to][r] = pk_h(ore[to][r], oim[to][r]);
  if (q > 0) {
#pragma unroll
    for (int to = 0; to < 3; ++to) st16(rs, toff[to][0], o[to]);
  } else {
#pragma unroll
    for (int to = 0; to < 3; ++to)
#pragma unroll
      for (int r = 0; r < 4; ++r) __builtin_amdgcn_raw_buffer_store_b32(o[to][r], rs, toff[to][r], 0, 0);
    __builtin_amdgcn_raw_buffer_store_b32(lane ? o[1][0] : o[0][0], rs, lane == 0 ? toff[0][0] + 4 : (lane == 16 ? toff[1][0] + 4 : DROP), 0, 0);
  }
}

template <int NT, int LDW, int NCG, int NTASK, bool PERM>
DI void retile(const u16* __restrict__ W, __amdgpu_buffer_rsrc_t rs, u32 dstbase, int wid, int nw, char* lds, int lane) {
  const int li = lane & 15, g = lane >> 4, q = li >> 2, p = li & 3;
  u32 dst[NT];
#pragma unroll
  for (int i = 0; i < NT; ++i) {
    const int task = wid + i * nw;
    const int t = task < NTASK ? task : 0;
    const int ks = t / NCG, cg = t - ks * NCG;
    const u16* src = W + (size_t)(ks * 32 + (lane >> 3)) * LDW + cg * 64 + (lane & 7) * 8;
#pragma unroll
    for (int j = 0; j < 4; ++j) GLDS16(src + (size_t)j * 8 * LDW, lds + i * 4096 + j * 1024);
    dst[i] = task < NTASK ? dstbase + (u32)t * 4096u + lane * 16 : DROP;
  }
  const char* rb = lds + (PERM ? 4 * g + q : 8 * g + q) * 128 + 8 * p;
  WAITV(0);
#pragma unroll
  for (int i = 0; i < NT; ++i)
#pragma unroll
    for (int f = 0; f < 4; ++f) {
      s4 lo = __builtin_amdgcn_ds_read_tr16_b64_v4i16((LDS_AS s4*)(rb + i * 4096 + f * 32));
      s4 hi = __builtin_amdgcn_ds_read_tr16_b64_v4i16((LDS_AS s4*)(rb + i * 4096 + f * 32 + (PERM ? 16 : 4) * 128));
      u2v a = __builtin_bit_cast(u2v, lo), b = __builtin_bit_cast(u2v, hi);
      st16(rs, dst[i] + f * 1024, u4v{a[0], a[1], b[0], b[1]});
    }
}
constexpr int NRT1 = 20 * 27, NRT3 = 27 * 10;
constexpr int K2_FFT_BLOCKS = 168, K2_PREP_BLOCKS = 88;

extern "C" __global__ __launch_bounds__(512, 2) void k2_kernel() {
  LOAD_ARGS();
  char* ws = ap_->ws; const u16* w_in = ap_->w_in;
  PINS(ws); PINS(w_in);
  const u32* T = (const u32*)(ws + WS_T); const u32* Wp = (const u32*)(ws + WS_WP); const u32* W2 = (const u32*)(ws + WS_W2);
  const u32* wmax = (const u32*)(ws + WS_WMAX); float* unscale = (float*)(ws + WS_UNS);
  extern __shared__ __attribute__((aligned(16))) char smem[];
  const int tid = threadIdx.x, lane = tid & 63;
  const int wave = __builtin_amdgcn_readfirstlane(tid >> 6);
  const int li = lane & 15, g = lane >> 4;
  if (blockIdx.x >= K2_FFT_BLOCKS) {
    const int wid = (blockIdx.x - K2_FFT_BLOCKS) * 8 + wave;
    const __amdgpu_buffer_rsrc_t rs = ws_rsrc(ws);
    if (wid < 256) st16(rs, (u32)WS_STATS + wid * 1024 + lane * 16, u4v{~0u, ~0u, ~0u, ~0u});
    else if (wid < 264) st16(rs, (u32)WS_CNT + (wid - 256) * 1024 + lane * 16, u4v{0u, 0u, 0u, 0u});
    const int pb = blockIdx.x - K2_FFT_BLOCKS;
    const int task = wave < 6 ? pb * 6 + wave : (wave == 6 && pb < 12 ? 528 + pb : NRT1);
    if (task < NRT1) retile<1, FF, 27, NRT1, false>(w_in, rs, (u32)WS_WT1, task, 0, smem + wave * 8192, lane);
    return;
  }
  const int P = (blockIdx.x & 7) * 21 + (blockIdx.x >> 3);
  if (P > 160) return;
  const int u = wave & 3, hh = wave >> 2;
  const int U = 4 * P + u;
#pragma unroll
  for (int i = 0; i < 2; ++i) {
    const int bb = (wave * 2 + i) * 16 + (lane >> 2);
    GLDS16(T + ((size_t)bb * TROW_UNITS + 4 * P + ((lane & 3) ^ ((lane >> 4) & 3))) * 4, smem + (wave * 2 + i) * 1024);
  }
  const u4v mxv = *(const u4v*)(wmax + lane * 4);
  u4v Wv[2];
#pragma unroll
  for (int t = 0; t < 2; ++t) Wv[t] = *(const u4v*)(Wp + ((size_t)((U < NUNITS ? U : 0) * 4 + 2 * hh + t) * 64 + lane) * 4);
  const bool special = P >= 120;
  const int cB = special ? 4 : (P / 40 + 1);
  const int ur = special ? U - 480 : U % 160;
  const bool e320 = special ? ur == 160 : ur == 80;
  const bool edge = hh == 0 && (ur == 0 || e320);
  u4v w2, w4;
  if (edge) {
    w2 = *(const u4v*)(W2 + ((special ? 0 : cB) * 2 + (e320 ? 1 : 0)) * 256 + lane * 4);
    w4 = *(const u4v*)(W2 + (4 * 2 + (e320 ? 1 : 0)) * 256 + lane * 4);
  }
  {
    char* cb = smem + 16384 + wave * 2048 + lane * 16;
    const int cc = wave < 4 ? cB : 0;
    const float inv = u < 2 ? (1.0f / 128.0f) : (1.0f / 2048.0f);
    const float amp = u == 0 ? 1.f : ((u == 1 && special) ? 0.125f : 0.25f);
    f4 c, s;
#pragma unroll
    for (int jj = 0; jj < 4; ++jj) {
      const int a = (u & 1) ? li * (cc + 8 * (4 * g + jj)) : (4 * g + jj) * (cc + 8 * li);
      const float ang = (float)a * inv;
      c[jj] = cosr(ang) * amp; s[jj] = sinr(ang) * amp;
    }
    if (u < 2) {
      *(u4v*)(cb) = u4v{pk_h(c[0], s[0]), pk_h(c[1], s[1]), pk_h(c[2], s[2]), pk_h(c[3], s[3])};
      *(u4v*)(cb + 1024) = u4v{pk_h(-s[0], c[0]), pk_h(-s[1], c[1]), pk_h(-s[2], c[2]), pk_h(-s[3], c[3])};
    } else {
      *(f4*)(cb) = c;
      *(f4*)(cb + 1024) = s;
    }
  }
  asm volatile("s_waitcnt lgkmcnt(0)" ::: "memory");
  __builtin_amdgcn_s_barrier();
  asm volatile("" ::: "memory");
  u4v Fre, Fim, P1re[2], P1im[2], P4re[2], P4im[2];
  f4 tfr[2], tfs[2], tir[2], tis[2];
  {
    const char* cb = smem + 16384 + lane * 16;
    Fre = *(const u4v*)(cb + 4 * 2048); Fim = *(const u4v*)(cb + 4 * 2048 + 1024);
#pragma unroll
    for (int c = 0; c < 2; ++c) {
      const char* cs = cb + ((c == 0 && special) ? 8192 : 0);
      P1re[c] = *(const u4v*)(cs); P1im[c] = *(const u4v*)(cs + 1024);
      P4re[c] = *(const u4v*)(cs + 2048); P4im[c] = *(const u4v*)(cs + 3072);
      tfr[c] = *(const f4*)(cs + 4096); tfs[c] = *(const f4*)(cs + 5120);
      tir[c] = *(const f4*)(cs + 6144); tis[c] = *(const f4*)(cs + 7168);
    }
    PIN(Fre); PIN(Fim);
#pragma unroll
    for (int c = 0; c < 2; ++c) { PIN(P1re[c]); PIN(P1im[c]); PIN(P4re[c]); PIN(P4im[c]); PIN(tfr[c]); PIN(tfs[c]); PIN(tir[c]); PIN(tis[c]); }
  }
  asm volatile("s_waitcnt vmcnt(0)" ::: "memory");
  __builtin_amdgcn_s_barrier();
  asm volatile("" ::: "memory");
  const int loff = (64 * g + li) * 64 + ((u ^ (li >> 2)) & 3) * 16 + hh * 8;
  u4v X[2];
#pragma unroll
  for (int jj = 0; jj < 4; ++jj) {
    const u2v L = *(const u2v*)(smem + loff + jj * 1024);
    X[0][jj] = L[0]; X[1][jj] = L[1];
  }
  if (special) {
#pragma unroll
    for (int jj = 0; jj < 4; ++jj) {
      const u32 au = X[0][jj], bu = X[1][jj] ^ 0x80000000u;
      const h2 A = __builtin_bit_cast(h2, au);
      const h2 Bc = __builtin_bit_cast(h2, bu);
      X[0][jj] = __builtin_bit_cast(u32, A + Bc);
      X[1][jj] = __builtin_bit_cast(u32, A - Bc);
    }
  }
  const f4 z4 = f4{0, 0, 0, 0};
  f4 re[2], im[2];
#pragma unroll
  for (int t = 0; t < 2; ++t) { re[t] = mfma_h(X[t], P1re[t], z4); im[t] = mfma_h(X[t], P1im[t], z4); }
  const u32 mx = max64u(max(max(mxv[0], mxv[1]), max(mxv[2], mxv[3])));
  int E = (int)(mx >> 7);
  E = E < 1 ? 1 : (E > 253 ? 253 : E);
  const float sig = __uint_as_float((u32)(254 - E) << 23) * 0.25f;
#pragma unroll
  for (int t = 0; t < 2; ++t) {
    const u4v v = cmul_pack<false>(re[t], im[t], tfr[t], -tfs[t]);
    re[t] = mfma_h(Fre, v, z4); im[t] = mfma_h(Fim, v, z4);
  }
  f4 wr4[2], wi4[2];
#pragma unroll
  for (int t = 0; t < 2; ++t)
#pragma unroll
    for (int r = 0; r < 4; ++r) { wr4[t][r] = bflo(Wv[t][r]); wi4[t][r] = bfhi(Wv[t][r]); }
  if (edge) {
#pragma unroll
    for (int r = 0; r < 4; ++r) {
      wr4[0][r] = (wr4[0][r] + bflo(w2[r])) * 0.5f; wi4[0][r] = (wi4[0][r] + bfhi(w2[r])) * 0.5f;
      if (special) { wr4[1][r] = (wr4[1][r] + bflo(w4[r])) * 0.5f; wi4[1][r] = (wi4[1][r] + bfhi(w4[r])) * 0.5f; }
    }
    if (U == 0 && lane == 0) unscale[0] = __uint_as_float((u32)E << 23);
  }
#pragma unroll
  for (int t = 0; t < 2; ++t) {
    const u4v v = cmul_pack<true>(re[t], im[t], wr4[t] * sig, wi4[t] * sig);
    im[t] = mfma_h(v, Fre, z4); re[t] = mfma_h(v, Fim, z4);
  }
#pragma unroll
  for (int t = 0; t < 2; ++t) {
    const u4v v = cmul_pack<true>(re[t], im[t], tir[t], tis[t]);
    im[t] = mfma_h(P4re[t], v, z4); re[t] = mfma_h(P4im[t], v, z4);
  }
  if (special) {
    const f4 yr = re[0], yi = im[0], zr = re[1], zi = im[1];
    re[0] = yr + zr; im[0] = yi + zi;
    re[1] = yr - zr; im[1] = zi - yi;
  }
#pragma unroll
  for (int r = 0; r < 4; ++r) *(u2v*)(smem + loff + r * 1024) = u2v{pk_h(re[0][r], im[0][r]), pk_h(re[1][r], im[1][r])};
  asm volatile("s_waitcnt lgkmcnt(0)" ::: "memory");
  __builtin_amdgcn_s_barrier();
  asm volatile("" ::: "memory");
  const __amdgpu_buffer_rsrc_t rs = ws_rsrc(ws);
#pragma unroll
  for (int i = 0; i < 2; ++i) {
    const u4v o = *(const u4v*)(smem + (wave * 2 + i) * 1024 + lane * 16);
    st16(rs, (u32)WS_TP + P * 16384 + (wave * 2 + i) * 1024 + (lane & ~3) * 16 + ((lane & 3) ^ ((lane >> 4) & 3)) * 16, o);
  }
  { char* scratch = smem + 49152; if (wave == 0) CODE_TOUCH(k3_kernel, g1_kernel, 0) }
  WAITV(0);
}

extern "C" __global__ __launch_bounds__(256, 2) void k3_kernel() {
  LOAD_ARGS();
  char* ws = ap_->ws; const u16* x = ap_->x; const u16* w_sp = ap_->w_sp;
  PINS(x); PINS(ws); PINS(w_sp);
  const u32* Tp = (const u32*)(ws + WS_TP);
  constexpr int K3_Y = 10752, K3_CB = K3_Y + 10240;
  __shared__ __attribute__((aligned(16))) char smem[K3_CB + 8192 + 512];
  const int tid = threadIdx.x, lane = tid & 63;
  const int wave = __builtin_amdgcn_readfirstlane(tid >> 6);
  const int li = lane & 15, g = lane >> 4;
  const int b = (blockIdx.x & 7) * 32 + (blockIdx.x >> 3);
#pragma unroll
  for (int i = 0; i < 2; ++i) {
    int U = (wave * 2 + i) * 64 + lane;
    U = U < 480 ? U : 479;
    GLDS16(Tp + (size_t)(U >> 2) * 4096 + b * 16 + (U & 3) * 4, smem + (wave * 2 + i) * 1024);
  }
#pragma unroll
  for (int it = 0; it < 4; ++it) {
    const int i = it < 2 ? wave * 2 + it : 6 + it;
    if (it < 2 || wave == 3) {
      const int k2 = (lane >> 2) + 16 * (4 * i + (lane & 3));
      const int pos = k2 <= 320 ? 2 * k2 : 2 * (640 - k2) + 1;
      GLDS4(Tp + (size_t)(120 + (pos >> 4)) * 4096 + b * 16 + (pos & 15), smem + 8192 + i * 256);
    }
  }
  char* scratch = smem + K3_CB + 8192;
  if (wave == 0) GLDS4(ws + WS_UNS, scratch + 256);
  const int a = 2 * wave + (lane >> 5), j = lane & 31;
  const int row = 256 * a + b;
  const u16* xr = x + (size_t)row * D;
  u4v x0 = *(const u4v*)(xr + 8 * j);
  u4v x1 = *(const u4v*)(xr + 256 + 8 * j);
  u2v x2 = *(const u2v*)(xr + 512 + 4 * j);
  char* cb = smem + K3_CB;
  const int t = wave;
  u4v Bre[3], Bim[3];
  if (wave < 3) {
#pragma unroll
    for (int tk = 0; tk < 3; ++tk) {
      const float sc = (tk < 2 || g < 2) ? 1.f : 0.f;
#pragma unroll
      for (int jj = 0; jj < 4; ++jj) {
        const float ang = (float)((16 * tk + 4 * g + jj) * (16 * t + li)) * (1.0f / 40.0f);
        const float c = cosr(ang) * sc, s = sinr(ang) * sc;
        Bre[tk][jj] = pk_h(c, -s);
        Bim[tk][jj] = pk_h(s, c);
      }
      PIN(Bre[tk]); PIN(Bim[tk]);
    }
  } else {
#pragma unroll
    for (int tt = 0; tt < 3; ++tt) {
      f4 twr, twi;
#pragma unroll
      for (int jj = 0; jj < 4; ++jj) {
        const float ang = (float)((16 * tt + li) * (4 * g + jj)) * (1.0f / 640.0f);
        twr[jj] = cosr(ang) * 0.125f; twi[jj] = sinr(ang) * 0.125f;
      }
      *(f4*)(cb + tt * 2048 + lane * 16) = twr;
      *(f4*)(cb + tt * 2048 + 1024 + lane * 16) = twi;
    }
    u4v gre, gim;
#pragma unroll
    for (int jj = 0; jj < 4; ++jj) {
      const float ang = (float)(li * (4 * g + jj)) * 0.0625f;
      const float c = cosr(ang), s = sinr(ang);
      gre[jj] = pk_h(c, -s); gim[jj] = pk_h(s, c);
    }
    *(u4v*)(cb + 6144 + lane * 16) = gre;
    *(u4v*)(cb + 7168 + lane * 16) = gim;
  }
  asm volatile("s_waitcnt vmcnt(0)" ::: "memory");
  PIN(x0); PIN(x1); PIN(x2);
  __syncthreads();
  char* yb = smem + K3_Y;
  if (wave < 3) {
    const f4 z4 = f4{0, 0, 0, 0};
    u4v af[4][3];
#pragma unroll
    for (int q = 0; q < 4; ++q) {
      const char* base = smem + (q == 0 ? 8192 : (q - 1) * 2560);
#pragma unroll
      for (int tk = 0; tk < 3; ++tk) af[q][tk] = *(const u4v*)(base + (tk < 2 ? (64 * tk + lane) : (128 + (lane & 31))) * 16);
    }
    const f4 twr = *(const f4*)(cb + t * 2048 + lane * 16), twi = *(const f4*)(cb + t * 2048 + 1024 + lane * 16);
    const u4v Gre = *(const u4v*)(cb + 6144 + lane * 16), Gim = *(const u4v*)(cb + 7168 + lane * 16);
    const float usc = *(const float*)(scratch + 256);
    SB();
    f4 vre[4], vim[4];
#pragma unroll
    for (int q = 0; q < 4; ++q) {
      f4 re = z4, im = z4;
#pragma unroll
      for (int tk = 0; tk < 3; ++tk) {
        re = mfma_h(af[q][tk], Bre[tk], re);
        im = mfma_h(af[q][tk], Bim[tk], im);
      }
      vre[q] = re; vim[q] = im;
    }
#pragma unroll
    for (int q = 0; q < 4; ++q) {
      const u4v v = cmul_pack<false>(vre[q], vim[q], twr, twi);
      vre[q] = mfma_h(v, Gre, z4);
      vim[q] = mfma_h(v, Gim, z4);
    }
    const float fs = usc * (2.0f * 1024.0f * 128.0f / (2048.0f * 640.0f));
    const float fh = fs * 0.70710678118f;
    const f4 E = (vre[0] + vim[0]) * fs, O = (vre[0] - vim[0]) * fs;
    const f4 s13 = vre[1] + vre[3], dx = vre[1] - vre[3], sy = vim[1] + vim[3], dy = vim[3] - vim[1];
    const f4 Ep = E + vre[2] * fs, Em = E - vre[2] * fs, Om = O - vim[2] * fs, Op = O + vim[2] * fs;
    const f4 A = dx - sy, B = dx + sy;
    f4 y[8];
    y[0] = Ep + s13 * fs; y[4] = Ep - s13 * fs; y[2] = Em + dy * fs; y[6] = Em - dy * fs;
    y[1] = Om + A * fh; y[5] = Om - A * fh; y[3] = Op - B * fh; y[7] = Op + B * fh;
    if (t < 2 || lane < 32) {
      char* yp = yb + (40 * li + 16 * t + 4 * g) * 2;
#pragma unroll
      for (int aa = 0; aa < 8; ++aa) *(u2v*)(yp + aa * 1280) = u2v{pk_bf(y[aa][0], y[aa][1]), pk_bf(y[aa][2], y[aa][3])};
    }
  }
  else {
    CODE_TOUCH(g1_kernel, g2_kernel, (blockIdx.x >> 3) & 1)
#pragma unroll
    for (int i = 0; i < 2; ++i) touch((const char*)w_sp + ((size_t)(512 + i * 256 + blockIdx.x) * 64 + lane) * 128);
  }
  asm volatile("s_waitcnt lgkmcnt(0)" ::: "memory");
  __builtin_amdgcn_s_barrier();
  asm volatile("" ::: "memory");
  {
    const __amdgpu_buffer_rsrc_t rs = ws_rsrc(ws);
    const u4v y0 = *(const u4v*)(yb + a * 1280 + 16 * j);
    const u4v y1 = *(const u4v*)(yb + a * 1280 + 512 + 16 * j);
    const u2v y2 = *(const u2v*)(yb + a * 1280 + 1024 + 8 * j);
    u32 hx[10];
#pragma unroll
    for (int i = 0; i < 4; ++i) {
      hx[i] = pk_bf(bflo(x0[i]) + bflo(y0[i]), bfhi(x0[i]) + bfhi(y0[i]));
      hx[4 + i] = pk_bf(bflo(x1[i]) + bflo(y1[i]), bfhi(x1[i]) + bfhi(y1[i]));
    }
    hx[8] = pk_bf(bflo(x2[0]) + bflo(y2[0]), bfhi(x2[0]) + bfhi(y2[0]));
    hx[9] = pk_bf(bflo(x2[1]) + bflo(y2[1]), bfhi(x2[1]) + bfhi(y2[1]));
    auto pairup = [&](u32 v0, u32 v1) {
      return u4v{v0, v1, (u32)__builtin_amdgcn_update_dpp(0, (int)v0, 0xB1, 0xf, 0xf, true), (u32)__builtin_amdgcn_update_dpp(0, (int)v1, 0xB1, 0xf, 0xf, true)};
    };
    const u32 drop = (lane & 1) ? DROP : 0u;
    const u32 ho = (u32)WS_H + row * (D * 2);
    st16(rs, ho + 16 * j, u4v{hx[0], hx[1], hx[2], hx[3]});
    st16(rs, ho + 512 + 16 * j, u4v{hx[4], hx[5], hx[6], hx[7]});
    st16(rs, (ho + 1024 + 8 * j) | drop, pairup(hx[8], hx[9]));
    f2 v[10];
#pragma unroll
    for (int i = 0; i < 10; ++i) v[i] = f2{bflo(hx[i]), bfhi(hx[i])};
    ln_row(v);
#pragma unroll
    for (int i = 0; i < 10; ++i) hx[i] = pk_bf(v[i][0], v[i][1]);
    const u32 lo = (u32)WS_L2 + (row >> 4) * 20480 + (row & 15) * 64;
    st16(rs, lo + (j >> 2) * 1024 + (j & 3) * 16, u4v{hx[0], hx[1], hx[2], hx[3]});
    st16(rs, lo + (8 + (j >> 2)) * 1024 + (j & 3) * 16, u4v{hx[4], hx[5], hx[6], hx[7]});
    st16(rs, (lo + (16 + (j >> 3)) * 1024 + (j & 7) * 8) | drop, pairup(hx[8], hx[9]));
  }
  WAITV(0);
}

constexpr int FPD = 2;
#include <type_traits>
typedef unsigned long long u64;
typedef __attribute__((address_space(1))) unsigned long long gu64;
#define WAITL0() do { __builtin_amdgcn_sched_barrier(0); asm volatile("s_waitcnt lgkmcnt(0)" ::: "memory"); __builtin_amdgcn_sched_barrier(0); } while (0)
#define BAR() do { asm volatile("" ::: "memory"); __builtin_amdgcn_sched_barrier(0); __builtin_amdgcn_s_barrier(); __builtin_amdgcn_sched_barrier(0); asm volatile("" ::: "memory"); } while (0)
#define SGB(mask, n) __builtin_amdgcn_sched_group_barrier(mask, n, 0)
DI f4 mfma_b(u4v a, u4v b, f4 c) {
  return __builtin_amdgcn_mfma_f32_16x16x32_bf16(__builtin_bit_cast(bf8, a), __builtin_bit_cast(bf8, b), c, 0, 0, 0);
}
DI f2 gelu2(f2 x) {
  const float k1 = -2.0f * 0.7978845608028654f * 1.4426950408889634f;
  const float k2 = k1 * 0.044715f;
  f2 p = x * x * k2 + k1;
  f2 a = x * p;
  f2 e = f2{__builtin_amdgcn_exp2f(a[0]), __builtin_amdgcn_exp2f(a[1])} + 1.0f;
  f2 r = f2{__builtin_amdgcn_rcpf(e[0]), __builtin_amdgcn_rcpf(e[1])};
  return x * r;
}
DI u2v gelu4(f4 a) {
  f2 g0 = gelu2(f2{a[0], a[1]}), g1 = gelu2(f2{a[2], a[3]});
  return u2v{pk_bf(g0[0], g0[1]), pk_bf(g1[0], g1[1])};
}
template <int N> DI void waitv() { asm volatile("s_waitcnt vmcnt(%0)" ::"n"(N) : "memory"); }

constexpr int G1_DEPTH = 3, G1_AD = 5;
extern "C" __global__ __launch_bounds__(512) void g1_kernel() {
  LOAD_ARGS();
  char* ws = ap_->ws; const u16* w_sp = ap_->w_sp; const u16* w_out = ap_->w_out;
  PINS(ws); PINS(w_sp); PINS(w_out);
  const u32* l2f = (const u32*)(ws + WS_L2); const u32* Wt1 = (const u32*)(ws + WS_WT1);
  u64* stats = (u64*)(ws + WS_STATS);
  __shared__ __attribute__((aligned(16))) char smem[140 * 1024 + 4 * 4608 + 256];
  const int tid = threadIdx.x, lane = tid & 63;
  const int wave = __builtin_amdgcn_readfirstlane(tid >> 6);
  const int li = lane & 15, g = lane >> 4;
  const int xcd = blockIdx.x & 7, idx = blockIdx.x >> 3;
  const int mt = (xcd >> 1) * 4 + (idx >> 3), nb = (xcd & 1) * 8 + (idx & 7);
  const int row0 = 1024 * (mt & 1) + 32 * (mt >> 1);
  const int zs = nb < 6 ? 4 * nb : 24 + 3 * (nb - 6);
  const int z1s = nb < 6 ? 3 * nb : (nb < 12 ? 18 + 4 * (nb - 6) : 42 + 3 * (nb - 12));
  const int type = nb < 6 ? 0 : (nb < 12 ? 1 : 2);
  if (wave >= 4) {
    const int lw = wave - 4;
    auto slot_frag = [&](int s) { return s < 3 ? 54 + zs + s : (s == 3 ? (type == 0 ? 54 + zs + 3 : (type == 1 ? z1s + 3 : z1s)) : z1s + s - 4); };
    const u32* bsrc[4];
#pragma unroll
    for (int j = 0; j < 4; ++j) {
      int i = lw + 4 * j; i = i < 14 ? i : 0;
      const int kk = i / 7, s = i - 7 * kk;
      bsrc[j] = Wt1 + (size_t)(kk * 108 + slot_frag(s)) * 256 + lane * 4;
    }
    auto stage = [&](int kt) {
      char* sb = smem + kt * 14336 + lw * 1024;
#pragma unroll
      for (int j = 0; j < 3; ++j) GLDS16(bsrc[j] + (size_t)kt * 2 * 108 * 256, sb + j * 4096);
      if (lw < 2) GLDS16(bsrc[3] + (size_t)kt * 2 * 108 * 256, sb + 3 * 4096);
    };
#pragma unroll
    for (int s = 0; s < G1_DEPTH; ++s) stage(s);
    for (int kt = 0; kt < 10 - G1_DEPTH; ++kt) {
      if (lw < 2) waitv<4 * (G1_DEPTH - 1)>(); else waitv<3 * (G1_DEPTH - 1)>();
      BAR();
      stage(kt + G1_DEPTH);
    }
    if (lw < 2) waitv<8>(); else waitv<6>(); BAR();
    if (lw < 2) waitv<4>(); else waitv<3>(); BAR();
    waitv<0>();
    char* scratch = smem + 140 * 1024 + 4 * 4608;
    if (lw < 2) touch((const char*)w_sp + ((size_t)(blockIdx.x * 2 + lw) * 64 + lane) * 128);
    if (lw >= 2) {
      CODE_TOUCH(g2_kernel, end_kernel, lw - 2)
      const int task = (lw - 2) * 256 + blockIdx.x;
      BAR();
      if (task < NRT3) retile<1, D, 10, NRT3, true>(w_out, ws_rsrc(ws), (u32)WS_WT3, task, 0, smem + lw * 4096, lane);
    } else {
      BAR();
    }
    waitv<0>();
    return;
  }
  const int rg0 = (row0 + wave * 256) >> 4;
  const u32* asrc = l2f + (size_t)rg0 * 20 * 256 + li * 16 + g * 4;
  u4v fa[G1_AD][2];
#pragma unroll
  for (int k = 0; k < G1_AD; ++k) { fa[k][0] = *(const u4v*)(asrc + k * 256); fa[k][1] = *(const u4v*)(asrc + (20 + k) * 256); __builtin_amdgcn_sched_barrier(0); }
  f4 acc[7][2];
#pragma unroll
  for (int s = 0; s < 7; ++s) { acc[s][0] = f4{0, 0, 0, 0}; acc[s][1] = f4{0, 0, 0, 0}; }
  u4v fw[2][7];
  const char* wb = smem + lane * 16;
  auto rd = [&](int k) {
#pragma unroll
    for (int s = 0; s < 7; ++s) fw[k & 1][s] = *(const u4v*)(wb + (k * 7 + s) * 1024);
  };
  auto mm = [&](int k) {
#pragma unroll
    for (int s = 0; s < 7; ++s) {
      acc[s][0] = mfma_b(fw[k & 1][s], fa[k % G1_AD][0], acc[s][0]);
      acc[s][1] = mfma_b(fw[k & 1][s], fa[k % G1_AD][1], acc[s][1]);
    }
    if (k + G1_AD < 20) { fa[k % G1_AD][0] = *(const u4v*)(asrc + (k + G1_AD) * 256); fa[k % G1_AD][1] = *(const u4v*)(asrc + (20 + G1_AD + k) * 256); }
  };
  BAR(); rd(0);
#pragma unroll
  for (int k = 1; k < 20; ++k) {
    if (!(k & 1)) BAR(); else __builtin_amdgcn_sched_barrier(0);
    rd(k);
    mm(k - 1);
#pragma unroll
    for (int i = 0; i < 7; ++i) { SGB(0x100, 1); SGB(0x8, 2); }
  }
  __builtin_amdgcn_sched_barrier(0);
  mm(19);
  const int n2 = type == 0 ? 4 : 3;
  f4 zv[4][2];
  u2v z3[2];
  float lsum[2], lm2[2];
#pragma unroll
  for (int sf = 0; sf < 2; ++sf) {
    f2 s = f2{0.f, 0.f};
#pragma unroll
    for (int j = 0; j < 4; ++j) {
      const u2v t = gelu4(acc[j][sf]);
      if (j == 3) z3[sf] = t;
      zv[j][sf] = f4{bflo(t[0]), bfhi(t[0]), bflo(t[1]), bfhi(t[1])};
      if (j < 3 || type == 0) s += f2{zv[j][sf][0], zv[j][sf][1]} + f2{zv[j][sf][2], zv[j][sf][3]};
    }
    const float st = xsum_g(s[0] + s[1]);
    const float lmean = st * (type == 0 ? (1.0f / 64.0f) : (1.0f / 48.0f));
    f2 q = f2{0.f, 0.f};
#pragma unroll
    for (int j = 0; j < 4; ++j) {
      const f4 d = zv[j][sf] - lmean;
      if (j < 3 || type == 0) { q += f2{d[0], d[1]} * f2{d[0], d[1]}; q += f2{d[2], d[3]} * f2{d[2], d[3]}; }
    }
    lsum[sf] = st; lm2[sf] = xsum_g(q[0] + q[1]);
  }
  gu64* st = (gu64*)stats + (size_t)mt * 16 * 128 + wave * 32;
  if (lane < 32) {
    u32 a = __float_as_uint(g ? lsum[1] : lsum[0]), b = __float_as_uint(g ? lm2[1] : lm2[0]);
    if (a == ~0u) a = 0x7fc00000u;
    __hip_atomic_store(st + nb * 128 + lane, ((u64)b << 32) | a, __ATOMIC_RELAXED, __HIP_MEMORY_SCOPE_AGENT);
  }
  __builtin_amdgcn_sched_barrier(0);
  const __amdgpu_buffer_rsrc_t rs = ws_rsrc(ws);
  u4v z1o[3];
#pragma unroll
  for (int j = 0; j < 3; ++j) {
    const u2v a = gelu4(acc[4 + j][0]), b = gelu4(acc[4 + j][1]);
    z1o[j] = u4v{a[0], a[1], b[0], b[1]};
    PIN(z1o[j]);
  }
  __builtin_amdgcn_sched_barrier(0);
  u64 gv[2][4];
  for (int spin = 0; spin < (1 << 16); ++spin) {
    bool ok = true;
#pragma unroll
    for (int sf = 0; sf < 2; ++sf)
#pragma unroll
      for (int i = 0; i < 4; ++i) {
        gv[sf][i] = __hip_atomic_load(st + (4 * g + i) * 128 + sf * 16 + li, __ATOMIC_RELAXED, __HIP_MEMORY_SCOPE_AGENT);
        ok &= ((u32)gv[sf][i] != ~0u);
      }
    if (__all(ok)) break;
    __builtin_amdgcn_s_sleep(1);
  }
  {
    const u32 zo = (u32)WS_Z1 + ((rg0 >> 1) * 54 + z1s) * 1024 + lane * 16;
#pragma unroll
    for (int j = 0; j < 3; ++j) st16(rs, zo + j * 1024, z1o[j]);
    if (type == 1) st16(rs, zo + 3 * 1024, u4v{z3[0][0], z3[0][1], z3[1][0], z3[1][1]});
  }
  char* tb = smem + 140 * 1024 + wave * 4608;
#pragma unroll
  for (int sf = 0; sf < 2; ++sf) {
    float s = 0.f;
#pragma unroll
    for (int i = 0; i < 4; ++i) s += __uint_as_float((u32)gv[sf][i]);
    const float mean = xsum_g(s) * (1.0f / 864.0f);
    float q = 0.f;
#pragma unroll
    for (int i = 0; i < 4; ++i) {
      const bool big = (4 * g + i) < 6;
      const float dm = __uint_as_float((u32)gv[sf][i]) * (big ? (1.0f / 64.0f) : (1.0f / 48.0f)) - mean;
      q += __uint_as_float((u32)(gv[sf][i] >> 32)) + (big ? 64.f : 48.f) * dm * dm;
    }
    const float var = xsum_g(q) * (1.0f / 864.0f);
    const float mb = rbf(mean);
    const float rinv = __builtin_amdgcn_rcpf(rbf(__builtin_amdgcn_sqrtf(rbf(rbf(var) + 1e-5f))));
#pragma unroll
    for (int j = 0; j < 4; ++j) {
      const f4 o = (zv[j][sf] - mb) * rinv;
      *(u2v*)(tb + (sf * 16 + li) * 144 + j * 32 + g * 8) = u2v{pk_bf(o[0], o[1]), pk_bf(o[2], o[3])};
    }
  }
  {
    const int q = li >> 2, p = li & 3;
#pragma unroll
    for (int j = 0; j < 4; ++j) {
      s4 lo = __builtin_amdgcn_ds_read_tr16_b64_v4i16((LDS_AS s4*)(tb + (8 * g + q) * 144 + j * 32 + 8 * p));
      s4 hi = __builtin_amdgcn_ds_read_tr16_b64_v4i16((LDS_AS s4*)(tb + (8 * g + 4 + q) * 144 + j * 32 + 8 * p));
      u2v a = __builtin_bit_cast(u2v, lo), b = __builtin_bit_cast(u2v, hi);
      if (j < n2) st16(rs, (u32)WS_Z2 + (((zs + j) * 16 + li) * S + row0 + wave * 256 + 8 * g) * 2, u4v{a[0], a[1], b[0], b[1]});
    }
  }
}

typedef __attribute__((address_space(1))) unsigned int gu32;
constexpr int G2_NST = 7, G2_STB = 22 * 1024;
extern "C" __global__ __launch_bounds__(512) void g2_kernel() {
  LOAD_ARGS();
  char* ws = ap_->ws; const u16* w_sp = ap_->w_sp; const u16* b_sp = ap_->b_sp; u16* out = ap_->out;
  PINS(w_sp); PINS(b_sp); PINS(ws); PINS(out);
  const u16* z2t = (const u16*)(ws + WS_Z2); const u32* z1f = (const u32*)(ws + WS_Z1);
  __shared__ __attribute__((aligned(16))) char smem[G2_NST * G2_STB + 256];
  const int tid = threadIdx.x, lane = tid & 63;
  const int wave = __builtin_amdgcn_readfirstlane(tid >> 6);
  const int li = lane & 15, g = lane >> 4;
  const int xcd = blockIdx.x & 7, idx = blockIdx.x >> 3;
  const int tt = (xcd >> 1) * 8 + (idx >> 2), ft = (xcd & 1) * 4 + (idx & 3);
  if (wave >= 4) {
    const int lw = wave - 4;
    const u16* src[6];
    const int swz = ((lane & 7) ^ (lane >> 3)) * 8;
#pragma unroll
    for (int j = 0; j < 6; ++j) {
      int i = lw + 4 * j; i = i < 22 ? i : 8;
      if (j < 2) src[j] = w_sp + (size_t)(tt * 64 + 8 * i + (lane >> 3)) * S + swz;
      else { int r = ft * 112 + 8 * (i - 8) + (lane >> 3); r = r < FH ? r : FH - 1; src[j] = z2t + (size_t)r * S + swz; }
    }
    auto stage = [&](int kt, int buf) {
      char* sb = smem + buf * G2_STB + lw * 1024;
      const int kk = kt * 64;
#pragma unroll
      for (int j = 0; j < 5; ++j) GLDS16(src[j] + kk, sb + j * 4096);
      if (lw < 2) GLDS16(src[5] + kk, sb + 5 * 4096);
    };
    const bool big = lw < 2;
    stage(0, 0); stage(1, 1); stage(2, 2);
    if (big) waitv<12>(); else waitv<10>(); BAR();
    stage(3, 3); stage(4, 4);
    if (big) waitv<18>(); else waitv<15>(); BAR();
    stage(5, 5); stage(6, 6);
    if (big) waitv<24>(); else waitv<20>(); BAR();
    stage(7, 0); stage(8, 1);
    int nbuf = 2;
    for (int kt = 3; kt < 26; ++kt) {
      if (big) waitv<30>(); else waitv<25>();
      BAR();
      stage(kt + 6, nbuf);
      nbuf = nbuf + 1 == G2_NST ? 0 : nbuf + 1;
    }
    const u32* wsrc = (const u32*)(ws + WS_WT3) + (size_t)(ft * 5) * 256 + lane * 4;
    for (int ps = 0; ps < 6; ++ps) {
      if (big) waitv<30>(); else waitv<25>();
      BAR();
      char* sb = smem + nbuf * G2_STB + lw * 1024;
#pragma unroll
      for (int j = 0; j < 6; ++j) {
        const int i = ps * 22 + lw + 4 * j, k = i / 5;
        if (j < 5 || big) GLDS16(wsrc + (size_t)(k * 35 + i) * 256, sb + j * 4096);
      }
      nbuf = nbuf + 1 == G2_NST ? 0 : nbuf + 1;
    }
    BAR();
    if (lw < 3) GLDS16(wsrc + (size_t)(26 * 35 + 132 + lw) * 256, smem + nbuf * G2_STB + lw * 1024);
    waitv<0>();
    BAR();
    return;
  }
  const int wt = wave & 1, wf = wave >> 1;
  const int n0 = (ft & 1) ? 3 : 4;
  const int f0 = wf ? n0 : 0, nf = wf ? 7 - n0 : n0;
  u4v z1v[4];
  float bias[2];
#pragma unroll
  for (int j = 0; j < 4; ++j) {
    int ff = ft * 7 + f0 + j; ff = ff < 54 ? ff : 53;
    z1v[j] = *(const u4v*)(z1f + ((size_t)(tt * 2 + wt) * 54 + ff) * 256 + lane * 4);
  }
  bias[0] = bflo(b_sp[tt * 64 + wt * 32 + li]); bias[1] = bflo(b_sp[tt * 64 + wt * 32 + 16 + li]);
  f4 acc[4][2];
#pragma unroll
  for (int j = 0; j < 4; ++j) { acc[j][0] = f4{0, 0, 0, 0}; acc[j][1] = f4{0, 0, 0, 0}; }
  const int sw = (g ^ (li & 7)) * 16;
  const int toff = (wt * 32 + li) * 128 + sw;
  const int foff = 8192 + (f0 * 16 + li) * 128 + sw;
  auto body = [&](auto NFc) {
    constexpr int NF = decltype(NFc)::value;
    u4v fb[2][2][2], fa[2][2][NF];
    auto rd = [&](int set, int buf) {
      const char* sb = smem + buf * G2_STB;
#pragma unroll
      for (int kk = 0; kk < 2; ++kk) {
        fb[set][kk][0] = *(const u4v*)(sb + (toff ^ (kk * 64)));
        fb[set][kk][1] = *(const u4v*)(sb + (toff ^ (kk * 64)) + 2048);
#pragma unroll
        for (int j = 0; j < NF; ++j) fa[set][kk][j] = *(const u4v*)(sb + (foff ^ (kk * 64)) + j * 2048);
      }
    };
    auto mm = [&](int set) {
#pragma unroll
      for (int kk = 0; kk < 2; ++kk)
#pragma unroll
        for (int j = 0; j < NF; ++j) {
          acc[j][0] = mfma_b(fa[set][kk][j], fb[set][kk][0], acc[j][0]);
          acc[j][1] = mfma_b(fa[set][kk][j], fb[set][kk][1], acc[j][1]);
        }
    };
    auto sched = [&]() {
#pragma unroll
      for (int i = 0; i < 2 * NF + 4; ++i) { SGB(0x100, 1); SGB(0x8, 1); }
      SGB(0x8, 2 * NF - 4);
    };
    BAR(); rd(0, 0); WAITL0();
    int buf = 1;
    for (int kt = 1; kt < 31; kt += 2) {
      BAR(); rd(1, buf); mm(0); sched(); WAITL0();
      buf = buf + 1 == G2_NST ? 0 : buf + 1;
      BAR(); rd(0, buf); mm(1); sched(); WAITL0();
      buf = buf + 1 == G2_NST ? 0 : buf + 1;
    }
    BAR(); rd(1, buf); mm(0); sched(); WAITL0();
#pragma unroll
    for (int j = 0; j < 4; ++j) PIN(z1v[j]);
    PIN(bias[0]); PIN(bias[1]);
    BAR();
    mm(1);
  };
  if (nf == 4) body(std::integral_constant<int, 4>{}); else body(std::integral_constant<int, 3>{});
  const __amdgpu_buffer_rsrc_t rs = ws_rsrc(ws);
  const int ff0 = ft * 7 + f0;
#pragma unroll
  for (int tf = 0; tf < 2; ++tf) {
    const int tfr = tt * 4 + wt * 2 + tf;
    u2v o[4];
#pragma unroll
    for (int j = 0; j < 4; ++j) {
      const u32 za = z1v[j][2 * tf], zb = z1v[j][2 * tf + 1];
      const f4 v = (acc[j][tf] + bias[tf]) * f4{bflo(za), bfhi(za), bflo(zb), bfhi(zb)};
      o[j] = u2v{pk_bf(v[0], v[1]), pk_bf(v[2], v[3])};
    }
    const u32 base = (u32)WS_P + (tfr * 27 + (ff0 >> 1)) * 1024 + lane * 16;
    if (!(ff0 & 1)) {
      st16(rs, base, u4v{o[0][0], o[0][1], o[1][0], o[1][1]});
      if (nf == 4) { if (ff0 + 3 < 54) st16(rs, base + 1024, u4v{o[2][0], o[2][1], o[3][0], o[3][1]}); }
      else st8(rs, base + 1024, o[2]);
    } else {
      st8(rs, base + 8, o[0]);
      st16(rs, base + 1024, u4v{o[1][0], o[1][1], o[2][0], o[2][1]});
    }
  }
  gu32* cnt = (gu32*)(ws + WS_CNT);
  waitv<0>();
  if (lane == 0) __hip_atomic_store(cnt + (tt * 2 + wt) * 32 + ft * 2 + wf, 1u, __ATOMIC_RELAXED, __HIP_MEMORY_SCOPE_AGENT);
  const int trow = tt * 64 + wave * 16 + li;
  const u16* h = (const u16*)(ws + WS_H);
  u2v hv[5];
#pragma unroll
  for (int j = 0; j < 5; ++j) hv[j] = *(const u2v*)(h + (size_t)trow * D + ft * 80 + j * 16 + g * 4);
  for (int spin = 0; spin < (1 << 16); ++spin) {
    const u32 c = __hip_atomic_load(cnt + (tt * 2 + (wave >> 1)) * 32 + li, __ATOMIC_RELAXED, __HIP_MEMORY_SCOPE_AGENT);
    if (__all(c != 0u)) break;
    __builtin_amdgcn_s_sleep(1);
  }
  asm volatile("" ::: "memory");
  u4v fb[27];
  const u32 poff = (u32)WS_P + (tt * 4 + wave) * 27 * 1024 + lane * 16;
#pragma unroll
  for (int k = 0; k < 3 * FPD; ++k) { fb[k] = __builtin_amdgcn_raw_buffer_load_b128(rs, poff + k * 1024, 0, AUX_SC1); SB(); }
  f4 acc3[5];
#pragma unroll
  for (int j = 0; j < 5; ++j) acc3[j] = f4{0, 0, 0, 0};
  u4v fa[2][15];
  u32 wo[3] = {(u32)lane * 16, (u32)lane * 16 + 3 * G2_STB - 8192, (u32)lane * 16 + 5 * G2_STB};
  PIN(wo[1]); PIN(wo[2]);
  auto rd3 = [&](int kt) {
#pragma unroll
    for (int ii = 0; ii < 15; ++ii) {
      const int i = kt * 15 + ii;
      const int off = ((32 + i / 22) % G2_NST) * G2_STB + (i % 22) * 1024;
      const int w = off < 3 * G2_STB - 8192 ? 0 : (off < 5 * G2_STB ? 1 : 2);
      fa[kt & 1][ii] = *(const u4v*)(smem + wo[w] + (off - (w == 0 ? 0 : (w == 1 ? 3 * G2_STB - 8192 : 5 * G2_STB))));
    }
  };
  auto mm3 = [&](int kt) {
#pragma unroll
    for (int kk = 0; kk < 3; ++kk)
#pragma unroll
      for (int j = 0; j < 5; ++j) acc3[j] = mfma_b(fa[kt & 1][kk * 5 + j], fb[kt * 3 + kk], acc3[j]);
  };
  BAR();
  rd3(0);
#pragma unroll
  for (int kt = 1; kt < 9; ++kt) {
    SB();
    if (kt - 1 + FPD < 9) {
#pragma unroll
      for (int k = 0; k < 3; ++k) fb[(kt - 1 + FPD) * 3 + k] = __builtin_amdgcn_raw_buffer_load_b128(rs, poff + ((kt - 1 + FPD) * 3 + k) * 1024, 0, AUX_SC1);
      SB();
    }
    rd3(kt);
    mm3(kt - 1);
#pragma unroll
    for (int i = 0; i < 15; ++i) { SGB(0x100, 1); SGB(0x8, 1); }
  }
  SB();
  mm3(8);
  {
    u2v o[5];
#pragma unroll
    for (int j = 0; j < 5; ++j) {
      u32 t0 = pk_bf(acc3[j][0], acc3[j][1]), t1 = pk_bf(acc3[j][2], acc3[j][3]);
      o[j] = u2v{pk_bf(bflo(t0) + bflo(hv[j][0]), bfhi(t0) + bfhi(hv[j][0])), pk_bf(bflo(t1) + bflo(hv[j][1]), bfhi(t1) + bfhi(hv[j][1]))};
    }
    const __amdgpu_buffer_rsrc_t ro = __builtin_amdgcn_make_buffer_rsrc((void*)out, 0, S * D * 2, 0x00020000);
    const u32 oo = (trow * D + ft * 80) * 2;
#pragma unroll
    for (int j = 0; j < 4; j += 2) {
      auto s0 = __builtin_amdgcn_permlane16_swap(o[j][0], o[j + 1][0], false, false);
      auto s1 = __builtin_amdgcn_permlane16_swap(o[j][1], o[j + 1][1], false, false);
      __builtin_amdgcn_raw_buffer_store_b128(u4v{s0[0], s1[0], s0[1], s1[1]}, ro, oo + (j + (g & 1)) * 32 + (g >> 1) * 16, 0, 0);
    }
    __builtin_amdgcn_raw_buffer_store_b64(o[4], ro, oo + 128 + g * 8, 0, 0);
  }
}

extern "C" __global__ void end_kernel() {}


extern "C" void kernel_launch(void* const* d_in, const int* in_sizes, int n_in, void* d_out, int out_size, void* d_ws,
                              size_t ws_size, hipStream_t stream) {
  const u16* x = (const u16*)d_in[0];
  const u16* wr = (const u16*)d_in[1];
  const u16* wi = (const u16*)d_in[2];
  const u16* w_in = (const u16*)d_in[4];
  const u16* w_sp = (const u16*)d_in[5];
  const u16* b_sp = (const u16*)d_in[6];
  const u16* w_out = (const u16*)d_in[7];
  (void)hipFuncSetAttribute((const void*)k2_kernel, hipFuncAttributeMaxDynamicSharedMemorySize, 84 * 1024);
  k1_kernel<<<256, 512, 0, stream>>>(x, wr, wi, (char*)d_ws, w_sp, w_in, w_out, b_sp, (u16*)d_out);
  k2_kernel<<<K2_FFT_BLOCKS + K2_PREP_BLOCKS, 512, 84 * 1024, stream>>>();
  k3_kernel<<<256, 256, 0, stream>>>();
  g1_kernel<<<256, 512, 0, stream>>>();
  g2_kernel<<<256, 512, 0, stream>>>();
}
```
